```python
import torch

def _softmax(x, axis=-1):
    return torch.softmax(x, dim=axis)
def _gelu(x):
    return torch.nn.functional.gelu(x, approximate="tanh")
def _silu(x):
    return torch.nn.functional.silu(x)
def _sigmoid(x):
    return torch.sigmoid(x)
def _layernorm(x, axis=-1, eps=1e-5):
    m = x.mean(dim=axis, keepdim=True)
    v = x.var(dim=axis, keepdim=True, unbiased=False)
    return (x - m) / torch.sqrt(v + eps)
def _scatter_add(tgt, idx, src, axis=0):
    return tgt.clone().index_add_(axis, idx, src)
def _segment_sum(src, seg, n):
    out = torch.zeros((n,) + src.shape[1:], dtype=src.dtype)
    return out.index_add_(0, seg, src)
def _pad(x, pw):
    flat = tuple(v for pair in reversed(pw) for v in pair)
    return torch.nn.functional.pad(x, flat)

kernel_name = 'parallel_sum__sliding_block__dense_activation_gelu__prompt'

def setup_inputs():

    _w = (lambda a: (lambda t: t.to(torch.bfloat16) if t.is_floating_point() else t)(torch.as_tensor(a)))
    def _A_setup():
        B, S, D = 1, 2048, 512
        return {
            "x": _w((lambda *s: torch.randn(*s, dtype=torch.bfloat16) * 0.05)(B, S, D)),
            "wqkv": _w((lambda *s: torch.randn(*s, dtype=torch.bfloat16) * 0.05)(D, 3 * D)),
            "wo": _w((lambda *s: torch.randn(*s, dtype=torch.bfloat16) * 0.05)(D, D)),
        }
    _A_in = _A_setup()

    def _B_setup():
        B, S, D, F = 1, 2048, 512, 1792
        return {
            "x": _w((lambda *s: torch.randn(*s, dtype=torch.bfloat16) * 0.05)(B, S, D)),
            "w1": _w((lambda *s: torch.randn(*s, dtype=torch.bfloat16) * 0.8)(D, F) * (D ** -0.5)),
            "b1": _w((lambda *s: torch.randn(*s, dtype=torch.bfloat16) * 0.8)(F) * 0.01),
            "w2": _w((lambda *s: torch.randn(*s, dtype=torch.bfloat16) * 0.8)(F, D) * (F ** -0.5)),
            "b2": _w((lambda *s: torch.randn(*s, dtype=torch.bfloat16) * 0.8)(D) * 0.01),
        }
    _B_in = _B_setup()

    return {
        "A_x": _A_in["x"],
        "A_wqkv": _A_in["wqkv"],
        "A_wo": _A_in["wo"],
        "B_x": _B_in["x"],
        "B_w1": _B_in["w1"],
        "B_b1": _B_in["b1"],
        "B_w2": _B_in["w2"],
        "B_b2": _B_in["b2"],
    }

def reference(A_x, A_wqkv, A_wo, B_x, B_w1, B_b1, B_w2, B_b2):
    def _A_ref(x, wqkv, wo):
        B, S, D = x.shape
        H = 32; Dh = D // H; W = 256
        qkv = (lambda x, *d: x.permute(*d))((x @ wqkv).reshape(B, S, 3, H, Dh), 2, 0, 3, 1, 4)
        q, k, v = qkv[0], qkv[1], qkv[2]
        scores = torch.einsum("bhqd,bhkd->bhqk", q, k) * (Dh ** -0.5)
        idx = torch.arange(S)
        mask = (idx[:, None] - idx[None, :] >= 0) & (idx[:, None] - idx[None, :] < W)
        scores = torch.where(mask[None, None], scores, -1e9)
        p = _softmax(scores, axis=-1)
        o = torch.einsum("bhqk,bhkd->bhqd", p, v)
        return (lambda x, *d: x.permute(*d))(o, 0, 2, 1, 3).reshape(B, S, D) @ wo

    def _B_ref(x, w1, b1, w2, b2):
        h = torch.einsum("bsd,df->bsf", x, w1) + b1
        h = _gelu(h)
        return torch.einsum("bsf,fd->bsd", h, w2) + b2

    x = A_x
    xn = _layernorm(x, axis=-1)
    return x + _A_ref(xn, A_wqkv, A_wo) + _B_ref(xn, B_w1, B_b1, B_w2, B_b2)
```

```cpp
#include <hip/hip_runtime.h>
#include <hip/hip_bf16.h>
#include <stdint.h>
#include <type_traits>

typedef unsigned v4u __attribute__((ext_vector_type(4)));
typedef unsigned v2u __attribute__((ext_vector_type(2)));
typedef float f32x2 __attribute__((ext_vector_type(2)));
typedef float f32x4 __attribute__((ext_vector_type(4)));
typedef float f32x16 __attribute__((ext_vector_type(16)));
typedef short bf16x8 __attribute__((ext_vector_type(8)));
typedef short s16x4 __attribute__((ext_vector_type(4)));
typedef __bf16 bf16x2_t __attribute__((ext_vector_type(2)));
typedef unsigned long long u64;
#define GAS __attribute__((address_space(1)))
#define LAS __attribute__((address_space(3)))
#define DI __device__ __forceinline__
#define RLX_AGENT __ATOMIC_RELAXED, __HIP_MEMORY_SCOPE_AGENT

constexpr int SEQ = 2048, DM = 512, FF = 1792;
constexpr unsigned WS_EPOCH = 0;
constexpr unsigned WS_FLAG = 4096;
constexpr unsigned WS_STATS = 32768;
constexpr unsigned WS_KV = 65536;
constexpr unsigned WS_AF = WS_KV + 16 * 64 * 4096;
constexpr unsigned WS_END = WS_AF + 64 * 144 * 1024;
constexpr unsigned WS_STAMP = WS_END;
enum { FL_KV = 0, FL_H = 1, FL_O = 2 };
constexpr int STG = 45056;
constexpr int RD = 12, RING = RD * 2048;
constexpr int L_K = 4 * RING;
constexpr int VB = 1040;
constexpr int L_V = L_K + 24 * 1024;
constexpr int L_CS = 148480;
constexpr int L_RS = L_CS + 3584;
constexpr int L_CNT = L_RS + 512;
constexpr int LDS_BYTES = L_CNT + 64;
static_assert(L_V + 24 * VB <= L_CS && 3 * STG <= L_CS && LDS_BYTES <= 163840, "lds map");
constexpr int AW = 8;
constexpr unsigned SPIN_CAP = 1u << 18;

#define MFMA32(a, b, c) __builtin_amdgcn_mfma_f32_32x32x16_bf16(__builtin_bit_cast(bf16x8, a), __builtin_bit_cast(bf16x8, b), c, 0, 0, 0)
#define MFMA16(a, b, c) __builtin_amdgcn_mfma_f32_16x16x32_bf16(__builtin_bit_cast(bf16x8, a), __builtin_bit_cast(bf16x8, b), c, 0, 0, 0)
#define SBAR() __builtin_amdgcn_sched_barrier(0)

#define STAMP_AT(i) do { } while (0)

DI float asf(unsigned u) { return __builtin_bit_cast(float, u); }
DI unsigned asu(float f) { return __builtin_bit_cast(unsigned, f); }
DI unsigned pk2(float lo, float hi) { return __builtin_bit_cast(unsigned, __builtin_convertvector((f32x2){lo, hi}, bf16x2_t)); }
DI float bflo(unsigned u) { return __builtin_bit_cast(float, u << 16); }
DI float bfhi(unsigned u) { return __builtin_bit_cast(float, u & 0xffff0000u); }
DI v4u make_srd(const void* p, unsigned bytes) { const u64 a = (u64)p; return (v4u){(unsigned)a, (unsigned)(a >> 32) & 0xffffu, bytes, 0x00020000u}; }
DI void dma16(v4u srd, unsigned voff, unsigned soff, unsigned lds) {
  asm volatile("s_mov_b32 m0, %3\n\ts_nop 0\n\tbuffer_load_dwordx4 %0, %1, %2 offen lds" :: "v"(voff), "s"(srd), "s"(soff), "s"(lds) : "memory");
}
DI v4u ld16_sc1_async(v4u srd, unsigned voff, unsigned soff) {
  v4u r; asm volatile("buffer_load_dwordx4 %0, %1, %2, %3 offen sc1" : "=v"(r) : "v"(voff), "s"(srd), "s"(soff) : "memory"); return r;
}
template <int N> DI void wait_vm() { asm volatile("s_waitcnt vmcnt(%0)" :: "i"(N) : "memory"); }
DI void bar() { asm volatile("s_waitcnt lgkmcnt(0)\n\ts_barrier" ::: "memory"); }
DI v4u lds128(unsigned a) { return *(const LAS v4u*)(size_t)a; }
DI void sts128(unsigned a, v4u v) { *(LAS v4u*)(size_t)a = v; }
DI v2u ldtr(unsigned a) { return __builtin_bit_cast(v2u, __builtin_amdgcn_ds_read_tr16_b64_v4i16((LAS s16x4*)(size_t)a)); }
DI v4u ldtr2(unsigned a, int second) { const v2u lo = ldtr(a), hi = ldtr(a + second); return (v4u){lo.x, lo.y, hi.x, hi.y}; }
template <int CTRL> DI float dpp(float v) { return __builtin_bit_cast(float, __builtin_amdgcn_update_dpp(0, __builtin_bit_cast(int, v), CTRL, 0xf, 0xf, true)); }
DI float rdl(float v, int l) { return __builtin_bit_cast(float, __builtin_amdgcn_readlane(__builtin_bit_cast(int, v), l)); }
DI float wave_sum(float v) {
  v += dpp<0xB1>(v); v += dpp<0x4E>(v); v += dpp<0x141>(v); v += dpp<0x140>(v);
  return (rdl(v, 0) + rdl(v, 16)) + (rdl(v, 32) + rdl(v, 48));
}
DI v4u pack8(const f32x16& a, int s) { return (v4u){pk2(a[8 * s], a[8 * s + 1]), pk2(a[8 * s + 2], a[8 * s + 3]), pk2(a[8 * s + 4], a[8 * s + 5]), pk2(a[8 * s + 6], a[8 * s + 7])}; }
DI void poll_flags(const GAS u64* p, u64 tag) {
  for (unsigned spin = 0; spin < SPIN_CAP; ++spin) {
    const u64 v = __hip_atomic_load(p, RLX_AGENT);
    if (__all(v == tag)) break;
    __builtin_amdgcn_s_sleep(1);
  }
  asm volatile("" ::: "memory");
}
constexpr u64 upper_mask(int reg) {
  u64 m = 0;
  for (int l = 0; l < 64; ++l) { const int row = (reg & 3) + 8 * (reg >> 2) + 4 * (l >> 5); if (row > (l & 31)) m |= 1ull << l; }
  return m;
}
template <int REG> DI bool upper() { return __builtin_amdgcn_inverse_ballot_w64(upper_mask(REG)); }
template <int I, int N, class Fn> DI void static_for(Fn&& f) { if constexpr (I < N) { f(std::integral_constant<int, I>{}); static_for<I + 1, N>(f); } }
DI float max3(float a, float b, float c) { return __builtin_fmaxf(__builtin_fmaxf(a, b), c); }

constexpr int g2_younger(int i) {
  int n = 0;
  if (i < AW) { n += 2 * (AW - 1 - i); for (int t = 0; t < i; ++t) n += ((t + RD < 36) ? 2 : 0) + ((t + AW < 28) ? 2 : 0); }
  else for (int t = i - AW + 1; t < i; ++t) n += ((t + RD < 36) ? 2 : 0) + ((t + AW < 28) ? 2 : 0);
  return n;
}

template <bool EDGE>
DI v4u attn_unit(v4u q, unsigned kb, unsigned vb, int imin, const unsigned (&am)[8]) {
  const f32x16 zero = {};
  v4u kf[9];
#pragma unroll
  for (int i = 0; i < 9; ++i) kf[i] = lds128(kb + i * 2048);
  float mx;
  {
    f32x16 su, sl = MFMA32(kf[8], q, zero);
    if (!EDGE || imin == 0) su = MFMA32(kf[0], q, zero);
    else for (int e = 0; e < 16; ++e) su[e] = -INFINITY;
    static_for<0, 16>([&](auto e) { sl[e.value] = upper<e.value>() ? su[e.value] : sl[e.value]; });
    float a = __builtin_fmaxf(sl[0], sl[1]), b = __builtin_fmaxf(sl[2], sl[3]);
#pragma unroll
    for (int e = 4; e < 16; e += 4) { a = max3(a, sl[e], sl[e + 1]); b = max3(b, sl[e + 2], sl[e + 3]); }
#pragma unroll
    for (int i = 1; i < 8; ++i) {
      if (EDGE && i < imin) continue;
      const f32x16 s = MFMA32(kf[i], q, zero);
#pragma unroll
      for (int e = 0; e < 16; e += 4) { a = max3(a, s[e], s[e + 1]); b = max3(b, s[e + 2], s[e + 3]); }
    }
    mx = __builtin_fmaxf(a, b);
    const auto sw = __builtin_amdgcn_permlane32_swap(__builtin_bit_cast(unsigned, mx), __builtin_bit_cast(unsigned, mx), false, false);
    mx = __builtin_fmaxf(asf(sw[0]), asf(sw[1]));
  }
  f32x16 negm;
#pragma unroll
  for (int e = 0; e < 16; ++e) negm[e] = -mx;
  f32x16 o = {};
#pragma unroll
  for (int i = 1; i < 8; ++i) {
    if (EDGE && i < imin) continue;
    f32x16 s = MFMA32(kf[i], q, negm);
    const v4u v0 = lds128(vb + i * 2 * VB), v1 = lds128(vb + i * 2 * VB + VB);
#pragma unroll
    for (int e = 0; e < 16; ++e) s[e] = __builtin_amdgcn_exp2f(s[e]);
    o = MFMA32(v0, pack8(s, 0), o);
    o = MFMA32(v1, pack8(s, 1), o);
  }
  {
    f32x16 su, sl = MFMA32(kf[8], q, negm);
    if (!EDGE || imin == 0) su = MFMA32(kf[0], q, negm);
    else for (int e = 0; e < 16; ++e) su[e] = -INFINITY;
    static_for<0, 16>([&](auto e) { sl[e.value] = __builtin_amdgcn_exp2f(upper<e.value>() ? su[e.value] : sl[e.value]); });
    const v4u p0 = pack8(sl, 0), p1 = pack8(sl, 1);
    const v4u m0 = {am[0], am[1], am[2], am[3]}, m1 = {am[4], am[5], am[6], am[7]};
    o = MFMA32(lds128(vb + 16 * VB), p0 & ~m0, o);
    o = MFMA32(lds128(vb + 17 * VB), p1 & ~m1, o);
    if (!EDGE || imin == 0) {
      o = MFMA32(lds128(vb), p0 & m0, o);
      o = MFMA32(lds128(vb + VB), p1 & m1, o);
    }
  }
  const float rl = __builtin_amdgcn_rcpf(o[8]);
#pragma unroll
  for (int e = 0; e < 8; ++e) o[e] *= rl;
  return pack8(o, 0);
}

__global__ __launch_bounds__(512, 2) void fused_block(const void* __restrict__ x, const void* __restrict__ wqkv, const void* __restrict__ wo,
                                                      const void* __restrict__ w1, const void* __restrict__ b1, const void* __restrict__ w2,
                                                      const void* __restrict__ b2, void* __restrict__ out, char* __restrict__ ws) {
  extern __shared__ __attribute__((aligned(16))) char smem[];
  const unsigned lds0 = (unsigned)(size_t)smem;
  const int tid = threadIdx.x, lane = tid & 63, wave = __builtin_amdgcn_readfirstlane(tid >> 6);
  const int m = wave & 3;
  const bool isG = wave < 4;
  const int xcd = blockIdx.x & 7, idx = blockIdx.x >> 3;
  const int r = (xcd >> 2) * 8 + (idx >> 2), j = (xcd & 3) * 4 + (idx & 3);
  const int rt = xcd * 4 + (idx >> 3), ct = idx & 7;
  const int r32 = lane & 31, h = lane >> 5, g = lane >> 4, q4 = (lane >> 2) & 3, p4 = lane & 3;
  const int hcol0 = 224 * (j >> 1) + 96 * (j & 1);
  STAMP_AT(0);

  const v4u sx = make_srd(x, SEQ * DM * 2), sws = make_srd(ws, WS_END);
  const __amdgpu_buffer_rsrc_t rws = __builtin_amdgcn_make_buffer_rsrc(ws, 0, WS_END, 0x00020000);
  const v4u sw = isG ? make_srd(w1, DM * FF * 2) : make_srd(wqkv, DM * 3 * DM * 2);

  v4u xrow;
  asm volatile("buffer_load_dwordx4 %0, %1, %2, %3 offen" : "=v"(xrow) : "v"(lane * 16), "s"(sx), "s"((128 * r + 8 * j + wave) * 1024) : "memory");
  const unsigned vx0 = (16 * wave + (lane >> 3)) * 1024 + (((lane & 7) ^ g) * 16);
  const unsigned vx1 = (16 * wave + 8 + (lane >> 3)) * 1024 + (((lane & 7) ^ (4 + g)) * 16);
  const unsigned vw = isG ? g * (FF * 2) + ((q4 ^ g) * 64 + p4 * 16) : (lane >> 2) * (3 * DM * 2) + p4 * 16;
  auto stage = [&](int s, unsigned buf) {
    dma16(sx, vx0, r * 131072 + s * 128, buf + wave * 2048);
    dma16(sx, vx1, r * 131072 + s * 128, buf + wave * 2048 + 1024);
    if (isG) {
#pragma unroll
      for (int i = 0; i < 4; ++i) dma16(sw, vw, (64 * s + 16 * m + 4 * i) * (FF * 2) + hcol0 * 2, buf + 16384 + m * 4096 + i * 1024);
    } else {
#pragma unroll
      for (int t = 0; t < 3; ++t) dma16(sw, vw, (64 * s + 16 * m) * (3 * DM * 2) + (512 * t + 32 * j) * 2, buf + 32768 + (t * 4 + m) * 1024);
    }
  };
  stage(0, lds0); stage(1, lds0 + STG); stage(2, lds0 + 2 * STG);
  const u64 tag = *(const u64*)(ws + WS_EPOCH) * 6364136223846793005ull + 1442695040888963407ull;
  const unsigned tag32 = (unsigned)(tag >> 32);
  if (tid == 0) *(LAS unsigned*)(size_t)(lds0 + L_CNT) = 0u;
  asm volatile("s_waitcnt vmcnt(15)" : "+v"(xrow) :: "memory");
  {
    float e[8];
#pragma unroll
    for (int i = 0; i < 4; ++i) { e[2 * i] = bflo(xrow[i]); e[2 * i + 1] = bfhi(xrow[i]); }
    const float mean = wave_sum(((e[0] + e[1]) + (e[2] + e[3])) + ((e[4] + e[5]) + (e[6] + e[7]))) * (1.f / DM);
    float sq = 0.f;
#pragma unroll
    for (int i = 0; i < 8; ++i) { const float d = e[i] - mean; sq = __builtin_fmaf(d, d, sq); }
    const float var = wave_sum(sq) * (1.f / DM) + 1e-5f;
    if (lane < 2) {
      const v2u gr = {__builtin_bit_cast(unsigned, lane ? var : mean), tag32};
      __builtin_amdgcn_raw_buffer_store_b64(gr, rws, WS_STATS + (128 * r + 8 * j + wave) * 16 + lane * 8, 0, 16);
    }
  }

  f32x16 acc[4] = {};
  f32x4 cs[4] = {};
  const unsigned one2 = 0x3f803f80u;
  const unsigned selv = ((lane & 15) == (g & 1)) ? one2 : 0u;
  const v4u sel = {selv, selv, selv, selv};
  unsigned xo[4], wb[4];
#pragma unroll
  for (int p = 0; p < 4; ++p) xo[p] = (32 * m + r32) * 128 + (((2 * ((p + m) & 3) + h) ^ ((r32 >> 1) & 7)) * 16);
#pragma unroll
  for (int t = 0; t < 4; ++t)
    wb[t] = isG ? 16384 + (8 * h + q4) * 256 + ((t ^ q4) * 64) + (g & 1) * 32 + p4 * 8 : 32768 + t * 4096 + (8 * h + q4) * 64 + (g & 1) * 32 + p4 * 8;
  v4u st = {};
  unsigned bias[4] = {};
  unsigned buf = lds0;
  for (int s = 0; s < 8; ++s) {
    if (s == 0) { if (isG) wait_vm<13>(); else wait_vm<11>(); }
    else if (s == 7) wait_vm<0>();
    else { if (isG) wait_vm<6>(); else wait_vm<5>(); }
    bar();
    if (s >= 1 && s <= 5) stage(s + 2, buf == lds0 ? lds0 + 2 * STG : buf - STG);
    if (s == 7) {
      st = __builtin_amdgcn_raw_buffer_load_b128(rws, WS_STATS + (128 * r + 32 * m + r32) * 16, 0, 16);
      if (isG) {
#pragma unroll
        for (int t = 0; t < 4; ++t) bias[t] = ((const GAS unsigned short*)b1)[hcol0 + 32 * t + r32];
      }
    }
    if (isG) {
#pragma unroll
      for (int p = 0; p < 4; ++p) {
        const unsigned kb = buf + ((p + m) & 3) * 4096;
        const v4u xf = lds128(buf + xo[p]);
#pragma unroll
        for (int t = 0; t < 4; ++t) {
          const v4u wf = ldtr2(kb + wb[t], 1024);
          acc[t] = MFMA32(wf, xf, acc[t]);
          if (p == 0) cs[t] = MFMA16(sel, wf, cs[t]);
        }
      }
    } else {
#pragma unroll
      for (int p = 0; p < 4; ++p) {
        const unsigned kb = buf + ((p + m) & 3) * 1024 + wb[0];
        const v4u xf = lds128(buf + xo[p]);
#pragma unroll
        for (int t = 0; t < 3; ++t) {
          const v4u wf = ldtr2(kb + t * 4096, 256);
          acc[t] = t < 2 ? MFMA32(wf, xf, acc[t]) : MFMA32(xf, wf, acc[t]);
          if (p == 0) cs[t] = MFMA16(sel, wf, cs[t]);
        }
      }
    }
    buf = buf == lds0 + 2 * STG ? lds0 : buf + STG;
  }
  if (lane < 16) {
#pragma unroll
    for (int t = 0; t < 4; ++t) {
      if (t == 3 && !isG) break;
      LAS float* c = (LAS float*)(size_t)(lds0 + L_CS + ((isG ? t : 4 + t) * 32 + lane) * 16 + m * 4);
      c[0] = cs[t][0]; c[64] = cs[t][1];
    }
  }
  bar();
  STAMP_AT(1);
  for (unsigned spin = 0; spin < SPIN_CAP; ++spin) {
    if (__all(st[1] == tag32 && st[3] == tag32)) break;
    asm volatile("" ::: "memory");
    st = __builtin_amdgcn_raw_buffer_load_b128(rws, WS_STATS + (128 * r + 32 * m + r32) * 16, 0, 16);
  }
  const float mean = asf(st[0]), var = asf(st[2]);
  const float rstd = __builtin_amdgcn_rsqf(var), sd = var * rstd;
  v4u xaug;
  {
    const unsigned hi = pk2(-mean, sd);
    const unsigned lo = pk2(-mean - bflo(hi), sd - bfhi(hi));
    xaug = (v4u){(hi & 0xffffu) | (hi << 16), (lo & 0xffffu) | (hi & 0xffff0000u), lo >> 16, 0u};
    if (h) xaug = (v4u){0u, 0u, 0u, 0u};
  }
  auto waug = [&](int tile, unsigned b16) {
    const f32x4 c4 = __builtin_bit_cast(f32x4, lds128(lds0 + L_CS + (tile * 32 + r32) * 16));
    const float c = (c4[0] + c4[1]) + (c4[2] + c4[3]);
    const unsigned hi = pk2(c, 0.f);
    const unsigned lo = pk2(c - bflo(hi), 0.f);
    return (v4u){hi | (lo << 16), hi | (b16 << 16), b16, 0u};
  };
  const int orow = 64 * rt + (tid >> 3), ocol = 64 * ct + (tid & 7) * 8;
  const v4u xres = *(const GAS v4u*)((const char*)x + (size_t)orow * 1024 + ocol * 2);
  const v4u bres = *(const GAS v4u*)((const char*)b2 + ocol * 2);
  const int T = 4 * r + m;
  GAS u64* flags = (GAS u64*)(ws + WS_FLAG);

  if (isG) {
    const v4u sw2 = make_srd(w2, FF * DM * 2), swo = make_srd(wo, DM * DM * 2);
    const unsigned ring = lds0 + m * RING;
    const unsigned vw2 = (lane >> 3) * 1024 + ((((lane >> 2) & 1) ^ (g & 1)) * 64) + p4 * 16;
    auto wdma = [&](int kk) {
#pragma unroll
      for (int i = 0; i < 2; ++i) {
        if (kk < 28) dma16(sw2, vw2, (448 * m + 16 * kk + 8 * i) * 1024 + 128 * ct, ring + (kk % RD) * 2048 + i * 1024);
        else dma16(swo, vw2, (128 * m + 16 * (kk - 28) + 8 * i) * 1024 + 128 * ct, ring + (kk % RD) * 2048 + i * 1024);
      }
    };
    asm volatile("" :: "v"(bias[0]), "v"(bias[1]), "v"(bias[2]), "v"(bias[3]), "v"(xres), "v"(bres));
#pragma unroll
    for (int kk = 0; kk < RD; ++kk) wdma(kk);
    const float C0 = 2.3022081986f, C1 = C0 * 0.044715f;
    const float ga = -C1 * rstd * rstd * rstd, gb = -C0 * rstd;
#pragma unroll
    for (int t = 0; t < 4; ++t) {
      if (t == 0 && (j & 1)) continue;
      f32x16 a = MFMA32(waug(t, bias[t]), xaug, acc[t]);
#pragma unroll
      for (int e = 0; e < 16; ++e) {
        const float v = a[e];
        const float z = __builtin_amdgcn_exp2f(__builtin_fmaf(v * v, ga, gb) * v);
        a[e] = v * __builtin_amdgcn_rcpf(__builtin_fmaf(z, sd, sd));
      }
#pragma unroll
      for (int s = 0; s < 2; ++s)
        __builtin_amdgcn_raw_buffer_store_b128(pack8(a, s), rws, lane * 16, WS_AF + (T * 144 + (hcol0 >> 4) + 2 * t + s) * 1024, 16);
    }
    STAMP_AT(2);
    wait_vm<0>();
    if (lane == 0) __hip_atomic_store(flags + (FL_H * 64 + T) * 16 + j, tag, RLX_AGENT);
    STAMP_AT(3);
    poll_flags(flags + (FL_H * 64 + 2 * rt + ((lane >> 2) & 1)) * 16 + 4 * m + p4, tag);
    STAMP_AT(4);

    f32x16 c2[2][2] = {};
    v4u af[AW][2];
    const unsigned a0 = WS_AF + (2 * rt * 144 + 28 * m) * 1024;
    unsigned rb[2];
#pragma unroll
    for (int c = 0; c < 2; ++c) rb[c] = ring + (4 * h + q4) * 128 + ((c ^ (q4 >> 1)) * 64) + (g & 1) * 32 + p4 * 8;
#pragma unroll
    for (int kk = 0; kk < AW; ++kk) {
      af[kk][0] = ld16_sc1_async(sws, lane * 16, a0 + kk * 1024);
      af[kk][1] = ld16_sc1_async(sws, lane * 16, a0 + (144 + kk) * 1024);
    }
    static_for<0, 28>([&](auto kc) {
      constexpr int kk = kc.value;
      asm volatile("s_waitcnt vmcnt(%2)" : "+v"(af[kk % AW][0]), "+v"(af[kk % AW][1]) : "i"(g2_younger(kk)) : "memory");
      v4u w0 = ldtr2(rb[0] + (kk % RD) * 2048, 1024), w1f = ldtr2(rb[1] + (kk % RD) * 2048, 1024);
      asm volatile("" : "+v"(w0), "+v"(w1f) :: "memory");
      if (kk + RD < 36) wdma(kk + RD);
      c2[0][0] = MFMA32(w0, af[kk % AW][0], c2[0][0]);
      c2[0][1] = MFMA32(w1f, af[kk % AW][0], c2[0][1]);
      c2[1][0] = MFMA32(w0, af[kk % AW][1], c2[1][0]);
      c2[1][1] = MFMA32(w1f, af[kk % AW][1], c2[1][1]);
      SBAR();
      if (kk + AW < 28) {
        af[kk % AW][0] = ld16_sc1_async(sws, lane * 16, a0 + (kk + AW) * 1024);
        af[kk % AW][1] = ld16_sc1_async(sws, lane * 16, a0 + (144 + kk + AW) * 1024);
      }
    });
    STAMP_AT(5);
    poll_flags(flags + (FL_O * 64 + 2 * rt + ((lane >> 2) & 1)) * 16 + 4 * m + p4, tag);
    STAMP_AT(6);
    const unsigned o0 = WS_AF + (2 * rt * 144 + 112 + 8 * m) * 1024;
#pragma unroll
    for (int kk = 0; kk < 8; ++kk) {
      af[kk][0] = ld16_sc1_async(sws, lane * 16, o0 + kk * 1024);
      af[kk][1] = ld16_sc1_async(sws, lane * 16, o0 + (144 + kk) * 1024);
    }
    static_for<0, 8>([&](auto kc) {
      constexpr int kk = kc.value;
      const v4u w0 = ldtr2(rb[0] + ((28 + kk) % RD) * 2048, 1024), w1f = ldtr2(rb[1] + ((28 + kk) % RD) * 2048, 1024);
      asm volatile("s_waitcnt vmcnt(%2)" : "+v"(af[kk][0]), "+v"(af[kk][1]) : "i"(14 - 2 * kk) : "memory");
      c2[0][0] = MFMA32(w0, af[kk][0], c2[0][0]);
      c2[0][1] = MFMA32(w1f, af[kk][0], c2[0][1]);
      c2[1][0] = MFMA32(w0, af[kk][1], c2[1][0]);
      c2[1][1] = MFMA32(w1f, af[kk][1], c2[1][1]);
    });
    asm volatile("" ::: "memory");
#pragma unroll
    for (int mt = 0; mt < 2; ++mt)
#pragma unroll
      for (int c = 0; c < 2; ++c)
#pragma unroll
        for (int qd = 0; qd < 4; ++qd) {
          const f32x4 v = {c2[mt][c][4 * qd], c2[mt][c][4 * qd + 1], c2[mt][c][4 * qd + 2], c2[mt][c][4 * qd + 3]};
          sts128(ring + ((mt * 8 + c * 4 + qd) * 66 + lane) * 16, __builtin_bit_cast(v4u, v));
        }
    STAMP_AT(7);
  } else {
    if (h == 0) *(LAS float*)(size_t)(lds0 + L_RS + (m * 32 + r32) * 4) = rstd;
    const unsigned vpos = (g & 1) * 512 + h * 256 + (lane & 15) * 16;
    const unsigned kvo = WS_KV + (j * 64 + T) * 4096;
    {
      f32x16 a = MFMA32(waug(5, 0u), xaug, acc[1]);
#pragma unroll
      for (int e = 0; e < 16; ++e) a[e] *= rstd;
#pragma unroll
      for (int e = 0; e < 2; ++e) {
        const v4u f = pack8(a, e);
        __builtin_amdgcn_raw_buffer_store_b128(f, rws, lane * 16, kvo + e * 1024, 16);
        sts128(lds0 + L_K + ((8 + m) * 2 + e) * 1024 + lane * 16, f);
      }
    }
    {
      f32x16 a = MFMA32(xaug, waug(6, 0u), acc[2]);
#pragma unroll
      for (int qd = 0; qd < 4; ++qd) {
        const f32x4 rs = __builtin_bit_cast(f32x4, lds128(lds0 + L_RS + (m * 32 + 8 * qd + 4 * h) * 4));
#pragma unroll
        for (int e = 0; e < 4; ++e) a[4 * qd + e] *= rs[e];
      }
#pragma unroll
      for (int s = 0; s < 2; ++s) {
        const v4u f = pack8(a, s);
        __builtin_amdgcn_raw_buffer_store_b128(f, rws, lane * 16, kvo + 2048 + s * 1024, 16);
        sts128(lds0 + L_V + ((8 + m) * 2 + s) * VB + vpos, f);
      }
    }
    v4u qf[2];
    {
      f32x16 a = MFMA32(waug(4, 0u), xaug, acc[0]);
      const float qs = rstd * (0.25f * 1.4426950408889634f);
#pragma unroll
      for (int e = 0; e < 16; ++e) a[e] *= qs;
      qf[0] = pack8(a, 0); qf[1] = pack8(a, 1);
    }
    if (lane < 6) sts128(lds0 + L_V + (6 * m + lane) * VB + 1024, (v4u){one2, one2, one2, one2});
    STAMP_AT(2);
    wait_vm<0>();
    if (lane == 0) __hip_atomic_store(flags + (FL_KV * 64 + T) * 16 + j, tag, RLX_AGENT);
    STAMP_AT(3);
    unsigned am[8];
    static_for<0, 8>([&](auto d) { am[d.value] = upper<2 * d.value>() ? 0xffffffffu : (upper<2 * d.value + 1>() ? 0xffff0000u : 0u); });
    const int T0 = 4 * r - 8 + 2 * m;
    if (T0 >= 0) {
      poll_flags(flags + (FL_KV * 64 + T0 + (lane & 1)) * 16 + j, tag);
      STAMP_AT(4);
      v4u kv[8];
#pragma unroll
      for (int i = 0; i < 8; ++i) kv[i] = __builtin_amdgcn_raw_buffer_load_b128(rws, lane * 16, WS_KV + (j * 64 + T0) * 4096 + i * 1024, 16);
#pragma unroll
      for (int i = 0; i < 2; ++i) {
        sts128(lds0 + L_K + ((2 * m + i) * 2) * 1024 + lane * 16, kv[4 * i]);
        sts128(lds0 + L_K + ((2 * m + i) * 2 + 1) * 1024 + lane * 16, kv[4 * i + 1]);
        sts128(lds0 + L_V + ((2 * m + i) * 2) * VB + vpos, kv[4 * i + 2]);
        sts128(lds0 + L_V + ((2 * m + i) * 2 + 1) * VB + vpos, kv[4 * i + 3]);
      }
    }
    asm volatile("s_waitcnt lgkmcnt(0)" ::: "memory");
    LAS unsigned* cnt = (LAS unsigned*)(size_t)(lds0 + L_CNT);
    if (lane == 0) __hip_atomic_fetch_add(cnt, 1u, __ATOMIC_RELAXED, __HIP_MEMORY_SCOPE_WORKGROUP);
    for (unsigned spin = 0; spin < SPIN_CAP * 8; ++spin) {
      if (__builtin_amdgcn_readfirstlane(__hip_atomic_load(cnt, __ATOMIC_RELAXED, __HIP_MEMORY_SCOPE_WORKGROUP)) >= 4u) break;
      __builtin_amdgcn_s_sleep(1);
    }
    asm volatile("" ::: "memory");
    STAMP_AT(5);
    const int imin = 8 - T > 0 ? 8 - T : 0;
    for (int e = 0; e < 2; ++e) {
      const v4u q = e ? qf[1] : qf[0];
      const unsigned kb = lds0 + L_K + (m * 2 + e) * 1024 + lane * 16;
      const unsigned vb = lds0 + L_V + m * 2 * VB + ((g & 1) ? 1024 : e * 512 + h * 256 + (lane & 15) * 16);
      const v4u o = r >= 2 ? attn_unit<false>(q, kb, vb, 0, am) : attn_unit<true>(q, kb, vb, imin, am);
      __builtin_amdgcn_raw_buffer_store_b128(o, rws, lane * 16, WS_AF + (T * 144 + 112 + 2 * j + e) * 1024, 16);
    }
    STAMP_AT(6);
    wait_vm<0>();
    if (lane == 0) __hip_atomic_store(flags + (FL_O * 64 + T) * 16 + j, tag, RLX_AGENT);
    STAMP_AT(7);
    if (blockIdx.x == 0 && wave == 7) {
#pragma unroll
      for (int i = 0; i < 4; ++i) poll_flags(flags + (FL_KV * 64 + 4 * (4 * i + g)) * 16 + (lane & 15), tag);
      if (lane == 0) __hip_atomic_store((GAS u64*)(ws + WS_EPOCH), tag, RLX_AGENT);
    }
  }
  bar();
  {
    const int row = tid >> 3, ch = tid & 7;
    const unsigned a = lds0 + (((row >> 5) * 8 + ch) * 66 + (row & 31)) * 16;
    f32x4 lo = {}, hi = {};
#pragma unroll
    for (int w = 0; w < 4; ++w) {
      lo += __builtin_bit_cast(f32x4, lds128(a + w * RING));
      hi += __builtin_bit_cast(f32x4, lds128(a + w * RING + 512));
    }
    v4u o;
    o[0] = pk2(lo[0] + bflo(xres[0]) + bflo(bres[0]), lo[1] + bfhi(xres[0]) + bfhi(bres[0]));
    o[1] = pk2(lo[2] + bflo(xres[1]) + bflo(bres[1]), lo[3] + bfhi(xres[1]) + bfhi(bres[1]));
    o[2] = pk2(hi[0] + bflo(xres[2]) + bflo(bres[2]), hi[1] + bfhi(xres[2]) + bfhi(bres[2]));
    o[3] = pk2(hi[2] + bflo(xres[3]) + bflo(bres[3]), hi[3] + bfhi(xres[3]) + bfhi(bres[3]));
    *(GAS v4u*)((char*)out + (size_t)orow * 1024 + ocol * 2) = o;
  }
  STAMP_AT(8);
}

extern "C" void kernel_launch(void* const* d_in, const int* in_sizes, int n_in, void* d_out, int out_size, void* d_ws, size_t ws_size,
                              hipStream_t stream) {
  (void)in_sizes; (void)n_in; (void)out_size; (void)ws_size;
  (void)hipFuncSetAttribute((const void*)fused_block, hipFuncAttributeMaxDynamicSharedMemorySize, LDS_BYTES);
  fused_block<<<256, 512, LDS_BYTES, stream>>>(d_in[0], d_in[1], d_in[2], d_in[4], d_in[5], d_in[6], d_in[7], d_out, (char*)d_ws);
}
```

```cpp
#include <hip/hip_runtime.h>
#include <hip/hip_bf16.h>
#include <stdint.h>
#include <type_traits>

typedef unsigned v4u __attribute__((ext_vector_type(4)));
typedef unsigned v2u __attribute__((ext_vector_type(2)));
typedef float f32x2 __attribute__((ext_vector_type(2)));
typedef float f32x4 __attribute__((ext_vector_type(4)));
typedef float f32x16 __attribute__((ext_vector_type(16)));
typedef short bf16x8 __attribute__((ext_vector_type(8)));
typedef short s16x4 __attribute__((ext_vector_type(4)));
typedef __bf16 bf16x2_t __attribute__((ext_vector_type(2)));
typedef unsigned long long u64;
#define GAS __attribute__((address_space(1)))
#define LAS __attribute__((address_space(3)))
#define DI __device__ __forceinline__
#define RLX_AGENT __ATOMIC_RELAXED, __HIP_MEMORY_SCOPE_AGENT

constexpr int SEQ = 2048, DM = 512, FF = 1792;
constexpr unsigned WS_EPOCH = 0;
constexpr unsigned WS_STATS = 32768;
constexpr unsigned WS_FLAG = 65536;
constexpr unsigned WS_KV = WS_FLAG + 4 * 1024 * 128;
constexpr int AFT = 145;
constexpr unsigned WS_AF = WS_KV + 16 * 64 * 4096;
constexpr unsigned WS_END = WS_AF + 64 * AFT * 1024;
constexpr unsigned WS_STAMP = WS_END;
enum { FL_KV = 0, FL_H0 = 1, FL_H1 = 2, FL_O = 3 };
constexpr int STG = 28672;
constexpr int L_WA = 0;
constexpr int L_WG = 3 * STG;
constexpr int VB = 1040;
constexpr int KVT = 2048 + 2 * VB;
constexpr int L_KV = 65536 - 8 * KVT;
constexpr int RD = 8, NR = 12;
constexpr int PART = 16 * 66 * 16;
constexpr int L_CS = L_WG + 4 * PART;
constexpr int L_RS = L_CS + 3584;
constexpr int L_CNT = L_RS + 512;
constexpr int LDS_BYTES = L_CNT + 64;
static_assert(L_KV + 12 * KVT <= L_WG && 4 * PART >= 65536 && PART >= RD * 2048 && LDS_BYTES <= 163840, "lds map");
constexpr int AW = 6;
constexpr unsigned SPIN_CAP = 1u << 18;

#define MFMA32(a, b, c) __builtin_amdgcn_mfma_f32_32x32x16_bf16(__builtin_bit_cast(bf16x8, a), __builtin_bit_cast(bf16x8, b), c, 0, 0, 0)
#define MFMA16(a, b, c) __builtin_amdgcn_mfma_f32_16x16x32_bf16(__builtin_bit_cast(bf16x8, a), __builtin_bit_cast(bf16x8, b), c, 0, 0, 0)
#define SBAR() __builtin_amdgcn_sched_barrier(0)

#define STAMP_AT(i) do { } while (0)

DI float asf(unsigned u) { return __builtin_bit_cast(float, u); }
DI unsigned asu(float f) { return __builtin_bit_cast(unsigned, f); }
DI unsigned pk2(float lo, float hi) { return __builtin_bit_cast(unsigned, __builtin_convertvector((f32x2){lo, hi}, bf16x2_t)); }
DI float bflo(unsigned u) { return __builtin_bit_cast(float, u << 16); }
DI float bfhi(unsigned u) { return __builtin_bit_cast(float, u & 0xffff0000u); }
DI v4u make_srd(const void* p, unsigned bytes) { const u64 a = (u64)p; return (v4u){(unsigned)a, (unsigned)(a >> 32) & 0xffffu, bytes, 0x00020000u}; }
DI void dma16(v4u srd, unsigned voff, unsigned soff, unsigned lds) {
  asm volatile("s_mov_b32 m0, %3\n\ts_nop 0\n\tbuffer_load_dwordx4 %0, %1, %2 offen lds" :: "v"(voff), "s"(srd), "s"(soff), "s"(lds) : "memory");
}
DI void dma16_a(v4u srd, unsigned voff, unsigned soff, unsigned lds) { dma16(srd, voff, soff, lds); }
DI void dma16_b(v4u srd, unsigned voff1, unsigned soff) {
  asm volatile("buffer_load_dwordx4 %0, %1, %2 offen offset:1024 lds" :: "v"(voff1), "s"(srd), "s"(soff) : "memory");
}
DI v4u ld16_sc1_async(v4u srd, unsigned voff, unsigned soff) {
  v4u r; asm volatile("buffer_load_dwordx4 %0, %1, %2, %3 offen sc1" : "=v"(r) : "v"(voff), "s"(srd), "s"(soff) : "memory"); return r;
}
DI v4u ld16_async(v4u srd, unsigned voff, unsigned soff) {
  v4u r; asm volatile("buffer_load_dwordx4 %0, %1, %2, %3 offen" : "=v"(r) : "v"(voff), "s"(srd), "s"(soff) : "memory"); return r;
}
DI unsigned ld2_async(v4u srd, unsigned voff, unsigned soff) {
  unsigned r; asm volatile("buffer_load_ushort %0, %1, %2, %3 offen" : "=v"(r) : "v"(voff), "s"(srd), "s"(soff) : "memory"); return r;
}
DI void st16_sc1(v4u v, v4u srd, unsigned voff, unsigned soff) {
  asm volatile("buffer_store_dwordx4 %0, %1, %2, %3 offen sc1\n\ts_nop 1" :: "v"(v), "v"(voff), "s"(srd), "s"(soff) : "memory");
}
template <int N> DI void wait_vm1(v4u& a) { asm volatile("s_waitcnt vmcnt(%1)" : "+v"(a) : "i"(N) : "memory"); }
template <int N> DI void wait_vm2(v4u& a, v4u& b) { asm volatile("s_waitcnt vmcnt(%2)" : "+v"(a), "+v"(b) : "i"(N) : "memory"); }
template <int N> DI void wait_vm3(v4u& a, v4u& b, v4u& c) { asm volatile("s_waitcnt vmcnt(%3)" : "+v"(a), "+v"(b), "+v"(c) : "i"(N) : "memory"); }
DI u64 load_s(const void* p) { u64 v; asm volatile("s_load_dwordx2 %0, %1, 0x0\n\ts_waitcnt lgkmcnt(0)" : "=&s"(v) : "s"(p) : "memory"); return v; }
template <class V> DI void pin(V& v) { asm volatile("" : "+v"(v)); }
template <int N> DI void wait_vm() { asm volatile("s_waitcnt vmcnt(%0)" :: "i"(N) : "memory"); }
DI void bar() { asm volatile("s_waitcnt lgkmcnt(0)\n\ts_barrier" ::: "memory"); }
DI v4u lds128(unsigned a) { return *(const LAS v4u*)(size_t)a; }
DI void sts128(unsigned a, v4u v) { *(LAS v4u*)(size_t)a = v; }
DI v2u ldtr(unsigned a) { return __builtin_bit_cast(v2u, __builtin_amdgcn_ds_read_tr16_b64_v4i16((LAS s16x4*)(size_t)a)); }
DI v4u ldtr2(unsigned a, int second) { const v2u lo = ldtr(a), hi = ldtr(a + second); return (v4u){lo.x, lo.y, hi.x, hi.y}; }
template <int CTRL> DI float dpp(float v) { return __builtin_bit_cast(float, __builtin_amdgcn_update_dpp(0, __builtin_bit_cast(int, v), CTRL, 0xf, 0xf, true)); }
DI float rdl(float v, int l) { return __builtin_bit_cast(float, __builtin_amdgcn_readlane(__builtin_bit_cast(int, v), l)); }
DI float wave_sum(float v) {
  v += dpp<0xB1>(v); v += dpp<0x4E>(v); v += dpp<0x141>(v); v += dpp<0x140>(v);
  return (rdl(v, 0) + rdl(v, 16)) + (rdl(v, 32) + rdl(v, 48));
}
DI v4u pack8(const f32x16& a, int s) { return (v4u){pk2(a[8 * s], a[8 * s + 1]), pk2(a[8 * s + 2], a[8 * s + 3]), pk2(a[8 * s + 4], a[8 * s + 5]), pk2(a[8 * s + 6], a[8 * s + 7])}; }
DI void poll_flags(const GAS u64* p, u64 tag) {
#pragma unroll 1
  for (unsigned spin = 0; spin < SPIN_CAP; ++spin) {
    const u64 v = __hip_atomic_load(p, RLX_AGENT);
    if (__all(v == tag)) break;
    __builtin_amdgcn_s_sleep(2);
  }
  asm volatile("" ::: "memory");
}
constexpr u64 upper_mask(int reg) {
  u64 m = 0;
  for (int l = 0; l < 64; ++l) { const int row = (reg & 3) + 8 * (reg >> 2) + 4 * (l >> 5); if (row > (l & 31)) m |= 1ull << l; }
  return m;
}
template <int REG> DI bool upper() { return __builtin_amdgcn_inverse_ballot_w64(upper_mask(REG)); }
template <int I, int N, class Fn> DI void static_for(Fn&& f) { if constexpr (I < N) { f(std::integral_constant<int, I>{}); static_for<I + 1, N>(f); } }
DI float max3(float a, float b, float c) { return __builtin_fmaxf(__builtin_fmaxf(a, b), c); }

constexpr int H1_LD = 7, H1_CK = 16 - AW, O_LD = 19, O_CK = 22;
constexpr int g2_issued(int t) { return ((t >= 1 && t - 1 + NR + RD < 36) ? 2 : 0) + ((t + AW < 28) ? 2 : 0) + (t == H1_LD) + (t == O_LD) + (t == O_CK ? 18 : 0); }
constexpr int g2_range(int a, int b) { int n = 0; for (int t = a; t < b; ++t) n += g2_issued(t); return n; }
#define EXP2(v) __builtin_amdgcn_exp2f(v)
#define PIN(v) pin(v)
extern "C" __device__ float mul_unpacked(float, float) __asm("llvm.amdgcn.fmul.legacy");
#define MUL1(a, b) mul_unpacked(a, b)
#define FMA1(a, b, c) __builtin_amdgcn_div_fmasf(a, b, c, false)
DI v2u ld8_sc1_async(v4u srd, unsigned voff) {
  v2u r; asm volatile("buffer_load_dwordx2 %0, %1, %2, 0 offen sc1" : "=v"(r) : "v"(voff), "s"(srd) : "memory"); return r;
}
template <int N> DI void wait_vmf(v2u& a) { asm volatile("s_waitcnt vmcnt(%1)" : "+v"(a) : "i"(N) : "memory"); }
DI void arrive(unsigned cnt, int lane) {
  asm volatile("s_waitcnt lgkmcnt(0)" ::: "memory");
  if (lane == 0) __hip_atomic_fetch_add((LAS unsigned*)(size_t)cnt, 1u, __ATOMIC_RELAXED, __HIP_MEMORY_SCOPE_WORKGROUP);
}
DI void arrive_nowait(unsigned cnt) {
  asm volatile("s_mov_b64 exec, 1\n\tds_add_u32 %0, %1\n\ts_mov_b64 exec, -1" :: "v"(cnt), "v"(1u) : "memory");
}
DI unsigned peek(unsigned cnt) { return __hip_atomic_load((LAS unsigned*)(size_t)cnt, __ATOMIC_RELAXED, __HIP_MEMORY_SCOPE_WORKGROUP); }
DI void await(unsigned cnt, unsigned n) {
#pragma unroll 1
  for (unsigned spin = 0; spin < SPIN_CAP * 8; ++spin) {
    if (__builtin_amdgcn_readfirstlane(peek(cnt)) >= n) break;
    __builtin_amdgcn_s_sleep(1);
  }
  asm volatile("" ::: "memory");
}

__global__ __launch_bounds__(512, 2) void fused_block(const void* __restrict__ x, const void* __restrict__ wqkv, const void* __restrict__ wo,
                                                      const void* __restrict__ w1, const void* __restrict__ b1, const void* __restrict__ w2,
                                                      const void* __restrict__ b2, void* __restrict__ out, char* __restrict__ ws) {
  constexpr unsigned lds0 = 0;
  const int wave = __builtin_amdgcn_readfirstlane(threadIdx.x >> 6);
  const int m = wave & 3;
  const bool isG = wave < 4;
  const int xcd = blockIdx.x & 7, idx = blockIdx.x >> 3;
  const int r = (xcd >> 2) * 8 + (idx >> 2), j = (xcd & 3) * 4 + (idx & 3);
  const int rt = (xcd >> 1) * 8 + (idx >> 2), ct = (xcd & 1) * 4 + (idx & 3);
  int lane = threadIdx.x & 63, r32, h, g, q4, p4;
  STAMP_AT(0);
  asm volatile("" :: "s"(x), "s"(wqkv), "s"(wo), "s"(w1), "s"(b1), "s"(w2), "s"(b2), "s"(out), "s"(ws));

  const v4u sx = make_srd(x, SEQ * DM * 2), sws = make_srd(ws, WS_END);
  const __amdgpu_buffer_rsrc_t rws = __builtin_amdgcn_make_buffer_rsrc(ws, 0, WS_END, 0x00020000);
  const unsigned one2 = 0x3f803f80u;
  const int T = 4 * r + m;
  auto flag_off = [&](int kind, int tile, int jj) { return WS_FLAG + ((kind * 64 + tile) * 16 + jj) * 128; };
  auto flag = [&](int kind, int tile, int jj) { return (GAS u64*)(ws + flag_off(kind, tile, jj)); };
  u64 tag;
  unsigned taglo, tag32;
  auto get_tag = [&]() {
    tag = load_s(ws + WS_EPOCH);
    taglo = (unsigned)tag * 0x9E3779B1u + 0x7F4A7C15u; tag32 = (unsigned)(tag >> 32) * 0x85EBCA77u + 0x165667B1u;
    tag = ((u64)tag32 << 32) | taglo;
  };
  v4u xres, bres, xaug, st;
  float mean, var, rstd, sd;
  v4u sel;
  unsigned xo[4], swz;
  auto lane_setup = [&]() {
    lane = threadIdx.x & 63; PIN(lane);
    r32 = lane & 31; h = lane >> 5; g = lane >> 4; q4 = (lane >> 2) & 3; p4 = lane & 3;
    const unsigned selv = ((lane & 15) == (g & 1)) ? one2 : 0u;
    sel = (v4u){selv, selv, selv, selv};
#pragma unroll
    for (int p = 0; p < 4; ++p) xo[p] = (32 * m + r32) * 128 + (((2 * ((p + m) & 3) + h) ^ ((r32 >> 1) & 7)) * 16);
    swz = ((((lane >> 2) & 1) ^ (g & 1)) * 64) + p4 * 16;
  };

  auto stat_load = [&]() { st = ld16_sc1_async(sws, WS_STATS + (128 * r + 32 * m + r32) * 16, 0); };
  auto res_loads = [&]() {
    int t2 = threadIdx.x; PIN(t2);
    const int row = 64 * rt + (t2 >> 3), col = 64 * ct + (t2 & 7) * 8;
    xres = ld16_async(sx, row * 1024 + col * 2, 0);
    bres = ld16_async(make_srd(b2, DM * 2), col * 2, 0);
  };
  auto row_stats = [&]() {
    if (!__all(st[1] == tag32 && st[3] == tag32)) {
#pragma unroll 1
      for (unsigned spin = 0; spin < SPIN_CAP; ++spin) {
        st = ld16_sc1_async(sws, WS_STATS + (128 * r + 32 * m + r32) * 16, 0);
        wait_vm1<0>(st);
        if (__all(st[1] == tag32 && st[3] == tag32)) break;
      }
    }
    mean = asf(st[0]); var = asf(st[2]);
    PIN(mean); PIN(var);
  };
  auto make_xaug = [&]() {
    PIN(mean); PIN(var);
    rstd = __builtin_amdgcn_rsqf(var); sd = var * rstd;
    const unsigned hi = pk2(-mean, sd);
    const unsigned lo = pk2(-mean - bflo(hi), sd - bfhi(hi));
    xaug = (v4u){(hi & 0xffffu) | (hi << 16), (lo & 0xffffu) | (hi & 0xffff0000u), lo >> 16, 0u};
    if (h) xaug = (v4u){0u, 0u, 0u, 0u};
    PIN(xaug);
  };
  auto cswr = [&](int tile, f32x4 c) {
    if (lane < 16) { LAS float* p = (LAS float*)(size_t)(lds0 + L_CS + (tile * 32 + lane) * 16 + m * 4); p[0] = c[0]; p[64] = c[1]; }
  };
  auto csrd = [&](int tile) { return __builtin_bit_cast(f32x4, lds128(lds0 + L_CS + (tile * 32 + r32) * 16)); };
  auto waug = [&](f32x4 c4, unsigned b16) {
    const float c = ((c4[0] + c4[1]) + c4[2]) + c4[3];
    const unsigned hi = pk2(c, 0.f);
    const unsigned lo = pk2(c - bflo(hi), 0.f);
    return (v4u){hi | (lo << 16), hi | (b16 << 16), b16, 0u};
  };

  const v4u sw1 = make_srd(w1, DM * FF * 2);
  const int hcol0 = 448 * (j >> 2) + 128 * (j & 3);
  const bool heavy = (j & 3) != 3;
  unsigned bias0, bias1;
  auto bias_load = [&](int P) {
    bias0 = ld2_async(make_srd(b1, FF * 2), r32 * 2, (hcol0 + 64 * P) * 2);
    bias1 = ld2_async(make_srd(b1, FF * 2), r32 * 2, (hcol0 + 64 * P + 32) * 2);
  };
  auto w1dma = [&](int P, int c0, int c1) {
    const unsigned vw1 = (lane >> 3) * (FF * 2) + swz, vw1b = vw1 + 8 * (FF * 2) - 1024;
    for (int c = c0; c < c1; ++c) {
      const unsigned so = (64 * ((c + r) & 7) + 16 * m) * (FF * 2) + (hcol0 + 64 * P) * 2;
      dma16_a(sw1, vw1, so, lds0 + (P ? L_WA : L_WG) + c * 8192 + m * 2048); dma16_b(sw1, vw1b, so);
    }
  };
  f32x16 hac[2];
  unsigned rk[4][2];
  auto hsetup = [&](int P) {
#pragma unroll
    for (int p = 0; p < 4; ++p)
#pragma unroll
      for (int c = 0; c < 2; ++c) {
        rk[p][c] = lds0 + (P ? L_WA : L_WG) + ((p + m) & 3) * 2048 + h * 1024 + q4 * 128 + ((c ^ (q4 >> 1)) * 64) + (g & 1) * 32 + p4 * 8;
        PIN(rk[p][c]);
      }
  };
  auto hmm = [&](auto pc, const v4u (&xf)[32], auto&& hook) {
    constexpr int P = pc.value;
    constexpr int D = 4;
    const unsigned cnt = lds0 + L_CNT + (P ? 0 : 16);
    f32x4 cs[2] = {}, c0, c1;
    v4u wf[D][2], au0, au1;
    unsigned seen = 0;
    hac[0] = (f32x16){}; hac[1] = (f32x16){};
    auto rd = [&](int ks, int t) { wf[ks % D][t] = ldtr2(rk[ks & 3][t] + (ks >> 2) * 8192, 512); };
#pragma unroll
    for (int ks = 0; ks < D; ++ks) { rd(ks, 0); rd(ks, 1); }
    SBAR();
    static_for<0, 32>([&](auto kc) {
      constexpr int ks = kc.value;
      hook(kc);
      if constexpr (ks == 8 || ks == 16) arrive_nowait(cnt);
      if constexpr (ks == 10 || ks == 18 || ks == 29) seen = peek(cnt);
#pragma unroll
      for (int t = 0; t < 2; ++t) {
        hac[t] = MFMA32(wf[ks % D][t], xf[ks], hac[t]);
        if ((ks & 3) == 0 && ks < 28) { cs[t] = MFMA16(sel, wf[ks % D][t], cs[t]); PIN(cs[t]); }
        if (ks == 26) { cs[t] = MFMA16(sel, wf[28 % D][t], cs[t]); PIN(cs[t]); }
        SBAR();
        if constexpr (ks + D < 32) rd(ks + D, t);
        if constexpr (ks == 31) { if (t == 0) { au0 = waug(c0, bias0); PIN(au0); } else { au1 = waug(c1, bias1); PIN(au1); } }
        SBAR();
      }
      if constexpr (ks == 11 || ks == 19 || ks == 30) {
        constexpr unsigned need = 4 * (P + (ks == 11 ? 1 : ks == 19 ? 2 : 3));
        PIN(hac[0]); PIN(hac[1]);
        if (__builtin_amdgcn_readfirstlane(seen) < need) await(cnt, need);
        asm volatile("" ::: "memory");
        if constexpr (ks == 30) { c0 = csrd(2 * P); c1 = csrd(2 * P + 1); }
      }
      if constexpr (ks == 27) { cswr(2 * P, cs[0]); cswr(2 * P + 1, cs[1]); arrive_nowait(cnt); }
      SBAR();
    });
    hac[0] = MFMA32(au0, xaug, hac[0]);
    hac[1] = MFMA32(au1, xaug, hac[1]);
    SBAR();
  };
  auto hgelu = [&](auto pc) {
    constexpr int P = pc.value;
    const float C0 = 2.3022081986f, C1 = C0 * 0.044715f;
    const float ga = -C1 * rstd * rstd * rstd, gb = -C0 * rstd;
    auto mul = [&](f32x2 a, f32x2 b) { return P ? a * b : (f32x2){MUL1(a[0], b[0]), MUL1(a[1], b[1])}; };
    auto fma = [&](f32x2 a, float b, float c) {
      return P ? __builtin_elementwise_fma(a, (f32x2){b, b}, (f32x2){c, c}) : (f32x2){FMA1(a[0], b, c), FMA1(a[1], b, c)}; };
#pragma unroll
    for (int t = 0; t < 2; ++t) {
      f32x2 v[8], z[8];
#pragma unroll
      for (int e = 0; e < 8; ++e) { v[e] = (f32x2){hac[t][2 * e], hac[t][2 * e + 1]}; z[e] = mul(v[e], v[e]); }
      SBAR();
#pragma unroll
      for (int e = 0; e < 8; ++e) z[e] = fma(z[e], ga, gb);
      SBAR();
#pragma unroll
      for (int e = 0; e < 8; ++e) z[e] = mul(z[e], v[e]);
      SBAR();
#pragma unroll
      for (int e = 0; e < 8; ++e) z[e] = (f32x2){EXP2(z[e][0]), EXP2(z[e][1])};
      SBAR();
#pragma unroll
      for (int e = 0; e < 8; ++e) z[e] = fma(z[e], sd, sd);
      SBAR();
#pragma unroll
      for (int e = 0; e < 8; ++e) z[e] = (f32x2){__builtin_amdgcn_rcpf(z[e][0]), __builtin_amdgcn_rcpf(z[e][1])};
      SBAR();
#pragma unroll
      for (int e = 0; e < 8; ++e) z[e] = mul(z[e], v[e]);
      SBAR();
#pragma unroll
      for (int s = 0; s < 2; ++s) {
        const v4u f = {pk2(z[4 * s][0], z[4 * s][1]), pk2(z[4 * s + 1][0], z[4 * s + 1][1]), pk2(z[4 * s + 2][0], z[4 * s + 2][1]), pk2(z[4 * s + 3][0], z[4 * s + 3][1])};
        st16_sc1(f, sws, lane * 16, WS_AF + (T * AFT + (hcol0 >> 4) + 4 * P + 2 * t + s) * 1024);
      }
      SBAR();
    }
  };

  if (isG) {
    lane_setup();
    {
      const v4u swq = make_srd(wqkv, DM * 3 * DM * 2);
      const unsigned vx0 = (lane >> 3) * 1024 + (((lane & 7) ^ g) * 16), vx1 = (7 + (lane >> 3)) * 1024 + (((lane & 7) ^ (4 + g)) * 16);
      const unsigned vwq = (lane >> 2) * (3 * DM * 2) + p4 * 16;
      auto stage = [&](int s) {
        const unsigned buf = lds0 + (s % 3) * STG;
        const int kc = (s + r) & 7;
#pragma unroll
        for (int i = 0; i < 2; ++i) {
          const unsigned so = (128 * r + 32 * m + 16 * i) * 1024 + kc * 128;
          dma16_a(sx, vx0, so, buf + m * 4096 + i * 2048); dma16_b(sx, vx1, so);
        }
#pragma unroll
        for (int t = 0; t < 3; ++t) dma16(swq, vwq, (64 * kc + 16 * m) * (3 * DM * 2) + (512 * t + 32 * j) * 2, buf + 16384 + (t * 4 + m) * 1024);
      };
      v4u xf[32];
      stage(0); stage(1);
      get_tag();
      static_for<0, 8>([&](auto sc) {
        constexpr int s = sc.value;
        wait_vm<(s < 7) ? 7 : 11>();
        bar();
        STAMP_AT(10 + s);
#pragma unroll
        for (int p = 0; p < 4; ++p) xf[4 * s + p] = lds128(lds0 + (s % 3) * STG + xo[p]);
        SBAR();
        if constexpr (s < 6) stage(s + 2);
        if constexpr (s == 6) { stat_load(); bias_load(0); w1dma(0, 0, 4); }
        if constexpr (s == 7) w1dma(0, 4, 8);
      });
      wait_vm1<16>(st);
      PIN(bias0); PIN(bias1);
      row_stats();
      make_xaug();
      hsetup(0);
      wait_vm<8>();
      bar();
      STAMP_AT(1);
      __builtin_amdgcn_s_setprio(2);
      hmm(std::integral_constant<int, 0>{}, xf, [&](auto kc) { if constexpr (kc.value == 8) wait_vm<4>(); if constexpr (kc.value == 16) wait_vm<0>(); });
    }
    STAMP_AT(18);
    const v4u sw2 = make_srd(w2, FF * DM * 2), swo = make_srd(wo, DM * DM * 2);
    const unsigned vw2 = (lane >> 3) * 1024 + swz, vw2b = vw2 + 7 * 1024;
    const int ro3 = rt % 3;
    auto kmap = [&](int kk) {
      if (kk < 16) return 8 * (((kk >> 2) + rt) & 3) + (kk & 3);
      const int p = ((kk - 16) >> 2) + ro3;
      return 8 * (p >= 3 ? p - 3 : p) + 4 + (kk & 3);
    };
    auto omap = [&](int i) { return (i + rt) & 7; };
    auto wdmah = [&](int kk, int half) {
      const unsigned so = (kk < 28 ? 448 * m + 16 * kmap(kk) : 128 * m + 16 * omap(kk - 28)) * 1024 + 128 * ct;
      if (half == 0) dma16_a(kk < 28 ? sw2 : swo, vw2, so, lds0 + L_WG + m * PART + (kk % RD) * 2048); else dma16_b(kk < 28 ? sw2 : swo, vw2b, so);
    };
    auto wdma = [&](int kk) { wdmah(kk, 0); wdmah(kk, 1); };
    hgelu(std::integral_constant<int, 0>{});
    __builtin_amdgcn_s_setprio(0);
    STAMP_AT(19);
    unsigned rb2[2];
#pragma unroll
    for (int c = 0; c < 2; ++c) rb2[c] = lds0 + L_WG + m * PART + h * 512 + q4 * 128 + ((c ^ (q4 >> 1)) * 64) + (g & 1) * 32 + p4 * 8;
    v4u wq[36][2];
    auto wrd = [&](int kk) {
#pragma unroll
      for (int c = 0; c < 2; ++c) wq[kk][c] = ldtr2(rb2[c] + (kk % RD) * 2048, 1024);
    };
    auto wgot = [&](int kk) { PIN(wq[kk][0]); PIN(wq[kk][1]); };
    for (int kk = 0; kk < 4; ++kk) wdma(kk);
    wait_vm<8>();
    if (lane == 0) __hip_atomic_store(flag(FL_H0, T, j), tag, RLX_AGENT);
    STAMP_AT(2);
    for (int kk = 4; kk < 8; ++kk) wdma(kk);
    const int ftile = 2 * rt + ((lane >> 2) & 1);
    v4u af[28][2], of[8][2];
    v2u fl;
    const unsigned a0 = WS_AF + (2 * rt * AFT + 28 * m) * 1024, o0 = WS_AF + (2 * rt * AFT + 112 + 8 * m) * 1024;
    static_for<0, 3>([&](auto ic) {
      constexpr int i = ic.value;
      if constexpr (i < 2) wait_vm<9>();
      for (int kk = 4 * i; kk < 4 * i + 4; ++kk) wrd(kk);
      for (int kk = 4 * i; kk < 4 * i + 4; ++kk) wgot(kk);
      for (int kk = 4 * i; kk < 4 * i + 4; ++kk) wdma(kk + RD);
      if constexpr (i == 0) fl = ld8_sc1_async(sws, flag_off(FL_H0, ftile, 4 * m + p4));
      if constexpr (i == 1) {
        STAMP_AT(3);
        wait_vmf<8>(fl);
        if (!__all(fl[0] == taglo && fl[1] == tag32)) poll_flags(flag(FL_H0, ftile, 4 * m + p4), tag);
        STAMP_AT(4);
#pragma unroll
        for (int kk = 0; kk < AW; ++kk) {
          af[kk][0] = ld16_sc1_async(sws, lane * 16, a0 + kmap(kk) * 1024);
          af[kk][1] = ld16_sc1_async(sws, lane * 16, a0 + (AFT + kmap(kk)) * 1024);
        }
      }
    });
    __builtin_amdgcn_s_setprio(2);
    f32x16 c2[2][2] = {};
    auto mm2 = [&](int kk, const v4u (&a)[2], auto&& fill) {
      SBAR();
#pragma unroll
      for (int i = 0; i < 4; ++i) { c2[i >> 1][i & 1] = MFMA32(wq[kk][i & 1], a[i >> 1], c2[i >> 1][i & 1]); SBAR(); fill(i); SBAR(); }
    };
    static_for<0, 28>([&](auto kc) {
      constexpr int kk = kc.value;
      wait_vm2<(kk < 4) ? 2 * (AW - 1 - kk) + 8 + g2_range(0, kk) : (kk < AW) ? g2_range(0, kk) : g2_range(kk - AW + 1, kk)>(af[kk][0], af[kk][1]);
      if constexpr (kk == H1_CK) {
        STAMP_AT(20);
        wait_vmf<g2_range(H1_LD, H1_CK) - 1>(fl);
        if (!__all(fl[0] == taglo && fl[1] == tag32)) poll_flags(flag(FL_H1, ftile, 4 * m + p4), tag);
        STAMP_AT(21);
      }
      if constexpr (kk == H1_LD) fl = ld8_sc1_async(sws, flag_off(FL_H1, ftile, 4 * m + p4));
      if constexpr (kk == O_LD) fl = ld8_sc1_async(sws, flag_off(FL_O, ftile, 4 * m + p4));
      constexpr bool dw = kk >= 1 && kk - 1 + NR + RD < 36;
      if constexpr (dw) wgot(kk - 1 + NR);
      mm2(kk, af[kk], [&](int i) {
        if constexpr (dw) { if (i < 2) wdmah(kk - 1 + NR + RD, i); }
        if constexpr (kk + NR < 28) { if (i == 1) wrd(kk + NR); }
        if constexpr (kk == 27) { if (i == 0) wrd(28); }
        if constexpr (kk + AW < 28) { if (i >= 2) af[kk + AW][i - 2] = ld16_sc1_async(sws, lane * 16, a0 + (AFT * (i - 2) + kmap(kk + AW)) * 1024); }
      });
      if constexpr (kk == O_CK) {
        STAMP_AT(5);
        wait_vmf<g2_range(O_LD, O_CK) - 1>(fl);
        if (!__all(fl[0] == taglo && fl[1] == tag32)) poll_flags(flag(FL_O, ftile, 4 * m + p4), tag);
        STAMP_AT(6);
        res_loads();
#pragma unroll
        for (int i = 0; i < 8; ++i) {
          of[i][0] = ld16_sc1_async(sws, lane * 16, o0 + omap(i) * 1024);
          of[i][1] = ld16_sc1_async(sws, lane * 16, o0 + (AFT + omap(i)) * 1024);
        }
      }
    });
    static_for<0, 8>([&](auto kc) {
      constexpr int kk = kc.value;
      if (kk < 7) wrd(28 + kk + 1);
      wait_vm2<14 - 2 * kk>(of[kk][0], of[kk][1]);
      mm2(28 + kk, of[kk], [&](int) {});
    });
    PIN(xres); PIN(bres);
    asm volatile("" ::: "memory");
#pragma unroll
    for (int mt = 0; mt < 2; ++mt)
#pragma unroll
      for (int c = 0; c < 2; ++c)
#pragma unroll
        for (int qd = 0; qd < 4; ++qd) {
          const f32x4 v = {c2[mt][c][4 * qd], c2[mt][c][4 * qd + 1], c2[mt][c][4 * qd + 2], c2[mt][c][4 * qd + 3]};
          sts128(lds0 + L_WG + m * PART + ((mt * 8 + c * 4 + qd) * 66 + lane) * 16, __builtin_bit_cast(v4u, v));
        }
    STAMP_AT(7);
  } else {
    __builtin_amdgcn_s_setprio(1);
    lane_setup();
    v4u xr[2];
    xr[0] = ld16_async(sx, lane * 16, (128 * r + 8 * j + 2 * m) * 1024); xr[1] = ld16_async(sx, lane * 16, (128 * r + 8 * j + 2 * m + 1) * 1024);
    get_tag();
    if (m == 0 && lane < 8) *(LAS unsigned*)(size_t)(lds0 + L_CNT + lane * 4) = 0u;
    auto make_stats = [&](int u) {
      if (u == 0) wait_vm2<0>(xr[0], xr[1]);
      float e[8];
#pragma unroll
      for (int i = 0; i < 4; ++i) { e[2 * i] = bflo(xr[u][i]); e[2 * i + 1] = bfhi(xr[u][i]); }
      const float mu = wave_sum(((e[0] + e[1]) + (e[2] + e[3])) + ((e[4] + e[5]) + (e[6] + e[7]))) * (1.f / DM);
      float sq = 0.f;
#pragma unroll
      for (int i = 0; i < 8; ++i) { const float d = e[i] - mu; sq = __builtin_fmaf(d, d, sq); }
      const float va = wave_sum(sq) * (1.f / DM) + 1e-5f;
      if (lane < 2) {
        const v2u gr = {asu(lane ? va : mu), tag32};
        __builtin_amdgcn_raw_buffer_store_b64(gr, rws, WS_STATS + (128 * r + 8 * j + 2 * m + u) * 16 + lane * 8, 0, 16);
      }
    };
    const unsigned acnt = lds0 + L_CNT;
    const unsigned vpos = 2048 + (g & 1) * 512 + h * 256 + (lane & 15) * 16;
    v4u qf[2];
    {
      v4u xf[32];
      f32x16 acc[3] = {};
      f32x4 cs[3] = {};
      v4u wf[2][3];
      unsigned wb[4];
#pragma unroll
      for (int p = 0; p < 4; ++p) wb[p] = 16384 + ((p + m) & 3) * 1024 + (8 * h + q4) * 64 + (g & 1) * 32 + p4 * 8;
      auto rd = [&](int ks, int t) {
        const unsigned buf = lds0 + ((ks >> 2) % 3) * STG;
        if (t == 0) xf[ks] = lds128(buf + xo[ks & 3]);
        wf[ks & 1][t] = ldtr2(buf + wb[ks & 3] + t * 4096, 256);
      };
      auto mm = [&](int ks, int t) {
        acc[t] = t == 2 ? MFMA32(xf[ks], wf[ks & 1][t], acc[t]) : MFMA32(wf[ks & 1][t], xf[ks], acc[t]);
        if ((ks & 3) == 0) { cs[t] = MFMA16(sel, wf[ks & 1][t], cs[t]); PIN(cs[t]); }
        SBAR();
      };
      make_stats(0); make_stats(1);
      bar();
      STAMP_AT(10);
#pragma unroll
      for (int t = 0; t < 3; ++t) rd(0, t);
      SBAR();
      static_for<0, 8>([&](auto sc) {
        constexpr int s = sc.value;
#pragma unroll
        for (int p = 0; p < 3; ++p) {
#pragma unroll
          for (int t = 0; t < 3; ++t) { mm(4 * s + p, t); rd(4 * s + p + 1, t); SBAR(); }
          if (s == 7 && p == 1) { cswr(4, cs[0]); cswr(5, cs[1]); cswr(6, cs[2]); SBAR(); }
        }
        if constexpr (s == 5) stat_load();
        if constexpr (s == 6) {
          wait_vm1<0>(st);
          row_stats();
          make_xaug();
          if (h == 0) *(LAS float*)(size_t)(lds0 + L_RS + (m * 32 + r32) * 4) = rstd;
        }
        if constexpr (s < 7) {
          bar();
          STAMP_AT(11 + s);
          rd(4 * s + 4, 0); rd(4 * s + 4, 1); rd(4 * s + 4, 2);
          SBAR();
          mm(4 * s + 3, 0); mm(4 * s + 3, 1); mm(4 * s + 3, 2);
        } else {
          mm(31, 1);
          bar();
        }
      });
      STAMP_AT(1);
      __builtin_amdgcn_s_setprio(3);
      const f32x4 c5 = csrd(5), c6 = csrd(6), c4 = csrd(4);
      SBAR();
      mm(31, 2); mm(31, 0);
      acc[1] = MFMA32(waug(c5, 0u), xaug, acc[1]);
      acc[2] = MFMA32(xaug, waug(c6, 0u), acc[2]);
      acc[0] = MFMA32(waug(c4, 0u), xaug, acc[0]);
      SBAR();
      if (heavy) { bias_load(1); w1dma(1, 0, 4); }
      SBAR();
      f32x4 rs[4];
#pragma unroll
      for (int qd = 0; qd < 4; ++qd) rs[qd] = __builtin_bit_cast(f32x4, lds128(lds0 + L_RS + (m * 32 + 8 * qd + 4 * h) * 4));
      const unsigned kvo = WS_KV + (j * 64 + T) * 4096;
#pragma unroll
      for (int e = 0; e < 16; ++e) acc[1][e] = MUL1(acc[1][e], rstd);
#pragma unroll
      for (int e = 0; e < 2; ++e) {
        const v4u f = pack8(acc[1], e);
        st16_sc1(f, sws, lane * 16, kvo + e * 1024);
        sts128(lds0 + L_KV + (8 + m) * KVT + e * 1024 + lane * 16, f);
      }
#pragma unroll
      for (int qd = 0; qd < 4; ++qd)
#pragma unroll
        for (int e = 0; e < 4; ++e) acc[2][4 * qd + e] = MUL1(acc[2][4 * qd + e], rs[qd][e]);
#pragma unroll
      for (int s = 0; s < 2; ++s) {
        const v4u f = pack8(acc[2], s);
        st16_sc1(f, sws, lane * 16, kvo + 2048 + s * 1024);
        sts128(lds0 + L_KV + (8 + m) * KVT + s * VB + vpos, f);
      }
      SBAR();
      const float qs = rstd * (0.25f * 1.4426950408889634f);
#pragma unroll
      for (int e = 0; e < 16; ++e) acc[0][e] = MUL1(acc[0][e], qs);
      qf[0] = pack8(acc[0], 0); qf[1] = pack8(acc[0], 1);
      PIN(qf[0]); PIN(qf[1]);
      STAMP_AT(2);
      const int T0 = 4 * r - 8 + 2 * m;
      const bool live = T0 >= 0;
      auto kv_load = [&](v4u (&kv)[8], bool up) {
        const unsigned src = WS_KV + (j * 64 + (live ? T0 : 0)) * 4096, vo = !up ? 0x40000000u : live ? lane * 16 : h * 512;
#pragma unroll
        for (int i = 0; i < 8; ++i) kv[i] = ld16_sc1_async(sws, vo, src + (live ? i : (i & 3)) * 1024);
      };
      auto kv_stage = [&](v4u (&kv)[8]) {
        const unsigned on = (lane < 2 || live) ? one2 : 0u;
        if (lane < 6) sts128(lds0 + L_KV + (lane < 2 ? 8 + m : 2 * m + ((lane - 2) >> 1)) * KVT + 2048 + (lane & 1) * VB + 1024, (v4u){on, on, on, on});
        const v4u zz = {};
#pragma unroll
        for (int i = 0; i < 2; ++i) {
          sts128(lds0 + L_KV + (2 * m + i) * KVT + lane * 16, kv[4 * i]);
          sts128(lds0 + L_KV + (2 * m + i) * KVT + 1024 + lane * 16, kv[4 * i + 1]);
          sts128(lds0 + L_KV + (2 * m + i) * KVT + vpos, live ? kv[4 * i + 2] : zz);
          sts128(lds0 + L_KV + (2 * m + i) * KVT + VB + vpos, live ? kv[4 * i + 3] : zz);
        }
        arrive_nowait(acnt);
      };
      auto kv_wait = [&](v4u (&kv)[8]) {
        wait_vm<0>();
#pragma unroll
        for (int i = 0; i < 8; ++i) PIN(kv[i]);
      };
      bool early = false;
      if (heavy) {
        w1dma(1, 4, 8);
        hsetup(1);
        wait_vm<12>();
        PIN(bias0); PIN(bias1);
        arrive_nowait(acnt);
        await(acnt, 4);
        __builtin_amdgcn_s_setprio(1);
        STAMP_AT(3);
        v2u fl;
        v4u kv[8];
        hmm(std::integral_constant<int, 1>{}, xf, [&](auto kc) {
          constexpr int ks = kc.value;
          if constexpr (ks == 2) {
            wait_vm<8>();
            if (lane == 0) __hip_atomic_store(flag(FL_KV, T, j), tag, RLX_AGENT);
          }
          if constexpr (ks == 8) wait_vm<5>();
          if constexpr (ks == 14) fl = ld8_sc1_async(sws, flag_off(FL_KV, live ? T0 + (lane & 1) : 0, j));
          if constexpr (ks == 16) wait_vm<2>();
          if constexpr (ks == 25) {
            wait_vmf<0>(fl);
            early = __all(fl[0] == taglo && fl[1] == tag32);
            kv_load(kv, early);
          }
        });
        STAMP_AT(18);
        __builtin_amdgcn_s_setprio(3);
        hgelu(std::integral_constant<int, 1>{});
        STAMP_AT(4);
        wait_vm<4>();
#pragma unroll
        for (int i = 0; i < 8; ++i) PIN(kv[i]);
        if (early) kv_stage(kv);
      } else {
        wait_vm<0>();
        if (lane == 0) __hip_atomic_store(flag(FL_KV, T, j), tag, RLX_AGENT);
      }
      if (!early) {
        v4u kv[8];
        poll_flags(flag(FL_KV, live ? T0 + (lane & 1) : 0, j), tag);
        kv_load(kv, true); kv_wait(kv); kv_stage(kv);
      }
    }
    STAMP_AT(19);
    unsigned am[8];
    static_for<0, 8>([&](auto d) { am[d.value] = upper<2 * d.value>() ? 0xffffffffu : (upper<2 * d.value + 1>() ? 0xffff0000u : 0u); });
    await(acnt, heavy ? 20 : 4);
    STAMP_AT(5);
    v4u kf[9];
    const unsigned kb = lds0 + L_KV + m * KVT + lane * 16;
#pragma unroll
    for (int i = 0; i < 9; ++i) kf[i] = lds128(kb + i * KVT);
#pragma unroll 1
    for (int e = 0; e < 2; ++e) {
      const v4u q = e ? qf[1] : qf[0];
      const unsigned vb = lds0 + L_KV + m * KVT + 2048 + ((g & 1) ? 1024 : e * 512 + h * 256 + (lane & 15) * 16);
      const f32x16 zero = {};
      f32x16 s0 = MFMA32(kf[0], q, zero), s1 = MFMA32(kf[8], q, zero), s2 = MFMA32(kf[1], q, zero);
      SBAR();
      static_for<0, 16>([&](auto u) { s1[u.value] = upper<u.value>() ? s0[u.value] : s1[u.value]; });
      PIN(s1); SBAR();
      float a = -INFINITY, b = -INFINITY;
      auto mx8 = [&](const f32x16& s) {
#pragma unroll
        for (int u = 0; u < 16; u += 4) { a = max3(a, s[u], s[u + 1]); b = max3(b, s[u + 2], s[u + 3]); }
        PIN(a); PIN(b); SBAR();
      };
      s0 = MFMA32(kf[2], q, zero); SBAR(); mx8(s1);
      s1 = MFMA32(kf[3], q, zero); SBAR(); mx8(s2);
      s2 = MFMA32(kf[4], q, zero); SBAR(); mx8(s0);
      s0 = MFMA32(kf[5], q, zero); SBAR(); mx8(s1);
      s1 = MFMA32(kf[6], q, zero); SBAR(); mx8(s2);
      s2 = MFMA32(kf[7], q, zero); SBAR(); mx8(s0);
      v4u v0 = lds128(vb), v1 = lds128(vb + VB), v2 = lds128(vb + 8 * KVT), v3 = lds128(vb + 8 * KVT + VB);
      SBAR();
      mx8(s1); mx8(s2);
      float mx = __builtin_fmaxf(a, b);
      const auto sw = __builtin_amdgcn_permlane32_swap(asu(mx), asu(mx), false, false);
      mx = __builtin_fmaxf(asf(sw[0]), asf(sw[1]));
      if (e == 0) {
        wait_vm<0>();
        if (lane == 0) __hip_atomic_store(flag(FL_H1, T, j), tag, RLX_AGENT);
      }
      f32x16 negm;
#pragma unroll
      for (int u = 0; u < 16; ++u) negm[u] = -mx;
      s0 = MFMA32(kf[0], q, negm); s1 = MFMA32(kf[8], q, negm); s2 = MFMA32(kf[1], q, negm);
      kf[0] = lds128(kb + 1024); kf[8] = lds128(kb + 8 * KVT + 1024); kf[1] = lds128(kb + KVT + 1024);
      SBAR();
      f32x16 o;
      v4u p0, p1;
      {
        static_for<0, 16>([&](auto u) { s1[u.value] = EXP2(upper<u.value>() ? s0[u.value] : s1[u.value]); });
        p0 = pack8(s1, 0); p1 = pack8(s1, 1);
        const v4u m0 = {am[0], am[1], am[2], am[3]}, m1 = {am[4], am[5], am[6], am[7]};
        o = MFMA32(v0, p0 & m0, zero);
        o = MFMA32(v1, p1 & m1, o);
        o = MFMA32(v2, p0 & ~m0, o);
        p1 &= ~m1; v1 = v3;
        SBAR();
      }
      auto tile = [&](auto ic, f32x16& sc, f32x16& sn) {
        constexpr int i = ic.value;
        if constexpr (i < 7) { sn = MFMA32(kf[i + 1], q, negm); kf[i + 1] = lds128(kb + (i + 1) * KVT + 1024); }
        v0 = lds128(vb + i * KVT); v2 = lds128(vb + i * KVT + VB);
        SBAR();
#pragma unroll
        for (int u = 0; u < 6; ++u) sc[u] = EXP2(sc[u]);
        PIN(sc); SBAR();
        o = MFMA32(v1, p1, o);
        SBAR();
        sc[6] = EXP2(sc[6]); sc[7] = EXP2(sc[7]);
        p0 = pack8(sc, 0);
        sc[8] = EXP2(sc[8]); sc[9] = EXP2(sc[9]);
        PIN(sc); PIN(p0); SBAR();
        o = MFMA32(v0, p0, o);
        SBAR();
#pragma unroll
        for (int u = 10; u < 16; ++u) sc[u] = EXP2(sc[u]);
        p1 = pack8(sc, 1); v1 = v2;
        PIN(p1); SBAR();
      };
      tile(std::integral_constant<int, 1>{}, s2, s0); tile(std::integral_constant<int, 2>{}, s0, s2);
      tile(std::integral_constant<int, 3>{}, s2, s0); tile(std::integral_constant<int, 4>{}, s0, s2);
      tile(std::integral_constant<int, 5>{}, s2, s0); tile(std::integral_constant<int, 6>{}, s0, s2);
      tile(std::integral_constant<int, 7>{}, s2, s0);
      o = MFMA32(v1, p1, o);
      const float rl = __builtin_amdgcn_rcpf(o[8]);
#pragma unroll
      for (int u = 0; u < 8; ++u) o[u] = MUL1(o[u], rl);
      st16_sc1(pack8(o, 0), sws, lane * 16, WS_AF + (T * AFT + 112 + 2 * j + e) * 1024);
    }
    STAMP_AT(6);
    wait_vm<0>();
    if (lane == 0) __hip_atomic_store(flag(FL_O, T, j), tag, RLX_AGENT);
    STAMP_AT(7);
    res_loads();
    wait_vm2<0>(xres, bres);
    if (blockIdx.x == 0 && wave == 7) {
#pragma unroll 1
      for (int i = 0; i < 4; ++i) poll_flags(flag(FL_KV, 4 * (4 * i + g), lane & 15), tag);
      if (lane == 0) __hip_atomic_store((GAS u64*)(ws + WS_EPOCH), tag, RLX_AGENT);
    }
  }
  bar();
  {
    int t2 = threadIdx.x; PIN(t2);
    const int row = t2 >> 3, ch = t2 & 7, orow = 64 * rt + row, ocol = 64 * ct + ch * 8;
    const unsigned a = lds0 + L_WG + (((row >> 5) * 8 + ch) * 66 + (row & 31)) * 16;
    f32x4 lo = {}, hi = {};
#pragma unroll
    for (int w = 0; w < 4; ++w) {
      lo += __builtin_bit_cast(f32x4, lds128(a + w * PART));
      hi += __builtin_bit_cast(f32x4, lds128(a + w * PART + 512));
    }
    v4u o;
    o[0] = pk2(lo[0] + bflo(xres[0]) + bflo(bres[0]), lo[1] + bfhi(xres[0]) + bfhi(bres[0]));
    o[1] = pk2(lo[2] + bflo(xres[1]) + bflo(bres[1]), lo[3] + bfhi(xres[1]) + bfhi(bres[1]));
    o[2] = pk2(hi[0] + bflo(xres[2]) + bflo(bres[2]), hi[1] + bfhi(xres[2]) + bfhi(bres[2]));
    o[3] = pk2(hi[2] + bflo(xres[3]) + bflo(bres[3]), hi[3] + bfhi(xres[3]) + bfhi(bres[3]));
    *(GAS v4u*)((char*)out + (size_t)orow * 1024 + ocol * 2) = o;
  }
  STAMP_AT(8);
}

extern "C" void kernel_launch(void* const* d_in, const int* in_sizes, int n_in, void* d_out, int out_size, void* d_ws, size_t ws_size,
                              hipStream_t stream) {
  (void)in_sizes; (void)n_in; (void)out_size; (void)ws_size;
  (void)hipFuncSetAttribute((const void*)fused_block, hipFuncAttributeMaxDynamicSharedMemorySize, LDS_BYTES);
  fused_block<<<256, 512, LDS_BYTES, stream>>>(d_in[0], d_in[1], d_in[2], d_in[4], d_in[5], d_in[6], d_in[7], d_out, (char*)d_ws);
}
```

```cpp
#define CODEWARM 39000
#define TLBWARM 1
#include <hip/hip_runtime.h>
#include <hip/hip_bf16.h>
#include <stdint.h>
#include <type_traits>

typedef unsigned v4u __attribute__((ext_vector_type(4)));
typedef unsigned v2u __attribute__((ext_vector_type(2)));
typedef float f32x2 __attribute__((ext_vector_type(2)));
typedef float f32x4 __attribute__((ext_vector_type(4)));
typedef float f32x16 __attribute__((ext_vector_type(16)));
typedef short bf16x8 __attribute__((ext_vector_type(8)));
typedef short s16x4 __attribute__((ext_vector_type(4)));
typedef __bf16 bf16x2_t __attribute__((ext_vector_type(2)));
typedef unsigned long long u64;
#define GAS __attribute__((address_space(1)))
#define LAS __attribute__((address_space(3)))
#define DI __device__ __forceinline__
#define RLX_AGENT __ATOMIC_RELAXED, __HIP_MEMORY_SCOPE_AGENT

constexpr int SEQ = 2048, DM = 512, FF = 1792;
constexpr unsigned WS_EPOCH = 0;
constexpr unsigned WS_STATS = 32768;
constexpr unsigned WS_FLAG = 65536;
constexpr unsigned WS_KV = WS_FLAG + 5 * 1024 * 128;
constexpr int AFT = 145;
constexpr unsigned WS_AF = WS_KV + 16 * 64 * 4096;
constexpr unsigned WS_END = WS_AF + 64 * AFT * 1024;
constexpr unsigned WS_STAMP = WS_END;
enum { FL_KV = 0, FL_H0 = 1, FL_H1 = 2, FL_O0 = 3, FL_O1 = 4 };
constexpr int STG = 36864;
constexpr int L_WA = 0;
constexpr int L_WG = 86016;
constexpr int VB = 1040;
constexpr int KVT = 2048 + 2 * VB;
constexpr int L_KV = 65536 - 8 * KVT;
constexpr int RD = 8, NR = 12;
constexpr int PART = 16 * 66 * 16;
constexpr int L_CS = L_WG + 4 * PART;
constexpr int L_RS = L_CS + 3584;
constexpr int L_CNT = L_RS + 512;
constexpr int LDS_BYTES = L_CNT + 64;
static_assert(L_KV + 12 * KVT <= L_WG && L_WG >= 2 * STG && L_CS >= 3 * STG && PART >= RD * 2048 && LDS_BYTES <= 163840, "lds map");
#define AWIN 6
constexpr int AW = AWIN;
constexpr unsigned SPIN_CAP = 1u << 18;

#define MFMA32(a, b, c) __builtin_amdgcn_mfma_f32_32x32x16_bf16(__builtin_bit_cast(bf16x8, a), __builtin_bit_cast(bf16x8, b), c, 0, 0, 0)
#define MFMA16(a, b, c) __builtin_amdgcn_mfma_f32_16x16x32_bf16(__builtin_bit_cast(bf16x8, a), __builtin_bit_cast(bf16x8, b), c, 0, 0, 0)
#define SBAR() __builtin_amdgcn_sched_barrier(0)

#define STAMP_AT(i) do { } while (0)

DI float asf(unsigned u) { return __builtin_bit_cast(float, u); }
DI unsigned asu(float f) { return __builtin_bit_cast(unsigned, f); }
DI unsigned pk2(float lo, float hi) { return __builtin_bit_cast(unsigned, __builtin_convertvector((f32x2){lo, hi}, bf16x2_t)); }
DI float bflo(unsigned u) { return __builtin_bit_cast(float, u << 16); }
DI float bfhi(unsigned u) { return __builtin_bit_cast(float, u & 0xffff0000u); }
DI v4u make_srd(const void* p, unsigned bytes) { const u64 a = (u64)p; return (v4u){(unsigned)a, (unsigned)(a >> 32) & 0xffffu, bytes, 0x00020000u}; }
DI void dma16(v4u srd, unsigned voff, unsigned soff, unsigned lds) {
  asm volatile("s_mov_b32 m0, %3\n\ts_nop 0\n\tbuffer_load_dwordx4 %0, %1, %2 offen lds" :: "v"(voff), "s"(srd), "s"(soff), "s"(lds) : "memory");
}
DI void dma16_a(v4u srd, unsigned voff, unsigned soff, unsigned lds) { dma16(srd, voff, soff, lds); }
DI void dma16_b(v4u srd, unsigned voff1, unsigned soff) {
  asm volatile("buffer_load_dwordx4 %0, %1, %2 offen offset:1024 lds" :: "v"(voff1), "s"(srd), "s"(soff) : "memory");
}
DI v4u ld16_sc1_async(v4u srd, unsigned voff, unsigned soff) {
  v4u r; asm volatile("buffer_load_dwordx4 %0, %1, %2, %3 offen sc1" : "=v"(r) : "v"(voff), "s"(srd), "s"(soff) : "memory"); return r;
}
DI v4u ld16_async(v4u srd, unsigned voff, unsigned soff) {
  v4u r; asm volatile("buffer_load_dwordx4 %0, %1, %2, %3 offen" : "=v"(r) : "v"(voff), "s"(srd), "s"(soff) : "memory"); return r;
}
DI unsigned ld2_async(v4u srd, unsigned voff, unsigned soff) {
  unsigned r; asm volatile("buffer_load_ushort %0, %1, %2, %3 offen" : "=v"(r) : "v"(voff), "s"(srd), "s"(soff) : "memory"); return r;
}
DI void st16_sc1(v4u v, v4u srd, unsigned voff, unsigned soff) {
  asm volatile("buffer_store_dwordx4 %0, %1, %2, %3 offen sc1\n\ts_nop 1" :: "v"(v), "v"(voff), "s"(srd), "s"(soff) : "memory");
}
template <int N> DI void wait_vm1(v4u& a) { asm volatile("s_waitcnt vmcnt(%1)" : "+v"(a) : "i"(N) : "memory"); }
template <int N> DI void wait_vm2(v4u& a, v4u& b) { asm volatile("s_waitcnt vmcnt(%2)" : "+v"(a), "+v"(b) : "i"(N) : "memory"); }
template <int N> DI void wait_vm3(v4u& a, v4u& b, v4u& c) { asm volatile("s_waitcnt vmcnt(%3)" : "+v"(a), "+v"(b), "+v"(c) : "i"(N) : "memory"); }
DI u64 load_s(const void* p) { u64 v; asm volatile("s_load_dwordx2 %0, %1, 0x0\n\ts_waitcnt lgkmcnt(0)" : "=&s"(v) : "s"(p) : "memory"); return v; }
DI unsigned ld4_async(const void* p) { unsigned r; asm volatile("global_load_dword %0, %1, off" : "=v"(r) : "v"(p) : "memory"); return r; }
template <class V> DI void pin(V& v) { asm volatile("" : "+v"(v)); }
template <int N> DI void wait_vm() { asm volatile("s_waitcnt vmcnt(%0)" :: "i"(N) : "memory"); }
DI void bar() { asm volatile("s_waitcnt lgkmcnt(0)\n\ts_barrier" ::: "memory"); }
DI v4u lds128(unsigned a) { return *(const LAS v4u*)(size_t)a; }
DI void sts128(unsigned a, v4u v) { *(LAS v4u*)(size_t)a = v; }
DI v2u ldtr(unsigned a) { return __builtin_bit_cast(v2u, __builtin_amdgcn_ds_read_tr16_b64_v4i16((LAS s16x4*)(size_t)a)); }
DI v4u ldtr2(unsigned a, int second) { const v2u lo = ldtr(a), hi = ldtr(a + second); return (v4u){lo.x, lo.y, hi.x, hi.y}; }
template <int CTRL> DI float dpp(float v) { return __builtin_bit_cast(float, __builtin_amdgcn_update_dpp(0, __builtin_bit_cast(int, v), CTRL, 0xf, 0xf, true)); }
DI float rdl(float v, int l) { return __builtin_bit_cast(float, __builtin_amdgcn_readlane(__builtin_bit_cast(int, v), l)); }
DI v4u pack8(const f32x16& a, int s) { return (v4u){pk2(a[8 * s], a[8 * s + 1]), pk2(a[8 * s + 2], a[8 * s + 3]), pk2(a[8 * s + 4], a[8 * s + 5]), pk2(a[8 * s + 6], a[8 * s + 7])}; }
DI void poll_flags(const GAS u64* p, u64 tag) {
#pragma unroll 1
  for (unsigned spin = 0; spin < SPIN_CAP; ++spin) {
    const u64 v = __hip_atomic_load(p, RLX_AGENT);
    if (__all(v == tag)) break;
    __builtin_amdgcn_s_sleep(2);
  }
  asm volatile("" ::: "memory");
}
constexpr u64 upper_mask(int reg) {
  u64 m = 0;
  for (int l = 0; l < 64; ++l) { const int row = (reg & 3) + 8 * (reg >> 2) + 4 * (l >> 5); if (row > (l & 31)) m |= 1ull << l; }
  return m;
}
template <int REG> DI bool upper() { return __builtin_amdgcn_inverse_ballot_w64(upper_mask(REG)); }
template <int I, int N, class Fn> DI void static_for(Fn&& f) { if constexpr (I < N) { f(std::integral_constant<int, I>{}); static_for<I + 1, N>(f); } }
DI float max3(float a, float b, float c) { return __builtin_fmaxf(__builtin_fmaxf(a, b), c); }

constexpr int H1_CK = 16 - AW, H1_LD = H1_CK - 3, O_CK = 28 - AW, O_LD = O_CK - 3;
constexpr int g2_issued(int t) { return ((t >= 1 && t - 1 + NR + RD < 36) ? 2 : 0) + ((t + AW < 28) ? 2 : 0) + (t == H1_LD) + (t == O_LD) + (t == O_CK ? 10 : 0); }
constexpr int g2_range(int a, int b) { int n = 0; for (int t = a; t < b; ++t) n += g2_issued(t); return n; }
#define GELU_PK(P) (P == 1)
#define PRIO_G2 2
#define PRIO_ATT 3
#define PRIO_G1 2
#define ROT r
#define WSOFF 0
#define KVPUB 6
#define EXP2(v) __builtin_amdgcn_exp2f(v)
#define PIN(v) pin(v)
extern "C" __device__ float mul_unpacked(float, float) __asm("llvm.amdgcn.fmul.legacy");
#define MUL1(a, b) mul_unpacked(a, b)
#define FMA1(a, b, c) __builtin_amdgcn_div_fmasf(a, b, c, false)
DI v2u ld8_sc1_async(v4u srd, unsigned voff) {
  v2u r; asm volatile("buffer_load_dwordx2 %0, %1, %2, 0 offen sc1" : "=v"(r) : "v"(voff), "s"(srd) : "memory"); return r;
}
template <int N> DI void wait_vmf(v2u& a) { asm volatile("s_waitcnt vmcnt(%1)" : "+v"(a) : "i"(N) : "memory"); }
DI void arrive(unsigned cnt, int lane) {
  asm volatile("s_waitcnt lgkmcnt(0)" ::: "memory");
  if (lane == 0) __hip_atomic_fetch_add((LAS unsigned*)(size_t)cnt, 1u, __ATOMIC_RELAXED, __HIP_MEMORY_SCOPE_WORKGROUP);
}
DI void arrive_nowait(unsigned cnt) {
  asm volatile("s_mov_b64 exec, 1\n\tds_add_u32 %0, %1\n\ts_mov_b64 exec, -1" :: "v"(cnt), "v"(1u) : "memory");
}
DI unsigned peek(unsigned cnt) { return __hip_atomic_load((LAS unsigned*)(size_t)cnt, __ATOMIC_RELAXED, __HIP_MEMORY_SCOPE_WORKGROUP); }
DI void await(unsigned cnt, unsigned n) {
#pragma unroll 1
  for (unsigned spin = 0; spin < SPIN_CAP * 8; ++spin) {
    if (__builtin_amdgcn_readfirstlane(peek(cnt)) >= n) break;
    __builtin_amdgcn_s_sleep(1);
  }
  asm volatile("" ::: "memory");
}

__global__ __launch_bounds__(512, 2) void fused_block(const void* __restrict__ x, const void* __restrict__ wqkv, const void* __restrict__ wo,
                                                      const void* __restrict__ w1, const void* __restrict__ b1, const void* __restrict__ w2,
                                                      const void* __restrict__ b2, void* __restrict__ out, char* __restrict__ ws) {
  constexpr unsigned lds0 = 0;
  const int wave = __builtin_amdgcn_readfirstlane(threadIdx.x >> 6);
  const int m = wave & 3;
  const bool isG = wave < 4;
  const int xcd = blockIdx.x & 7, idx = blockIdx.x >> 3;
  const int r = (xcd >> 2) * 8 + (idx >> 2), j = (xcd & 3) * 4 + (idx & 3);
  const int rt = (xcd >> 1) * 8 + (idx >> 2), ct = (xcd & 1) * 4 + (idx & 3);
  int lane = threadIdx.x & 63, r32, h, g, q4, p4;
  STAMP_AT(0);
  asm volatile("" :: "s"(x), "s"(wqkv), "s"(wo), "s"(w1), "s"(b1), "s"(w2), "s"(b2), "s"(out), "s"(ws));

  const v4u sx = make_srd(x, SEQ * DM * 2), sws = make_srd(ws, WS_END);
  const __amdgpu_buffer_rsrc_t rws = __builtin_amdgcn_make_buffer_rsrc(ws, 0, WS_END, 0x00020000);
  const unsigned one2 = 0x3f803f80u;
  const int T = 4 * r + m;
  const int rot = ROT;
  auto flag_off = [&](int kind, int tile, int jj) { return WS_FLAG + ((kind * 64 + tile) * 16 + jj) * 128; };
  auto flag = [&](int kind, int tile, int jj) { return (GAS u64*)(ws + flag_off(kind, tile, jj)); };
  u64 tag;
  unsigned taglo, tag32;
  auto get_tag = [&]() {
    tag = load_s(ws + WS_EPOCH);
    taglo = (unsigned)tag * 0x9E3779B1u + 0x7F4A7C15u; tag32 = (unsigned)(tag >> 32) * 0x85EBCA77u + 0x165667B1u;
    tag = ((u64)tag32 << 32) | taglo;
  };
  v4u xres, bres, xaug, st;
  float mean, var, rstd, sd;
  v4u sel;
  unsigned xo[4], swz;
  auto lane_setup = [&]() {
    lane = threadIdx.x & 63; PIN(lane);
    r32 = lane & 31; h = lane >> 5; g = lane >> 4; q4 = (lane >> 2) & 3; p4 = lane & 3;
    const unsigned selv = ((lane & 15) == (g & 1)) ? one2 : 0u;
    sel = (v4u){selv, selv, selv, selv};
#pragma unroll
    for (int p = 0; p < 4; ++p) xo[p] = (32 * m + r32) * 128 + (((2 * ((p + m) & 3) + h) ^ ((r32 >> 1) & 7)) * 16);
    swz = ((((lane >> 2) & 1) ^ (g & 1)) * 64) + p4 * 16;
  };

  auto stat_load = [&]() { st = ld16_sc1_async(sws, WS_STATS + (128 * r + 32 * m + r32) * 16, 0); };
  auto res_loads = [&]() {
    int t2 = threadIdx.x; PIN(t2);
    const int row = 64 * rt + (t2 >> 3), col = 64 * ct + (t2 & 7) * 8;
    xres = ld16_async(sx, row * 1024 + col * 2, 0);
    bres = ld16_async(make_srd(b2, DM * 2), col * 2, 0);
  };
  auto row_stats = [&]() {
    if (!__all(st[1] == tag32 && st[3] == tag32)) {
#pragma unroll 1
      for (unsigned spin = 0; spin < SPIN_CAP; ++spin) {
        st = ld16_sc1_async(sws, WS_STATS + (128 * r + 32 * m + r32) * 16, 0);
        wait_vm1<0>(st);
        if (__all(st[1] == tag32 && st[3] == tag32)) break;
      }
    }
    mean = asf(st[0]); var = asf(st[2]);
    PIN(mean); PIN(var);
  };
  auto make_xaug = [&]() {
    PIN(mean); PIN(var);
    rstd = __builtin_amdgcn_rsqf(var); sd = var * rstd;
    const unsigned hi = pk2(-mean, sd);
    const unsigned lo = pk2(-mean - bflo(hi), FMA1(bfhi(hi), -1.f, sd));
    xaug = (v4u){(hi & 0xffffu) | (hi << 16), (lo & 0xffffu) | (hi & 0xffff0000u), lo >> 16, 0u};
    if (h) xaug = (v4u){0u, 0u, 0u, 0u};
    PIN(xaug);
  };
  auto cswr = [&](int tile, f32x4 c) {
    if (lane < 16) { LAS float* p = (LAS float*)(size_t)(lds0 + L_CS + (tile * 32 + lane) * 16 + m * 4); p[0] = c[0]; p[64] = c[1]; }
  };
  auto csrd = [&](int tile) { return __builtin_bit_cast(f32x4, lds128(lds0 + L_CS + (tile * 32 + r32) * 16)); };
  auto waug = [&](f32x4 c4, unsigned b16) {
    const float c = ((c4[0] + c4[1]) + c4[2]) + c4[3];
    const unsigned hi = pk2(c, 0.f);
    const unsigned lo = pk2(c - bflo(hi), 0.f);
    return (v4u){hi | (lo << 16), hi | (b16 << 16), b16, 0u};
  };

  const v4u sw1 = make_srd(w1, DM * FF * 2);
  const int hcol0 = 448 * (j >> 2) + 128 * (j & 3);
  const bool heavy = (j & 3) != 3;
  unsigned bias0, bias1;
  auto bias_load = [&](int P) {
    bias0 = ld2_async(make_srd(b1, FF * 2), r32 * 2, (hcol0 + 64 * P) * 2);
    bias1 = ld2_async(make_srd(b1, FF * 2), r32 * 2, (hcol0 + 64 * P + 32) * 2);
  };
  auto w1dma = [&](int P, int c, unsigned dst, int half) {
    const unsigned vw1 = (lane >> 3) * (FF * 2) + swz, vw1b = vw1 + 8 * (FF * 2) - 1024;
    const unsigned so = (64 * ((c + rot) & 7) + 16 * m) * (FF * 2) + (hcol0 + 64 * P) * 2;
    if (half == 0) dma16_a(sw1, vw1, so, dst + m * 2048); else dma16_b(sw1, vw1b, so);
  };
  f32x16 hac[2];
  unsigned rk[4][2];
  auto hsetup = [&](int P) {
#pragma unroll
    for (int p = 0; p < 4; ++p)
#pragma unroll
      for (int c = 0; c < 2; ++c) {
        rk[p][c] = lds0 + (P ? L_WA : 28672) + ((p + m) & 3) * 2048 + h * 1024 + q4 * 128 + ((c ^ (q4 >> 1)) * 64) + (g & 1) * 32 + p4 * 8;
        PIN(rk[p][c]);
      }
  };
  auto hmm = [&](auto pc, const v4u (&xf)[32], auto&& hook) {
    constexpr int P = pc.value;
    constexpr int D = 4;
    const unsigned cnt = lds0 + L_CNT;
    f32x4 cs[2] = {}, c0, c1;
    v4u wf[D][2], au0, au1;
    unsigned seen = 0;
    hac[0] = (f32x16){}; hac[1] = (f32x16){};
    auto rd = [&](int ks, int t) { wf[ks % D][t] = ldtr2(rk[ks & 3][t] + (ks >> 2) * 8192, 512); };
#pragma unroll
    for (int ks = 0; ks < D; ++ks) { rd(ks, 0); rd(ks, 1); }
    SBAR();
    static_for<0, 32>([&](auto kc) {
      constexpr int ks = kc.value;
      hook(kc);
      if constexpr (ks == 10) seen = peek(cnt + 20);
      if constexpr (ks == 29) seen = peek(cnt);
#pragma unroll
      for (int t = 0; t < 2; ++t) {
        hac[t] = MFMA32(wf[ks % D][t], xf[ks], hac[t]);
        if ((ks & 3) == 0 && ks < 28) { cs[t] = MFMA16(sel, wf[ks % D][t], cs[t]); PIN(cs[t]); }
        if (ks == 26) { cs[t] = MFMA16(sel, wf[28 % D][t], cs[t]); PIN(cs[t]); }
        SBAR();
        if constexpr (ks + D < 32) rd(ks + D, t);
        if constexpr (ks == 31) { if (t == 0) { au0 = waug(c0, bias0); PIN(au0); } else { au1 = waug(c1, bias1); PIN(au1); } }
        SBAR();
      }
      if constexpr (ks == 11 || ks == 30) {
        PIN(hac[0]); PIN(hac[1]);
        if (__builtin_amdgcn_readfirstlane(seen) < 4) await(ks == 11 ? cnt + 20 : cnt, 4);
        asm volatile("" ::: "memory");
        if constexpr (ks == 30) { c0 = csrd(2 * P); c1 = csrd(2 * P + 1); }
      }
      if constexpr (ks == 27) { cswr(2 * P, cs[0]); cswr(2 * P + 1, cs[1]); arrive_nowait(cnt); }
      SBAR();
    });
    hac[0] = MFMA32(au0, xaug, hac[0]);
    hac[1] = MFMA32(au1, xaug, hac[1]);
    SBAR();
  };
  auto hgelu = [&](auto pc, auto&& mid) {
    constexpr int P = pc.value;
    constexpr bool PK = GELU_PK(P);
    const float C0 = 2.3022081986f, C1 = C0 * 0.044715f;
    const float ga = -C1 * rstd * rstd * rstd, gb = -C0 * rstd;
    auto mul = [&](f32x2 a, f32x2 b) { return PK ? a * b : (f32x2){MUL1(a[0], b[0]), MUL1(a[1], b[1])}; };
    auto fma = [&](f32x2 a, float b, float c) {
      return PK ? __builtin_elementwise_fma(a, (f32x2){b, b}, (f32x2){c, c}) : (f32x2){FMA1(a[0], b, c), FMA1(a[1], b, c)}; };
#pragma unroll
    for (int t = 0; t < 2; ++t) {
      f32x2 v[8], z[8];
#pragma unroll
      for (int e = 0; e < 8; ++e) { v[e] = (f32x2){hac[t][2 * e], hac[t][2 * e + 1]}; z[e] = mul(v[e], v[e]); }
      SBAR();
#pragma unroll
      for (int e = 0; e < 8; ++e) z[e] = fma(z[e], ga, gb);
      SBAR();
#pragma unroll
      for (int e = 0; e < 8; ++e) z[e] = mul(z[e], v[e]);
      SBAR();
#pragma unroll
      for (int e = 0; e < 8; ++e) z[e] = (f32x2){EXP2(z[e][0]), EXP2(z[e][1])};
      SBAR();
#pragma unroll
      for (int e = 0; e < 8; ++e) z[e] = fma(z[e], sd, sd);
      SBAR();
#pragma unroll
      for (int e = 0; e < 8; ++e) z[e] = (f32x2){__builtin_amdgcn_rcpf(z[e][0]), __builtin_amdgcn_rcpf(z[e][1])};
      SBAR();
#pragma unroll
      for (int e = 0; e < 8; ++e) z[e] = mul(z[e], v[e]);
      SBAR();
#pragma unroll
      for (int s = 0; s < 2; ++s) {
        const v4u f = {pk2(z[4 * s][0], z[4 * s][1]), pk2(z[4 * s + 1][0], z[4 * s + 1][1]), pk2(z[4 * s + 2][0], z[4 * s + 2][1]), pk2(z[4 * s + 3][0], z[4 * s + 3][1])};
        st16_sc1(f, sws, lane * 16, WS_AF + (T * AFT + (hcol0 >> 4) + 4 * P + 2 * t + s) * 1024);
      }
      if (t == 0) mid();
      SBAR();
    }
  };

  if (isG) {
    lane_setup();
    const v4u sw2 = make_srd(w2, FF * DM * 2), swo = make_srd(wo, DM * DM * 2);
    const unsigned vw2 = (lane >> 3) * 1024 + swz, vw2b = vw2 + 7 * 1024;
    const int ro3 = rt % 3;
    auto kmap = [&](int kk) {
      if (kk < 16) return 8 * (((kk >> 2) + rt) & 3) + (kk & 3);
      const int p = ((kk - 16) >> 2) + ro3;
      return 8 * (p >= 3 ? p - 3 : p) + 4 + (kk & 3);
    };
    auto omap = [&](int i) { return 2 * ((i + rt) & 3) + (i >> 2); };
    auto wdmah = [&](int kk, int half) {
      const unsigned so = (kk < 28 ? 448 * m + 16 * kmap(kk) : 128 * m + 16 * omap(kk - 28)) * 1024 + 128 * ct;
      if (half == 0) dma16_a(kk < 28 ? sw2 : swo, vw2, so, lds0 + L_WG + m * PART + (kk % RD) * 2048); else dma16_b(kk < 28 ? sw2 : swo, vw2b, so);
    };
    auto wdma = [&](int kk) { wdmah(kk, 0); wdmah(kk, 1); };
    {
      const v4u swq = make_srd(wqkv, DM * 3 * DM * 2);
      const unsigned vx0 = (lane >> 3) * 1024 + (((lane & 7) ^ g) * 16), vx1 = (7 + (lane >> 3)) * 1024 + (((lane & 7) ^ (4 + g)) * 16);
      const unsigned vwq = (lane >> 2) * (3 * DM * 2) + p4 * 16;
      auto piece = [&](int s, int i) {
        const unsigned buf = lds0 + (s % 3) * STG;
        const int kc = (s + rot) & 7;
        if (i < 4) {
          const unsigned so = (128 * r + 32 * m + 16 * (i >> 1)) * 1024 + kc * 128;
          if (i & 1) dma16_b(sx, vx1, so); else dma16_a(sx, vx0, so, buf + m * 4096 + (i >> 1) * 2048);
        } else if (i < 7) dma16(swq, vwq, (64 * kc + 16 * m) * (3 * DM * 2) + (512 * (i - 4) + 32 * j) * 2, buf + 16384 + ((i - 4) * 4 + m) * 1024);
        else w1dma(0, s, buf + 28672, i - 7);
      };
#pragma unroll
      for (int i = 0; i < 18; ++i) { piece(i / 9, i % 9); if (i == 8) __builtin_amdgcn_s_barrier(); }
      __builtin_amdgcn_s_setprio(PRIO_G1);
      hsetup(0);
      f32x4 cs[2] = {};
      v4u xf[4], wf[4][2];
      hac[0] = (f32x16){}; hac[1] = (f32x16){};
      static_for<0, 8>([&](auto sc) {
        constexpr int s = sc.value;
        constexpr unsigned buf = (s % 3) * STG;
        auto fill = [&](int i) {
          if constexpr (s < 6) piece(s + 2, i);
          if constexpr (s == 6) { if (i == 0) { stat_load(); bias_load(0); } else wdmah((i - 1) >> 1, (i - 1) & 1); }
          if constexpr (s == 7) { if (heavy) { w1dma(1, i, lds0 + L_WA + i * 8192, 0); w1dma(1, i, 0, 1); } }
          SBAR();
        };
        auto rd = [&](int p) { xf[p] = lds128(buf + xo[p]); wf[p][0] = ldtr2(buf + rk[p][0], 512); wf[p][1] = ldtr2(buf + rk[p][1], 512); SBAR(); };
        if constexpr (s < 7) wait_vm<9>(); else { wait_vm1<8>(st); PIN(bias0); PIN(bias1); }
        bar();
        STAMP_AT(10 + s);
        rd(0);
        if constexpr (s < 7) fill(0);
        rd(1);
        if constexpr (s < 7) fill(1);
        if constexpr (s == 1) {
          const v2u tg = *(const LAS v2u*)(size_t)(lds0 + L_CNT + 32);
          taglo = __builtin_amdgcn_readfirstlane(tg[0]); tag32 = __builtin_amdgcn_readfirstlane(tg[1]);
          tag = ((u64)tag32 << 32) | taglo;
        }
#pragma unroll
        for (int p = 0; p < 4; ++p) {
#pragma unroll
          for (int t = 0; t < 2; ++t) {
            hac[t] = MFMA32(wf[p][t], xf[p], hac[t]);
            if (p == 0) { cs[t] = MFMA16(sel, wf[p][t], cs[t]); PIN(cs[t]); }
            SBAR();
            if (p < 2 && t == 0) rd(p + 2);
            if (s == 7) fill(2 * p + t); else if (2 * p + t < 7) fill(2 + 2 * p + t);
          }
          if (s == 7 && p == 1) {
            cswr(0, cs[0]); cswr(1, cs[1]);
            bar();
            STAMP_AT(1);
          }
        }
        if constexpr (s == 7) {
          const f32x4 c0 = csrd(0), c1 = csrd(1);
          row_stats(); make_xaug();
          hac[0] = MFMA32(waug(c0, bias0), xaug, hac[0]);
          hac[1] = MFMA32(waug(c1, bias1), xaug, hac[1]);
          SBAR();
        }
      });
    }
    STAMP_AT(18);
    if (heavy) { wait_vm<8>(); arrive_nowait(lds0 + L_CNT + 16); }
    hgelu(std::integral_constant<int, 0>{}, [&]() {
      wait_vm<2>();
      if (heavy) arrive_nowait(lds0 + L_CNT + 20);
    });
    __builtin_amdgcn_s_setprio(0);
    STAMP_AT(19);
    unsigned rb2[2];
#pragma unroll
    for (int c = 0; c < 2; ++c) rb2[c] = lds0 + L_WG + m * PART + h * 512 + q4 * 128 + ((c ^ (q4 >> 1)) * 64) + (g & 1) * 32 + p4 * 8;
    v4u wq[36][2];
    auto wrd = [&](int kk) {
#pragma unroll
      for (int c = 0; c < 2; ++c) wq[kk][c] = ldtr2(rb2[c] + (kk % RD) * 2048, 1024);
    };
    auto wgot = [&](int kk) { PIN(wq[kk][0]); PIN(wq[kk][1]); };
    const int ftile = 2 * rt + ((lane >> 2) & 1);
    v4u af[28][2], of[8][2];
    v2u fl;
    const unsigned a0 = WS_AF + (2 * rt * AFT + 28 * m) * 1024, o0 = WS_AF + (2 * rt * AFT + 112 + 8 * m) * 1024;
    for (int kk = 0; kk < 4; ++kk) wrd(kk);
    for (int kk = 4; kk < 8; ++kk) wdma(kk);
    for (int kk = 0; kk < 4; ++kk) wgot(kk);
    wait_vm<8>();
    if (lane == 0) __hip_atomic_store(flag(FL_H0, T, j), tag, RLX_AGENT);
    STAMP_AT(2);
    for (int kk = 8; kk < 12; ++kk) wdma(kk);
    static_for<1, 3>([&](auto ic) {
      constexpr int i = ic.value;
      wait_vm<(i == 1) ? 9 : 8>();
      for (int kk = 4 * i; kk < 4 * i + 4; ++kk) wrd(kk);
      for (int kk = 4 * i; kk < 4 * i + 4; ++kk) wgot(kk);
      if constexpr (i == 2) fl = ld8_sc1_async(sws, flag_off(FL_H0, ftile, 4 * m + p4));
      for (int kk = 4 * i; kk < 4 * i + 4; ++kk) wdma(kk + RD);
    });
    STAMP_AT(3);
    wait_vmf<8>(fl);
    if (!__all(fl[0] == taglo && fl[1] == tag32)) poll_flags(flag(FL_H0, ftile, 4 * m + p4), tag);
    STAMP_AT(4);
#pragma unroll
    for (int kk = 0; kk < AW; ++kk) {
      af[kk][0] = ld16_sc1_async(sws, lane * 16, a0 + kmap(kk) * 1024);
      af[kk][1] = ld16_sc1_async(sws, lane * 16, a0 + (AFT + kmap(kk)) * 1024);
    }
    __builtin_amdgcn_s_setprio(PRIO_G2);
    f32x16 c2[2][2] = {};
    auto mm2 = [&](int kk, const v4u (&a)[2], auto&& fill) {
      SBAR();
#pragma unroll
      for (int i = 0; i < 4; ++i) { c2[i >> 1][i & 1] = MFMA32(wq[kk][i & 1], a[i >> 1], c2[i >> 1][i & 1]); SBAR(); fill(i); SBAR(); }
    };
    static_for<0, 28>([&](auto kc) {
      constexpr int kk = kc.value;
      wait_vm2<(kk < AW) ? 2 * (AW - 1 - kk) + g2_range(0, kk) : g2_range(kk - AW + 1, kk)>(af[kk][0], af[kk][1]);
      if constexpr (kk == H1_CK) {
        STAMP_AT(20);
        wait_vmf<g2_range(H1_LD, H1_CK) - 1>(fl);
        if (!__all(fl[0] == taglo && fl[1] == tag32)) poll_flags(flag(FL_H1, ftile, 4 * m + p4), tag);
        STAMP_AT(21);
      }
      if constexpr (kk == H1_LD) fl = ld8_sc1_async(sws, flag_off(FL_H1, ftile, 4 * m + p4));
      if constexpr (kk == O_LD) fl = ld8_sc1_async(sws, flag_off(FL_O0, ftile, 4 * m + p4));
      constexpr bool dw = kk >= 1 && kk - 1 + NR + RD < 36;
      if constexpr (dw) wgot(kk - 1 + NR);
      mm2(kk, af[kk], [&](int i) {
        if constexpr (dw) { if (i < 2) wdmah(kk - 1 + NR + RD, i); }
        if constexpr (kk + NR < 28) { if (i == 1) wrd(kk + NR); }
        if constexpr (kk == 27) { if (i == 0) wrd(28); }
        if constexpr (kk + AW < 28) { if (i >= 2) af[kk + AW][i - 2] = ld16_sc1_async(sws, lane * 16, a0 + (AFT * (i - 2) + kmap(kk + AW)) * 1024); }
      });
      if constexpr (kk == O_CK) {
        wait_vmf<g2_range(O_LD, O_CK) - 1>(fl);
        if (!__all(fl[0] == taglo && fl[1] == tag32)) poll_flags(flag(FL_O0, ftile, 4 * m + p4), tag);
        res_loads();
#pragma unroll
        for (int i = 0; i < 4; ++i) {
          of[i][0] = ld16_sc1_async(sws, lane * 16, o0 + omap(i) * 1024);
          of[i][1] = ld16_sc1_async(sws, lane * 16, o0 + (AFT + omap(i)) * 1024);
        }
      }
    });
    static_for<0, 8>([&](auto kc) {
      constexpr int kk = kc.value;
      if (kk < 7) wrd(28 + kk + 1);
      if constexpr (kk == 1) {
        STAMP_AT(5);
        poll_flags(flag(FL_O1, ftile, 4 * m + p4), tag);
        STAMP_AT(6);
#pragma unroll
        for (int i = 4; i < 8; ++i) {
          of[i][0] = ld16_sc1_async(sws, lane * 16, o0 + omap(i) * 1024);
          of[i][1] = ld16_sc1_async(sws, lane * 16, o0 + (AFT + omap(i)) * 1024);
        }
      }
      wait_vm2<(kk == 0) ? 6 : 14 - 2 * kk>(of[kk][0], of[kk][1]);
      mm2(28 + kk, of[kk], [&](int) {});
    });
    PIN(xres); PIN(bres);
    asm volatile("" ::: "memory");
#pragma unroll
    for (int mt = 0; mt < 2; ++mt)
#pragma unroll
      for (int c = 0; c < 2; ++c)
#pragma unroll
        for (int qd = 0; qd < 4; ++qd) {
          const f32x4 v = {c2[mt][c][4 * qd], c2[mt][c][4 * qd + 1], c2[mt][c][4 * qd + 2], c2[mt][c][4 * qd + 3]};
          sts128(lds0 + L_WG + m * PART + ((mt * 8 + c * 4 + qd) * 66 + lane) * 16, __builtin_bit_cast(v4u, v));
        }
    STAMP_AT(7);
  } else {
    __builtin_amdgcn_s_setprio(1);
    lane_setup();
    v4u xr[2];
    xr[0] = ld16_async(sx, lane * 16, (128 * r + 8 * j + 2 * m) * 1024); xr[1] = ld16_async(sx, lane * 16, (128 * r + 8 * j + 2 * m + 1) * 1024);
    unsigned warm2;
    {
      constexpr int NWS = (WS_END + 65535) / 65536;
      static_assert(NWS + 28 + 8 + 28 + 32 + 2 <= 32 * 12, "touch list");
      int t = (idx * 4 + m) * 3 + (lane < 3 ? lane : 0);
      const char* p = ws;
      if (t >= NWS) { t -= NWS; p = (const char*)w2;
        if (t >= 28) { t -= 28; p = (const char*)wo;
          if (t >= 8) { t -= 8; p = (const char*)w1;
            if (t >= 28) { t -= 28; p = (const char*)out;
              if (t >= 32) { p = (const char*)(t == 32 ? b1 : b2); t = 0; } } } } }
      warm2 = ld4_async(p + t * 65536);
    }
    unsigned warm;
    {
      u64 pc; asm volatile("s_getpc_b64 %0" : "=s"(pc));
      warm = ld2_async(make_srd((const void*)(pc & ~127ull), CODEWARM), lane < 3 ? lane * 128 : 0x40000000u, (idx * 4 + m) * 384);
    }
    __builtin_amdgcn_s_barrier();
    get_tag();
    if (m == 0 && lane < 10) *(LAS unsigned*)(size_t)(lds0 + L_CNT + lane * 4) = lane == 8 ? taglo : lane == 9 ? tag32 : 0u;
    auto make_stats = [&]() {
      wait_vm2<0>(xr[0], xr[1]);
      PIN(warm);
      PIN(warm2);
      float piv[2], a[4];
#pragma unroll
      for (int u = 0; u < 2; ++u) {
        piv[u] = rdl(bflo(xr[u][0]), 0);
        float s1 = 0.f, s2 = 0.f;
#pragma unroll
        for (int i = 0; i < 8; ++i) { const float d = ((i & 1) ? bfhi(xr[u][i >> 1]) : bflo(xr[u][i >> 1])) - piv[u]; s1 += d; s2 = __builtin_fmaf(d, d, s2); }
        a[2 * u] = s1; a[2 * u + 1] = s2;
      }
#pragma unroll
      for (int i = 0; i < 4; ++i) a[i] += dpp<0xB1>(a[i]);
#pragma unroll
      for (int i = 0; i < 4; ++i) a[i] += dpp<0x4E>(a[i]);
      float t = p4 == 0 ? a[0] : p4 == 1 ? a[1] : p4 == 2 ? a[2] : a[3];
      t += dpp<0x114>(t); t += dpp<0x118>(t);
      const auto s16 = __builtin_amdgcn_permlane16_swap(asu(t), asu(t), false, false);
      t = asf(s16[0]) + asf(s16[1]);
      const auto s32 = __builtin_amdgcn_permlane32_swap(asu(t), asu(t), false, false);
      t = (asf(s32[0]) + asf(s32[1])) * (1.f / DM);
      const float e1 = dpp<0xA0>(t);
      const float val = (p4 & 1) ? __builtin_fmaxf(__builtin_fmaf(-e1, e1, t), 0.f) + 1e-5f : t + (p4 ? piv[1] : piv[0]);
      if ((lane >> 2) == 3) {
        const v2u gr = {asu(val), tag32};
        __builtin_amdgcn_raw_buffer_store_b64(gr, rws, WS_STATS + (128 * r + 8 * j + 2 * m) * 16 + p4 * 8, 0, 16);
      }
    };
    const unsigned acnt = lds0 + L_CNT;
    const unsigned vpos = 2048 + (g & 1) * 512 + h * 256 + (lane & 15) * 16;
    v4u qf[2], am0, am1;
    {
      v4u xf[32];
      f32x16 acc[3] = {};
      f32x4 cs[3] = {};
      v4u wf[4][3];
      unsigned wb[4];
#pragma unroll
      for (int p = 0; p < 4; ++p) wb[p] = 16384 + ((p + m) & 3) * 1024 + (8 * h + q4) * 64 + (g & 1) * 32 + p4 * 8;
      auto rd = [&](int ks, int t) {
        const unsigned buf = lds0 + ((ks >> 2) % 3) * STG;
        if (t == 0) xf[ks] = lds128(buf + xo[ks & 3]);
        wf[ks & 3][t] = ldtr2(buf + wb[ks & 3] + t * 4096, 256);
      };
      auto mm = [&](int ks, int t) {
        acc[t] = t == 2 ? MFMA32(xf[ks], wf[ks & 3][t], acc[t]) : MFMA32(wf[ks & 3][t], xf[ks], acc[t]);
        if ((ks & 3) == 0) { cs[t] = MFMA16(sel, wf[ks & 3][t], cs[t]); PIN(cs[t]); }
        SBAR();
      };
      make_stats();
      bar();
      STAMP_AT(10);
#pragma unroll
      for (int t = 0; t < 6; ++t) rd(t / 3, t % 3);
      SBAR();
      static_for<0, 8>([&](auto sc) {
        constexpr int s = sc.value;
#pragma unroll
        for (int p = 0; p < 2; ++p) {
#pragma unroll
          for (int t = 0; t < 3; ++t) { mm(4 * s + p, t); rd(4 * s + p + 2, t); SBAR(); }
          if (s == 7 && p == 0) { cswr(4, cs[0]); cswr(5, cs[1]); cswr(6, cs[2]); SBAR(); }
        }
        if constexpr (s == 5) stat_load();
        if constexpr (s == 6) {
          wait_vm1<0>(st);
          row_stats();
        }
        mm(4 * s + 2, 0); mm(4 * s + 2, 1);
        bar();
        if constexpr (s < 7) {
          STAMP_AT(11 + s);
          mm(4 * s + 2, 2);
          rd(4 * s + 4, 0); rd(4 * s + 4, 1); rd(4 * s + 4, 2); SBAR();
#pragma unroll
          for (int t = 0; t < 3; ++t) { mm(4 * s + 3, t); rd(4 * s + 5, t); SBAR(); }
        }
      });
      STAMP_AT(1);
      __builtin_amdgcn_s_setprio(3);
      const f32x4 c5 = csrd(5), c6 = csrd(6), c4 = csrd(4);
      SBAR();
      mm(30, 2); mm(31, 1);
      make_xaug();
      SBAR();
      mm(31, 2); mm(31, 0);
      if (h == 0) *(LAS float*)(size_t)(lds0 + L_RS + (m * 32 + r32) * 4) = rstd;
      acc[1] = MFMA32(waug(c5, 0u), xaug, acc[1]);
      acc[2] = MFMA32(xaug, waug(c6, 0u), acc[2]);
      acc[0] = MFMA32(waug(c4, 0u), xaug, acc[0]);
      SBAR();
      f32x4 rs[4];
#pragma unroll
      for (int qd = 0; qd < 4; ++qd) rs[qd] = __builtin_bit_cast(f32x4, lds128(lds0 + L_RS + (m * 32 + 8 * qd + 4 * h) * 4));
      const unsigned kvo = WS_KV + (j * 64 + T) * 4096;
#pragma unroll
      for (int e = 0; e < 16; ++e) acc[1][e] = MUL1(acc[1][e], rstd);
#pragma unroll
      for (int e = 0; e < 2; ++e) {
        const v4u f = pack8(acc[1], e);
        st16_sc1(f, sws, lane * 16, kvo + e * 1024);
        sts128(lds0 + L_KV + (8 + m) * KVT + e * 1024 + lane * 16, f);
      }
#pragma unroll
      for (int qd = 0; qd < 4; ++qd)
#pragma unroll
        for (int e = 0; e < 4; ++e) acc[2][4 * qd + e] = MUL1(acc[2][4 * qd + e], rs[qd][e]);
#pragma unroll
      for (int s = 0; s < 2; ++s) {
        const v4u f = pack8(acc[2], s);
        st16_sc1(f, sws, lane * 16, kvo + 2048 + s * 1024);
        sts128(lds0 + L_KV + (8 + m) * KVT + s * VB + vpos, f);
      }
      SBAR();
      const float qs = rstd * (0.25f * 1.4426950408889634f);
#pragma unroll
      for (int e = 0; e < 16; ++e) acc[0][e] = MUL1(acc[0][e], qs);
      qf[0] = pack8(acc[0], 0); qf[1] = pack8(acc[0], 1);
      PIN(qf[0]); PIN(qf[1]);
      STAMP_AT(2);
      const int T0 = 4 * r - 8 + 2 * m;
      const bool live = T0 >= 0;
      auto kv_load = [&](v4u (&kv)[8], int i0, int i1) {
        const unsigned src = WS_KV + (j * 64 + (live ? T0 : 0)) * 4096, vo = live ? lane * 16 : h * 512;
#pragma unroll
        for (int i = i0; i < i1; ++i) kv[i] = ld16_sc1_async(sws, vo, src + (live ? i : (i & 3)) * 1024);
      };
      auto kv_stage = [&](v4u (&kv)[8]) {
        const unsigned on = (lane < 2 || live) ? one2 : 0u;
        if (lane < 6) sts128(lds0 + L_KV + (lane < 2 ? 8 + m : 2 * m + ((lane - 2) >> 1)) * KVT + 2048 + (lane & 1) * VB + 1024, (v4u){on, on, on, on});
        const v4u zz = {};
#pragma unroll
        for (int i = 0; i < 2; ++i) {
          sts128(lds0 + L_KV + (2 * m + i) * KVT + lane * 16, kv[4 * i]);
          sts128(lds0 + L_KV + (2 * m + i) * KVT + 1024 + lane * 16, kv[4 * i + 1]);
          sts128(lds0 + L_KV + (2 * m + i) * KVT + vpos, live ? kv[4 * i + 2] : zz);
          sts128(lds0 + L_KV + (2 * m + i) * KVT + VB + vpos, live ? kv[4 * i + 3] : zz);
        }
        arrive_nowait(acnt);
      };
      auto kv_wait = [&](v4u (&kv)[8]) {
        wait_vm<0>();
#pragma unroll
        for (int i = 0; i < 8; ++i) PIN(kv[i]);
      };
      if (heavy) {
        bias_load(1);
        hsetup(1);
        await(acnt + 16, 4);
        STAMP_AT(3);
        v2u fl;
        v4u kv[8];
        hmm(std::integral_constant<int, 1>{}, xf, [&](auto kc) {
          constexpr int ks = kc.value;
          if constexpr (ks == KVPUB) {
            wait_vm<0>();
            PIN(bias0); PIN(bias1);
            if (lane == 0) __hip_atomic_store(flag(FL_KV, T, j), tag, RLX_AGENT);
          }
          if constexpr (ks == 24) fl = ld8_sc1_async(sws, flag_off(FL_KV, live ? T0 + (lane & 1) : 0, j));
        });
        STAMP_AT(18);
        wait_vmf<0>(fl);
        if (!__all(fl[0] == taglo && fl[1] == tag32)) poll_flags(flag(FL_KV, live ? T0 + (lane & 1) : 0, j), tag);
        kv_load(kv, 0, 8);
        hgelu(std::integral_constant<int, 1>{}, [&]() {});
        STAMP_AT(4);
        wait_vm<4>();
#pragma unroll
        for (int i = 0; i < 8; ++i) PIN(kv[i]);
        kv_stage(kv);
      } else {
        wait_vm<0>();
        if (lane == 0) __hip_atomic_store(flag(FL_KV, T, j), tag, RLX_AGENT);
        v4u kv[8];
        poll_flags(flag(FL_KV, live ? T0 + (lane & 1) : 0, j), tag);
        kv_load(kv, 0, 8); kv_wait(kv); kv_stage(kv);
      }
    }
    STAMP_AT(19);
    const unsigned kb = lds0 + L_KV + m * KVT + lane * 16;
    unsigned vb[2];
#pragma unroll
    for (int e = 0; e < 2; ++e) { vb[e] = lds0 + L_KV + m * KVT + 2048 + ((g & 1) ? 1024 : e * 512 + h * 256 + (lane & 15) * 16); PIN(vb[e]); }
    {
      const int dq = r32 - 4 * h;
#pragma unroll
      for (int d = 0; d < 8; ++d) { const int row = 2 * (d & 1) + 8 * (d >> 1); (d < 4 ? am0 : am1)[d & 3] = row > dq ? 0xffffffffu : row == dq ? 0xffff0000u : 0u; }
      PIN(am0); PIN(am1);
    }
    await(acnt, heavy ? 8 : 4);
    __builtin_amdgcn_s_setprio(PRIO_ATT);
    STAMP_AT(5);
    const f32x16 zero = {};
    auto o_store = [&](f32x16& o, int e) {
      const float rl = __builtin_amdgcn_rcpf(o[8]);
#pragma unroll
      for (int u = 0; u < 8; ++u) o[u] = MUL1(o[u], rl);
      st16_sc1(pack8(o, 0), sws, lane * 16, WS_AF + (T * AFT + 112 + 2 * j + e) * 1024);
    };
    int redo = 0;
    {
      f32x16 o[2], sa, sb, su;
      bool pend = false;
      v4u kn, ku, va, vc, vn0, vn1, vu0, vu1, p0, p1, pu;
      auto kaddr = [&](int n) { return kb + ((n & 7) + 1) * KVT + (n >> 3) * 1024; };
      auto vaddr = [&](int n) { return vb[n >> 3] + ((n & 7) + 1) * KVT; };
      kn = lds128(kaddr(0)); vn0 = lds128(vaddr(0)); vn1 = lds128(vaddr(0) + VB);
      sa = MFMA32(kn, qf[0], zero);
      kn = lds128(kaddr(1));
      SBAR();
      auto step = [&](auto nc, f32x16& sc, f32x16& sn) {
        constexpr int n = nc.value, e = n >> 3, i = n & 7;
        if constexpr (n < 15) sn = MFMA32(kn, qf[(n + 1) >> 3], zero);
        if constexpr (n < 14) kn = lds128(kaddr(n + 2));
        if constexpr (i == 5) ku = lds128(kb + e * 1024);
        v4u v0 = vn0, v1 = vn1;
        if constexpr (n < 15) { vn0 = lds128(vaddr(n + 1)); vn1 = lds128(vaddr(n + 1) + VB); }
        if constexpr (i == 6) { vu0 = lds128(vb[e]); vu1 = lds128(vb[e] + VB); }
        SBAR();
        if constexpr (i == 7) {
          static_for<0, 16>([&](auto u) { sc[u.value] = upper<u.value>() ? su[u.value] : sc[u.value]; });
          PIN(sc); SBAR();
        }
#pragma unroll
        for (int u = 0; u < 6; ++u) sc[u] = EXP2(sc[u]);
        PIN(sc); SBAR();
        if constexpr (n == 8) { o[0] = MFMA32(vc, p1, o[0]); SBAR(); }
        else if constexpr (n > 0) { o[e] = MFMA32(vc, p1, o[e]); SBAR(); }
        sc[6] = EXP2(sc[6]); sc[7] = EXP2(sc[7]);
        p0 = pack8(sc, 0);
        sc[8] = EXP2(sc[8]); sc[9] = EXP2(sc[9]);
        if constexpr (i == 7) { va = p0 & am0; p0 &= ~am0; PIN(va); }
        PIN(sc); PIN(p0); SBAR();
        if constexpr (n == 8) { o[0] = MFMA32(vu1, pu, o[0]); SBAR(); }
        o[e] = MFMA32(v0, p0, i == 0 ? zero : o[e]);
        SBAR();
        if constexpr (i == 6) { su = MFMA32(ku, qf[e], zero); SBAR(); }
#pragma unroll
        for (int u = 10; u < 13; ++u) sc[u] = EXP2(sc[u]);
        if constexpr (i == 7) { PIN(sc); SBAR(); o[e] = MFMA32(vu0, va, o[e]); SBAR(); }
#pragma unroll
        for (int u = 13; u < 16; ++u) sc[u] = EXP2(sc[u]);
        p1 = pack8(sc, 1); vc = v1;
        if constexpr (i == 7) { pu = p1 & am1; p1 &= ~am1; PIN(pu); }
        PIN(p1); SBAR();
        if constexpr (n == 8) {
          if (__all(o[0][8] >= 0x1p-30f && o[0][8] <= 0x1p30f)) o_store(o[0], 0); else redo = 1;
          SBAR();
        }
        if constexpr (i == 2) pend = !redo;
        if constexpr (i >= 2) {
          if (pend && (i == 7 || (__builtin_amdgcn_s_getreg(7 | (3 << 11)) & 15) == 0)) {
            wait_vm<0>();
            if (lane == 0) __hip_atomic_store(flag(FL_H1 + e, T, j), tag, RLX_AGENT);
            pend = false;
          }
        }
      };
      static_for<0, 8>([&](auto hc) {
        step(std::integral_constant<int, 2 * hc.value>{}, sa, sb);
        step(std::integral_constant<int, 2 * hc.value + 1>{}, sb, sa);
      });
      o[1] = MFMA32(vc, p1, o[1]);
      o[1] = MFMA32(vu1, pu, o[1]);
      if (__all(o[1][8] >= 0x1p-30f && o[1][8] <= 0x1p30f)) o_store(o[1], 1); else redo |= 2;
    }
    if (redo) {
#pragma unroll 1
      for (int e = 0; e < 2; ++e) {
        if (!((redo >> e) & 1)) continue;
        const v4u q = e ? qf[1] : qf[0];
        const unsigned vbe = e ? vb[1] : vb[0];
        auto merged = [&](const f32x16& c) {
          f32x16 sl = MFMA32(lds128(kb + 8 * KVT + e * 1024), q, c);
          const f32x16 su = MFMA32(lds128(kb + e * 1024), q, c);
          static_for<0, 16>([&](auto u) { sl[u.value] = upper<u.value>() ? su[u.value] : sl[u.value]; });
          return sl;
        };
        float mx = -INFINITY;
#pragma unroll 1
        for (int i = 0; i < 8; ++i) {
          const f32x16 sc = i ? MFMA32(lds128(kb + i * KVT + e * 1024), q, zero) : merged(zero);
#pragma unroll
          for (int u = 0; u < 16; u += 2) mx = max3(mx, sc[u], sc[u + 1]);
        }
        const auto sw = __builtin_amdgcn_permlane32_swap(asu(mx), asu(mx), false, false);
        mx = __builtin_fmaxf(asf(sw[0]), asf(sw[1]));
        f32x16 negm, o;
#pragma unroll
        for (int u = 0; u < 16; ++u) negm[u] = -mx;
        {
          f32x16 sc = merged(negm);
#pragma unroll
          for (int u = 0; u < 16; ++u) sc[u] = EXP2(sc[u]);
          const v4u p0 = pack8(sc, 0), p1 = pack8(sc, 1);
          o = MFMA32(lds128(vbe), p0 & am0, zero);
          o = MFMA32(lds128(vbe + VB), p1 & am1, o);
          o = MFMA32(lds128(vbe + 8 * KVT), p0 & ~am0, o);
          o = MFMA32(lds128(vbe + 8 * KVT + VB), p1 & ~am1, o);
        }
#pragma unroll 1
        for (int i = 1; i < 8; ++i) {
          f32x16 sc = MFMA32(lds128(kb + i * KVT + e * 1024), q, negm);
#pragma unroll
          for (int u = 0; u < 16; ++u) sc[u] = EXP2(sc[u]);
          o = MFMA32(lds128(vbe + i * KVT), pack8(sc, 0), o);
          o = MFMA32(lds128(vbe + i * KVT + VB), pack8(sc, 1), o);
        }
        o_store(o, e);
      }
      wait_vm<0>();
      if (lane == 0) __hip_atomic_store(flag(FL_O0, T, j), tag, RLX_AGENT);
    }
    STAMP_AT(6);
    wait_vm<0>();
    if (lane == 0) __hip_atomic_store(flag(FL_O1, T, j), tag, RLX_AGENT);
    STAMP_AT(7);
    res_loads();
    wait_vm2<0>(xres, bres);
    if (blockIdx.x == 0 && wave == 7) {
#pragma unroll 1
      for (int i = 0; i < 4; ++i) poll_flags(flag(FL_KV, 4 * (4 * i + g), lane & 15), tag);
      if (lane == 0) __hip_atomic_store((GAS u64*)(ws + WS_EPOCH), tag, RLX_AGENT);
    }
  }
  bar();
  {
    int t2 = threadIdx.x; PIN(t2);
    const int row = t2 >> 3, ch = t2 & 7, orow = 64 * rt + row, ocol = 64 * ct + ch * 8;
    const unsigned a = lds0 + L_WG + (((row >> 5) * 8 + ch) * 66 + (row & 31)) * 16;
    f32x4 lo = {}, hi = {};
#pragma unroll
    for (int w = 0; w < 4; ++w) {
      lo += __builtin_bit_cast(f32x4, lds128(a + w * PART));
      hi += __builtin_bit_cast(f32x4, lds128(a + w * PART + 512));
    }
    v4u o;
    o[0] = pk2(lo[0] + bflo(xres[0]) + bflo(bres[0]), lo[1] + bfhi(xres[0]) + bfhi(bres[0]));
    o[1] = pk2(lo[2] + bflo(xres[1]) + bflo(bres[1]), lo[3] + bfhi(xres[1]) + bfhi(bres[1]));
    o[2] = pk2(hi[0] + bflo(xres[2]) + bflo(bres[2]), hi[1] + bfhi(xres[2]) + bfhi(bres[2]));
    o[3] = pk2(hi[2] + bflo(xres[3]) + bflo(bres[3]), hi[3] + bfhi(xres[3]) + bfhi(bres[3]));
    *(GAS v4u*)((char*)out + (size_t)orow * 1024 + ocol * 2) = o;
  }
  STAMP_AT(8);
}

extern "C" void kernel_launch(void* const* d_in, const int* in_sizes, int n_in, void* d_out, int out_size, void* d_ws, size_t ws_size,
                              hipStream_t stream) {
  (void)in_sizes; (void)n_in; (void)out_size; (void)ws_size;
  (void)hipFuncSetAttribute((const void*)fused_block, hipFuncAttributeMaxDynamicSharedMemorySize, LDS_BYTES);
  fused_block<<<256, 512, LDS_BYTES, stream>>>(d_in[0], d_in[1], d_in[2], d_in[4], d_in[5], d_in[6], d_in[7], d_out, (char*)d_ws + WSOFF);
}
```

```cpp
#include <hip/hip_runtime.h>
#include <hip/hip_bf16.h>
#include <stdint.h>
#include <type_traits>

typedef unsigned v4u __attribute__((ext_vector_type(4)));
typedef unsigned v2u __attribute__((ext_vector_type(2)));
typedef float f32x2 __attribute__((ext_vector_type(2)));
typedef float f32x4 __attribute__((ext_vector_type(4)));
typedef float f32x16 __attribute__((ext_vector_type(16)));
typedef short bf16x8 __attribute__((ext_vector_type(8)));
typedef short s16x4 __attribute__((ext_vector_type(4)));
typedef __bf16 bf16x2_t __attribute__((ext_vector_type(2)));
typedef unsigned long long u64;
#define GAS __attribute__((address_space(1)))
#define LAS __attribute__((address_space(3)))
#define DI __device__ __forceinline__
#define RLX_AGENT __ATOMIC_RELAXED, __HIP_MEMORY_SCOPE_AGENT

constexpr int SEQ = 2048, DM = 512, FF = 1792;
constexpr unsigned WS_EPOCH = 0;
constexpr unsigned WS_STATS = 32768;
constexpr unsigned WS_FLAG = 65536;
constexpr unsigned WS_KV = WS_FLAG + 5 * 1024 * 128;
constexpr int AFT = 145;
constexpr unsigned WS_AF = WS_KV + 16 * 64 * 4096;
constexpr unsigned WS_END = WS_AF + 64 * AFT * 1024;
constexpr unsigned WS_STAMP = WS_END;
enum { FL_KV = 0, FL_H0 = 1, FL_H1 = 2, FL_O0 = 3, FL_O1 = 4 };
constexpr int STG = 36864;
constexpr int L_WA = 0;
constexpr int L_WG = 86016;
constexpr int VB = 1040;
constexpr int KVT = 2048 + 2 * VB;
constexpr int L_KV = 65536 - 8 * KVT;
constexpr int RD = 8, NR = 12;
constexpr int PART = 16 * 66 * 16;
constexpr int L_CS = L_WG + 4 * PART;
constexpr int L_RS = L_CS + 3584;
constexpr int L_CNT = L_RS + 512;
constexpr int LDS_BYTES = L_CNT + 64;
static_assert(L_KV + 12 * KVT <= L_WG && L_WG >= 2 * STG && L_CS >= 3 * STG && PART >= RD * 2048 && LDS_BYTES <= 163840, "lds map");
#define AWIN 6
constexpr int AW = AWIN;
constexpr unsigned SPIN_CAP = 1u << 18;

#define MFMA32(a, b, c) __builtin_amdgcn_mfma_f32_32x32x16_bf16(__builtin_bit_cast(bf16x8, a), __builtin_bit_cast(bf16x8, b), c, 0, 0, 0)
#define MFMA16(a, b, c) __builtin_amdgcn_mfma_f32_16x16x32_bf16(__builtin_bit_cast(bf16x8, a), __builtin_bit_cast(bf16x8, b), c, 0, 0, 0)
#define SBAR() __builtin_amdgcn_sched_barrier(0)

#define STAMP_AT(i) do { } while (0)

DI float asf(unsigned u) { return __builtin_bit_cast(float, u); }
DI unsigned asu(float f) { return __builtin_bit_cast(unsigned, f); }
DI unsigned pk2(float lo, float hi) { return __builtin_bit_cast(unsigned, __builtin_convertvector((f32x2){lo, hi}, bf16x2_t)); }
DI float bflo(unsigned u) { return __builtin_bit_cast(float, u << 16); }
DI float bfhi(unsigned u) { return __builtin_bit_cast(float, u & 0xffff0000u); }
DI v4u make_srd(const void* p, unsigned bytes) { const u64 a = (u64)p; return (v4u){(unsigned)a, (unsigned)(a >> 32) & 0xffffu, bytes, 0x00020000u}; }
DI void dma16(v4u srd, unsigned voff, unsigned soff, unsigned lds) {
  asm volatile("s_mov_b32 m0, %3\n\ts_nop 0\n\tbuffer_load_dwordx4 %0, %1, %2 offen lds" :: "v"(voff), "s"(srd), "s"(soff), "s"(lds) : "memory");
}
DI void dma16_a(v4u srd, unsigned voff, unsigned soff, unsigned lds) { dma16(srd, voff, soff, lds); }
DI void dma16_b(v4u srd, unsigned voff1, unsigned soff) {
  asm volatile("buffer_load_dwordx4 %0, %1, %2 offen offset:1024 lds" :: "v"(voff1), "s"(srd), "s"(soff) : "memory");
}
DI v4u ld16_sc1_async(v4u srd, unsigned voff, unsigned soff) {
  v4u r; asm volatile("buffer_load_dwordx4 %0, %1, %2, %3 offen sc1" : "=v"(r) : "v"(voff), "s"(srd), "s"(soff) : "memory"); return r;
}
DI v4u ld16_async(v4u srd, unsigned voff, unsigned soff) {
  v4u r; asm volatile("buffer_load_dwordx4 %0, %1, %2, %3 offen" : "=v"(r) : "v"(voff), "s"(srd), "s"(soff) : "memory"); return r;
}
DI unsigned ld2_async(v4u srd, unsigned voff, unsigned soff) {
  unsigned r; asm volatile("buffer_load_ushort %0, %1, %2, %3 offen" : "=v"(r) : "v"(voff), "s"(srd), "s"(soff) : "memory"); return r;
}
DI void st16_sc1(v4u v, v4u srd, unsigned voff, unsigned soff) {
  asm volatile("buffer_store_dwordx4 %0, %1, %2, %3 offen sc1\n\ts_nop 1" :: "v"(v), "v"(voff), "s"(srd), "s"(soff) : "memory");
}
template <int N> DI void wait_vm1(v4u& a) { asm volatile("s_waitcnt vmcnt(%1)" : "+v"(a) : "i"(N) : "memory"); }
template <int N> DI void wait_vm2(v4u& a, v4u& b) { asm volatile("s_waitcnt vmcnt(%2)" : "+v"(a), "+v"(b) : "i"(N) : "memory"); }
template <int N> DI void wait_vm3(v4u& a, v4u& b, v4u& c) { asm volatile("s_waitcnt vmcnt(%3)" : "+v"(a), "+v"(b), "+v"(c) : "i"(N) : "memory"); }
DI u64 load_s(const void* p) { u64 v; asm volatile("s_load_dwordx2 %0, %1, 0x0\n\ts_waitcnt lgkmcnt(0)" : "=&s"(v) : "s"(p) : "memory"); return v; }
template <class V> DI void pin(V& v) { asm volatile("" : "+v"(v)); }
template <int N> DI void wait_vm() { asm volatile("s_waitcnt vmcnt(%0)" :: "i"(N) : "memory"); }
DI void bar() { asm volatile("s_waitcnt lgkmcnt(0)\n\ts_barrier" ::: "memory"); }
DI v4u lds128(unsigned a) { return *(const LAS v4u*)(size_t)a; }
DI void sts128(unsigned a, v4u v) { *(LAS v4u*)(size_t)a = v; }
DI v2u ldtr(unsigned a) { return __builtin_bit_cast(v2u, __builtin_amdgcn_ds_read_tr16_b64_v4i16((LAS s16x4*)(size_t)a)); }
DI v4u ldtr2(unsigned a, int second) { const v2u lo = ldtr(a), hi = ldtr(a + second); return (v4u){lo.x, lo.y, hi.x, hi.y}; }
template <int CTRL> DI float dpp(float v) { return __builtin_bit_cast(float, __builtin_amdgcn_update_dpp(0, __builtin_bit_cast(int, v), CTRL, 0xf, 0xf, true)); }
DI float rdl(float v, int l) { return __builtin_bit_cast(float, __builtin_amdgcn_readlane(__builtin_bit_cast(int, v), l)); }
DI v4u pack8(const f32x16& a, int s) { return (v4u){pk2(a[8 * s], a[8 * s + 1]), pk2(a[8 * s + 2], a[8 * s + 3]), pk2(a[8 * s + 4], a[8 * s + 5]), pk2(a[8 * s + 6], a[8 * s + 7])}; }
DI void poll_flags(const GAS u64* p, u64 tag) {
#pragma unroll 1
  for (unsigned spin = 0; spin < SPIN_CAP; ++spin) {
    const u64 v = __hip_atomic_load(p, RLX_AGENT);
    if (__all(v == tag)) break;
    __builtin_amdgcn_s_sleep(2);
  }
  asm volatile("" ::: "memory");
}
constexpr u64 upper_mask(int reg) {
  u64 m = 0;
  for (int l = 0; l < 64; ++l) { const int row = (reg & 3) + 8 * (reg >> 2) + 4 * (l >> 5); if (row > (l & 31)) m |= 1ull << l; }
  return m;
}
template <int REG> DI bool upper() { return __builtin_amdgcn_inverse_ballot_w64(upper_mask(REG)); }
template <int I, int N, class Fn> DI void static_for(Fn&& f) { if constexpr (I < N) { f(std::integral_constant<int, I>{}); static_for<I + 1, N>(f); } }
DI float max3(float a, float b, float c) { return __builtin_fmaxf(__builtin_fmaxf(a, b), c); }

constexpr int H1_CK = 16 - AW, H1_LD = H1_CK - 3, O_CK = 28 - AW, O_LD = O_CK - 3;
constexpr int g2_issued(int t) { return ((t >= 1 && t - 1 + NR + RD < 36) ? 2 : 0) + ((t + AW < 28) ? 2 : 0) + (t == H1_LD) + (t == O_LD) + (t == O_CK ? 10 : 0); }
constexpr int g2_range(int a, int b) { int n = 0; for (int t = a; t < b; ++t) n += g2_issued(t); return n; }
#define GELU_PK(P) (P == 1)
#define PRIO_G2 2
#define PRIO_ATT 3
#define PRIO_G1 2
#define ROT r
#define KVPUB 6
#define EXP2(v) __builtin_amdgcn_exp2f(v)
#define PIN(v) pin(v)
extern "C" __device__ float mul_unpacked(float, float) __asm("llvm.amdgcn.fmul.legacy");
#define MUL1(a, b) mul_unpacked(a, b)
#define FMA1(a, b, c) __builtin_amdgcn_div_fmasf(a, b, c, false)
DI v2u ld8_sc1_async(v4u srd, unsigned voff) {
  v2u r; asm volatile("buffer_load_dwordx2 %0, %1, %2, 0 offen sc1" : "=v"(r) : "v"(voff), "s"(srd) : "memory"); return r;
}
template <int N> DI void wait_vmf(v2u& a) { asm volatile("s_waitcnt vmcnt(%1)" : "+v"(a) : "i"(N) : "memory"); }
DI void arrive(unsigned cnt, int lane) {
  asm volatile("s_waitcnt lgkmcnt(0)" ::: "memory");
  if (lane == 0) __hip_atomic_fetch_add((LAS unsigned*)(size_t)cnt, 1u, __ATOMIC_RELAXED, __HIP_MEMORY_SCOPE_WORKGROUP);
}
DI void arrive_nowait(unsigned cnt) {
  asm volatile("s_mov_b64 exec, 1\n\tds_add_u32 %0, %1\n\ts_mov_b64 exec, -1" :: "v"(cnt), "v"(1u) : "memory");
}
DI unsigned peek(unsigned cnt) { return __hip_atomic_load((LAS unsigned*)(size_t)cnt, __ATOMIC_RELAXED, __HIP_MEMORY_SCOPE_WORKGROUP); }
DI void await(unsigned cnt, unsigned n) {
#pragma unroll 1
  for (unsigned spin = 0; spin < SPIN_CAP * 8; ++spin) {
    if (__builtin_amdgcn_readfirstlane(peek(cnt)) >= n) break;
    __builtin_amdgcn_s_sleep(1);
  }
  asm volatile("" ::: "memory");
}

__global__ __launch_bounds__(512, 2) void fused_block(const void* __restrict__ x, const void* __restrict__ wqkv, const void* __restrict__ wo,
                                                      const void* __restrict__ w1, const void* __restrict__ b1, const void* __restrict__ w2,
                                                      const void* __restrict__ b2, void* __restrict__ out, char* __restrict__ ws) {
  constexpr unsigned lds0 = 0;
  const int wave = __builtin_amdgcn_readfirstlane(threadIdx.x >> 6);
  const int m = wave & 3;
  const bool isG = wave < 4;
  const int xcd = blockIdx.x & 7, idx = blockIdx.x >> 3;
  const int r = (xcd >> 2) * 8 + (idx >> 2), j = (xcd & 3) * 4 + (idx & 3);
  const int rt = (xcd >> 1) * 8 + (idx >> 2), ct = (xcd & 1) * 4 + (idx & 3);
  int lane = threadIdx.x & 63, r32, h, g, q4, p4;
  STAMP_AT(0);
  asm volatile("" :: "s"(x), "s"(wqkv), "s"(wo), "s"(w1), "s"(b1), "s"(w2), "s"(b2), "s"(out), "s"(ws));

  const v4u sx = make_srd(x, SEQ * DM * 2), sws = make_srd(ws, WS_END);
  const __amdgpu_buffer_rsrc_t rws = __builtin_amdgcn_make_buffer_rsrc(ws, 0, WS_END, 0x00020000);
  const unsigned one2 = 0x3f803f80u;
  const int T = 4 * r + m;
  const int rot = ROT;
  auto flag_off = [&](int kind, int tile, int jj) { return WS_FLAG + ((kind * 64 + tile) * 16 + jj) * 128; };
  auto flag = [&](int kind, int tile, int jj) { return (GAS u64*)(ws + flag_off(kind, tile, jj)); };
  u64 tag;
  unsigned taglo, tag32;
  auto get_tag = [&]() {
    tag = load_s(ws + WS_EPOCH);
    taglo = (unsigned)tag * 0x9E3779B1u + 0x7F4A7C15u; tag32 = (unsigned)(tag >> 32) * 0x85EBCA77u + 0x165667B1u;
    tag = ((u64)tag32 << 32) | taglo;
  };
  v4u xres, bres, xaug, st;
  float mean, var, rstd, sd;
  v4u sel;
  unsigned xo[4], swz;
  auto lane_setup = [&]() {
    lane = threadIdx.x & 63; PIN(lane);
    r32 = lane & 31; h = lane >> 5; g = lane >> 4; q4 = (lane >> 2) & 3; p4 = lane & 3;
    const unsigned selv = ((lane & 15) == (g & 1)) ? one2 : 0u;
    sel = (v4u){selv, selv, selv, selv};
#pragma unroll
    for (int p = 0; p < 4; ++p) xo[p] = (32 * m + r32) * 128 + (((2 * ((p + m) & 3) + h) ^ ((r32 >> 1) & 7)) * 16);
    swz = ((((lane >> 2) & 1) ^ (g & 1)) * 64) + p4 * 16;
  };

  auto stat_load = [&]() { st = ld16_sc1_async(sws, WS_STATS + (128 * r + 32 * m + r32) * 16, 0); };
  auto res_loads = [&]() {
    int t2 = threadIdx.x; PIN(t2);
    const int row = 64 * rt + (t2 >> 3), col = 64 * ct + (t2 & 7) * 8;
    xres = ld16_async(sx, row * 1024 + col * 2, 0);
    bres = ld16_async(make_srd(b2, DM * 2), col * 2, 0);
  };
  auto row_stats = [&]() {
    if (!__all(st[1] == tag32 && st[3] == tag32)) {
#pragma unroll 1
      for (unsigned spin = 0; spin < SPIN_CAP; ++spin) {
        st = ld16_sc1_async(sws, WS_STATS + (128 * r + 32 * m + r32) * 16, 0);
        wait_vm1<0>(st);
        if (__all(st[1] == tag32 && st[3] == tag32)) break;
      }
    }
    mean = asf(st[0]); var = asf(st[2]);
    PIN(mean); PIN(var);
  };
  auto make_xaug = [&]() {
    PIN(mean); PIN(var);
    rstd = __builtin_amdgcn_rsqf(var); sd = var * rstd;
    const unsigned hi = pk2(-mean, sd);
    const unsigned lo = pk2(-mean - bflo(hi), FMA1(bfhi(hi), -1.f, sd));
    xaug = (v4u){(hi & 0xffffu) | (hi << 16), (lo & 0xffffu) | (hi & 0xffff0000u), lo >> 16, 0u};
    if (h) xaug = (v4u){0u, 0u, 0u, 0u};
    PIN(xaug);
  };
  auto cswr = [&](int tile, f32x4 c) {
    if (lane < 16) { LAS float* p = (LAS float*)(size_t)(lds0 + L_CS + (tile * 32 + lane) * 16 + m * 4); p[0] = c[0]; p[64] = c[1]; }
  };
  auto csrd = [&](int tile) { return __builtin_bit_cast(f32x4, lds128(lds0 + L_CS + (tile * 32 + r32) * 16)); };
  auto waug = [&](f32x4 c4, unsigned b16) {
    const float c = ((c4[0] + c4[1]) + c4[2]) + c4[3];
    const unsigned hi = pk2(c, 0.f);
    const unsigned lo = pk2(c - bflo(hi), 0.f);
    return (v4u){hi | (lo << 16), hi | (b16 << 16), b16, 0u};
  };

  const v4u sw1 = make_srd(w1, DM * FF * 2);
  const int hcol0 = 448 * (j >> 2) + 128 * (j & 3);
  const bool heavy = (j & 3) != 3;
  unsigned bias0, bias1;
  auto bias_load = [&](int P) {
    bias0 = ld2_async(make_srd(b1, FF * 2), r32 * 2, (hcol0 + 64 * P) * 2);
    bias1 = ld2_async(make_srd(b1, FF * 2), r32 * 2, (hcol0 + 64 * P + 32) * 2);
  };
  auto w1dma = [&](int P, int c, unsigned dst, int half) {
    const unsigned vw1 = (lane >> 3) * (FF * 2) + swz, vw1b = vw1 + 8 * (FF * 2) - 1024;
    const unsigned so = (64 * ((c + rot) & 7) + 16 * m) * (FF * 2) + (hcol0 + 64 * P) * 2;
    if (half == 0) dma16_a(sw1, vw1, so, dst + m * 2048); else dma16_b(sw1, vw1b, so);
  };
  f32x16 hac[2];
  unsigned rk[4][2];
  auto hsetup = [&](int P) {
#pragma unroll
    for (int p = 0; p < 4; ++p)
#pragma unroll
      for (int c = 0; c < 2; ++c) {
        rk[p][c] = lds0 + (P ? L_WA : 28672) + ((p + m) & 3) * 2048 + h * 1024 + q4 * 128 + ((c ^ (q4 >> 1)) * 64) + (g & 1) * 32 + p4 * 8;
        PIN(rk[p][c]);
      }
  };
  auto hmm = [&](auto pc, const v4u (&xf)[32], auto&& hook) {
    constexpr int P = pc.value;
    constexpr int D = 4;
    const unsigned cnt = lds0 + L_CNT;
    f32x4 cs[2] = {}, c0, c1;
    v4u wf[D][2], au0, au1;
    unsigned seen = 0;
    hac[0] = (f32x16){}; hac[1] = (f32x16){};
    auto rd = [&](int ks, int t) { wf[ks % D][t] = ldtr2(rk[ks & 3][t] + (ks >> 2) * 8192, 512); };
#pragma unroll
    for (int ks = 0; ks < D; ++ks) { rd(ks, 0); rd(ks, 1); }
    SBAR();
    static_for<0, 32>([&](auto kc) {
      constexpr int ks = kc.value;
      hook(kc);
      if constexpr (ks == 10) seen = peek(cnt + 20);
      if constexpr (ks == 29) seen = peek(cnt);
#pragma unroll
      for (int t = 0; t < 2; ++t) {
        hac[t] = MFMA32(wf[ks % D][t], xf[ks], hac[t]);
        if ((ks & 3) == 0 && ks < 28) { cs[t] = MFMA16(sel, wf[ks % D][t], cs[t]); PIN(cs[t]); }
        if (ks == 26) { cs[t] = MFMA16(sel, wf[28 % D][t], cs[t]); PIN(cs[t]); }
        SBAR();
        if constexpr (ks + D < 32) rd(ks + D, t);
        if constexpr (ks == 31) { if (t == 0) { au0 = waug(c0, bias0); PIN(au0); } else { au1 = waug(c1, bias1); PIN(au1); } }
        SBAR();
      }
      if constexpr (ks == 11 || ks == 30) {
        PIN(hac[0]); PIN(hac[1]);
        if (__builtin_amdgcn_readfirstlane(seen) < 4) await(ks == 11 ? cnt + 20 : cnt, 4);
        asm volatile("" ::: "memory");
        if constexpr (ks == 30) { c0 = csrd(2 * P); c1 = csrd(2 * P + 1); }
      }
      if constexpr (ks == 27) { cswr(2 * P, cs[0]); cswr(2 * P + 1, cs[1]); arrive_nowait(cnt); }
      SBAR();
    });
    hac[0] = MFMA32(au0, xaug, hac[0]);
    hac[1] = MFMA32(au1, xaug, hac[1]);
    SBAR();
  };
  auto hgelu = [&](auto pc, auto&& mid) {
    constexpr int P = pc.value;
    constexpr bool PK = GELU_PK(P);
    const float C0 = 2.3022081986f, C1 = C0 * 0.044715f;
    const float ga = -C1 * rstd * rstd * rstd, gb = -C0 * rstd;
    auto mul = [&](f32x2 a, f32x2 b) { return PK ? a * b : (f32x2){MUL1(a[0], b[0]), MUL1(a[1], b[1])}; };
    auto fma = [&](f32x2 a, float b, float c) {
      return PK ? __builtin_elementwise_fma(a, (f32x2){b, b}, (f32x2){c, c}) : (f32x2){FMA1(a[0], b, c), FMA1(a[1], b, c)}; };
#pragma unroll
    for (int t = 0; t < 2; ++t) {
      f32x2 v[8], z[8];
#pragma unroll
      for (int e = 0; e < 8; ++e) { v[e] = (f32x2){hac[t][2 * e], hac[t][2 * e + 1]}; z[e] = mul(v[e], v[e]); }
      SBAR();
#pragma unroll
      for (int e = 0; e < 8; ++e) z[e] = fma(z[e], ga, gb);
      SBAR();
#pragma unroll
      for (int e = 0; e < 8; ++e) z[e] = mul(z[e], v[e]);
      SBAR();
#pragma unroll
      for (int e = 0; e < 8; ++e) z[e] = (f32x2){EXP2(z[e][0]), EXP2(z[e][1])};
      SBAR();
#pragma unroll
      for (int e = 0; e < 8; ++e) z[e] = fma(z[e], sd, sd);
      SBAR();
#pragma unroll
      for (int e = 0; e < 8; ++e) z[e] = (f32x2){__builtin_amdgcn_rcpf(z[e][0]), __builtin_amdgcn_rcpf(z[e][1])};
      SBAR();
#pragma unroll
      for (int e = 0; e < 8; ++e) z[e] = mul(z[e], v[e]);
      SBAR();
#pragma unroll
      for (int s = 0; s < 2; ++s) {
        const v4u f = {pk2(z[4 * s][0], z[4 * s][1]), pk2(z[4 * s + 1][0], z[4 * s + 1][1]), pk2(z[4 * s + 2][0], z[4 * s + 2][1]), pk2(z[4 * s + 3][0], z[4 * s + 3][1])};
        st16_sc1(f, sws, lane * 16, WS_AF + (T * AFT + (hcol0 >> 4) + 4 * P + 2 * t + s) * 1024);
      }
      if (t == 0) mid();
      SBAR();
    }
  };

  if (isG) {
    lane_setup();
    const v4u sw2 = make_srd(w2, FF * DM * 2), swo = make_srd(wo, DM * DM * 2);
    const unsigned vw2 = (lane >> 3) * 1024 + swz, vw2b = vw2 + 7 * 1024;
    const int ro3 = rt % 3;
    auto kmap = [&](int kk) {
      if (kk < 16) return 8 * (((kk >> 2) + rt) & 3) + (kk & 3);
      const int p = ((kk - 16) >> 2) + ro3;
      return 8 * (p >= 3 ? p - 3 : p) + 4 + (kk & 3);
    };
    auto omap = [&](int i) { return 2 * ((i + rt) & 3) + (i >> 2); };
    auto wdmah = [&](int kk, int half) {
      const unsigned so = (kk < 28 ? 448 * m + 16 * kmap(kk) : 128 * m + 16 * omap(kk - 28)) * 1024 + 128 * ct;
      if (half == 0) dma16_a(kk < 28 ? sw2 : swo, vw2, so, lds0 + L_WG + m * PART + (kk % RD) * 2048); else dma16_b(kk < 28 ? sw2 : swo, vw2b, so);
    };
    auto wdma = [&](int kk) { wdmah(kk, 0); wdmah(kk, 1); };
    {
      const v4u swq = make_srd(wqkv, DM * 3 * DM * 2);
      const unsigned vx0 = (lane >> 3) * 1024 + (((lane & 7) ^ g) * 16), vx1 = (7 + (lane >> 3)) * 1024 + (((lane & 7) ^ (4 + g)) * 16);
      const unsigned vwq = (lane >> 2) * (3 * DM * 2) + p4 * 16;
      auto piece = [&](int s, int i) {
        const unsigned buf = lds0 + (s % 3) * STG;
        const int kc = (s + rot) & 7;
        if (i < 4) {
          const unsigned so = (128 * r + 32 * m + 16 * (i >> 1)) * 1024 + kc * 128;
          if (i & 1) dma16_b(sx, vx1, so); else dma16_a(sx, vx0, so, buf + m * 4096 + (i >> 1) * 2048);
        } else if (i < 7) dma16(swq, vwq, (64 * kc + 16 * m) * (3 * DM * 2) + (512 * (i - 4) + 32 * j) * 2, buf + 16384 + ((i - 4) * 4 + m) * 1024);
        else w1dma(0, s, buf + 28672, i - 7);
      };
#pragma unroll
      for (int i = 0; i < 18; ++i) { piece(i / 9, i % 9); if (i == 8) __builtin_amdgcn_s_barrier(); }
      __builtin_amdgcn_s_setprio(PRIO_G1);
      hsetup(0);
      f32x4 cs[2] = {};
      v4u xf[4], wf[4][2];
      hac[0] = (f32x16){}; hac[1] = (f32x16){};
      static_for<0, 8>([&](auto sc) {
        constexpr int s = sc.value;
        constexpr unsigned buf = (s % 3) * STG;
        auto fill = [&](int i) {
          if constexpr (s < 6) piece(s + 2, i);
          if constexpr (s == 6) { if (i == 0) { stat_load(); bias_load(0); } else wdmah((i - 1) >> 1, (i - 1) & 1); }
          if constexpr (s == 7) { if (heavy) { w1dma(1, i, lds0 + L_WA + i * 8192, 0); w1dma(1, i, 0, 1); } }
          SBAR();
        };
        auto rd = [&](int p) { xf[p] = lds128(buf + xo[p]); wf[p][0] = ldtr2(buf + rk[p][0], 512); wf[p][1] = ldtr2(buf + rk[p][1], 512); SBAR(); };
        if constexpr (s < 7) wait_vm<9>(); else { wait_vm1<8>(st); PIN(bias0); PIN(bias1); }
        bar();
        STAMP_AT(10 + s);
        rd(0);
        if constexpr (s < 7) fill(0);
        rd(1);
        if constexpr (s < 7) fill(1);
        if constexpr (s == 1) {
          const v2u tg = *(const LAS v2u*)(size_t)(lds0 + L_CNT + 32);
          taglo = __builtin_amdgcn_readfirstlane(tg[0]); tag32 = __builtin_amdgcn_readfirstlane(tg[1]);
          tag = ((u64)tag32 << 32) | taglo;
        }
#pragma unroll
        for (int p = 0; p < 4; ++p) {
#pragma unroll
          for (int t = 0; t < 2; ++t) {
            hac[t] = MFMA32(wf[p][t], xf[p], hac[t]);
            if (p == 0) { cs[t] = MFMA16(sel, wf[p][t], cs[t]); PIN(cs[t]); }
            SBAR();
            if (p < 2 && t == 0) rd(p + 2);
            if (s == 7) fill(2 * p + t); else if (2 * p + t < 7) fill(2 + 2 * p + t);
          }
          if (s == 7 && p == 1) {
            cswr(0, cs[0]); cswr(1, cs[1]);
            bar();
            STAMP_AT(1);
          }
        }
        if constexpr (s == 7) {
          const f32x4 c0 = csrd(0), c1 = csrd(1);
          row_stats(); make_xaug();
          hac[0] = MFMA32(waug(c0, bias0), xaug, hac[0]);
          hac[1] = MFMA32(waug(c1, bias1), xaug, hac[1]);
          SBAR();
        }
      });
    }
    STAMP_AT(18);
    if (heavy) { wait_vm<8>(); arrive_nowait(lds0 + L_CNT + 16); }
    hgelu(std::integral_constant<int, 0>{}, [&]() {
      wait_vm<2>();
      if (heavy) arrive_nowait(lds0 + L_CNT + 20);
    });
    __builtin_amdgcn_s_setprio(0);
    STAMP_AT(19);
    unsigned rb2[2];
#pragma unroll
    for (int c = 0; c < 2; ++c) rb2[c] = lds0 + L_WG + m * PART + h * 512 + q4 * 128 + ((c ^ (q4 >> 1)) * 64) + (g & 1) * 32 + p4 * 8;
    v4u wq[36][2];
    auto wrd = [&](int kk) {
#pragma unroll
      for (int c = 0; c < 2; ++c) wq[kk][c] = ldtr2(rb2[c] + (kk % RD) * 2048, 1024);
    };
    auto wgot = [&](int kk) { PIN(wq[kk][0]); PIN(wq[kk][1]); };
    const int ftile = 2 * rt + ((lane >> 2) & 1);
    v4u af[28][2], of[8][2];
    v2u fl;
    const unsigned a0 = WS_AF + (2 * rt * AFT + 28 * m) * 1024, o0 = WS_AF + (2 * rt * AFT + 112 + 8 * m) * 1024;
    for (int kk = 0; kk < 4; ++kk) wrd(kk);
    for (int kk = 4; kk < 8; ++kk) wdma(kk);
    for (int kk = 0; kk < 4; ++kk) wgot(kk);
    wait_vm<8>();
    if (lane == 0) __hip_atomic_store(flag(FL_H0, T, j), tag, RLX_AGENT);
    STAMP_AT(2);
    for (int kk = 8; kk < 12; ++kk) wdma(kk);
    static_for<1, 3>([&](auto ic) {
      constexpr int i = ic.value;
      wait_vm<(i == 1) ? 9 : 8>();
      for (int kk = 4 * i; kk < 4 * i + 4; ++kk) wrd(kk);
      for (int kk = 4 * i; kk < 4 * i + 4; ++kk) wgot(kk);
      if constexpr (i == 2) fl = ld8_sc1_async(sws, flag_off(FL_H0, ftile, 4 * m + p4));
      for (int kk = 4 * i; kk < 4 * i + 4; ++kk) wdma(kk + RD);
    });
    STAMP_AT(3);
    wait_vmf<8>(fl);
    if (!__all(fl[0] == taglo && fl[1] == tag32)) poll_flags(flag(FL_H0, ftile, 4 * m + p4), tag);
    STAMP_AT(4);
#pragma unroll
    for (int kk = 0; kk < AW; ++kk) {
      af[kk][0] = ld16_sc1_async(sws, lane * 16, a0 + kmap(kk) * 1024);
      af[kk][1] = ld16_sc1_async(sws, lane * 16, a0 + (AFT + kmap(kk)) * 1024);
    }
    __builtin_amdgcn_s_setprio(PRIO_G2);
    f32x16 c2[2][2] = {};
    auto mm2 = [&](int kk, const v4u (&a)[2], auto&& fill) {
      SBAR();
#pragma unroll
      for (int i = 0; i < 4; ++i) { c2[i >> 1][i & 1] = MFMA32(wq[kk][i & 1], a[i >> 1], c2[i >> 1][i & 1]); SBAR(); fill(i); SBAR(); }
    };
    static_for<0, 28>([&](auto kc) {
      constexpr int kk = kc.value;
      wait_vm2<(kk < AW) ? 2 * (AW - 1 - kk) + g2_range(0, kk) : g2_range(kk - AW + 1, kk)>(af[kk][0], af[kk][1]);
      if constexpr (kk == H1_CK) {
        STAMP_AT(20);
        wait_vmf<g2_range(H1_LD, H1_CK) - 1>(fl);
        if (!__all(fl[0] == taglo && fl[1] == tag32)) poll_flags(flag(FL_H1, ftile, 4 * m + p4), tag);
        STAMP_AT(21);
      }
      if constexpr (kk == H1_LD) fl = ld8_sc1_async(sws, flag_off(FL_H1, ftile, 4 * m + p4));
      if constexpr (kk == O_LD) fl = ld8_sc1_async(sws, flag_off(FL_O0, ftile, 4 * m + p4));
      constexpr bool dw = kk >= 1 && kk - 1 + NR + RD < 36;
      if constexpr (dw) wgot(kk - 1 + NR);
      mm2(kk, af[kk], [&](int i) {
        if constexpr (dw) { if (i < 2) wdmah(kk - 1 + NR + RD, i); }
        if constexpr (kk + NR < 28) { if (i == 1) wrd(kk + NR); }
        if constexpr (kk == 27) { if (i == 0) wrd(28); }
        if constexpr (kk + AW < 28) { if (i >= 2) af[kk + AW][i - 2] = ld16_sc1_async(sws, lane * 16, a0 + (AFT * (i - 2) + kmap(kk + AW)) * 1024); }
      });
      if constexpr (kk == O_CK) {
        wait_vmf<g2_range(O_LD, O_CK) - 1>(fl);
        if (!__all(fl[0] == taglo && fl[1] == tag32)) poll_flags(flag(FL_O0, ftile, 4 * m + p4), tag);
        res_loads();
#pragma unroll
        for (int i = 0; i < 4; ++i) {
          of[i][0] = ld16_sc1_async(sws, lane * 16, o0 + omap(i) * 1024);
          of[i][1] = ld16_sc1_async(sws, lane * 16, o0 + (AFT + omap(i)) * 1024);
        }
      }
    });
    static_for<0, 8>([&](auto kc) {
      constexpr int kk = kc.value;
      if (kk < 7) wrd(28 + kk + 1);
      if constexpr (kk == 1) {
        STAMP_AT(5);
        poll_flags(flag(FL_O1, ftile, 4 * m + p4), tag);
        STAMP_AT(6);
#pragma unroll
        for (int i = 4; i < 8; ++i) {
          of[i][0] = ld16_sc1_async(sws, lane * 16, o0 + omap(i) * 1024);
          of[i][1] = ld16_sc1_async(sws, lane * 16, o0 + (AFT + omap(i)) * 1024);
        }
      }
      wait_vm2<(kk == 0) ? 6 : 14 - 2 * kk>(of[kk][0], of[kk][1]);
      mm2(28 + kk, of[kk], [&](int) {});
    });
    PIN(xres); PIN(bres);
    asm volatile("" ::: "memory");
#pragma unroll
    for (int mt = 0; mt < 2; ++mt)
#pragma unroll
      for (int c = 0; c < 2; ++c)
#pragma unroll
        for (int qd = 0; qd < 4; ++qd) {
          const f32x4 v = {c2[mt][c][4 * qd], c2[mt][c][4 * qd + 1], c2[mt][c][4 * qd + 2], c2[mt][c][4 * qd + 3]};
          sts128(lds0 + L_WG + m * PART + ((mt * 8 + c * 4 + qd) * 66 + lane) * 16, __builtin_bit_cast(v4u, v));
        }
    STAMP_AT(7);
  } else {
    __builtin_amdgcn_s_setprio(1);
    lane_setup();
    v4u xr[2];
    xr[0] = ld16_async(sx, lane * 16, (128 * r + 8 * j + 2 * m) * 1024); xr[1] = ld16_async(sx, lane * 16, (128 * r + 8 * j + 2 * m + 1) * 1024);
    __builtin_amdgcn_s_barrier();
    get_tag();
    if (m == 0 && lane < 10) *(LAS unsigned*)(size_t)(lds0 + L_CNT + lane * 4) = lane == 8 ? taglo : lane == 9 ? tag32 : 0u;
    auto make_stats = [&]() {
      wait_vm2<0>(xr[0], xr[1]);
      float piv[2], a[4];
#pragma unroll
      for (int u = 0; u < 2; ++u) {
        piv[u] = rdl(bflo(xr[u][0]), 0);
        float s1 = 0.f, s2 = 0.f;
#pragma unroll
        for (int i = 0; i < 8; ++i) { const float d = ((i & 1) ? bfhi(xr[u][i >> 1]) : bflo(xr[u][i >> 1])) - piv[u]; s1 += d; s2 = __builtin_fmaf(d, d, s2); }
        a[2 * u] = s1; a[2 * u + 1] = s2;
      }
#pragma unroll
      for (int i = 0; i < 4; ++i) a[i] += dpp<0xB1>(a[i]);
#pragma unroll
      for (int i = 0; i < 4; ++i) a[i] += dpp<0x4E>(a[i]);
      float t = p4 == 0 ? a[0] : p4 == 1 ? a[1] : p4 == 2 ? a[2] : a[3];
      t += dpp<0x114>(t); t += dpp<0x118>(t);
      const auto s16 = __builtin_amdgcn_permlane16_swap(asu(t), asu(t), false, false);
      t = asf(s16[0]) + asf(s16[1]);
      const auto s32 = __builtin_amdgcn_permlane32_swap(asu(t), asu(t), false, false);
      t = (asf(s32[0]) + asf(s32[1])) * (1.f / DM);
      const float e1 = dpp<0xA0>(t);
      const float val = (p4 & 1) ? __builtin_fmaxf(__builtin_fmaf(-e1, e1, t), 0.f) + 1e-5f : t + (p4 ? piv[1] : piv[0]);
      if ((lane >> 2) == 3) {
        const v2u gr = {asu(val), tag32};
        __builtin_amdgcn_raw_buffer_store_b64(gr, rws, WS_STATS + (128 * r + 8 * j + 2 * m) * 16 + p4 * 8, 0, 16);
      }
    };
    const unsigned acnt = lds0 + L_CNT;
    const unsigned vpos = 2048 + (g & 1) * 512 + h * 256 + (lane & 15) * 16;
    v4u qf[2], am0, am1;
    {
      v4u xf[32];
      f32x16 acc[3] = {};
      f32x4 cs[3] = {};
      v4u wf[4][3];
      unsigned wb[4];
#pragma unroll
      for (int p = 0; p < 4; ++p) wb[p] = 16384 + ((p + m) & 3) * 1024 + (8 * h + q4) * 64 + (g & 1) * 32 + p4 * 8;
      auto rd = [&](int ks, int t) {
        const unsigned buf = lds0 + ((ks >> 2) % 3) * STG;
        if (t == 0) xf[ks] = lds128(buf + xo[ks & 3]);
        wf[ks & 3][t] = ldtr2(buf + wb[ks & 3] + t * 4096, 256);
      };
      auto mm = [&](int ks, int t) {
        acc[t] = t == 2 ? MFMA32(xf[ks], wf[ks & 3][t], acc[t]) : MFMA32(wf[ks & 3][t], xf[ks], acc[t]);
        if ((ks & 3) == 0) { cs[t] = MFMA16(sel, wf[ks & 3][t], cs[t]); PIN(cs[t]); }
        SBAR();
      };
      make_stats();
      bar();
      STAMP_AT(10);
#pragma unroll
      for (int t = 0; t < 6; ++t) rd(t / 3, t % 3);
      SBAR();
      static_for<0, 8>([&](auto sc) {
        constexpr int s = sc.value;
#pragma unroll
        for (int p = 0; p < 2; ++p) {
#pragma unroll
          for (int t = 0; t < 3; ++t) { mm(4 * s + p, t); rd(4 * s + p + 2, t); SBAR(); }
          if (s == 7 && p == 0) { cswr(4, cs[0]); cswr(5, cs[1]); cswr(6, cs[2]); SBAR(); }
        }
        if constexpr (s == 5) stat_load();
        if constexpr (s == 6) { wait_vm1<0>(st); row_stats(); }
        mm(4 * s + 2, 0); mm(4 * s + 2, 1);
        bar();
        if constexpr (s < 7) {
          STAMP_AT(11 + s);
          mm(4 * s + 2, 2);
          rd(4 * s + 4, 0); rd(4 * s + 4, 1); rd(4 * s + 4, 2); SBAR();
#pragma unroll
          for (int t = 0; t < 3; ++t) { mm(4 * s + 3, t); rd(4 * s + 5, t); SBAR(); }
        }
      });
      STAMP_AT(1);
      __builtin_amdgcn_s_setprio(3);
      const f32x4 c5 = csrd(5), c6 = csrd(6), c4 = csrd(4);
      SBAR();
      mm(30, 2); mm(31, 1);
      make_xaug();
      SBAR();
      mm(31, 2); mm(31, 0);
      if (h == 0) *(LAS float*)(size_t)(lds0 + L_RS + (m * 32 + r32) * 4) = rstd;
      acc[1] = MFMA32(waug(c5, 0u), xaug, acc[1]);
      acc[2] = MFMA32(xaug, waug(c6, 0u), acc[2]);
      acc[0] = MFMA32(waug(c4, 0u), xaug, acc[0]);
      SBAR();
      f32x4 rs[4];
#pragma unroll
      for (int qd = 0; qd < 4; ++qd) rs[qd] = __builtin_bit_cast(f32x4, lds128(lds0 + L_RS + (m * 32 + 8 * qd + 4 * h) * 4));
      const unsigned kvo = WS_KV + (j * 64 + T) * 4096;
#pragma unroll
      for (int e = 0; e < 16; ++e) acc[1][e] = MUL1(acc[1][e], rstd);
#pragma unroll
      for (int e = 0; e < 2; ++e) {
        const v4u f = pack8(acc[1], e);
        st16_sc1(f, sws, lane * 16, kvo + e * 1024);
        sts128(lds0 + L_KV + (8 + m) * KVT + e * 1024 + lane * 16, f);
      }
#pragma unroll
      for (int qd = 0; qd < 4; ++qd)
#pragma unroll
        for (int e = 0; e < 4; ++e) acc[2][4 * qd + e] = MUL1(acc[2][4 * qd + e], rs[qd][e]);
#pragma unroll
      for (int s = 0; s < 2; ++s) {
        const v4u f = pack8(acc[2], s);
        st16_sc1(f, sws, lane * 16, kvo + 2048 + s * 1024);
        sts128(lds0 + L_KV + (8 + m) * KVT + s * VB + vpos, f);
      }
      SBAR();
      const float qs = rstd * (0.25f * 1.4426950408889634f);
#pragma unroll
      for (int e = 0; e < 16; ++e) acc[0][e] = MUL1(acc[0][e], qs);
      qf[0] = pack8(acc[0], 0); qf[1] = pack8(acc[0], 1);
      PIN(qf[0]); PIN(qf[1]);
      STAMP_AT(2);
      const int T0 = 4 * r - 8 + 2 * m;
      const bool live = T0 >= 0;
      auto kv_load = [&](v4u (&kv)[8], int i0, int i1) {
        const unsigned src = WS_KV + (j * 64 + (live ? T0 : 0)) * 4096, vo = live ? lane * 16 : h * 512;
#pragma unroll
        for (int i = i0; i < i1; ++i) kv[i] = ld16_sc1_async(sws, vo, src + (live ? i : (i & 3)) * 1024);
      };
      auto kv_stage = [&](v4u (&kv)[8]) {
        const unsigned on = (lane < 2 || live) ? one2 : 0u;
        if (lane < 6) sts128(lds0 + L_KV + (lane < 2 ? 8 + m : 2 * m + ((lane - 2) >> 1)) * KVT + 2048 + (lane & 1) * VB + 1024, (v4u){on, on, on, on});
        const v4u zz = {};
#pragma unroll
        for (int i = 0; i < 2; ++i) {
          sts128(lds0 + L_KV + (2 * m + i) * KVT + lane * 16, kv[4 * i]);
          sts128(lds0 + L_KV + (2 * m + i) * KVT + 1024 + lane * 16, kv[4 * i + 1]);
          sts128(lds0 + L_KV + (2 * m + i) * KVT + vpos, live ? kv[4 * i + 2] : zz);
          sts128(lds0 + L_KV + (2 * m + i) * KVT + VB + vpos, live ? kv[4 * i + 3] : zz);
        }
        arrive_nowait(acnt);
      };
      auto kv_wait = [&](v4u (&kv)[8]) {
        wait_vm<0>();
#pragma unroll
        for (int i = 0; i < 8; ++i) PIN(kv[i]);
      };
      if (heavy) {
        bias_load(1);
        hsetup(1);
        await(acnt + 16, 4);
        STAMP_AT(3);
        v2u fl;
        v4u kv[8];
        hmm(std::integral_constant<int, 1>{}, xf, [&](auto kc) {
          constexpr int ks = kc.value;
          if constexpr (ks == KVPUB) {
            wait_vm<0>();
            PIN(bias0); PIN(bias1);
            if (lane == 0) __hip_atomic_store(flag(FL_KV, T, j), tag, RLX_AGENT);
          }
          if constexpr (ks == 24) fl = ld8_sc1_async(sws, flag_off(FL_KV, live ? T0 + (lane & 1) : 0, j));
        });
        STAMP_AT(18);
        wait_vmf<0>(fl);
        if (!__all(fl[0] == taglo && fl[1] == tag32)) poll_flags(flag(FL_KV, live ? T0 + (lane & 1) : 0, j), tag);
        kv_load(kv, 0, 8);
        hgelu(std::integral_constant<int, 1>{}, [&]() {});
        STAMP_AT(4);
        wait_vm<4>();
#pragma unroll
        for (int i = 0; i < 8; ++i) PIN(kv[i]);
        kv_stage(kv);
      } else {
        wait_vm<0>();
        if (lane == 0) __hip_atomic_store(flag(FL_KV, T, j), tag, RLX_AGENT);
        v4u kv[8];
        poll_flags(flag(FL_KV, live ? T0 + (lane & 1) : 0, j), tag);
        kv_load(kv, 0, 8); kv_wait(kv); kv_stage(kv);
      }
    }
    STAMP_AT(19);
    const unsigned kb = lds0 + L_KV + m * KVT + lane * 16;
    unsigned vb[2];
#pragma unroll
    for (int e = 0; e < 2; ++e) { vb[e] = lds0 + L_KV + m * KVT + 2048 + ((g & 1) ? 1024 : e * 512 + h * 256 + (lane & 15) * 16); PIN(vb[e]); }
    {
      const int dq = r32 - 4 * h;
#pragma unroll
      for (int d = 0; d < 8; ++d) { const int row = 2 * (d & 1) + 8 * (d >> 1); (d < 4 ? am0 : am1)[d & 3] = row > dq ? 0xffffffffu : row == dq ? 0xffff0000u : 0u; }
      PIN(am0); PIN(am1);
    }
    await(acnt, heavy ? 8 : 4);
    __builtin_amdgcn_s_setprio(PRIO_ATT);
    STAMP_AT(5);
    const f32x16 zero = {};
    auto o_store = [&](f32x16& o, int e) {
      const float rl = __builtin_amdgcn_rcpf(o[8]);
#pragma unroll
      for (int u = 0; u < 8; ++u) o[u] = MUL1(o[u], rl);
      st16_sc1(pack8(o, 0), sws, lane * 16, WS_AF + (T * AFT + 112 + 2 * j + e) * 1024);
    };
    int redo = 0;
    {
      f32x16 o[2], sa, sb, su;
      bool pend = false;
      v4u kn, ku, va, vc, vn0, vn1, vu0, vu1, p0, p1, pu;
      auto kaddr = [&](int n) { return kb + ((n & 7) + 1) * KVT + (n >> 3) * 1024; };
      auto vaddr = [&](int n) { return vb[n >> 3] + ((n & 7) + 1) * KVT; };
      kn = lds128(kaddr(0)); vn0 = lds128(vaddr(0)); vn1 = lds128(vaddr(0) + VB);
      sa = MFMA32(kn, qf[0], zero);
      kn = lds128(kaddr(1));
      SBAR();
      auto step = [&](auto nc, f32x16& sc, f32x16& sn) {
        constexpr int n = nc.value, e = n >> 3, i = n & 7;
        if constexpr (n < 15) sn = MFMA32(kn, qf[(n + 1) >> 3], zero);
        if constexpr (n < 14) kn = lds128(kaddr(n + 2));
        if constexpr (i == 5) ku = lds128(kb + e * 1024);
        v4u v0 = vn0, v1 = vn1;
        if constexpr (n < 15) { vn0 = lds128(vaddr(n + 1)); vn1 = lds128(vaddr(n + 1) + VB); }
        if constexpr (i == 6) { vu0 = lds128(vb[e]); vu1 = lds128(vb[e] + VB); }
        SBAR();
        if constexpr (i == 7) {
          static_for<0, 16>([&](auto u) { sc[u.value] = upper<u.value>() ? su[u.value] : sc[u.value]; });
          PIN(sc); SBAR();
        }
#pragma unroll
        for (int u = 0; u < 6; ++u) sc[u] = EXP2(sc[u]);
        PIN(sc); SBAR();
        if constexpr (n == 8) { o[0] = MFMA32(vc, p1, o[0]); SBAR(); }
        else if constexpr (n > 0) { o[e] = MFMA32(vc, p1, o[e]); SBAR(); }
        sc[6] = EXP2(sc[6]); sc[7] = EXP2(sc[7]);
        p0 = pack8(sc, 0);
        sc[8] = EXP2(sc[8]); sc[9] = EXP2(sc[9]);
        if constexpr (i == 7) { va = p0 & am0; p0 &= ~am0; PIN(va); }
        PIN(sc); PIN(p0); SBAR();
        if constexpr (n == 8) { o[0] = MFMA32(vu1, pu, o[0]); SBAR(); }
        o[e] = MFMA32(v0, p0, i == 0 ? zero : o[e]);
        SBAR();
        if constexpr (i == 6) { su = MFMA32(ku, qf[e], zero); SBAR(); }
#pragma unroll
        for (int u = 10; u < 13; ++u) sc[u] = EXP2(sc[u]);
        if constexpr (i == 7) { PIN(sc); SBAR(); o[e] = MFMA32(vu0, va, o[e]); SBAR(); }
#pragma unroll
        for (int u = 13; u < 16; ++u) sc[u] = EXP2(sc[u]);
        p1 = pack8(sc, 1); vc = v1;
        if constexpr (i == 7) { pu = p1 & am1; p1 &= ~am1; PIN(pu); }
        PIN(p1); SBAR();
        if constexpr (n == 8) {
          if (__all(o[0][8] >= 0x1p-30f && o[0][8] <= 0x1p30f)) o_store(o[0], 0); else redo = 1;
          SBAR();
        }
        if constexpr (i == 2) pend = !redo;
        if constexpr (i >= 2) {
          if (pend && (i == 7 || (__builtin_amdgcn_s_getreg(7 | (3 << 11)) & 15) == 0)) {
            wait_vm<0>();
            if (lane == 0) __hip_atomic_store(flag(FL_H1 + e, T, j), tag, RLX_AGENT);
            pend = false;
          }
        }
      };
      static_for<0, 8>([&](auto hc) {
        step(std::integral_constant<int, 2 * hc.value>{}, sa, sb);
        step(std::integral_constant<int, 2 * hc.value + 1>{}, sb, sa);
      });
      o[1] = MFMA32(vc, p1, o[1]);
      o[1] = MFMA32(vu1, pu, o[1]);
      if (__all(o[1][8] >= 0x1p-30f && o[1][8] <= 0x1p30f)) o_store(o[1], 1); else redo |= 2;
    }
    if (redo) {
#pragma unroll 1
      for (int e = 0; e < 2; ++e) {
        if (!((redo >> e) & 1)) continue;
        const v4u q = e ? qf[1] : qf[0];
        const unsigned vbe = e ? vb[1] : vb[0];
        auto merged = [&](const f32x16& c) {
          f32x16 sl = MFMA32(lds128(kb + 8 * KVT + e * 1024), q, c);
          const f32x16 su = MFMA32(lds128(kb + e * 1024), q, c);
          static_for<0, 16>([&](auto u) { sl[u.value] = upper<u.value>() ? su[u.value] : sl[u.value]; });
          return sl;
        };
        float mx = -INFINITY;
#pragma unroll 1
        for (int i = 0; i < 8; ++i) {
          const f32x16 sc = i ? MFMA32(lds128(kb + i * KVT + e * 1024), q, zero) : merged(zero);
#pragma unroll
          for (int u = 0; u < 16; u += 2) mx = max3(mx, sc[u], sc[u + 1]);
        }
        const auto sw = __builtin_amdgcn_permlane32_swap(asu(mx), asu(mx), false, false);
        mx = __builtin_fmaxf(asf(sw[0]), asf(sw[1]));
        f32x16 negm, o;
#pragma unroll
        for (int u = 0; u < 16; ++u) negm[u] = -mx;
        {
          f32x16 sc = merged(negm);
#pragma unroll
          for (int u = 0; u < 16; ++u) sc[u] = EXP2(sc[u]);
          const v4u p0 = pack8(sc, 0), p1 = pack8(sc, 1);
          o = MFMA32(lds128(vbe), p0 & am0, zero);
          o = MFMA32(lds128(vbe + VB), p1 & am1, o);
          o = MFMA32(lds128(vbe + 8 * KVT), p0 & ~am0, o);
          o = MFMA32(lds128(vbe + 8 * KVT + VB), p1 & ~am1, o);
        }
#pragma unroll 1
        for (int i = 1; i < 8; ++i) {
          f32x16 sc = MFMA32(lds128(kb + i * KVT + e * 1024), q, negm);
#pragma unroll
          for (int u = 0; u < 16; ++u) sc[u] = EXP2(sc[u]);
          o = MFMA32(lds128(vbe + i * KVT), pack8(sc, 0), o);
          o = MFMA32(lds128(vbe + i * KVT + VB), pack8(sc, 1), o);
        }
        o_store(o, e);
      }
      wait_vm<0>();
      if (lane == 0) __hip_atomic_store(flag(FL_O0, T, j), tag, RLX_AGENT);
    }
    STAMP_AT(6);
    wait_vm<0>();
    if (lane == 0) __hip_atomic_store(flag(FL_O1, T, j), tag, RLX_AGENT);
    STAMP_AT(7);
    res_loads();
    wait_vm2<0>(xres, bres);
    if (blockIdx.x == 0 && wave == 7) {
#pragma unroll 1
      for (int i = 0; i < 4; ++i) poll_flags(flag(FL_KV, 4 * (4 * i + g), lane & 15), tag);
      if (lane == 0) __hip_atomic_store((GAS u64*)(ws + WS_EPOCH), tag, RLX_AGENT);
    }
  }
  bar();
  {
    int t2 = threadIdx.x; PIN(t2);
    const int row = t2 >> 3, ch = t2 & 7, orow = 64 * rt + row, ocol = 64 * ct + ch * 8;
    const unsigned a = lds0 + L_WG + (((row >> 5) * 8 + ch) * 66 + (row & 31)) * 16;
    f32x4 lo = {}, hi = {};
#pragma unroll
    for (int w = 0; w < 4; ++w) {
      lo += __builtin_bit_cast(f32x4, lds128(a + w * PART));
      hi += __builtin_bit_cast(f32x4, lds128(a + w * PART + 512));
    }
    v4u o;
    o[0] = pk2(lo[0] + bflo(xres[0]) + bflo(bres[0]), lo[1] + bfhi(xres[0]) + bfhi(bres[0]));
    o[1] = pk2(lo[2] + bflo(xres[1]) + bflo(bres[1]), lo[3] + bfhi(xres[1]) + bfhi(bres[1]));
    o[2] = pk2(hi[0] + bflo(xres[2]) + bflo(bres[2]), hi[1] + bfhi(xres[2]) + bfhi(bres[2]));
    o[3] = pk2(hi[2] + bflo(xres[3]) + bflo(bres[3]), hi[3] + bfhi(xres[3]) + bfhi(bres[3]));
    *(GAS v4u*)((char*)out + (size_t)orow * 1024 + ocol * 2) = o;
  }
  STAMP_AT(8);
}

extern "C" void kernel_launch(void* const* d_in, const int* in_sizes, int n_in, void* d_out, int out_size, void* d_ws, size_t ws_size,
                              hipStream_t stream) {
  (void)in_sizes; (void)n_in; (void)out_size; (void)ws_size;
  (void)hipFuncSetAttribute((const void*)fused_block, hipFuncAttributeMaxDynamicSharedMemorySize, LDS_BYTES);
  fused_block<<<256, 512, LDS_BYTES, stream>>>(d_in[0], d_in[1], d_in[2], d_in[4], d_in[5], d_in[6], d_in[7], d_out, (char*)d_ws);
}
```

```cpp
#include <hip/hip_runtime.h>
#include <hip/hip_bf16.h>
#include <stdint.h>
#include <type_traits>

typedef unsigned v4u __attribute__((ext_vector_type(4)));
typedef unsigned v2u __attribute__((ext_vector_type(2)));
typedef float f32x2 __attribute__((ext_vector_type(2)));
typedef float f32x4 __attribute__((ext_vector_type(4)));
typedef float f32x16 __attribute__((ext_vector_type(16)));
typedef short bf16x8 __attribute__((ext_vector_type(8)));
typedef short s16x4 __attribute__((ext_vector_type(4)));
typedef __bf16 bf16x2_t __attribute__((ext_vector_type(2)));
typedef unsigned long long u64;
#define GAS __attribute__((address_space(1)))
#define LAS __attribute__((address_space(3)))
#define DI __device__ __forceinline__
#define RLX_AGENT __ATOMIC_RELAXED, __HIP_MEMORY_SCOPE_AGENT

constexpr int SEQ = 2048, DM = 512, FF = 1792;
constexpr unsigned WS_EPOCH = 0;
constexpr unsigned WS_STATS = 32768;
constexpr unsigned WS_FLAG = 65536;
constexpr unsigned WS_KV = WS_FLAG + 5 * 1024 * 128;
constexpr int AFT = 145;
constexpr unsigned WS_AF = WS_KV + 16 * 64 * 4096;
constexpr unsigned WS_END = WS_AF + 64 * AFT * 1024;
constexpr unsigned WS_STAMP = WS_END;
enum { FL_KV = 0, FL_H0 = 1, FL_H1 = 2, FL_O0 = 3, FL_O1 = 4 };
constexpr int STG = 36864;
constexpr int L_WA = 0;
constexpr int L_WG = 86016;
constexpr int VB = 1040;
constexpr int KVT = 2048 + 2 * VB;
constexpr int L_KV = 65536 - 8 * KVT;
constexpr int RD = 8, NR = 12;
constexpr int PART = 16 * 66 * 16;
constexpr int L_CS = L_WG + 4 * PART;
constexpr int L_RS = L_CS + 3584;
constexpr int L_CNT = L_RS + 512;
constexpr int LDS_BYTES = L_CNT + 64;
static_assert(L_KV + 12 * KVT <= L_WG && L_WG >= 2 * STG && L_CS >= 3 * STG && PART >= RD * 2048 && LDS_BYTES <= 163840, "lds map");
#define AWIN 6
constexpr int AW = AWIN;
constexpr unsigned SPIN_CAP = 1u << 23;

#define MFMA32(a, b, c) __builtin_amdgcn_mfma_f32_32x32x16_bf16(__builtin_bit_cast(bf16x8, a), __builtin_bit_cast(bf16x8, b), c, 0, 0, 0)
#define MFMA16(a, b, c) __builtin_amdgcn_mfma_f32_16x16x32_bf16(__builtin_bit_cast(bf16x8, a), __builtin_bit_cast(bf16x8, b), c, 0, 0, 0)
#define SBAR() __builtin_amdgcn_sched_barrier(0)

#define STAMP_AT(i) do { } while (0)

DI float asf(unsigned u) { return __builtin_bit_cast(float, u); }
DI unsigned asu(float f) { return __builtin_bit_cast(unsigned, f); }
DI unsigned pk2(float lo, float hi) { return __builtin_bit_cast(unsigned, __builtin_convertvector((f32x2){lo, hi}, bf16x2_t)); }
DI float bflo(unsigned u) { return __builtin_bit_cast(float, u << 16); }
DI float bfhi(unsigned u) { return __builtin_bit_cast(float, u & 0xffff0000u); }
DI v4u make_srd(const void* p, unsigned bytes) { const u64 a = (u64)p; return (v4u){(unsigned)a, (unsigned)(a >> 32) & 0xffffu, bytes, 0x00020000u}; }
DI void dma16(v4u srd, unsigned voff, unsigned soff, unsigned lds) {
  asm volatile("s_mov_b32 m0, %3\n\ts_nop 0\n\tbuffer_load_dwordx4 %0, %1, %2 offen lds" :: "v"(voff), "s"(srd), "s"(soff), "s"(lds) : "memory");
}
DI void dma16_a(v4u srd, unsigned voff, unsigned soff, unsigned lds) { dma16(srd, voff, soff, lds); }
DI void dma16_b(v4u srd, unsigned voff1, unsigned soff) {
  asm volatile("buffer_load_dwordx4 %0, %1, %2 offen offset:1024 lds" :: "v"(voff1), "s"(srd), "s"(soff) : "memory");
}
DI v4u ld16_sc1_async(v4u srd, unsigned voff, unsigned soff) {
  v4u r; asm volatile("buffer_load_dwordx4 %0, %1, %2, %3 offen sc1" : "=v"(r) : "v"(voff), "s"(srd), "s"(soff) : "memory"); return r;
}
DI v4u ld16_async(v4u srd, unsigned voff, unsigned soff) {
  v4u r; asm volatile("buffer_load_dwordx4 %0, %1, %2, %3 offen" : "=v"(r) : "v"(voff), "s"(srd), "s"(soff) : "memory"); return r;
}
DI unsigned ld2_async(v4u srd, unsigned voff, unsigned soff) {
  unsigned r; asm volatile("buffer_load_ushort %0, %1, %2, %3 offen" : "=v"(r) : "v"(voff), "s"(srd), "s"(soff) : "memory"); return r;
}
DI void st16_sc1(v4u v, v4u srd, unsigned voff, unsigned soff) {
  asm volatile("buffer_store_dwordx4 %0, %1, %2, %3 offen sc1\n\ts_nop 1" :: "v"(v), "v"(voff), "s"(srd), "s"(soff) : "memory");
}
template <int N> DI void wait_vm1(v4u& a) { asm volatile("s_waitcnt vmcnt(%1)" : "+v"(a) : "i"(N) : "memory"); }
template <int N> DI void wait_vm2(v4u& a, v4u& b) { asm volatile("s_waitcnt vmcnt(%2)" : "+v"(a), "+v"(b) : "i"(N) : "memory"); }
template <int N> DI void wait_vm3(v4u& a, v4u& b, v4u& c) { asm volatile("s_waitcnt vmcnt(%3)" : "+v"(a), "+v"(b), "+v"(c) : "i"(N) : "memory"); }
template <class V> DI void pin(V& v) { asm volatile("" : "+v"(v)); }
template <int N> DI void wait_vm() { asm volatile("s_waitcnt vmcnt(%0)" :: "i"(N) : "memory"); }
DI void bar() { asm volatile("s_waitcnt lgkmcnt(0)\n\ts_barrier" ::: "memory"); }
DI v4u lds128(unsigned a) { return *(const LAS v4u*)(size_t)a; }
DI void sts128(unsigned a, v4u v) { *(LAS v4u*)(size_t)a = v; }
DI v2u ldtr(unsigned a) { return __builtin_bit_cast(v2u, __builtin_amdgcn_ds_read_tr16_b64_v4i16((LAS s16x4*)(size_t)a)); }
DI v4u ldtr2(unsigned a, int second) { const v2u lo = ldtr(a), hi = ldtr(a + second); return (v4u){lo.x, lo.y, hi.x, hi.y}; }
template <int CTRL> DI float dpp(float v) { return __builtin_bit_cast(float, __builtin_amdgcn_update_dpp(0, __builtin_bit_cast(int, v), CTRL, 0xf, 0xf, true)); }
DI float rdl(float v, int l) { return __builtin_bit_cast(float, __builtin_amdgcn_readlane(__builtin_bit_cast(int, v), l)); }
DI v4u pack8(const f32x16& a, int s) { return (v4u){pk2(a[8 * s], a[8 * s + 1]), pk2(a[8 * s + 2], a[8 * s + 3]), pk2(a[8 * s + 4], a[8 * s + 5]), pk2(a[8 * s + 6], a[8 * s + 7])}; }
DI void poll_flags(const GAS u64* p, u64 tag) {
#pragma unroll 1
  for (unsigned spin = 0; spin < SPIN_CAP; ++spin) {
    const u64 v = __hip_atomic_load(p, RLX_AGENT);
    if (__all(v == tag)) break;
    __builtin_amdgcn_s_sleep(2);
  }
  asm volatile("" ::: "memory");
}
constexpr u64 upper_mask(int reg) {
  u64 m = 0;
  for (int l = 0; l < 64; ++l) { const int row = (reg & 3) + 8 * (reg >> 2) + 4 * (l >> 5); if (row > (l & 31)) m |= 1ull << l; }
  return m;
}
template <int REG> DI bool upper() { return __builtin_amdgcn_inverse_ballot_w64(upper_mask(REG)); }
template <int I, int N, class Fn> DI void static_for(Fn&& f) { if constexpr (I < N) { f(std::integral_constant<int, I>{}); static_for<I + 1, N>(f); } }
DI float max3(float a, float b, float c) { return __builtin_fmaxf(__builtin_fmaxf(a, b), c); }

constexpr int H1_CK = 16 - AW, H1_LD = H1_CK - 3, O_CK = 28 - AW, O_LD = O_CK - 3;
constexpr int g2_issued(int t) { return ((t >= 1 && t - 1 + NR + RD < 36) ? 2 : 0) + ((t + AW < 28) ? 2 : 0) + (t == H1_LD) + (t == O_LD) + (t == O_CK ? 10 : 0); }
constexpr int g2_range(int a, int b) { int n = 0; for (int t = a; t < b; ++t) n += g2_issued(t); return n; }
#define GELU_PK(P) (P == 1)
#define PRIO_G2 2
#define PRIO_ATT 3
#define PRIO_G1 2
#define ROT r
#define CODEWARM 39000
#define KVPUB 6
#define EXP2(v) __builtin_amdgcn_exp2f(v)
#define PIN(v) pin(v)
extern "C" __device__ float mul_unpacked(float, float) __asm("llvm.amdgcn.fmul.legacy");
#define MUL1(a, b) mul_unpacked(a, b)
#define FMA1(a, b, c) __builtin_amdgcn_div_fmasf(a, b, c, false)
DI v2u ld8_sc1_async(v4u srd, unsigned voff) {
  v2u r; asm volatile("buffer_load_dwordx2 %0, %1, %2, 0 offen sc1" : "=v"(r) : "v"(voff), "s"(srd) : "memory"); return r;
}
template <int N> DI void wait_vmf(v2u& a) { asm volatile("s_waitcnt vmcnt(%1)" : "+v"(a) : "i"(N) : "memory"); }
DI void arrive(unsigned cnt, int lane) {
  asm volatile("s_waitcnt lgkmcnt(0)" ::: "memory");
  if (lane == 0) __hip_atomic_fetch_add((LAS unsigned*)(size_t)cnt, 1u, __ATOMIC_RELAXED, __HIP_MEMORY_SCOPE_WORKGROUP);
}
DI void arrive_nowait(unsigned cnt) {
  asm volatile("s_mov_b64 exec, 1\n\tds_add_u32 %0, %1\n\ts_mov_b64 exec, -1" :: "v"(cnt), "v"(1u) : "memory");
}
DI unsigned peek(unsigned cnt) { return __hip_atomic_load((LAS unsigned*)(size_t)cnt, __ATOMIC_RELAXED, __HIP_MEMORY_SCOPE_WORKGROUP); }
DI void await(unsigned cnt, unsigned n) {
#pragma unroll 1
  for (unsigned spin = 0; spin < SPIN_CAP * 32; ++spin) {
    if (__builtin_amdgcn_readfirstlane(peek(cnt)) >= n) break;
    __builtin_amdgcn_s_sleep(1);
  }
  asm volatile("" ::: "memory");
}

__global__ __launch_bounds__(512, 2) void fused_block(const void* __restrict__ x, const void* __restrict__ wqkv, const void* __restrict__ wo,
                                                      const void* __restrict__ w1, const void* __restrict__ b1, const void* __restrict__ w2,
                                                      const void* __restrict__ b2, void* __restrict__ out, char* __restrict__ ws) {
  constexpr unsigned lds0 = 0;
  const int wave = __builtin_amdgcn_readfirstlane(threadIdx.x >> 6);
  const int m = wave & 3;
  const bool isG = wave < 4;
  const int xcd = blockIdx.x & 7, idx = blockIdx.x >> 3;
  const int r = (xcd >> 2) * 8 + (idx >> 2), j = (xcd & 3) * 4 + (idx & 3);
  const int rt = (xcd >> 1) * 8 + (idx >> 2), ct = (xcd & 1) * 4 + (idx & 3);
  int lane = threadIdx.x & 63, r32, h, g, q4, p4;
  STAMP_AT(0);
  asm volatile("" :: "s"(x), "s"(wqkv), "s"(wo), "s"(w1), "s"(b1), "s"(w2), "s"(b2), "s"(out), "s"(ws));

  const v4u sx = make_srd(x, SEQ * DM * 2), sws = make_srd(ws, WS_END);
  const __amdgpu_buffer_rsrc_t rws = __builtin_amdgcn_make_buffer_rsrc(ws, 0, WS_END, 0x00020000);
  const unsigned one2 = 0x3f803f80u;
  const int T = 4 * r + m;
  const int rot = ROT;
  auto flag_off = [&](int kind, int tile, int jj) { return WS_FLAG + ((kind * 64 + tile) * 16 + jj) * 128; };
  auto flag = [&](int kind, int tile, int jj) { return (GAS u64*)(ws + flag_off(kind, tile, jj)); };
  u64 tag;
  unsigned taglo, tag32;
  v4u xres, bres, xaug, st;
  float mean, var, rstd, sd;
  v4u sel;
  unsigned xo[4], swz;
  auto lane_setup = [&]() {
    lane = threadIdx.x & 63; PIN(lane);
    r32 = lane & 31; h = lane >> 5; g = lane >> 4; q4 = (lane >> 2) & 3; p4 = lane & 3;
    const unsigned selv = ((lane & 15) == (g & 1)) ? one2 : 0u;
    sel = (v4u){selv, selv, selv, selv};
#pragma unroll
    for (int p = 0; p < 4; ++p) xo[p] = (32 * m + r32) * 128 + (((2 * ((p + m) & 3) + h) ^ ((r32 >> 1) & 7)) * 16);
    swz = ((((lane >> 2) & 1) ^ (g & 1)) * 64) + p4 * 16;
  };

  auto stat_load = [&]() { st = ld16_sc1_async(sws, WS_STATS + (128 * r + 32 * m + r32) * 16, 0); };
  auto res_loads = [&]() {
    int t2 = threadIdx.x; PIN(t2);
    const int row = 64 * rt + (t2 >> 3), col = 64 * ct + (t2 & 7) * 8;
    xres = ld16_async(sx, row * 1024 + col * 2, 0);
    bres = ld16_async(make_srd(b2, DM * 2), col * 2, 0);
  };
  auto row_stats = [&]() {
    if (!__all(st[1] == tag32 && st[3] == tag32)) {
#pragma unroll 1
      for (unsigned spin = 0; spin < SPIN_CAP; ++spin) {
        st = ld16_sc1_async(sws, WS_STATS + (128 * r + 32 * m + r32) * 16, 0);
        wait_vm1<0>(st);
        if (__all(st[1] == tag32 && st[3] == tag32)) break;
      }
    }
    mean = asf(st[0]); var = asf(st[2]);
    PIN(mean); PIN(var);
  };
  auto make_xaug = [&]() {
    PIN(mean); PIN(var);
    rstd = __builtin_amdgcn_rsqf(var); sd = var * rstd;
    const unsigned hi = pk2(-mean, sd);
    const unsigned lo = pk2(-mean - bflo(hi), FMA1(bfhi(hi), -1.f, sd));
    xaug = (v4u){(hi & 0xffffu) | (hi << 16), (lo & 0xffffu) | (hi & 0xffff0000u), lo >> 16, 0u};
    if (h) xaug = (v4u){0u, 0u, 0u, 0u};
    PIN(xaug);
  };
  auto cswr = [&](int tile, f32x4 c) {
    if (lane < 16) { LAS float* p = (LAS float*)(size_t)(lds0 + L_CS + (tile * 32 + lane) * 16 + m * 4); p[0] = c[0]; p[64] = c[1]; }
  };
  auto csrd = [&](int tile) { return __builtin_bit_cast(f32x4, lds128(lds0 + L_CS + (tile * 32 + r32) * 16)); };
  auto waug = [&](f32x4 c4, unsigned b16) {
    const float c = ((c4[0] + c4[1]) + c4[2]) + c4[3];
    const unsigned hi = pk2(c, 0.f);
    const unsigned lo = pk2(c - bflo(hi), 0.f);
    return (v4u){hi | (lo << 16), hi | (b16 << 16), b16, 0u};
  };

  const v4u sw1 = make_srd(w1, DM * FF * 2);
  const int hcol0 = 448 * (j >> 2) + 128 * (j & 3);
  const bool heavy = (j & 3) != 3;
  unsigned bias0, bias1;
  auto bias_load = [&](int P) {
    bias0 = ld2_async(make_srd(b1, FF * 2), r32 * 2, (hcol0 + 64 * P) * 2);
    bias1 = ld2_async(make_srd(b1, FF * 2), r32 * 2, (hcol0 + 64 * P + 32) * 2);
  };
  auto w1dma = [&](int P, int c, unsigned dst, int half) {
    const unsigned vw1 = (lane >> 3) * (FF * 2) + swz, vw1b = vw1 + 8 * (FF * 2) - 1024;
    const unsigned so = (64 * ((c + rot) & 7) + 16 * m) * (FF * 2) + (hcol0 + 64 * P) * 2;
    if (half == 0) dma16_a(sw1, vw1, so, dst + m * 2048); else dma16_b(sw1, vw1b, so);
  };
  f32x16 hac[2];
  unsigned rk[4][2];
  auto hsetup = [&](int P) {
#pragma unroll
    for (int p = 0; p < 4; ++p)
#pragma unroll
      for (int c = 0; c < 2; ++c) {
        rk[p][c] = lds0 + (P ? L_WA : 28672) + ((p + m) & 3) * 2048 + h * 1024 + q4 * 128 + ((c ^ (q4 >> 1)) * 64) + (g & 1) * 32 + p4 * 8;
        PIN(rk[p][c]);
      }
  };
  auto hmm = [&](auto pc, const v4u (&xf)[32], auto&& hook) {
    constexpr int P = pc.value;
    constexpr int D = 4;
    const unsigned cnt = lds0 + L_CNT;
    f32x4 cs[2] = {}, c0, c1;
    v4u wf[D][2], au0, au1;
    unsigned seen = 0;
    hac[0] = (f32x16){}; hac[1] = (f32x16){};
    auto rd = [&](int ks, int t) { wf[ks % D][t] = ldtr2(rk[ks & 3][t] + (ks >> 2) * 8192, 512); };
#pragma unroll
    for (int ks = 0; ks < D; ++ks) { rd(ks, 0); rd(ks, 1); }
    SBAR();
    static_for<0, 32>([&](auto kc) {
      constexpr int ks = kc.value;
      hook(kc);
      if constexpr (ks == 10) seen = peek(cnt + 20);
      if constexpr (ks == 29) seen = peek(cnt);
#pragma unroll
      for (int t = 0; t < 2; ++t) {
        hac[t] = MFMA32(wf[ks % D][t], xf[ks], hac[t]);
        if ((ks & 3) == 0 && ks < 28) { cs[t] = MFMA16(sel, wf[ks % D][t], cs[t]); PIN(cs[t]); }
        if (ks == 26) { cs[t] = MFMA16(sel, wf[28 % D][t], cs[t]); PIN(cs[t]); }
        SBAR();
        if constexpr (ks + D < 32) rd(ks + D, t);
        if constexpr (ks == 31) { if (t == 0) { au0 = waug(c0, bias0); PIN(au0); } else { au1 = waug(c1, bias1); PIN(au1); } }
        SBAR();
      }
      if constexpr (ks == 11 || ks == 30) {
        PIN(hac[0]); PIN(hac[1]);
        if (__builtin_amdgcn_readfirstlane(seen) < 4) await(ks == 11 ? cnt + 20 : cnt, 4);
        asm volatile("" ::: "memory");
        if constexpr (ks == 30) { c0 = csrd(2 * P); c1 = csrd(2 * P + 1); }
      }
      if constexpr (ks == 27) { cswr(2 * P, cs[0]); cswr(2 * P + 1, cs[1]); arrive_nowait(cnt); }
      SBAR();
    });
    hac[0] = MFMA32(au0, xaug, hac[0]);
    hac[1] = MFMA32(au1, xaug, hac[1]);
    SBAR();
  };
  auto hgelu = [&](auto pc, auto&& mid) {
    constexpr int P = pc.value;
    constexpr bool PK = GELU_PK(P);
    const float C0 = 2.3022081986f, C1 = C0 * 0.044715f;
    const float ga = -C1 * rstd * rstd * rstd, gb = -C0 * rstd;
    auto mul = [&](f32x2 a, f32x2 b) { return PK ? a * b : (f32x2){MUL1(a[0], b[0]), MUL1(a[1], b[1])}; };
    auto fma = [&](f32x2 a, float b, float c) {
      return PK ? __builtin_elementwise_fma(a, (f32x2){b, b}, (f32x2){c, c}) : (f32x2){FMA1(a[0], b, c), FMA1(a[1], b, c)}; };
#pragma unroll
    for (int t = 0; t < 2; ++t) {
      f32x2 v[8], z[8];
#pragma unroll
      for (int e = 0; e < 8; ++e) { v[e] = (f32x2){hac[t][2 * e], hac[t][2 * e + 1]}; z[e] = mul(v[e], v[e]); }
      SBAR();
#pragma unroll
      for (int e = 0; e < 8; ++e) z[e] = fma(z[e], ga, gb);
      SBAR();
#pragma unroll
      for (int e = 0; e < 8; ++e) z[e] = mul(z[e], v[e]);
      SBAR();
#pragma unroll
      for (int e = 0; e < 8; ++e) z[e] = (f32x2){EXP2(z[e][0]), EXP2(z[e][1])};
      SBAR();
#pragma unroll
      for (int e = 0; e < 8; ++e) z[e] = fma(z[e], sd, sd);
      SBAR();
#pragma unroll
      for (int e = 0; e < 8; ++e) z[e] = (f32x2){__builtin_amdgcn_rcpf(z[e][0]), __builtin_amdgcn_rcpf(z[e][1])};
      SBAR();
#pragma unroll
      for (int e = 0; e < 8; ++e) z[e] = mul(z[e], v[e]);
      SBAR();
#pragma unroll
      for (int s = 0; s < 2; ++s) {
        const v4u f = {pk2(z[4 * s][0], z[4 * s][1]), pk2(z[4 * s + 1][0], z[4 * s + 1][1]), pk2(z[4 * s + 2][0], z[4 * s + 2][1]), pk2(z[4 * s + 3][0], z[4 * s + 3][1])};
        st16_sc1(f, sws, lane * 16, WS_AF + (T * AFT + (hcol0 >> 4) + 4 * P + 2 * t + s) * 1024);
      }
      if (t == 0) mid();
      SBAR();
    }
  };

  if (isG) {
    lane_setup();
    const v4u sw2 = make_srd(w2, FF * DM * 2), swo = make_srd(wo, DM * DM * 2);
    const unsigned vw2 = (lane >> 3) * 1024 + swz, vw2b = vw2 + 7 * 1024;
    const int ro3 = rt % 3;
    auto kmap = [&](int kk) {
      if (kk < 16) return 8 * (((kk >> 2) + rt) & 3) + (kk & 3);
      const int p = ((kk - 16) >> 2) + ro3;
      return 8 * (p >= 3 ? p - 3 : p) + 4 + (kk & 3);
    };
    auto omap = [&](int i) { return 2 * ((i + rt) & 3) + (i >> 2); };
    auto wdmah = [&](int kk, int half) {
      const unsigned so = (kk < 28 ? 448 * m + 16 * kmap(kk) : 128 * m + 16 * omap(kk - 28)) * 1024 + 128 * ct;
      if (half == 0) dma16_a(kk < 28 ? sw2 : swo, vw2, so, lds0 + L_WG + m * PART + (kk % RD) * 2048); else dma16_b(kk < 28 ? sw2 : swo, vw2b, so);
    };
    auto wdma = [&](int kk) { wdmah(kk, 0); wdmah(kk, 1); };
    {
      const v4u swq = make_srd(wqkv, DM * 3 * DM * 2);
      const unsigned vx0 = (lane >> 3) * 1024 + (((lane & 7) ^ g) * 16), vx1 = (7 + (lane >> 3)) * 1024 + (((lane & 7) ^ (4 + g)) * 16);
      const unsigned vwq = (lane >> 2) * (3 * DM * 2) + p4 * 16;
      auto piece = [&](int s, int i) {
        const unsigned buf = lds0 + (s % 3) * STG;
        const int kc = (s + rot) & 7;
        if (i < 4) {
          const unsigned so = (128 * r + 32 * m + 16 * (i >> 1)) * 1024 + kc * 128;
          if (i & 1) dma16_b(sx, vx1, so); else dma16_a(sx, vx0, so, buf + m * 4096 + (i >> 1) * 2048);
        } else if (i < 7) dma16(swq, vwq, (64 * kc + 16 * m) * (3 * DM * 2) + (512 * (i - 4) + 32 * j) * 2, buf + 16384 + ((i - 4) * 4 + m) * 1024);
        else w1dma(0, s, buf + 28672, i - 7);
      };
#pragma unroll
      for (int i = 0; i < 18; ++i) { piece(i / 9, i % 9); if (i == 8) __builtin_amdgcn_s_barrier(); }
      __builtin_amdgcn_s_setprio(PRIO_G1);
      hsetup(0);
      f32x4 cs[2] = {};
      v4u xf[4], wf[4][2];
      hac[0] = (f32x16){}; hac[1] = (f32x16){};
      static_for<0, 8>([&](auto sc) {
        constexpr int s = sc.value;
        constexpr unsigned buf = (s % 3) * STG;
        auto fill = [&](int i) {
          if constexpr (s < 6) piece(s + 2, i);
          if constexpr (s == 6) { if (i == 0) { stat_load(); bias_load(0); } else wdmah((i - 1) >> 1, (i - 1) & 1); }
          if constexpr (s == 7) { if (heavy) { w1dma(1, i, lds0 + L_WA + i * 8192, 0); w1dma(1, i, 0, 1); } }
          SBAR();
        };
        auto rd = [&](int p) { xf[p] = lds128(buf + xo[p]); wf[p][0] = ldtr2(buf + rk[p][0], 512); wf[p][1] = ldtr2(buf + rk[p][1], 512); SBAR(); };
        if constexpr (s < 7) wait_vm<9>(); else { wait_vm1<8>(st); PIN(bias0); PIN(bias1); }
        bar();
        STAMP_AT(10 + s);
        rd(0);
        if constexpr (s < 7) fill(0);
        rd(1);
        if constexpr (s < 7) fill(1);
        if constexpr (s == 1) {
          const v2u tg = *(const LAS v2u*)(size_t)(lds0 + L_CNT + 32);
          taglo = __builtin_amdgcn_readfirstlane(tg[0]); tag32 = __builtin_amdgcn_readfirstlane(tg[1]);
          tag = ((u64)tag32 << 32) | taglo;
        }
#pragma unroll
        for (int p = 0; p < 4; ++p) {
#pragma unroll
          for (int t = 0; t < 2; ++t) {
            hac[t] = MFMA32(wf[p][t], xf[p], hac[t]);
            if (p == 0) { cs[t] = MFMA16(sel, wf[p][t], cs[t]); PIN(cs[t]); }
            SBAR();
            if (p < 2 && t == 0) rd(p + 2);
            if (s == 7) fill(2 * p + t); else if (2 * p + t < 7) fill(2 + 2 * p + t);
          }
          if (s == 7 && p == 1) {
            cswr(0, cs[0]); cswr(1, cs[1]);
            bar();
            STAMP_AT(1);
          }
        }
        if constexpr (s == 7) {
          const f32x4 c0 = csrd(0), c1 = csrd(1);
          row_stats(); make_xaug();
          hac[0] = MFMA32(waug(c0, bias0), xaug, hac[0]);
          hac[1] = MFMA32(waug(c1, bias1), xaug, hac[1]);
          SBAR();
        }
      });
    }
    STAMP_AT(18);
    if (heavy) { wait_vm<8>(); arrive_nowait(lds0 + L_CNT + 16); }
    hgelu(std::integral_constant<int, 0>{}, [&]() {
      wait_vm<2>();
      if (heavy) arrive_nowait(lds0 + L_CNT + 20);
    });
    __builtin_amdgcn_s_setprio(0);
    STAMP_AT(19);
    unsigned rb2[2];
#pragma unroll
    for (int c = 0; c < 2; ++c) rb2[c] = lds0 + L_WG + m * PART + h * 512 + q4 * 128 + ((c ^ (q4 >> 1)) * 64) + (g & 1) * 32 + p4 * 8;
    v4u wq[36][2];
    auto wrd = [&](int kk) {
#pragma unroll
      for (int c = 0; c < 2; ++c) wq[kk][c] = ldtr2(rb2[c] + (kk % RD) * 2048, 1024);
    };
    auto wgot = [&](int kk) { PIN(wq[kk][0]); PIN(wq[kk][1]); };
    const int ftile = 2 * rt + ((lane >> 2) & 1);
    v4u af[28][2], of[8][2];
    v2u fl;
    const unsigned a0 = WS_AF + (2 * rt * AFT + 28 * m) * 1024, o0 = WS_AF + (2 * rt * AFT + 112 + 8 * m) * 1024;
    for (int kk = 0; kk < 4; ++kk) wrd(kk);
    for (int kk = 4; kk < 8; ++kk) wdma(kk);
    for (int kk = 0; kk < 4; ++kk) wgot(kk);
    wait_vm<8>();
    if (lane == 0) __hip_atomic_store(flag(FL_H0, T, j), tag, RLX_AGENT);
    STAMP_AT(2);
    for (int kk = 8; kk < 12; ++kk) wdma(kk);
    static_for<1, 3>([&](auto ic) {
      constexpr int i = ic.value;
      wait_vm<(i == 1) ? 9 : 8>();
      for (int kk = 4 * i; kk < 4 * i + 4; ++kk) wrd(kk);
      for (int kk = 4 * i; kk < 4 * i + 4; ++kk) wgot(kk);
      if constexpr (i == 2) fl = ld8_sc1_async(sws, flag_off(FL_H0, ftile, 4 * m + p4));
      for (int kk = 4 * i; kk < 4 * i + 4; ++kk) wdma(kk + RD);
    });
    STAMP_AT(3);
    wait_vmf<8>(fl);
    if (!__all(fl[0] == taglo && fl[1] == tag32)) poll_flags(flag(FL_H0, ftile, 4 * m + p4), tag);
    STAMP_AT(4);
#pragma unroll
    for (int kk = 0; kk < AW; ++kk) {
      af[kk][0] = ld16_sc1_async(sws, lane * 16, a0 + kmap(kk) * 1024);
      af[kk][1] = ld16_sc1_async(sws, lane * 16, a0 + (AFT + kmap(kk)) * 1024);
    }
    __builtin_amdgcn_s_setprio(PRIO_G2);
    f32x16 c2[2][2] = {};
    auto mm2 = [&](int kk, const v4u (&a)[2], auto&& fill) {
      SBAR();
#pragma unroll
      for (int i = 0; i < 4; ++i) { c2[i >> 1][i & 1] = MFMA32(wq[kk][i & 1], a[i >> 1], c2[i >> 1][i & 1]); SBAR(); fill(i); SBAR(); }
    };
    static_for<0, 28>([&](auto kc) {
      constexpr int kk = kc.value;
      wait_vm2<(kk < AW) ? 2 * (AW - 1 - kk) + g2_range(0, kk) : g2_range(kk - AW + 1, kk)>(af[kk][0], af[kk][1]);
      if constexpr (kk == H1_CK) {
        STAMP_AT(20);
        wait_vmf<g2_range(H1_LD, H1_CK) - 1>(fl);
        if (!__all(fl[0] == taglo && fl[1] == tag32)) poll_flags(flag(FL_H1, ftile, 4 * m + p4), tag);
        STAMP_AT(21);
      }
      if constexpr (kk == H1_LD) fl = ld8_sc1_async(sws, flag_off(FL_H1, ftile, 4 * m + p4));
      if constexpr (kk == O_LD) fl = ld8_sc1_async(sws, flag_off(FL_O0, ftile, 4 * m + p4));
      constexpr bool dw = kk >= 1 && kk - 1 + NR + RD < 36;
      if constexpr (dw) wgot(kk - 1 + NR);
      mm2(kk, af[kk], [&](int i) {
        if constexpr (dw) { if (i < 2) wdmah(kk - 1 + NR + RD, i); }
        if constexpr (kk + NR < 28) { if (i == 1) wrd(kk + NR); }
        if constexpr (kk == 27) { if (i == 0) wrd(28); }
        if constexpr (kk + AW < 28) { if (i >= 2) af[kk + AW][i - 2] = ld16_sc1_async(sws, lane * 16, a0 + (AFT * (i - 2) + kmap(kk + AW)) * 1024); }
      });
      if constexpr (kk == O_CK) {
        wait_vmf<g2_range(O_LD, O_CK) - 1>(fl);
        if (!__all(fl[0] == taglo && fl[1] == tag32)) poll_flags(flag(FL_O0, ftile, 4 * m + p4), tag);
        res_loads();
#pragma unroll
        for (int i = 0; i < 4; ++i) {
          of[i][0] = ld16_sc1_async(sws, lane * 16, o0 + omap(i) * 1024);
          of[i][1] = ld16_sc1_async(sws, lane * 16, o0 + (AFT + omap(i)) * 1024);
        }
      }
    });
    static_for<0, 8>([&](auto kc) {
      constexpr int kk = kc.value;
      if (kk < 7) wrd(28 + kk + 1);
      if constexpr (kk == 1) {
        STAMP_AT(5);
        poll_flags(flag(FL_O1, ftile, 4 * m + p4), tag);
        STAMP_AT(6);
#pragma unroll
        for (int i = 4; i < 8; ++i) {
          of[i][0] = ld16_sc1_async(sws, lane * 16, o0 + omap(i) * 1024);
          of[i][1] = ld16_sc1_async(sws, lane * 16, o0 + (AFT + omap(i)) * 1024);
        }
      }
      wait_vm2<(kk == 0) ? 6 : 14 - 2 * kk>(of[kk][0], of[kk][1]);
      mm2(28 + kk, of[kk], [&](int) {});
    });
    PIN(xres); PIN(bres);
    asm volatile("" ::: "memory");
#pragma unroll
    for (int mt = 0; mt < 2; ++mt)
#pragma unroll
      for (int c = 0; c < 2; ++c)
#pragma unroll
        for (int qd = 0; qd < 4; ++qd) {
          const f32x4 v = {c2[mt][c][4 * qd], c2[mt][c][4 * qd + 1], c2[mt][c][4 * qd + 2], c2[mt][c][4 * qd + 3]};
          sts128(lds0 + L_WG + m * PART + ((mt * 8 + c * 4 + qd) * 66 + lane) * 16, __builtin_bit_cast(v4u, v));
        }
    STAMP_AT(7);
  } else {
    __builtin_amdgcn_s_setprio(1);
    lane_setup();
    v2u ep = ld8_sc1_async(sws, WS_EPOCH);
    v4u xr[2];
    xr[0] = ld16_async(sx, lane * 16, (128 * r + 8 * j + 2 * m) * 1024); xr[1] = ld16_async(sx, lane * 16, (128 * r + 8 * j + 2 * m + 1) * 1024);
    unsigned warm;
    {
      u64 pc; asm volatile("s_getpc_b64 %0" : "=s"(pc));
      warm = ld2_async(make_srd((const void*)(pc & ~127ull), CODEWARM), lane < 3 ? lane * 128 : 0x40000000u, (idx * 4 + m) * 384);
    }
    __builtin_amdgcn_s_barrier();
    wait_vmf<3>(ep);
    taglo = __builtin_amdgcn_readfirstlane(ep[0]) * 0x9E3779B1u + 0x7F4A7C15u; tag32 = __builtin_amdgcn_readfirstlane(ep[1]) * 0x85EBCA77u + 0x165667B1u;
    tag = ((u64)tag32 << 32) | taglo;
    if (m == 0 && lane < 10) *(LAS unsigned*)(size_t)(lds0 + L_CNT + lane * 4) = lane == 8 ? taglo : lane == 9 ? tag32 : 0u;
    auto make_stats = [&]() {
      wait_vm2<0>(xr[0], xr[1]);
      PIN(warm);
      float piv[2], a[4];
#pragma unroll
      for (int u = 0; u < 2; ++u) {
        piv[u] = rdl(bflo(xr[u][0]), 0);
        float s1 = 0.f, s2 = 0.f;
#pragma unroll
        for (int i = 0; i < 8; ++i) { const float d = ((i & 1) ? bfhi(xr[u][i >> 1]) : bflo(xr[u][i >> 1])) - piv[u]; s1 += d; s2 = __builtin_fmaf(d, d, s2); }
        a[2 * u] = s1; a[2 * u + 1] = s2;
      }
#pragma unroll
      for (int i = 0; i < 4; ++i) a[i] += dpp<0xB1>(a[i]);
#pragma unroll
      for (int i = 0; i < 4; ++i) a[i] += dpp<0x4E>(a[i]);
      float t = p4 == 0 ? a[0] : p4 == 1 ? a[1] : p4 == 2 ? a[2] : a[3];
      t += dpp<0x114>(t); t += dpp<0x118>(t);
      const auto s16 = __builtin_amdgcn_permlane16_swap(asu(t), asu(t), false, false);
      t = asf(s16[0]) + asf(s16[1]);
      const auto s32 = __builtin_amdgcn_permlane32_swap(asu(t), asu(t), false, false);
      t = (asf(s32[0]) + asf(s32[1])) * (1.f / DM);
      const float e1 = dpp<0xA0>(t);
      const float val = (p4 & 1) ? __builtin_fmaxf(__builtin_fmaf(-e1, e1, t), 0.f) + 1e-5f : t + (p4 ? piv[1] : piv[0]);
      if ((lane >> 2) == 3) {
        const v2u gr = {asu(val), tag32};
        __builtin_amdgcn_raw_buffer_store_b64(gr, rws, WS_STATS + (128 * r + 8 * j + 2 * m) * 16 + p4 * 8, 0, 16);
      }
    };
    const unsigned acnt = lds0 + L_CNT;
    const unsigned vpos = 2048 + (g & 1) * 512 + h * 256 + (lane & 15) * 16;
    v4u qf[2], am0, am1;
    {
      v4u xf[32];
      f32x16 acc[3] = {};
      f32x4 cs[3] = {};
      v4u wf[4][3];
      unsigned wb[4];
#pragma unroll
      for (int p = 0; p < 4; ++p) wb[p] = 16384 + ((p + m) & 3) * 1024 + (8 * h + q4) * 64 + (g & 1) * 32 + p4 * 8;
      auto rd = [&](int ks, int t) {
        const unsigned buf = lds0 + ((ks >> 2) % 3) * STG;
        if (t == 0) xf[ks] = lds128(buf + xo[ks & 3]);
        wf[ks & 3][t] = ldtr2(buf + wb[ks & 3] + t * 4096, 256);
      };
      auto mm = [&](int ks, int t) {
        acc[t] = t == 2 ? MFMA32(xf[ks], wf[ks & 3][t], acc[t]) : MFMA32(wf[ks & 3][t], xf[ks], acc[t]);
        if ((ks & 3) == 0) { cs[t] = MFMA16(sel, wf[ks & 3][t], cs[t]); PIN(cs[t]); }
        SBAR();
      };
      make_stats();
      bar();
      STAMP_AT(10);
#pragma unroll
      for (int t = 0; t < 6; ++t) rd(t / 3, t % 3);
      SBAR();
      static_for<0, 8>([&](auto sc) {
        constexpr int s = sc.value;
#pragma unroll
        for (int p = 0; p < 2; ++p) {
#pragma unroll
          for (int t = 0; t < 3; ++t) { mm(4 * s + p, t); rd(4 * s + p + 2, t); SBAR(); }
          if (s == 7 && p == 0) { cswr(4, cs[0]); cswr(5, cs[1]); cswr(6, cs[2]); SBAR(); }
        }
        if constexpr (s == 5) stat_load();
        if constexpr (s == 6) {
          wait_vm1<0>(st);
          row_stats();
        }
        mm(4 * s + 2, 0); mm(4 * s + 2, 1);
        bar();
        if constexpr (s < 7) {
          STAMP_AT(11 + s);
          mm(4 * s + 2, 2);
          rd(4 * s + 4, 0); rd(4 * s + 4, 1); rd(4 * s + 4, 2); SBAR();
#pragma unroll
          for (int t = 0; t < 3; ++t) { mm(4 * s + 3, t); rd(4 * s + 5, t); SBAR(); }
        }
      });
      STAMP_AT(1);
      __builtin_amdgcn_s_setprio(3);
      const f32x4 c5 = csrd(5), c6 = csrd(6), c4 = csrd(4);
      SBAR();
      mm(30, 2); mm(31, 1);
      make_xaug();
      SBAR();
      mm(31, 2); mm(31, 0);
      if (h == 0) *(LAS float*)(size_t)(lds0 + L_RS + (m * 32 + r32) * 4) = rstd;
      acc[1] = MFMA32(waug(c5, 0u), xaug, acc[1]);
      acc[2] = MFMA32(xaug, waug(c6, 0u), acc[2]);
      acc[0] = MFMA32(waug(c4, 0u), xaug, acc[0]);
      SBAR();
      f32x4 rs[4];
#pragma unroll
      for (int qd = 0; qd < 4; ++qd) rs[qd] = __builtin_bit_cast(f32x4, lds128(lds0 + L_RS + (m * 32 + 8 * qd + 4 * h) * 4));
      const unsigned kvo = WS_KV + (j * 64 + T) * 4096;
#pragma unroll
      for (int e = 0; e < 16; ++e) acc[1][e] = MUL1(acc[1][e], rstd);
#pragma unroll
      for (int e = 0; e < 2; ++e) {
        const v4u f = pack8(acc[1], e);
        st16_sc1(f, sws, lane * 16, kvo + e * 1024);
        sts128(lds0 + L_KV + (8 + m) * KVT + e * 1024 + lane * 16, f);
      }
#pragma unroll
      for (int qd = 0; qd < 4; ++qd)
#pragma unroll
        for (int e = 0; e < 4; ++e) acc[2][4 * qd + e] = MUL1(acc[2][4 * qd + e], rs[qd][e]);
#pragma unroll
      for (int s = 0; s < 2; ++s) {
        const v4u f = pack8(acc[2], s);
        st16_sc1(f, sws, lane * 16, kvo + 2048 + s * 1024);
        sts128(lds0 + L_KV + (8 + m) * KVT + s * VB + vpos, f);
      }
      SBAR();
      const float qs = rstd * (0.25f * 1.4426950408889634f);
#pragma unroll
      for (int e = 0; e < 16; ++e) acc[0][e] = MUL1(acc[0][e], qs);
      qf[0] = pack8(acc[0], 0); qf[1] = pack8(acc[0], 1);
      PIN(qf[0]); PIN(qf[1]);
      STAMP_AT(2);
      const int T0 = 4 * r - 8 + 2 * m;
      const bool live = T0 >= 0;
      auto kv_load = [&](v4u (&kv)[8], int i0, int i1) {
        const unsigned src = WS_KV + (j * 64 + (live ? T0 : 0)) * 4096, vo = live ? lane * 16 : h * 512;
#pragma unroll
        for (int i = i0; i < i1; ++i) kv[i] = ld16_sc1_async(sws, vo, src + (live ? i : (i & 3)) * 1024);
      };
      auto kv_stage = [&](v4u (&kv)[8]) {
        const unsigned on = (lane < 2 || live) ? one2 : 0u;
        if (lane < 6) sts128(lds0 + L_KV + (lane < 2 ? 8 + m : 2 * m + ((lane - 2) >> 1)) * KVT + 2048 + (lane & 1) * VB + 1024, (v4u){on, on, on, on});
        const v4u zz = {};
#pragma unroll
        for (int i = 0; i < 2; ++i) {
          sts128(lds0 + L_KV + (2 * m + i) * KVT + lane * 16, kv[4 * i]);
          sts128(lds0 + L_KV + (2 * m + i) * KVT + 1024 + lane * 16, kv[4 * i + 1]);
          sts128(lds0 + L_KV + (2 * m + i) * KVT + vpos, live ? kv[4 * i + 2] : zz);
          sts128(lds0 + L_KV + (2 * m + i) * KVT + VB + vpos, live ? kv[4 * i + 3] : zz);
        }
        arrive_nowait(acnt);
      };
      auto kv_wait = [&](v4u (&kv)[8]) {
        wait_vm<0>();
#pragma unroll
        for (int i = 0; i < 8; ++i) PIN(kv[i]);
      };
      if (heavy) {
        bias_load(1);
        hsetup(1);
        await(acnt + 16, 4);
        STAMP_AT(3);
        v2u fl;
        v4u kv[8];
        hmm(std::integral_constant<int, 1>{}, xf, [&](auto kc) {
          constexpr int ks = kc.value;
          if constexpr (ks == KVPUB) {
            wait_vm<0>();
            PIN(bias0); PIN(bias1);
            if (lane == 0) __hip_atomic_store(flag(FL_KV, T, j), tag, RLX_AGENT);
          }
          if constexpr (ks == 24) fl = ld8_sc1_async(sws, flag_off(FL_KV, live ? T0 + (lane & 1) : 0, j));
        });
        STAMP_AT(18);
        wait_vmf<0>(fl);
        if (!__all(fl[0] == taglo && fl[1] == tag32)) poll_flags(flag(FL_KV, live ? T0 + (lane & 1) : 0, j), tag);
        kv_load(kv, 0, 8);
        hgelu(std::integral_constant<int, 1>{}, [&]() {});
        STAMP_AT(4);
        wait_vm<4>();
#pragma unroll
        for (int i = 0; i < 8; ++i) PIN(kv[i]);
        kv_stage(kv);
      } else {
        wait_vm<0>();
        if (lane == 0) __hip_atomic_store(flag(FL_KV, T, j), tag, RLX_AGENT);
        v4u kv[8];
        poll_flags(flag(FL_KV, live ? T0 + (lane & 1) : 0, j), tag);
        kv_load(kv, 0, 8); kv_wait(kv); kv_stage(kv);
      }
    }
    STAMP_AT(19);
    const unsigned kb = lds0 + L_KV + m * KVT + lane * 16;
    unsigned vb[2];
#pragma unroll
    for (int e = 0; e < 2; ++e) { vb[e] = lds0 + L_KV + m * KVT + 2048 + ((g & 1) ? 1024 : e * 512 + h * 256 + (lane & 15) * 16); PIN(vb[e]); }
    {
      const int dq = r32 - 4 * h;
#pragma unroll
      for (int d = 0; d < 8; ++d) { const int row = 2 * (d & 1) + 8 * (d >> 1); (d < 4 ? am0 : am1)[d & 3] = row > dq ? 0xffffffffu : row == dq ? 0xffff0000u : 0u; }
      PIN(am0); PIN(am1);
    }
    await(acnt, heavy ? 8 : 4);
    __builtin_amdgcn_s_setprio(PRIO_ATT);
    STAMP_AT(5);
    const f32x16 zero = {};
    auto o_store = [&](f32x16& o, int e) {
      const float rl = __builtin_amdgcn_rcpf(o[8]);
#pragma unroll
      for (int u = 0; u < 8; ++u) o[u] = MUL1(o[u], rl);
      st16_sc1(pack8(o, 0), sws, lane * 16, WS_AF + (T * AFT + 112 + 2 * j + e) * 1024);
    };
    int redo = 0;
    {
      f32x16 o[2], sa, sb, su;
      bool pend = false;
      v4u kn, ku, va, vc, vn0, vn1, vu0, vu1, p0, p1, pu;
      auto kaddr = [&](int n) { return kb + ((n & 7) + 1) * KVT + (n >> 3) * 1024; };
      auto vaddr = [&](int n) { return vb[n >> 3] + ((n & 7) + 1) * KVT; };
      kn = lds128(kaddr(0)); vn0 = lds128(vaddr(0)); vn1 = lds128(vaddr(0) + VB);
      sa = MFMA32(kn, qf[0], zero);
      kn = lds128(kaddr(1));
      SBAR();
      auto step = [&](auto nc, f32x16& sc, f32x16& sn) {
        constexpr int n = nc.value, e = n >> 3, i = n & 7;
        if constexpr (n < 15) sn = MFMA32(kn, qf[(n + 1) >> 3], zero);
        if constexpr (n < 14) kn = lds128(kaddr(n + 2));
        if constexpr (i == 5) ku = lds128(kb + e * 1024);
        v4u v0 = vn0, v1 = vn1;
        if constexpr (n < 15) { vn0 = lds128(vaddr(n + 1)); vn1 = lds128(vaddr(n + 1) + VB); }
        if constexpr (i == 6) { vu0 = lds128(vb[e]); vu1 = lds128(vb[e] + VB); }
        SBAR();
        if constexpr (i == 7) {
          static_for<0, 16>([&](auto u) { sc[u.value] = upper<u.value>() ? su[u.value] : sc[u.value]; });
          PIN(sc); SBAR();
        }
#pragma unroll
        for (int u = 0; u < 6; ++u) sc[u] = EXP2(sc[u]);
        PIN(sc); SBAR();
        if constexpr (n == 8) { o[0] = MFMA32(vc, p1, o[0]); SBAR(); }
        else if constexpr (n > 0) { o[e] = MFMA32(vc, p1, o[e]); SBAR(); }
        sc[6] = EXP2(sc[6]); sc[7] = EXP2(sc[7]);
        p0 = pack8(sc, 0);
        sc[8] = EXP2(sc[8]); sc[9] = EXP2(sc[9]);
        if constexpr (i == 7) { va = p0 & am0; p0 &= ~am0; PIN(va); }
        PIN(sc); PIN(p0); SBAR();
        if constexpr (n == 8) { o[0] = MFMA32(vu1, pu, o[0]); SBAR(); }
        o[e] = MFMA32(v0, p0, i == 0 ? zero : o[e]);
        SBAR();
        if constexpr (i == 6) { su = MFMA32(ku, qf[e], zero); SBAR(); }
#pragma unroll
        for (int u = 10; u < 13; ++u) sc[u] = EXP2(sc[u]);
        if constexpr (i == 7) { PIN(sc); SBAR(); o[e] = MFMA32(vu0, va, o[e]); SBAR(); }
#pragma unroll
        for (int u = 13; u < 16; ++u) sc[u] = EXP2(sc[u]);
        p1 = pack8(sc, 1); vc = v1;
        if constexpr (i == 7) { pu = p1 & am1; p1 &= ~am1; PIN(pu); }
        PIN(p1); SBAR();
        if constexpr (n == 8) {
          if (__all(o[0][8] >= 0x1p-30f && o[0][8] <= 0x1p30f)) o_store(o[0], 0); else redo = 1;
          SBAR();
        }
        if constexpr (i == 2) pend = !redo;
        if constexpr (i >= 2) {
          if (pend && (i == 7 || (__builtin_amdgcn_s_getreg(7 | (3 << 11)) & 15) == 0)) {
            wait_vm<0>();
            if (lane == 0) __hip_atomic_store(flag(FL_H1 + e, T, j), tag, RLX_AGENT);
            pend = false;
          }
        }
      };
      static_for<0, 8>([&](auto hc) {
        step(std::integral_constant<int, 2 * hc.value>{}, sa, sb);
        step(std::integral_constant<int, 2 * hc.value + 1>{}, sb, sa);
      });
      o[1] = MFMA32(vc, p1, o[1]);
      o[1] = MFMA32(vu1, pu, o[1]);
      if (__all(o[1][8] >= 0x1p-30f && o[1][8] <= 0x1p30f)) o_store(o[1], 1); else redo |= 2;
    }
    if (redo) {
#pragma unroll 1
      for (int e = 0; e < 2; ++e) {
        if (!((redo >> e) & 1)) continue;
        const v4u q = e ? qf[1] : qf[0];
        const unsigned vbe = e ? vb[1] : vb[0];
        auto merged = [&](const f32x16& c) {
          f32x16 sl = MFMA32(lds128(kb + 8 * KVT + e * 1024), q, c);
          const f32x16 su = MFMA32(lds128(kb + e * 1024), q, c);
          static_for<0, 16>([&](auto u) { sl[u.value] = upper<u.value>() ? su[u.value] : sl[u.value]; });
          return sl;
        };
        float mx = -INFINITY;
#pragma unroll 1
        for (int i = 0; i < 8; ++i) {
          const f32x16 sc = i ? MFMA32(lds128(kb + i * KVT + e * 1024), q, zero) : merged(zero);
#pragma unroll
          for (int u = 0; u < 16; u += 2) mx = max3(mx, sc[u], sc[u + 1]);
        }
        const auto sw = __builtin_amdgcn_permlane32_swap(asu(mx), asu(mx), false, false);
        mx = __builtin_fmaxf(asf(sw[0]), asf(sw[1]));
        f32x16 negm, o;
#pragma unroll
        for (int u = 0; u < 16; ++u) negm[u] = -mx;
        {
          f32x16 sc = merged(negm);
#pragma unroll
          for (int u = 0; u < 16; ++u) sc[u] = EXP2(sc[u]);
          const v4u p0 = pack8(sc, 0), p1 = pack8(sc, 1);
          o = MFMA32(lds128(vbe), p0 & am0, zero);
          o = MFMA32(lds128(vbe + VB), p1 & am1, o);
          o = MFMA32(lds128(vbe + 8 * KVT), p0 & ~am0, o);
          o = MFMA32(lds128(vbe + 8 * KVT + VB), p1 & ~am1, o);
        }
#pragma unroll 1
        for (int i = 1; i < 8; ++i) {
          f32x16 sc = MFMA32(lds128(kb + i * KVT + e * 1024), q, negm);
#pragma unroll
          for (int u = 0; u < 16; ++u) sc[u] = EXP2(sc[u]);
          o = MFMA32(lds128(vbe + i * KVT), pack8(sc, 0), o);
          o = MFMA32(lds128(vbe + i * KVT + VB), pack8(sc, 1), o);
        }
        o_store(o, e);
      }
      wait_vm<0>();
      if (lane == 0) __hip_atomic_store(flag(FL_O0, T, j), tag, RLX_AGENT);
    }
    STAMP_AT(6);
    wait_vm<0>();
    if (lane == 0) __hip_atomic_store(flag(FL_O1, T, j), tag, RLX_AGENT);
    STAMP_AT(7);
    res_loads();
    wait_vm2<0>(xres, bres);
    if (blockIdx.x == 0 && wave == 7) {
#pragma unroll 1
      for (int i = 0; i < 4; ++i) poll_flags(flag(FL_KV, 4 * (4 * i + g), lane & 15), tag);
      if (lane == 0) __hip_atomic_store((GAS u64*)(ws + WS_EPOCH), tag, RLX_AGENT);
    }
  }
  bar();
  {
    int t2 = threadIdx.x; PIN(t2);
    const int row = t2 >> 3, ch = t2 & 7, orow = 64 * rt + row, ocol = 64 * ct + ch * 8;
    const unsigned a = lds0 + L_WG + (((row >> 5) * 8 + ch) * 66 + (row & 31)) * 16;
    f32x4 lo = {}, hi = {};
#pragma unroll
    for (int w = 0; w < 4; ++w) {
      lo += __builtin_bit_cast(f32x4, lds128(a + w * PART));
      hi += __builtin_bit_cast(f32x4, lds128(a + w * PART + 512));
    }
    v4u o;
    o[0] = pk2(lo[0] + bflo(xres[0]) + bflo(bres[0]), lo[1] + bfhi(xres[0]) + bfhi(bres[0]));
    o[1] = pk2(lo[2] + bflo(xres[1]) + bflo(bres[1]), lo[3] + bfhi(xres[1]) + bfhi(bres[1]));
    o[2] = pk2(hi[0] + bflo(xres[2]) + bflo(bres[2]), hi[1] + bfhi(xres[2]) + bfhi(bres[2]));
    o[3] = pk2(hi[2] + bflo(xres[3]) + bflo(bres[3]), hi[3] + bfhi(xres[3]) + bfhi(bres[3]));
    *(GAS v4u*)((char*)out + (size_t)orow * 1024 + ocol * 2) = o;
  }
  STAMP_AT(8);
}

extern "C" void kernel_launch(void* const* d_in, const int* in_sizes, int n_in, void* d_out, int out_size, void* d_ws, size_t ws_size,
                              hipStream_t stream) {
  (void)in_sizes; (void)n_in; (void)out_size; (void)ws_size;
  (void)hipFuncSetAttribute((const void*)fused_block, hipFuncAttributeMaxDynamicSharedMemorySize, LDS_BYTES);
  fused_block<<<256, 512, LDS_BYTES, stream>>>(d_in[0], d_in[1], d_in[2], d_in[4], d_in[5], d_in[6], d_in[7], d_out, (char*)d_ws);
}
```

```cpp
#define CODEWARM 39000
#include <hip/hip_runtime.h>
#include <hip/hip_bf16.h>
#include <stdint.h>
#include <type_traits>

typedef unsigned v4u __attribute__((ext_vector_type(4)));
typedef unsigned v2u __attribute__((ext_vector_type(2)));
typedef float f32x2 __attribute__((ext_vector_type(2)));
typedef float f32x4 __attribute__((ext_vector_type(4)));
typedef float f32x16 __attribute__((ext_vector_type(16)));
typedef short bf16x8 __attribute__((ext_vector_type(8)));
typedef short s16x4 __attribute__((ext_vector_type(4)));
typedef __bf16 bf16x2_t __attribute__((ext_vector_type(2)));
typedef unsigned long long u64;
#define GAS __attribute__((address_space(1)))
#define LAS __attribute__((address_space(3)))
#define DI __device__ __forceinline__
#define RLX_AGENT __ATOMIC_RELAXED, __HIP_MEMORY_SCOPE_AGENT

constexpr int SEQ = 2048, DM = 512, FF = 1792;
constexpr unsigned WS_EPOCH = 0;
constexpr unsigned WS_STATS = 32768;
constexpr unsigned WS_FLAG = 65536;
constexpr unsigned WS_KV = WS_FLAG + 5 * 1024 * 128;
constexpr int AFT = 145;
constexpr unsigned WS_AF = WS_KV + 16 * 64 * 4096;
constexpr unsigned WS_END = WS_AF + 64 * AFT * 1024;
constexpr unsigned WS_STAMP = WS_END;
enum { FL_KV = 0, FL_H0 = 1, FL_H1 = 2, FL_O0 = 3, FL_O1 = 4 };
constexpr int STG = 36864;
constexpr int L_WA = 0;
constexpr int L_WG = 86016;
constexpr int VB = 1040;
constexpr int KVT = 2048 + 2 * VB;
constexpr int L_KV = 65536 - 8 * KVT;
constexpr int RD = 8, NR = 12;
constexpr int PART = 16 * 66 * 16;
constexpr int L_CS = L_WG + 4 * PART;
constexpr int L_RS = L_CS + 3584;
constexpr int L_CNT = L_RS + 512;
constexpr int LDS_BYTES = L_CNT + 64;
static_assert(L_KV + 12 * KVT <= L_WG && L_WG >= 2 * STG && L_CS >= 3 * STG && PART >= RD * 2048 && LDS_BYTES <= 163840, "lds map");
#define AWIN 6
constexpr int AW = AWIN;
constexpr unsigned SPIN_CAP = 1u << 18;

#define MFMA32(a, b, c) __builtin_amdgcn_mfma_f32_32x32x16_bf16(__builtin_bit_cast(bf16x8, a), __builtin_bit_cast(bf16x8, b), c, 0, 0, 0)
#define MFMA16(a, b, c) __builtin_amdgcn_mfma_f32_16x16x32_bf16(__builtin_bit_cast(bf16x8, a), __builtin_bit_cast(bf16x8, b), c, 0, 0, 0)
#define SBAR() __builtin_amdgcn_sched_barrier(0)

#define STAMP_AT(i) do { } while (0)

DI float asf(unsigned u) { return __builtin_bit_cast(float, u); }
DI unsigned asu(float f) { return __builtin_bit_cast(unsigned, f); }
DI unsigned pk2(float lo, float hi) { return __builtin_bit_cast(unsigned, __builtin_convertvector((f32x2){lo, hi}, bf16x2_t)); }
DI float bflo(unsigned u) { return __builtin_bit_cast(float, u << 16); }
DI float bfhi(unsigned u) { return __builtin_bit_cast(float, u & 0xffff0000u); }
DI v4u make_srd(const void* p, unsigned bytes) { const u64 a = (u64)p; return (v4u){(unsigned)a, (unsigned)(a >> 32) & 0xffffu, bytes, 0x00020000u}; }
DI void dma16(v4u srd, unsigned voff, unsigned soff, unsigned lds) {
  asm volatile("s_mov_b32 m0, %3\n\ts_nop 0\n\tbuffer_load_dwordx4 %0, %1, %2 offen lds" :: "v"(voff), "s"(srd), "s"(soff), "s"(lds) : "memory");
}
DI void dma16_a(v4u srd, unsigned voff, unsigned soff, unsigned lds) { dma16(srd, voff, soff, lds); }
DI void dma16_b(v4u srd, unsigned voff1, unsigned soff) {
  asm volatile("buffer_load_dwordx4 %0, %1, %2 offen offset:1024 lds" :: "v"(voff1), "s"(srd), "s"(soff) : "memory");
}
DI v4u ld16_sc1_async(v4u srd, unsigned voff, unsigned soff) {
  v4u r; asm volatile("buffer_load_dwordx4 %0, %1, %2, %3 offen sc1" : "=v"(r) : "v"(voff), "s"(srd), "s"(soff) : "memory"); return r;
}
DI v4u ld16_async(v4u srd, unsigned voff, unsigned soff) {
  v4u r; asm volatile("buffer_load_dwordx4 %0, %1, %2, %3 offen" : "=v"(r) : "v"(voff), "s"(srd), "s"(soff) : "memory"); return r;
}
DI unsigned ld2_async(v4u srd, unsigned voff, unsigned soff) {
  unsigned r; asm volatile("buffer_load_ushort %0, %1, %2, %3 offen" : "=v"(r) : "v"(voff), "s"(srd), "s"(soff) : "memory"); return r;
}
DI void st16_sc1(v4u v, v4u srd, unsigned voff, unsigned soff) {
  asm volatile("buffer_store_dwordx4 %0, %1, %2, %3 offen sc1\n\ts_nop 1" :: "v"(v), "v"(voff), "s"(srd), "s"(soff) : "memory");
}
template <int N> DI void wait_vm1(v4u& a) { asm volatile("s_waitcnt vmcnt(%1)" : "+v"(a) : "i"(N) : "memory"); }
template <int N> DI void wait_vm2(v4u& a, v4u& b) { asm volatile("s_waitcnt vmcnt(%2)" : "+v"(a), "+v"(b) : "i"(N) : "memory"); }
template <int N> DI void wait_vm3(v4u& a, v4u& b, v4u& c) { asm volatile("s_waitcnt vmcnt(%3)" : "+v"(a), "+v"(b), "+v"(c) : "i"(N) : "memory"); }
DI u64 load_s(const void* p) { u64 v; asm volatile("s_load_dwordx2 %0, %1, 0x0\n\ts_waitcnt lgkmcnt(0)" : "=&s"(v) : "s"(p) : "memory"); return v; }
DI unsigned ld4_async(const void* p) { unsigned r; asm volatile("global_load_dword %0, %1, off" : "=v"(r) : "v"(p) : "memory"); return r; }
template <class V> DI void pin(V& v) { asm volatile("" : "+v"(v)); }
template <int N> DI void wait_vm() { asm volatile("s_waitcnt vmcnt(%0)" :: "i"(N) : "memory"); }
DI void bar() { asm volatile("s_waitcnt lgkmcnt(0)\n\ts_barrier" ::: "memory"); }
DI v4u lds128(unsigned a) { return *(const LAS v4u*)(size_t)a; }
DI void sts128(unsigned a, v4u v) { *(LAS v4u*)(size_t)a = v; }
DI v2u ldtr(unsigned a) { return __builtin_bit_cast(v2u, __builtin_amdgcn_ds_read_tr16_b64_v4i16((LAS s16x4*)(size_t)a)); }
DI v4u ldtr2(unsigned a, int second) { const v2u lo = ldtr(a), hi = ldtr(a + second); return (v4u){lo.x, lo.y, hi.x, hi.y}; }
template <int CTRL> DI float dpp(float v) { return __builtin_bit_cast(float, __builtin_amdgcn_update_dpp(0, __builtin_bit_cast(int, v), CTRL, 0xf, 0xf, true)); }
DI float rdl(float v, int l) { return __builtin_bit_cast(float, __builtin_amdgcn_readlane(__builtin_bit_cast(int, v), l)); }
DI v4u pack8(const f32x16& a, int s) { return (v4u){pk2(a[8 * s], a[8 * s + 1]), pk2(a[8 * s + 2], a[8 * s + 3]), pk2(a[8 * s + 4], a[8 * s + 5]), pk2(a[8 * s + 6], a[8 * s + 7])}; }
DI void poll_flags(const GAS u64* p, u64 tag) {
#pragma unroll 1
  for (unsigned spin = 0; spin < SPIN_CAP; ++spin) {
    const u64 v = __hip_atomic_load(p, RLX_AGENT);
    if (__all(v == tag)) break;
    __builtin_amdgcn_s_sleep(2);
  }
  asm volatile("" ::: "memory");
}
constexpr u64 upper_mask(int reg) {
  u64 m = 0;
  for (int l = 0; l < 64; ++l) { const int row = (reg & 3) + 8 * (reg >> 2) + 4 * (l >> 5); if (row > (l & 31)) m |= 1ull << l; }
  return m;
}
template <int REG> DI bool upper() { return __builtin_amdgcn_inverse_ballot_w64(upper_mask(REG)); }
template <int I, int N, class Fn> DI void static_for(Fn&& f) { if constexpr (I < N) { f(std::integral_constant<int, I>{}); static_for<I + 1, N>(f); } }
DI float max3(float a, float b, float c) { return __builtin_fmaxf(__builtin_fmaxf(a, b), c); }

constexpr int H1_CK = 16 - AW, H1_LD = H1_CK - 3, O_CK = 28 - AW, O_LD = O_CK - 3;
constexpr int g2_issued(int t) { return ((t >= 1 && t - 1 + NR + RD < 36) ? 2 : 0) + ((t + AW < 28) ? 2 : 0) + (t == H1_LD) + (t == O_LD) + (t == O_CK ? 10 : 0); }
constexpr int g2_range(int a, int b) { int n = 0; for (int t = a; t < b; ++t) n += g2_issued(t); return n; }
#define GELU_PK(P) (P == 1)
#define PRIO_G2 2
#define PRIO_ATT 3
#define PRIO_G1 2
#define ROT r
#define WSOFF 0
#define KVPUB 6
#define EXP2(v) __builtin_amdgcn_exp2f(v)
#define PIN(v) pin(v)
extern "C" __device__ float mul_unpacked(float, float) __asm("llvm.amdgcn.fmul.legacy");
#define MUL1(a, b) mul_unpacked(a, b)
#define FMA1(a, b, c) __builtin_amdgcn_div_fmasf(a, b, c, false)
DI v2u ld8_sc1_async(v4u srd, unsigned voff) {
  v2u r; asm volatile("buffer_load_dwordx2 %0, %1, %2, 0 offen sc1" : "=v"(r) : "v"(voff), "s"(srd) : "memory"); return r;
}
template <int N> DI void wait_vmf(v2u& a) { asm volatile("s_waitcnt vmcnt(%1)" : "+v"(a) : "i"(N) : "memory"); }
DI void arrive(unsigned cnt, int lane) {
  asm volatile("s_waitcnt lgkmcnt(0)" ::: "memory");
  if (lane == 0) __hip_atomic_fetch_add((LAS unsigned*)(size_t)cnt, 1u, __ATOMIC_RELAXED, __HIP_MEMORY_SCOPE_WORKGROUP);
}
DI void arrive_nowait(unsigned cnt) {
  asm volatile("s_mov_b64 exec, 1\n\tds_add_u32 %0, %1\n\ts_mov_b64 exec, -1" :: "v"(cnt), "v"(1u) : "memory");
}
DI unsigned peek(unsigned cnt) { return __hip_atomic_load((LAS unsigned*)(size_t)cnt, __ATOMIC_RELAXED, __HIP_MEMORY_SCOPE_WORKGROUP); }
DI void await(unsigned cnt, unsigned n) {
#pragma unroll 1
  for (unsigned spin = 0; spin < SPIN_CAP * 8; ++spin) {
    if (__builtin_amdgcn_readfirstlane(peek(cnt)) >= n) break;
    __builtin_amdgcn_s_sleep(1);
  }
  asm volatile("" ::: "memory");
}

__global__ __launch_bounds__(512, 2) void fused_block(const void* __restrict__ x, const void* __restrict__ wqkv, const void* __restrict__ wo,
                                                      const void* __restrict__ w1, const void* __restrict__ b1, const void* __restrict__ w2,
                                                      const void* __restrict__ b2, void* __restrict__ out, char* __restrict__ ws) {
  constexpr unsigned lds0 = 0;
  const int wave = __builtin_amdgcn_readfirstlane(threadIdx.x >> 6);
  const int m = wave & 3;
  const bool isG = wave < 4;
  const int xcd = blockIdx.x & 7, idx = blockIdx.x >> 3;
  const int r = (xcd >> 2) * 8 + (idx >> 2), j = (xcd & 3) * 4 + (idx & 3);
  const int rt = (xcd >> 1) * 8 + (idx >> 2), ct = (xcd & 1) * 4 + (idx & 3);
  int lane = threadIdx.x & 63, r32, h, g, q4, p4;
  STAMP_AT(0);
  asm volatile("" :: "s"(x), "s"(wqkv), "s"(wo), "s"(w1), "s"(b1), "s"(w2), "s"(b2), "s"(out), "s"(ws));

  const v4u sx = make_srd(x, SEQ * DM * 2), sws = make_srd(ws, WS_END);
  const __amdgpu_buffer_rsrc_t rws = __builtin_amdgcn_make_buffer_rsrc(ws, 0, WS_END, 0x00020000);
  const unsigned one2 = 0x3f803f80u;
  const int T = 4 * r + m;
  const int rot = ROT;
  auto flag_off = [&](int kind, int tile, int jj) { return WS_FLAG + ((kind * 64 + tile) * 16 + jj) * 128; };
  auto flag = [&](int kind, int tile, int jj) { return (GAS u64*)(ws + flag_off(kind, tile, jj)); };
  u64 tag;
  unsigned taglo, tag32;
  auto get_tag = [&]() {
    tag = load_s(ws + WS_EPOCH);
    taglo = (unsigned)tag * 0x9E3779B1u + 0x7F4A7C15u; tag32 = (unsigned)(tag >> 32) * 0x85EBCA77u + 0x165667B1u;
    tag = ((u64)tag32 << 32) | taglo;
  };
  v4u xres, bres, xaug, st;
  float mean, var, rstd, sd;
  v4u sel;
  unsigned xo[4], swz;
  auto lane_setup = [&]() {
    lane = threadIdx.x & 63; PIN(lane);
    r32 = lane & 31; h = lane >> 5; g = lane >> 4; q4 = (lane >> 2) & 3; p4 = lane & 3;
    const unsigned selv = ((lane & 15) == (g & 1)) ? one2 : 0u;
    sel = (v4u){selv, selv, selv, selv};
#pragma unroll
    for (int p = 0; p < 4; ++p) xo[p] = (32 * m + r32) * 128 + (((2 * ((p + m) & 3) + h) ^ ((r32 >> 1) & 7)) * 16);
    swz = ((((lane >> 2) & 1) ^ (g & 1)) * 64) + p4 * 16;
  };

  auto stat_load = [&]() { st = ld16_sc1_async(sws, WS_STATS + (128 * r + 32 * m + r32) * 16, 0); };
  auto res_loads = [&]() {
    int t2 = threadIdx.x; PIN(t2);
    const int row = 64 * rt + (t2 >> 3), col = 64 * ct + (t2 & 7) * 8;
    xres = ld16_async(sx, row * 1024 + col * 2, 0);
    bres = ld16_async(make_srd(b2, DM * 2), col * 2, 0);
  };
  auto row_stats = [&]() {
    if (!__all(st[1] == tag32 && st[3] == tag32)) {
#pragma unroll 1
      for (unsigned spin = 0; spin < SPIN_CAP; ++spin) {
        st = ld16_sc1_async(sws, WS_STATS + (128 * r + 32 * m + r32) * 16, 0);
        wait_vm1<0>(st);
        if (__all(st[1] == tag32 && st[3] == tag32)) break;
      }
    }
    mean = asf(st[0]); var = asf(st[2]);
    PIN(mean); PIN(var);
  };
  auto make_xaug = [&]() {
    PIN(mean); PIN(var);
    rstd = __builtin_amdgcn_rsqf(var); sd = var * rstd;
    const unsigned hi = pk2(-mean, sd);
    const unsigned lo = pk2(-mean - bflo(hi), FMA1(bfhi(hi), -1.f, sd));
    xaug = (v4u){(hi & 0xffffu) | (hi << 16), (lo & 0xffffu) | (hi & 0xffff0000u), lo >> 16, 0u};
    if (h) xaug = (v4u){0u, 0u, 0u, 0u};
    PIN(xaug);
  };
  auto cswr = [&](int tile, f32x4 c) {
    if (lane < 16) { LAS float* p = (LAS float*)(size_t)(lds0 + L_CS + (tile * 32 + lane) * 16 + m * 4); p[0] = c[0]; p[64] = c[1]; }
  };
  auto csrd = [&](int tile) { return __builtin_bit_cast(f32x4, lds128(lds0 + L_CS + (tile * 32 + r32) * 16)); };
  auto waug = [&](f32x4 c4, unsigned b16) {
    const float c = ((c4[0] + c4[1]) + c4[2]) + c4[3];
    const unsigned hi = pk2(c, 0.f);
    const unsigned lo = pk2(c - bflo(hi), 0.f);
    return (v4u){hi | (lo << 16), hi | (b16 << 16), b16, 0u};
  };

  const v4u sw1 = make_srd(w1, DM * FF * 2);
  const int hcol0 = 448 * (j >> 2) + 128 * (j & 3);
  const bool heavy = (j & 3) != 3;
  unsigned bias0, bias1;
  auto bias_load = [&](int P) {
    bias0 = ld2_async(make_srd(b1, FF * 2), r32 * 2, (hcol0 + 64 * P) * 2);
    bias1 = ld2_async(make_srd(b1, FF * 2), r32 * 2, (hcol0 + 64 * P + 32) * 2);
  };
  auto w1dma = [&](int P, int c, unsigned dst, int half) {
    const unsigned vw1 = (lane >> 3) * (FF * 2) + swz, vw1b = vw1 + 8 * (FF * 2) - 1024;
    const unsigned so = (64 * ((c + rot) & 7) + 16 * m) * (FF * 2) + (hcol0 + 64 * P) * 2;
    if (half == 0) dma16_a(sw1, vw1, so, dst + m * 2048); else dma16_b(sw1, vw1b, so);
  };
  f32x16 hac[2];
  unsigned rk[4][2];
  auto hsetup = [&](int P) {
#pragma unroll
    for (int p = 0; p < 4; ++p)
#pragma unroll
      for (int c = 0; c < 2; ++c) {
        rk[p][c] = lds0 + (P ? L_WA : 28672) + ((p + m) & 3) * 2048 + h * 1024 + q4 * 128 + ((c ^ (q4 >> 1)) * 64) + (g & 1) * 32 + p4 * 8;
        PIN(rk[p][c]);
      }
  };
  auto hmm = [&](auto pc, const v4u (&xf)[32], auto&& hook) {
    constexpr int P = pc.value;
    constexpr int D = 4;
    const unsigned cnt = lds0 + L_CNT;
    f32x4 cs[2] = {}, c0, c1;
    v4u wf[D][2], au0, au1;
    unsigned seen = 0;
    hac[0] = (f32x16){}; hac[1] = (f32x16){};
    auto rd = [&](int ks, int t) { wf[ks % D][t] = ldtr2(rk[ks & 3][t] + (ks >> 2) * 8192, 512); };
#pragma unroll
    for (int ks = 0; ks < D; ++ks) { rd(ks, 0); rd(ks, 1); }
    SBAR();
    static_for<0, 32>([&](auto kc) {
      constexpr int ks = kc.value;
      hook(kc);
      if constexpr (ks == 10) seen = peek(cnt + 20);
      if constexpr (ks == 29) seen = peek(cnt);
#pragma unroll
      for (int t = 0; t < 2; ++t) {
        hac[t] = MFMA32(wf[ks % D][t], xf[ks], hac[t]);
        if ((ks & 3) == 0 && ks < 28) { cs[t] = MFMA16(sel, wf[ks % D][t], cs[t]); PIN(cs[t]); }
        if (ks == 26) { cs[t] = MFMA16(sel, wf[28 % D][t], cs[t]); PIN(cs[t]); }
        SBAR();
        if constexpr (ks + D < 32) rd(ks + D, t);
        if constexpr (ks == 31) { if (t == 0) { au0 = waug(c0, bias0); PIN(au0); } else { au1 = waug(c1, bias1); PIN(au1); } }
        SBAR();
      }
      if constexpr (ks == 11 || ks == 30) {
        PIN(hac[0]); PIN(hac[1]);
        if (__builtin_amdgcn_readfirstlane(seen) < 4) await(ks == 11 ? cnt + 20 : cnt, 4);
        asm volatile("" ::: "memory");
        if constexpr (ks == 30) { c0 = csrd(2 * P); c1 = csrd(2 * P + 1); }
      }
      if constexpr (ks == 27) { cswr(2 * P, cs[0]); cswr(2 * P + 1, cs[1]); arrive_nowait(cnt); }
      SBAR();
    });
    hac[0] = MFMA32(au0, xaug, hac[0]);
    hac[1] = MFMA32(au1, xaug, hac[1]);
    SBAR();
  };
  auto hgelu = [&](auto pc, auto&& mid) {
    constexpr int P = pc.value;
    constexpr bool PK = GELU_PK(P);
    const float C0 = 2.3022081986f, C1 = C0 * 0.044715f;
    const float ga = -C1 * rstd * rstd * rstd, gb = -C0 * rstd;
    auto mul = [&](f32x2 a, f32x2 b) { return PK ? a * b : (f32x2){MUL1(a[0], b[0]), MUL1(a[1], b[1])}; };
    auto fma = [&](f32x2 a, float b, float c) {
      return PK ? __builtin_elementwise_fma(a, (f32x2){b, b}, (f32x2){c, c}) : (f32x2){FMA1(a[0], b, c), FMA1(a[1], b, c)}; };
#pragma unroll
    for (int t = 0; t < 2; ++t) {
      f32x2 v[8], z[8];
#pragma unroll
      for (int e = 0; e < 8; ++e) { v[e] = (f32x2){hac[t][2 * e], hac[t][2 * e + 1]}; z[e] = mul(v[e], v[e]); }
      SBAR();
#pragma unroll
      for (int e = 0; e < 8; ++e) z[e] = fma(z[e], ga, gb);
      SBAR();
#pragma unroll
      for (int e = 0; e < 8; ++e) z[e] = mul(z[e], v[e]);
      SBAR();
#pragma unroll
      for (int e = 0; e < 8; ++e) z[e] = (f32x2){EXP2(z[e][0]), EXP2(z[e][1])};
      SBAR();
#pragma unroll
      for (int e = 0; e < 8; ++e) z[e] = fma(z[e], sd, sd);
      SBAR();
#pragma unroll
      for (int e = 0; e < 8; ++e) z[e] = (f32x2){__builtin_amdgcn_rcpf(z[e][0]), __builtin_amdgcn_rcpf(z[e][1])};
      SBAR();
#pragma unroll
      for (int e = 0; e < 8; ++e) z[e] = mul(z[e], v[e]);
      SBAR();
#pragma unroll
      for (int s = 0; s < 2; ++s) {
        const v4u f = {pk2(z[4 * s][0], z[4 * s][1]), pk2(z[4 * s + 1][0], z[4 * s + 1][1]), pk2(z[4 * s + 2][0], z[4 * s + 2][1]), pk2(z[4 * s + 3][0], z[4 * s + 3][1])};
        st16_sc1(f, sws, lane * 16, WS_AF + (T * AFT + (hcol0 >> 4) + 4 * P + 2 * t + s) * 1024);
      }
      if (t == 0) mid();
      SBAR();
    }
  };

  if (isG) {
    lane_setup();
    const v4u sw2 = make_srd(w2, FF * DM * 2), swo = make_srd(wo, DM * DM * 2);
    const unsigned vw2 = (lane >> 3) * 1024 + swz, vw2b = vw2 + 7 * 1024;
    const int ro3 = rt % 3;
    auto kmap = [&](int kk) {
      if (kk < 16) return 8 * (((kk >> 2) + rt) & 3) + (kk & 3);
      const int p = ((kk - 16) >> 2) + ro3;
      return 8 * (p >= 3 ? p - 3 : p) + 4 + (kk & 3);
    };
    auto omap = [&](int i) { return 2 * ((i + rt) & 3) + (i >> 2); };
    auto wdmah = [&](int kk, int half) {
      const unsigned so = (kk < 28 ? 448 * m + 16 * kmap(kk) : 128 * m + 16 * omap(kk - 28)) * 1024 + 128 * ct;
      if (half == 0) dma16_a(kk < 28 ? sw2 : swo, vw2, so, lds0 + L_WG + m * PART + (kk % RD) * 2048); else dma16_b(kk < 28 ? sw2 : swo, vw2b, so);
    };
    auto wdma = [&](int kk) { wdmah(kk, 0); wdmah(kk, 1); };
    {
      const v4u swq = make_srd(wqkv, DM * 3 * DM * 2);
      const unsigned vx0 = (lane >> 3) * 1024 + (((lane & 7) ^ g) * 16), vx1 = (7 + (lane >> 3)) * 1024 + (((lane & 7) ^ (4 + g)) * 16);
      const unsigned vwq = (lane >> 2) * (3 * DM * 2) + p4 * 16;
      auto piece = [&](int s, int i) {
        const unsigned buf = lds0 + (s % 3) * STG;
        const int kc = (s + rot) & 7;
        if (i < 4) {
          const unsigned so = (128 * r + 32 * m + 16 * (i >> 1)) * 1024 + kc * 128;
          if (i & 1) dma16_b(sx, vx1, so); else dma16_a(sx, vx0, so, buf + m * 4096 + (i >> 1) * 2048);
        } else if (i < 7) dma16(swq, vwq, (64 * kc + 16 * m) * (3 * DM * 2) + (512 * (i - 4) + 32 * j) * 2, buf + 16384 + ((i - 4) * 4 + m) * 1024);
        else w1dma(0, s, buf + 28672, i - 7);
      };
#pragma unroll
      for (int i = 0; i < 18; ++i) { piece(i / 9, i % 9); if (i == 8) __builtin_amdgcn_s_barrier(); }
      __builtin_amdgcn_s_setprio(PRIO_G1);
      hsetup(0);
      f32x4 cs[2] = {};
      v4u xf[4], wf[4][2];
      hac[0] = (f32x16){}; hac[1] = (f32x16){};
      static_for<0, 8>([&](auto sc) {
        constexpr int s = sc.value;
        constexpr unsigned buf = (s % 3) * STG;
        auto fill = [&](int i) {
          if constexpr (s < 6) piece(s + 2, i);
          if constexpr (s == 6) { if (i == 0) { stat_load(); bias_load(0); } else wdmah((i - 1) >> 1, (i - 1) & 1); }
          if constexpr (s == 7) { if (heavy) { w1dma(1, i, lds0 + L_WA + i * 8192, 0); w1dma(1, i, 0, 1); } }
          SBAR();
        };
        auto rd = [&](int p) { xf[p] = lds128(buf + xo[p]); wf[p][0] = ldtr2(buf + rk[p][0], 512); wf[p][1] = ldtr2(buf + rk[p][1], 512); SBAR(); };
        if constexpr (s < 7) wait_vm<9>(); else { wait_vm1<8>(st); PIN(bias0); PIN(bias1); }
        bar();
        STAMP_AT(10 + s);
        rd(0);
        if constexpr (s < 7) fill(0);
        rd(1);
        if constexpr (s < 7) fill(1);
        if constexpr (s == 1) {
          const v2u tg = *(const LAS v2u*)(size_t)(lds0 + L_CNT + 32);
          taglo = __builtin_amdgcn_readfirstlane(tg[0]); tag32 = __builtin_amdgcn_readfirstlane(tg[1]);
          tag = ((u64)tag32 << 32) | taglo;
        }
#pragma unroll
        for (int p = 0; p < 4; ++p) {
#pragma unroll
          for (int t = 0; t < 2; ++t) {
            hac[t] = MFMA32(wf[p][t], xf[p], hac[t]);
            if (p == 0) { cs[t] = MFMA16(sel, wf[p][t], cs[t]); PIN(cs[t]); }
            SBAR();
            if (p < 2 && t == 0) rd(p + 2);
            if (s == 7) fill(2 * p + t); else if (2 * p + t < 7) fill(2 + 2 * p + t);
          }
          if (s == 7 && p == 1) {
            cswr(0, cs[0]); cswr(1, cs[1]);
            bar();
            STAMP_AT(1);
          }
        }
        if constexpr (s == 7) {
          const f32x4 c0 = csrd(0), c1 = csrd(1);
          row_stats(); make_xaug();
          hac[0] = MFMA32(waug(c0, bias0), xaug, hac[0]);
          hac[1] = MFMA32(waug(c1, bias1), xaug, hac[1]);
          SBAR();
        }
      });
    }
    STAMP_AT(18);
    if (heavy) { wait_vm<8>(); arrive_nowait(lds0 + L_CNT + 16); }
    hgelu(std::integral_constant<int, 0>{}, [&]() {
      wait_vm<2>();
      if (heavy) arrive_nowait(lds0 + L_CNT + 20);
    });
    __builtin_amdgcn_s_setprio(0);
    STAMP_AT(19);
    unsigned rb2[2];
#pragma unroll
    for (int c = 0; c < 2; ++c) rb2[c] = lds0 + L_WG + m * PART + h * 512 + q4 * 128 + ((c ^ (q4 >> 1)) * 64) + (g & 1) * 32 + p4 * 8;
    v4u wq[36][2];
    auto wrd = [&](int kk) {
#pragma unroll
      for (int c = 0; c < 2; ++c) wq[kk][c] = ldtr2(rb2[c] + (kk % RD) * 2048, 1024);
    };
    auto wgot = [&](int kk) { PIN(wq[kk][0]); PIN(wq[kk][1]); };
    const int ftile = 2 * rt + ((lane >> 2) & 1);
    v4u af[28][2], of[8][2];
    v2u fl;
    const unsigned a0 = WS_AF + (2 * rt * AFT + 28 * m) * 1024, o0 = WS_AF + (2 * rt * AFT + 112 + 8 * m) * 1024;
    for (int kk = 0; kk < 4; ++kk) wrd(kk);
    for (int kk = 4; kk < 8; ++kk) wdma(kk);
    for (int kk = 0; kk < 4; ++kk) wgot(kk);
    wait_vm<8>();
    if (lane == 0) __hip_atomic_store(flag(FL_H0, T, j), tag, RLX_AGENT);
    STAMP_AT(2);
    for (int kk = 8; kk < 12; ++kk) wdma(kk);
    static_for<1, 3>([&](auto ic) {
      constexpr int i = ic.value;
      wait_vm<(i == 1) ? 9 : 8>();
      for (int kk = 4 * i; kk < 4 * i + 4; ++kk) wrd(kk);
      for (int kk = 4 * i; kk < 4 * i + 4; ++kk) wgot(kk);
      if constexpr (i == 2) fl = ld8_sc1_async(sws, flag_off(FL_H0, ftile, 4 * m + p4));
      for (int kk = 4 * i; kk < 4 * i + 4; ++kk) wdma(kk + RD);
    });
    STAMP_AT(3);
    wait_vmf<8>(fl);
    if (!__all(fl[0] == taglo && fl[1] == tag32)) poll_flags(flag(FL_H0, ftile, 4 * m + p4), tag);
    STAMP_AT(4);
#pragma unroll
    for (int kk = 0; kk < AW; ++kk) {
      af[kk][0] = ld16_sc1_async(sws, lane * 16, a0 + kmap(kk) * 1024);
      af[kk][1] = ld16_sc1_async(sws, lane * 16, a0 + (AFT + kmap(kk)) * 1024);
    }
    __builtin_amdgcn_s_setprio(PRIO_G2);
    f32x16 c2[2][2] = {};
    auto mm2 = [&](int kk, const v4u (&a)[2], auto&& fill) {
      SBAR();
#pragma unroll
      for (int i = 0; i < 4; ++i) { c2[i >> 1][i & 1] = MFMA32(wq[kk][i & 1], a[i >> 1], c2[i >> 1][i & 1]); SBAR(); fill(i); SBAR(); }
    };
    static_for<0, 28>([&](auto kc) {
      constexpr int kk = kc.value;
      wait_vm2<(kk < AW) ? 2 * (AW - 1 - kk) + g2_range(0, kk) : g2_range(kk - AW + 1, kk)>(af[kk][0], af[kk][1]);
      if constexpr (kk == H1_CK) {
        STAMP_AT(20);
        wait_vmf<g2_range(H1_LD, H1_CK) - 1>(fl);
        if (!__all(fl[0] == taglo && fl[1] == tag32)) poll_flags(flag(FL_H1, ftile, 4 * m + p4), tag);
        STAMP_AT(21);
      }
      if constexpr (kk == H1_LD) fl = ld8_sc1_async(sws, flag_off(FL_H1, ftile, 4 * m + p4));
      if constexpr (kk == O_LD) fl = ld8_sc1_async(sws, flag_off(FL_O0, ftile, 4 * m + p4));
      constexpr bool dw = kk >= 1 && kk - 1 + NR + RD < 36;
      if constexpr (dw) wgot(kk - 1 + NR);
      mm2(kk, af[kk], [&](int i) {
        if constexpr (dw) { if (i < 2) wdmah(kk - 1 + NR + RD, i); }
        if constexpr (kk + NR < 28) { if (i == 1) wrd(kk + NR); }
        if constexpr (kk == 27) { if (i == 0) wrd(28); }
        if constexpr (kk + AW < 28) { if (i >= 2) af[kk + AW][i - 2] = ld16_sc1_async(sws, lane * 16, a0 + (AFT * (i - 2) + kmap(kk + AW)) * 1024); }
      });
      if constexpr (kk == O_CK) {
        wait_vmf<g2_range(O_LD, O_CK) - 1>(fl);
        if (!__all(fl[0] == taglo && fl[1] == tag32)) poll_flags(flag(FL_O0, ftile, 4 * m + p4), tag);
        res_loads();
#pragma unroll
        for (int i = 0; i < 4; ++i) {
          of[i][0] = ld16_sc1_async(sws, lane * 16, o0 + omap(i) * 1024);
          of[i][1] = ld16_sc1_async(sws, lane * 16, o0 + (AFT + omap(i)) * 1024);
        }
      }
    });
    static_for<0, 8>([&](auto kc) {
      constexpr int kk = kc.value;
      if (kk < 7) wrd(28 + kk + 1);
      if constexpr (kk == 1) {
        STAMP_AT(5);
        poll_flags(flag(FL_O1, ftile, 4 * m + p4), tag);
        STAMP_AT(6);
#pragma unroll
        for (int i = 4; i < 8; ++i) {
          of[i][0] = ld16_sc1_async(sws, lane * 16, o0 + omap(i) * 1024);
          of[i][1] = ld16_sc1_async(sws, lane * 16, o0 + (AFT + omap(i)) * 1024);
        }
      }
      wait_vm2<(kk == 0) ? 6 : 14 - 2 * kk>(of[kk][0], of[kk][1]);
      mm2(28 + kk, of[kk], [&](int) {});
    });
    PIN(xres); PIN(bres);
    asm volatile("" ::: "memory");
#pragma unroll
    for (int mt = 0; mt < 2; ++mt)
#pragma unroll
      for (int c = 0; c < 2; ++c)
#pragma unroll
        for (int qd = 0; qd < 4; ++qd) {
          const f32x4 v = {c2[mt][c][4 * qd], c2[mt][c][4 * qd + 1], c2[mt][c][4 * qd + 2], c2[mt][c][4 * qd + 3]};
          sts128(lds0 + L_WG + m * PART + ((mt * 8 + c * 4 + qd) * 66 + lane) * 16, __builtin_bit_cast(v4u, v));
        }
    STAMP_AT(7);
  } else {
    __builtin_amdgcn_s_setprio(1);
    lane_setup();
    v4u xr[2];
    xr[0] = ld16_async(sx, lane * 16, (128 * r + 8 * j + 2 * m) * 1024); xr[1] = ld16_async(sx, lane * 16, (128 * r + 8 * j + 2 * m + 1) * 1024);
    unsigned warm;
    {
      u64 pc; asm volatile("s_getpc_b64 %0" : "=s"(pc));
      warm = ld2_async(make_srd((const void*)(pc & ~127ull), CODEWARM), lane < 3 ? lane * 128 : 0x40000000u, (idx * 4 + m) * 384);
    }
    __builtin_amdgcn_s_barrier();
    get_tag();
    if (m == 0 && lane < 10) *(LAS unsigned*)(size_t)(lds0 + L_CNT + lane * 4) = lane == 8 ? taglo : lane == 9 ? tag32 : 0u;
    auto make_stats = [&]() {
      wait_vm2<0>(xr[0], xr[1]);
      PIN(warm);
      float piv[2], a[4];
#pragma unroll
      for (int u = 0; u < 2; ++u) {
        piv[u] = rdl(bflo(xr[u][0]), 0);
        float s1 = 0.f, s2 = 0.f;
#pragma unroll
        for (int i = 0; i < 8; ++i) { const float d = ((i & 1) ? bfhi(xr[u][i >> 1]) : bflo(xr[u][i >> 1])) - piv[u]; s1 += d; s2 = __builtin_fmaf(d, d, s2); }
        a[2 * u] = s1; a[2 * u + 1] = s2;
      }
#pragma unroll
      for (int i = 0; i < 4; ++i) a[i] += dpp<0xB1>(a[i]);
#pragma unroll
      for (int i = 0; i < 4; ++i) a[i] += dpp<0x4E>(a[i]);
      float t = p4 == 0 ? a[0] : p4 == 1 ? a[1] : p4 == 2 ? a[2] : a[3];
      t += dpp<0x114>(t); t += dpp<0x118>(t);
      const auto s16 = __builtin_amdgcn_permlane16_swap(asu(t), asu(t), false, false);
      t = asf(s16[0]) + asf(s16[1]);
      const auto s32 = __builtin_amdgcn_permlane32_swap(asu(t), asu(t), false, false);
      t = (asf(s32[0]) + asf(s32[1])) * (1.f / DM);
      const float e1 = dpp<0xA0>(t);
      const float val = (p4 & 1) ? __builtin_fmaxf(__builtin_fmaf(-e1, e1, t), 0.f) + 1e-5f : t + (p4 ? piv[1] : piv[0]);
      if ((lane >> 2) == 3) {
        const v2u gr = {asu(val), tag32};
        __builtin_amdgcn_raw_buffer_store_b64(gr, rws, WS_STATS + (128 * r + 8 * j + 2 * m) * 16 + p4 * 8, 0, 16);
      }
    };
    const unsigned acnt = lds0 + L_CNT;
    const unsigned vpos = 2048 + (g & 1) * 512 + h * 256 + (lane & 15) * 16;
    v4u qf[2], am0, am1;
    {
      v4u xf[32];
      f32x16 acc[3] = {};
      f32x4 cs[3] = {};
      v4u wf[4][3];
      unsigned wb[4];
#pragma unroll
      for (int p = 0; p < 4; ++p) wb[p] = 16384 + ((p + m) & 3) * 1024 + (8 * h + q4) * 64 + (g & 1) * 32 + p4 * 8;
      auto rd = [&](int ks, int t) {
        const unsigned buf = lds0 + ((ks >> 2) % 3) * STG;
        if (t == 0) xf[ks] = lds128(buf + xo[ks & 3]);
        wf[ks & 3][t] = ldtr2(buf + wb[ks & 3] + t * 4096, 256);
      };
      auto mm = [&](int ks, int t) {
        acc[t] = t == 2 ? MFMA32(xf[ks], wf[ks & 3][t], acc[t]) : MFMA32(wf[ks & 3][t], xf[ks], acc[t]);
        if ((ks & 3) == 0) { cs[t] = MFMA16(sel, wf[ks & 3][t], cs[t]); PIN(cs[t]); }
        SBAR();
      };
      make_stats();
      bar();
      STAMP_AT(10);
#pragma unroll
      for (int t = 0; t < 6; ++t) rd(t / 3, t % 3);
      SBAR();
      static_for<0, 8>([&](auto sc) {
        constexpr int s = sc.value;
#pragma unroll
        for (int p = 0; p < 2; ++p) {
#pragma unroll
          for (int t = 0; t < 3; ++t) { mm(4 * s + p, t); rd(4 * s + p + 2, t); SBAR(); }
          if (s == 7 && p == 0) { cswr(4, cs[0]); cswr(5, cs[1]); cswr(6, cs[2]); SBAR(); }
        }
        if constexpr (s == 5) stat_load();
        if constexpr (s == 6) {
          wait_vm1<0>(st);
          row_stats();
        }
        mm(4 * s + 2, 0); mm(4 * s + 2, 1);
        bar();
        if constexpr (s < 7) {
          STAMP_AT(11 + s);
          mm(4 * s + 2, 2);
          rd(4 * s + 4, 0); rd(4 * s + 4, 1); rd(4 * s + 4, 2); SBAR();
#pragma unroll
          for (int t = 0; t < 3; ++t) { mm(4 * s + 3, t); rd(4 * s + 5, t); SBAR(); }
        }
      });
      STAMP_AT(1);
      __builtin_amdgcn_s_setprio(3);
      const f32x4 c5 = csrd(5), c6 = csrd(6), c4 = csrd(4);
      SBAR();
      mm(30, 2); mm(31, 1);
      make_xaug();
      SBAR();
      mm(31, 2); mm(31, 0);
      if (h == 0) *(LAS float*)(size_t)(lds0 + L_RS + (m * 32 + r32) * 4) = rstd;
      acc[1] = MFMA32(waug(c5, 0u), xaug, acc[1]);
      acc[2] = MFMA32(xaug, waug(c6, 0u), acc[2]);
      acc[0] = MFMA32(waug(c4, 0u), xaug, acc[0]);
      SBAR();
      f32x4 rs[4];
#pragma unroll
      for (int qd = 0; qd < 4; ++qd) rs[qd] = __builtin_bit_cast(f32x4, lds128(lds0 + L_RS + (m * 32 + 8 * qd + 4 * h) * 4));
      const unsigned kvo = WS_KV + (j * 64 + T) * 4096;
#pragma unroll
      for (int e = 0; e < 16; ++e) acc[1][e] = MUL1(acc[1][e], rstd);
#pragma unroll
      for (int e = 0; e < 2; ++e) {
        const v4u f = pack8(acc[1], e);
        st16_sc1(f, sws, lane * 16, kvo + e * 1024);
        sts128(lds0 + L_KV + (8 + m) * KVT + e * 1024 + lane * 16, f);
      }
#pragma unroll
      for (int qd = 0; qd < 4; ++qd)
#pragma unroll
        for (int e = 0; e < 4; ++e) acc[2][4 * qd + e] = MUL1(acc[2][4 * qd + e], rs[qd][e]);
#pragma unroll
      for (int s = 0; s < 2; ++s) {
        const v4u f = pack8(acc[2], s);
        st16_sc1(f, sws, lane * 16, kvo + 2048 + s * 1024);
        sts128(lds0 + L_KV + (8 + m) * KVT + s * VB + vpos, f);
      }
      SBAR();
      const float qs = rstd * (0.25f * 1.4426950408889634f);
#pragma unroll
      for (int e = 0; e < 16; ++e) acc[0][e] = MUL1(acc[0][e], qs);
      qf[0] = pack8(acc[0], 0); qf[1] = pack8(acc[0], 1);
      PIN(qf[0]); PIN(qf[1]);
      STAMP_AT(2);
      const int T0 = 4 * r - 8 + 2 * m;
      const bool live = T0 >= 0;
      auto kv_load = [&](v4u (&kv)[8], int i0, int i1) {
        const unsigned src = WS_KV + (j * 64 + (live ? T0 : 0)) * 4096, vo = live ? lane * 16 : h * 512;
#pragma unroll
        for (int i = i0; i < i1; ++i) kv[i] = ld16_sc1_async(sws, vo, src + (live ? i : (i & 3)) * 1024);
      };
      auto kv_stage = [&](v4u (&kv)[8]) {
        const unsigned on = (lane < 2 || live) ? one2 : 0u;
        if (lane < 6) sts128(lds0 + L_KV + (lane < 2 ? 8 + m : 2 * m + ((lane - 2) >> 1)) * KVT + 2048 + (lane & 1) * VB + 1024, (v4u){on, on, on, on});
        const v4u zz = {};
#pragma unroll
        for (int i = 0; i < 2; ++i) {
          sts128(lds0 + L_KV + (2 * m + i) * KVT + lane * 16, kv[4 * i]);
          sts128(lds0 + L_KV + (2 * m + i) * KVT + 1024 + lane * 16, kv[4 * i + 1]);
          sts128(lds0 + L_KV + (2 * m + i) * KVT + vpos, live ? kv[4 * i + 2] : zz);
          sts128(lds0 + L_KV + (2 * m + i) * KVT + VB + vpos, live ? kv[4 * i + 3] : zz);
        }
        arrive_nowait(acnt);
      };
      auto kv_wait = [&](v4u (&kv)[8]) {
        wait_vm<0>();
#pragma unroll
        for (int i = 0; i < 8; ++i) PIN(kv[i]);
      };
      if (heavy) {
        bias_load(1);
        hsetup(1);
        await(acnt + 16, 4);
        STAMP_AT(3);
        v2u fl;
        v4u kv[8];
        hmm(std::integral_constant<int, 1>{}, xf, [&](auto kc) {
          constexpr int ks = kc.value;
          if constexpr (ks == KVPUB) {
            wait_vm<0>();
            PIN(bias0); PIN(bias1);
            if (lane == 0) __hip_atomic_store(flag(FL_KV, T, j), tag, RLX_AGENT);
          }
          if constexpr (ks == 24) fl = ld8_sc1_async(sws, flag_off(FL_KV, live ? T0 + (lane & 1) : 0, j));
        });
        STAMP_AT(18);
        wait_vmf<0>(fl);
        if (!__all(fl[0] == taglo && fl[1] == tag32)) poll_flags(flag(FL_KV, live ? T0 + (lane & 1) : 0, j), tag);
        kv_load(kv, 0, 8);
        hgelu(std::integral_constant<int, 1>{}, [&]() {});
        STAMP_AT(4);
        wait_vm<4>();
#pragma unroll
        for (int i = 0; i < 8; ++i) PIN(kv[i]);
        kv_stage(kv);
      } else {
        wait_vm<0>();
        if (lane == 0) __hip_atomic_store(flag(FL_KV, T, j), tag, RLX_AGENT);
        v4u kv[8];
        poll_flags(flag(FL_KV, live ? T0 + (lane & 1) : 0, j), tag);
        kv_load(kv, 0, 8); kv_wait(kv); kv_stage(kv);
      }
    }
    STAMP_AT(19);
    const unsigned kb = lds0 + L_KV + m * KVT + lane * 16;
    unsigned vb[2];
#pragma unroll
    for (int e = 0; e < 2; ++e) { vb[e] = lds0 + L_KV + m * KVT + 2048 + ((g & 1) ? 1024 : e * 512 + h * 256 + (lane & 15) * 16); PIN(vb[e]); }
    {
      const int dq = r32 - 4 * h;
#pragma unroll
      for (int d = 0; d < 8; ++d) { const int row = 2 * (d & 1) + 8 * (d >> 1); (d < 4 ? am0 : am1)[d & 3] = row > dq ? 0xffffffffu : row == dq ? 0xffff0000u : 0u; }
      PIN(am0); PIN(am1);
    }
    await(acnt, heavy ? 8 : 4);
    __builtin_amdgcn_s_setprio(PRIO_ATT);
    STAMP_AT(5);
    const f32x16 zero = {};
    auto o_store = [&](f32x16& o, int e) {
      const float rl = __builtin_amdgcn_rcpf(o[8]);
#pragma unroll
      for (int u = 0; u < 8; ++u) o[u] = MUL1(o[u], rl);
      st16_sc1(pack8(o, 0), sws, lane * 16, WS_AF + (T * AFT + 112 + 2 * j + e) * 1024);
    };
    int redo = 0;
    {
      f32x16 o[2], sa, sb, su;
      bool pend = false;
      v4u kn, ku, va, vc, vn0, vn1, vu0, vu1, p0, p1, pu;
      auto kaddr = [&](int n) { return kb + ((n & 7) + 1) * KVT + (n >> 3) * 1024; };
      auto vaddr = [&](int n) { return vb[n >> 3] + ((n & 7) + 1) * KVT; };
      kn = lds128(kaddr(0)); vn0 = lds128(vaddr(0)); vn1 = lds128(vaddr(0) + VB);
      sa = MFMA32(kn, qf[0], zero);
      kn = lds128(kaddr(1));
      SBAR();
      auto step = [&](auto nc, f32x16& sc, f32x16& sn) {
        constexpr int n = nc.value, e = n >> 3, i = n & 7;
        if constexpr (n < 15) sn = MFMA32(kn, qf[(n + 1) >> 3], zero);
        if constexpr (n < 14) kn = lds128(kaddr(n + 2));
        if constexpr (i == 5) ku = lds128(kb + e * 1024);
        v4u v0 = vn0, v1 = vn1;
        if constexpr (n < 15) { vn0 = lds128(vaddr(n + 1)); vn1 = lds128(vaddr(n + 1) + VB); }
        if constexpr (i == 6) { vu0 = lds128(vb[e]); vu1 = lds128(vb[e] + VB); }
        SBAR();
        if constexpr (i == 7) {
          static_for<0, 16>([&](auto u) { sc[u.value] = upper<u.value>() ? su[u.value] : sc[u.value]; });
          PIN(sc); SBAR();
        }
#pragma unroll
        for (int u = 0; u < 6; ++u) sc[u] = EXP2(sc[u]);
        PIN(sc); SBAR();
        if constexpr (n == 8) { o[0] = MFMA32(vc, p1, o[0]); SBAR(); }
        else if constexpr (n > 0) { o[e] = MFMA32(vc, p1, o[e]); SBAR(); }
        sc[6] = EXP2(sc[6]); sc[7] = EXP2(sc[7]);
        p0 = pack8(sc, 0);
        sc[8] = EXP2(sc[8]); sc[9] = EXP2(sc[9]);
        if constexpr (i == 7) { va = p0 & am0; p0 &= ~am0; PIN(va); }
        PIN(sc); PIN(p0); SBAR();
        if constexpr (n == 8) { o[0] = MFMA32(vu1, pu, o[0]); SBAR(); }
        o[e] = MFMA32(v0, p0, i == 0 ? zero : o[e]);
        SBAR();
        if constexpr (i == 6) { su = MFMA32(ku, qf[e], zero); SBAR(); }
#pragma unroll
        for (int u = 10; u < 13; ++u) sc[u] = EXP2(sc[u]);
        if constexpr (i == 7) { PIN(sc); SBAR(); o[e] = MFMA32(vu0, va, o[e]); SBAR(); }
#pragma unroll
        for (int u = 13; u < 16; ++u) sc[u] = EXP2(sc[u]);
        p1 = pack8(sc, 1); vc = v1;
        if constexpr (i == 7) { pu = p1 & am1; p1 &= ~am1; PIN(pu); }
        PIN(p1); SBAR();
        if constexpr (n == 8) {
          if (__all(o[0][8] >= 0x1p-30f && o[0][8] <= 0x1p30f)) o_store(o[0], 0); else redo = 1;
          SBAR();
        }
        if constexpr (i == 2) pend = !redo;
        if constexpr (i >= 2) {
          if (pend && (i == 7 || (__builtin_amdgcn_s_getreg(7 | (3 << 11)) & 15) == 0)) {
            wait_vm<0>();
            if (lane == 0) __hip_atomic_store(flag(FL_H1 + e, T, j), tag, RLX_AGENT);
            pend = false;
          }
        }
      };
      static_for<0, 8>([&](auto hc) {
        step(std::integral_constant<int, 2 * hc.value>{}, sa, sb);
        step(std::integral_constant<int, 2 * hc.value + 1>{}, sb, sa);
      });
      o[1] = MFMA32(vc, p1, o[1]);
      o[1] = MFMA32(vu1, pu, o[1]);
      if (__all(o[1][8] >= 0x1p-30f && o[1][8] <= 0x1p30f)) o_store(o[1], 1); else redo |= 2;
    }
    if (redo) {
#pragma unroll 1
      for (int e = 0; e < 2; ++e) {
        if (!((redo >> e) & 1)) continue;
        const v4u q = e ? qf[1] : qf[0];
        const unsigned vbe = e ? vb[1] : vb[0];
        auto merged = [&](const f32x16& c) {
          f32x16 sl = MFMA32(lds128(kb + 8 * KVT + e * 1024), q, c);
          const f32x16 su = MFMA32(lds128(kb + e * 1024), q, c);
          static_for<0, 16>([&](auto u) { sl[u.value] = upper<u.value>() ? su[u.value] : sl[u.value]; });
          return sl;
        };
        float mx = -INFINITY;
#pragma unroll 1
        for (int i = 0; i < 8; ++i) {
          const f32x16 sc = i ? MFMA32(lds128(kb + i * KVT + e * 1024), q, zero) : merged(zero);
#pragma unroll
          for (int u = 0; u < 16; u += 2) mx = max3(mx, sc[u], sc[u + 1]);
        }
        const auto sw = __builtin_amdgcn_permlane32_swap(asu(mx), asu(mx), false, false);
        mx = __builtin_fmaxf(asf(sw[0]), asf(sw[1]));
        f32x16 negm, o;
#pragma unroll
        for (int u = 0; u < 16; ++u) negm[u] = -mx;
        {
          f32x16 sc = merged(negm);
#pragma unroll
          for (int u = 0; u < 16; ++u) sc[u] = EXP2(sc[u]);
          const v4u p0 = pack8(sc, 0), p1 = pack8(sc, 1);
          o = MFMA32(lds128(vbe), p0 & am0, zero);
          o = MFMA32(lds128(vbe + VB), p1 & am1, o);
          o = MFMA32(lds128(vbe + 8 * KVT), p0 & ~am0, o);
          o = MFMA32(lds128(vbe + 8 * KVT + VB), p1 & ~am1, o);
        }
#pragma unroll 1
        for (int i = 1; i < 8; ++i) {
          f32x16 sc = MFMA32(lds128(kb + i * KVT + e * 1024), q, negm);
#pragma unroll
          for (int u = 0; u < 16; ++u) sc[u] = EXP2(sc[u]);
          o = MFMA32(lds128(vbe + i * KVT), pack8(sc, 0), o);
          o = MFMA32(lds128(vbe + i * KVT + VB), pack8(sc, 1), o);
        }
        o_store(o, e);
      }
      wait_vm<0>();
      if (lane == 0) __hip_atomic_store(flag(FL_O0, T, j), tag, RLX_AGENT);
    }
    STAMP_AT(6);
    wait_vm<0>();
    if (lane == 0) __hip_atomic_store(flag(FL_O1, T, j), tag, RLX_AGENT);
    STAMP_AT(7);
    res_loads();
    wait_vm2<0>(xres, bres);
    if (blockIdx.x == 0 && wave == 7) {
#pragma unroll 1
      for (int i = 0; i < 4; ++i) poll_flags(flag(FL_KV, 4 * (4 * i + g), lane & 15), tag);
      if (lane == 0) __hip_atomic_store((GAS u64*)(ws + WS_EPOCH), tag, RLX_AGENT);
    }
  }
  bar();
  {
    int t2 = threadIdx.x; PIN(t2);
    const int row = t2 >> 3, ch = t2 & 7, orow = 64 * rt + row, ocol = 64 * ct + ch * 8;
    const unsigned a = lds0 + L_WG + (((row >> 5) * 8 + ch) * 66 + (row & 31)) * 16;
    f32x4 lo = {}, hi = {};
#pragma unroll
    for (int w = 0; w < 4; ++w) {
      lo += __builtin_bit_cast(f32x4, lds128(a + w * PART));
      hi += __builtin_bit_cast(f32x4, lds128(a + w * PART + 512));
    }
    v4u o;
    o[0] = pk2(lo[0] + bflo(xres[0]) + bflo(bres[0]), lo[1] + bfhi(xres[0]) + bfhi(bres[0]));
    o[1] = pk2(lo[2] + bflo(xres[1]) + bflo(bres[1]), lo[3] + bfhi(xres[1]) + bfhi(bres[1]));
    o[2] = pk2(hi[0] + bflo(xres[2]) + bflo(bres[2]), hi[1] + bfhi(xres[2]) + bfhi(bres[2]));
    o[3] = pk2(hi[2] + bflo(xres[3]) + bflo(bres[3]), hi[3] + bfhi(xres[3]) + bfhi(bres[3]));
    *(GAS v4u*)((char*)out + (size_t)orow * 1024 + ocol * 2) = o;
  }
  STAMP_AT(8);
}

extern "C" void kernel_launch(void* const* d_in, const int* in_sizes, int n_in, void* d_out, int out_size, void* d_ws, size_t ws_size,
                              hipStream_t stream) {
  (void)in_sizes; (void)n_in; (void)out_size; (void)ws_size;
  (void)hipFuncSetAttribute((const void*)fused_block, hipFuncAttributeMaxDynamicSharedMemorySize, LDS_BYTES);
  fused_block<<<256, 512, LDS_BYTES, stream>>>(d_in[0], d_in[1], d_in[2], d_in[4], d_in[5], d_in[6], d_in[7], d_out, (char*)d_ws + WSOFF);
}
```

```cpp
#include <hip/hip_runtime.h>
#include <hip/hip_bf16.h>
#include <stdint.h>
#include <type_traits>

typedef unsigned v4u __attribute__((ext_vector_type(4)));
typedef unsigned v2u __attribute__((ext_vector_type(2)));
typedef float f32x2 __attribute__((ext_vector_type(2)));
typedef float f32x4 __attribute__((ext_vector_type(4)));
typedef float f32x16 __attribute__((ext_vector_type(16)));
typedef short bf16x8 __attribute__((ext_vector_type(8)));
typedef short s16x4 __attribute__((ext_vector_type(4)));
typedef __bf16 bf16x2_t __attribute__((ext_vector_type(2)));
typedef unsigned long long u64;
#define GAS __attribute__((address_space(1)))
#define LAS __attribute__((address_space(3)))
#define DI __device__ __forceinline__
#define RLX_AGENT __ATOMIC_RELAXED, __HIP_MEMORY_SCOPE_AGENT

constexpr int SEQ = 2048, DM = 512, FF = 1792;
constexpr unsigned WS_EPOCH = 0;
constexpr unsigned WS_STATS = 32768;
constexpr unsigned WS_FLAG = 65536;
constexpr unsigned WS_KV = WS_FLAG + 5 * 1024 * 128;
constexpr int AFT = 145;
constexpr unsigned WS_AF = WS_KV + 16 * 64 * 4096;
constexpr unsigned WS_END = WS_AF + 64 * AFT * 1024;
constexpr unsigned WS_STAMP = WS_END;
enum { FL_KV = 0, FL_H0 = 1, FL_H1 = 2, FL_O0 = 3, FL_O1 = 4 };
constexpr int STG = 36864;
constexpr int L_WA = 0;
constexpr int L_WG = 86016;
constexpr int VB = 1040;
constexpr int KVT = 2048 + 2 * VB;
constexpr int L_KV = 65536 - 8 * KVT;
constexpr int RD = 8, NR = 12;
constexpr int PART = 16 * 66 * 16;
constexpr int L_CS = L_WG + 4 * PART;
constexpr int L_RS = L_CS + 3584;
constexpr int L_CNT = L_RS + 512;
constexpr int LDS_BYTES = L_CNT + 64;
static_assert(L_KV + 12 * KVT <= L_WG && L_WG >= 2 * STG && L_CS >= 3 * STG && PART >= RD * 2048 && LDS_BYTES <= 163840, "lds map");
#define AWIN 6
constexpr int AW = AWIN;
constexpr unsigned SPIN_CAP = 1u << 23;

#define MFMA32(a, b, c) __builtin_amdgcn_mfma_f32_32x32x16_bf16(__builtin_bit_cast(bf16x8, a), __builtin_bit_cast(bf16x8, b), c, 0, 0, 0)
#define MFMA16(a, b, c) __builtin_amdgcn_mfma_f32_16x16x32_bf16(__builtin_bit_cast(bf16x8, a), __builtin_bit_cast(bf16x8, b), c, 0, 0, 0)
#define SBAR() __builtin_amdgcn_sched_barrier(0)

#define STAMP_AT(i) do { } while (0)

DI float asf(unsigned u) { return __builtin_bit_cast(float, u); }
DI unsigned asu(float f) { return __builtin_bit_cast(unsigned, f); }
DI unsigned pk2(float lo, float hi) { return __builtin_bit_cast(unsigned, __builtin_convertvector((f32x2){lo, hi}, bf16x2_t)); }
DI float bflo(unsigned u) { return __builtin_bit_cast(float, u << 16); }
DI float bfhi(unsigned u) { return __builtin_bit_cast(float, u & 0xffff0000u); }
DI v4u make_srd(const void* p, unsigned bytes) { const u64 a = (u64)p; return (v4u){(unsigned)a, (unsigned)(a >> 32) & 0xffffu, bytes, 0x00020000u}; }
DI void dma16(v4u srd, unsigned voff, unsigned soff, unsigned lds) {
  asm volatile("s_mov_b32 m0, %3\n\ts_nop 0\n\tbuffer_load_dwordx4 %0, %1, %2 offen lds" :: "v"(voff), "s"(srd), "s"(soff), "s"(lds) : "memory");
}
DI void dma16_a(v4u srd, unsigned voff, unsigned soff, unsigned lds) { dma16(srd, voff, soff, lds); }
DI void dma16_b(v4u srd, unsigned voff1, unsigned soff) {
  asm volatile("buffer_load_dwordx4 %0, %1, %2 offen offset:1024 lds" :: "v"(voff1), "s"(srd), "s"(soff) : "memory");
}
DI v4u ld16_sc1_async(v4u srd, unsigned voff, unsigned soff) {
  v4u r; asm volatile("buffer_load_dwordx4 %0, %1, %2, %3 offen sc1" : "=v"(r) : "v"(voff), "s"(srd), "s"(soff) : "memory"); return r;
}
DI v4u ld16_async(v4u srd, unsigned voff, unsigned soff) {
  v4u r; asm volatile("buffer_load_dwordx4 %0, %1, %2, %3 offen" : "=v"(r) : "v"(voff), "s"(srd), "s"(soff) : "memory"); return r;
}
DI unsigned ld2_async(v4u srd, unsigned voff, unsigned soff) {
  unsigned r; asm volatile("buffer_load_ushort %0, %1, %2, %3 offen" : "=v"(r) : "v"(voff), "s"(srd), "s"(soff) : "memory"); return r;
}
DI void st16_sc1(v4u v, v4u srd, unsigned voff, unsigned soff) {
  asm volatile("buffer_store_dwordx4 %0, %1, %2, %3 offen sc1\n\ts_nop 1" :: "v"(v), "v"(voff), "s"(srd), "s"(soff) : "memory");
}
template <int N> DI void wait_vm1(v4u& a) { asm volatile("s_waitcnt vmcnt(%1)" : "+v"(a) : "i"(N) : "memory"); }
template <int N> DI void wait_vm2(v4u& a, v4u& b) { asm volatile("s_waitcnt vmcnt(%2)" : "+v"(a), "+v"(b) : "i"(N) : "memory"); }
template <int N> DI void wait_vm3(v4u& a, v4u& b, v4u& c) { asm volatile("s_waitcnt vmcnt(%3)" : "+v"(a), "+v"(b), "+v"(c) : "i"(N) : "memory"); }
template <class V> DI void pin(V& v) { asm volatile("" : "+v"(v)); }
template <int N> DI void wait_vm() { asm volatile("s_waitcnt vmcnt(%0)" :: "i"(N) : "memory"); }
DI void bar() { asm volatile("s_waitcnt lgkmcnt(0)\n\ts_barrier" ::: "memory"); }
DI v4u lds128(unsigned a) { return *(const LAS v4u*)(size_t)a; }
DI void sts128(unsigned a, v4u v) { *(LAS v4u*)(size_t)a = v; }
DI v2u ldtr(unsigned a) { return __builtin_bit_cast(v2u, __builtin_amdgcn_ds_read_tr16_b64_v4i16((LAS s16x4*)(size_t)a)); }
DI v4u ldtr2(unsigned a, int second) { const v2u lo = ldtr(a), hi = ldtr(a + second); return (v4u){lo.x, lo.y, hi.x, hi.y}; }
template <int CTRL> DI float dpp(float v) { return __builtin_bit_cast(float, __builtin_amdgcn_update_dpp(0, __builtin_bit_cast(int, v), CTRL, 0xf, 0xf, true)); }
DI float rdl(float v, int l) { return __builtin_bit_cast(float, __builtin_amdgcn_readlane(__builtin_bit_cast(int, v), l)); }
DI v4u pack8(const f32x16& a, int s) { return (v4u){pk2(a[8 * s], a[8 * s + 1]), pk2(a[8 * s + 2], a[8 * s + 3]), pk2(a[8 * s + 4], a[8 * s + 5]), pk2(a[8 * s + 6], a[8 * s + 7])}; }
DI void poll_flags(const GAS u64* p, u64 tag) {
#pragma unroll 1
  for (unsigned spin = 0; spin < SPIN_CAP; ++spin) {
    const u64 v = __hip_atomic_load(p, RLX_AGENT);
    if (__all(v == tag)) break;
    __builtin_amdgcn_s_sleep(2);
  }
  asm volatile("" ::: "memory");
}
constexpr u64 upper_mask(int reg) {
  u64 m = 0;
  for (int l = 0; l < 64; ++l) { const int row = (reg & 3) + 8 * (reg >> 2) + 4 * (l >> 5); if (row > (l & 31)) m |= 1ull << l; }
  return m;
}
template <int REG> DI bool upper() { return __builtin_amdgcn_inverse_ballot_w64(upper_mask(REG)); }
template <int I, int N, class Fn> DI void static_for(Fn&& f) { if constexpr (I < N) { f(std::integral_constant<int, I>{}); static_for<I + 1, N>(f); } }
DI float max3(float a, float b, float c) { return __builtin_fmaxf(__builtin_fmaxf(a, b), c); }

constexpr int H1_CK = 16 - AW, H1_LD = H1_CK - 3, O_CK = 28 - AW, O_LD = O_CK - 3;
constexpr int g2_issued(int t) { return ((t >= 1 && t - 1 + NR + RD < 36) ? 2 : 0) + ((t + AW < 28) ? 2 : 0) + (t == H1_LD) + (t == O_LD) + (t == O_CK ? 10 : 0); }
constexpr int g2_range(int a, int b) { int n = 0; for (int t = a; t < b; ++t) n += g2_issued(t); return n; }
#define GELU_PK(P) (P == 1)
#define PRIO_G2 2
#define PRIO_ATT 3
#define PRIO_G1 2
#define ROT r
#define CODEWARM 39000
#define KVPUB 6
#define EXP2(v) __builtin_amdgcn_exp2f(v)
#define PIN(v) pin(v)
extern "C" __device__ float mul_unpacked(float, float) __asm("llvm.amdgcn.fmul.legacy");
#define MUL1(a, b) mul_unpacked(a, b)
#define FMA1(a, b, c) __builtin_amdgcn_div_fmasf(a, b, c, false)
DI v2u ld8_sc1_async(v4u srd, unsigned voff) {
  v2u r; asm volatile("buffer_load_dwordx2 %0, %1, %2, 0 offen sc1" : "=v"(r) : "v"(voff), "s"(srd) : "memory"); return r;
}
template <int N> DI void wait_vmf(v2u& a) { asm volatile("s_waitcnt vmcnt(%1)" : "+v"(a) : "i"(N) : "memory"); }
DI void arrive(unsigned cnt, int lane) {
  asm volatile("s_waitcnt lgkmcnt(0)" ::: "memory");
  if (lane == 0) __hip_atomic_fetch_add((LAS unsigned*)(size_t)cnt, 1u, __ATOMIC_RELAXED, __HIP_MEMORY_SCOPE_WORKGROUP);
}
DI void arrive_nowait(unsigned cnt) {
  asm volatile("s_mov_b64 exec, 1\n\tds_add_u32 %0, %1\n\ts_mov_b64 exec, -1" :: "v"(cnt), "v"(1u) : "memory");
}
DI unsigned peek(unsigned cnt) { return __hip_atomic_load((LAS unsigned*)(size_t)cnt, __ATOMIC_RELAXED, __HIP_MEMORY_SCOPE_WORKGROUP); }
DI void await(unsigned cnt, unsigned n) {
#pragma unroll 1
  for (unsigned spin = 0; spin < SPIN_CAP * 32; ++spin) {
    if (__builtin_amdgcn_readfirstlane(peek(cnt)) >= n) break;
    __builtin_amdgcn_s_sleep(1);
  }
  asm volatile("" ::: "memory");
}

__global__ __launch_bounds__(512, 2) void fused_block(const void* __restrict__ x, const void* __restrict__ wqkv, const void* __restrict__ wo,
                                                      const void* __restrict__ w1, const void* __restrict__ b1, const void* __restrict__ w2,
                                                      const void* __restrict__ b2, void* __restrict__ out, char* __restrict__ ws) {
  constexpr unsigned lds0 = 0;
  const int wave = __builtin_amdgcn_readfirstlane(threadIdx.x >> 6);
  const int m = wave & 3;
  const bool isG = wave < 4;
  const int xcd = blockIdx.x & 7, idx = blockIdx.x >> 3;
  const int r = (xcd >> 2) * 8 + (idx >> 2), j = (xcd & 3) * 4 + (idx & 3);
  const int rt = (xcd >> 1) * 8 + (idx >> 2), ct = (xcd & 1) * 4 + (idx & 3);
  int lane = threadIdx.x & 63, r32, h, g, q4, p4;
  STAMP_AT(0);
  asm volatile("" :: "s"(x), "s"(wqkv), "s"(wo), "s"(w1), "s"(b1), "s"(w2), "s"(b2), "s"(out), "s"(ws));

  const v4u sx = make_srd(x, SEQ * DM * 2), sws = make_srd(ws, WS_END);
  const __amdgpu_buffer_rsrc_t rws = __builtin_amdgcn_make_buffer_rsrc(ws, 0, WS_END, 0x00020000);
  const unsigned one2 = 0x3f803f80u;
  const int T = 4 * r + m;
  const int rot = ROT;
  auto flag_off = [&](int kind, int tile, int jj) { return WS_FLAG + ((kind * 64 + tile) * 16 + jj) * 128; };
  auto flag = [&](int kind, int tile, int jj) { return (GAS u64*)(ws + flag_off(kind, tile, jj)); };
  u64 tag;
  unsigned taglo, tag32;
  auto get_tag = [&]() {
    const v2u tg = *(const LAS v2u*)(size_t)(lds0 + L_CNT + 32);
    taglo = __builtin_amdgcn_readfirstlane(tg[0]); tag32 = __builtin_amdgcn_readfirstlane(tg[1]);
    tag = ((u64)tag32 << 32) | taglo;
  };
  v4u xres, bres, xaug, st;
  float mean, var, rstd, sd;
  v4u sel;
  unsigned xo[4], swz;
  auto lane_setup = [&]() {
    lane = threadIdx.x & 63; PIN(lane);
    r32 = lane & 31; h = lane >> 5; g = lane >> 4; q4 = (lane >> 2) & 3; p4 = lane & 3;
    const unsigned selv = ((lane & 15) == (g & 1)) ? one2 : 0u;
    sel = (v4u){selv, selv, selv, selv};
#pragma unroll
    for (int p = 0; p < 4; ++p) xo[p] = (32 * m + r32) * 128 + (((2 * ((p + m) & 3) + h) ^ ((r32 >> 1) & 7)) * 16);
    swz = ((((lane >> 2) & 1) ^ (g & 1)) * 64) + p4 * 16;
  };

  auto stat_load = [&]() { st = ld16_sc1_async(sws, WS_STATS + (128 * r + 32 * m + r32) * 16, 0); };
  auto res_loads = [&]() {
    int t2 = threadIdx.x; PIN(t2);
    const int row = 64 * rt + (t2 >> 3), col = 64 * ct + (t2 & 7) * 8;
    xres = ld16_async(sx, row * 1024 + col * 2, 0);
    bres = ld16_async(make_srd(b2, DM * 2), col * 2, 0);
  };
  auto row_stats = [&]() {
    if (!__all(st[1] == tag32 && st[3] == tag32)) {
#pragma unroll 1
      for (unsigned spin = 0; spin < SPIN_CAP; ++spin) {
        st = ld16_sc1_async(sws, WS_STATS + (128 * r + 32 * m + r32) * 16, 0);
        wait_vm1<0>(st);
        if (__all(st[1] == tag32 && st[3] == tag32)) break;
      }
    }
    mean = asf(st[0]); var = asf(st[2]);
    PIN(mean); PIN(var);
  };
  auto make_xaug = [&]() {
    PIN(mean); PIN(var);
    rstd = __builtin_amdgcn_rsqf(var); sd = var * rstd;
    const unsigned hi = pk2(-mean, sd);
    const unsigned lo = pk2(-mean - bflo(hi), FMA1(bfhi(hi), -1.f, sd));
    xaug = (v4u){(hi & 0xffffu) | (hi << 16), (lo & 0xffffu) | (hi & 0xffff0000u), lo >> 16, 0u};
    if (h) xaug = (v4u){0u, 0u, 0u, 0u};
    PIN(xaug);
  };
  auto cswr = [&](int tile, f32x4 c) {
    if (lane < 16) { LAS float* p = (LAS float*)(size_t)(lds0 + L_CS + (tile * 32 + lane) * 16 + m * 4); p[0] = c[0]; p[64] = c[1]; }
  };
  auto csrd = [&](int tile) { return __builtin_bit_cast(f32x4, lds128(lds0 + L_CS + (tile * 32 + r32) * 16)); };
  auto waug = [&](f32x4 c4, unsigned b16) {
    const float c = ((c4[0] + c4[1]) + c4[2]) + c4[3];
    const unsigned hi = pk2(c, 0.f);
    const unsigned lo = pk2(c - bflo(hi), 0.f);
    return (v4u){hi | (lo << 16), hi | (b16 << 16), b16, 0u};
  };

  const v4u sw1 = make_srd(w1, DM * FF * 2);
  const int hcol0 = 448 * (j >> 2) + 128 * (j & 3);
  const bool heavy = (j & 3) != 3;
  unsigned bias0, bias1;
  auto bias_load = [&](int P) {
    bias0 = ld2_async(make_srd(b1, FF * 2), r32 * 2, (hcol0 + 64 * P) * 2);
    bias1 = ld2_async(make_srd(b1, FF * 2), r32 * 2, (hcol0 + 64 * P + 32) * 2);
  };
  auto w1dma = [&](int P, int c, unsigned dst, int half) {
    const unsigned vw1 = (lane >> 3) * (FF * 2) + swz, vw1b = vw1 + 8 * (FF * 2) - 1024;
    const unsigned so = (64 * ((c + rot) & 7) + 16 * m) * (FF * 2) + (hcol0 + 64 * P) * 2;
    if (half == 0) dma16_a(sw1, vw1, so, dst + m * 2048); else dma16_b(sw1, vw1b, so);
  };
  f32x16 hac[2];
  unsigned rk[4][2];
  auto hsetup = [&](int P) {
#pragma unroll
    for (int p = 0; p < 4; ++p)
#pragma unroll
      for (int c = 0; c < 2; ++c) {
        rk[p][c] = lds0 + (P ? L_WA : 28672) + ((p + m) & 3) * 2048 + h * 1024 + q4 * 128 + ((c ^ (q4 >> 1)) * 64) + (g & 1) * 32 + p4 * 8;
        PIN(rk[p][c]);
      }
  };
  auto hmm = [&](auto pc, const v4u (&xf)[32], auto&& hook) {
    constexpr int P = pc.value;
    constexpr int D = 4;
    const unsigned cnt = lds0 + L_CNT;
    f32x4 cs[2] = {}, c0, c1;
    v4u wf[D][2], au0, au1;
    unsigned seen = 0;
    hac[0] = (f32x16){}; hac[1] = (f32x16){};
    auto rd = [&](int ks, int t) { wf[ks % D][t] = ldtr2(rk[ks & 3][t] + (ks >> 2) * 8192, 512); };
#pragma unroll
    for (int ks = 0; ks < D; ++ks) { rd(ks, 0); rd(ks, 1); }
    SBAR();
    static_for<0, 32>([&](auto kc) {
      constexpr int ks = kc.value;
      hook(kc);
      if constexpr (ks == 10) seen = peek(cnt + 20);
      if constexpr (ks == 29) seen = peek(cnt);
#pragma unroll
      for (int t = 0; t < 2; ++t) {
        hac[t] = MFMA32(wf[ks % D][t], xf[ks], hac[t]);
        if ((ks & 3) == 0 && ks < 28) { cs[t] = MFMA16(sel, wf[ks % D][t], cs[t]); PIN(cs[t]); }
        if (ks == 26) { cs[t] = MFMA16(sel, wf[28 % D][t], cs[t]); PIN(cs[t]); }
        SBAR();
        if constexpr (ks + D < 32) rd(ks + D, t);
        if constexpr (ks == 31) { if (t == 0) { au0 = waug(c0, bias0); PIN(au0); } else { au1 = waug(c1, bias1); PIN(au1); } }
        SBAR();
      }
      if constexpr (ks == 11 || ks == 30) {
        PIN(hac[0]); PIN(hac[1]);
        if (__builtin_amdgcn_readfirstlane(seen) < 4) await(ks == 11 ? cnt + 20 : cnt, 4);
        asm volatile("" ::: "memory");
        if constexpr (ks == 30) { c0 = csrd(2 * P); c1 = csrd(2 * P + 1); }
      }
      if constexpr (ks == 27) { cswr(2 * P, cs[0]); cswr(2 * P + 1, cs[1]); arrive_nowait(cnt); }
      SBAR();
    });
    hac[0] = MFMA32(au0, xaug, hac[0]);
    hac[1] = MFMA32(au1, xaug, hac[1]);
    SBAR();
  };
  auto hgelu = [&](auto pc, auto&& mid) {
    constexpr int P = pc.value;
    constexpr bool PK = GELU_PK(P);
    const float C0 = 2.3022081986f, C1 = C0 * 0.044715f;
    const float ga = -C1 * rstd * rstd * rstd, gb = -C0 * rstd;
    auto mul = [&](f32x2 a, f32x2 b) { return PK ? a * b : (f32x2){MUL1(a[0], b[0]), MUL1(a[1], b[1])}; };
    auto fma = [&](f32x2 a, float b, float c) {
      return PK ? __builtin_elementwise_fma(a, (f32x2){b, b}, (f32x2){c, c}) : (f32x2){FMA1(a[0], b, c), FMA1(a[1], b, c)}; };
#pragma unroll
    for (int t = 0; t < 2; ++t) {
      f32x2 v[8], z[8];
#pragma unroll
      for (int e = 0; e < 8; ++e) { v[e] = (f32x2){hac[t][2 * e], hac[t][2 * e + 1]}; z[e] = mul(v[e], v[e]); }
      SBAR();
#pragma unroll
      for (int e = 0; e < 8; ++e) z[e] = fma(z[e], ga, gb);
      SBAR();
#pragma unroll
      for (int e = 0; e < 8; ++e) z[e] = mul(z[e], v[e]);
      SBAR();
#pragma unroll
      for (int e = 0; e < 8; ++e) z[e] = (f32x2){EXP2(z[e][0]), EXP2(z[e][1])};
      SBAR();
#pragma unroll
      for (int e = 0; e < 8; ++e) z[e] = fma(z[e], sd, sd);
      SBAR();
#pragma unroll
      for (int e = 0; e < 8; ++e) z[e] = (f32x2){__builtin_amdgcn_rcpf(z[e][0]), __builtin_amdgcn_rcpf(z[e][1])};
      SBAR();
#pragma unroll
      for (int e = 0; e < 8; ++e) z[e] = mul(z[e], v[e]);
      SBAR();
#pragma unroll
      for (int s = 0; s < 2; ++s) {
        const v4u f = {pk2(z[4 * s][0], z[4 * s][1]), pk2(z[4 * s + 1][0], z[4 * s + 1][1]), pk2(z[4 * s + 2][0], z[4 * s + 2][1]), pk2(z[4 * s + 3][0], z[4 * s + 3][1])};
        st16_sc1(f, sws, lane * 16, WS_AF + (T * AFT + (hcol0 >> 4) + 4 * P + 2 * t + s) * 1024);
      }
      if (t == 0) mid();
      SBAR();
    }
  };

  if (isG) {
    lane_setup();
    const v4u sw2 = make_srd(w2, FF * DM * 2), swo = make_srd(wo, DM * DM * 2);
    const unsigned vw2 = (lane >> 3) * 1024 + swz, vw2b = vw2 + 7 * 1024;
    const int ro3 = rt % 3;
    auto kmap = [&](int kk) {
      if (kk < 16) return 8 * (((kk >> 2) + rt) & 3) + (kk & 3);
      const int p = ((kk - 16) >> 2) + ro3;
      return 8 * (p >= 3 ? p - 3 : p) + 4 + (kk & 3);
    };
    auto omap = [&](int i) { return 2 * ((i + rt) & 3) + (i >> 2); };
    auto wdmah = [&](int kk, int half) {
      const unsigned so = (kk < 28 ? 448 * m + 16 * kmap(kk) : 128 * m + 16 * omap(kk - 28)) * 1024 + 128 * ct;
      if (half == 0) dma16_a(kk < 28 ? sw2 : swo, vw2, so, lds0 + L_WG + m * PART + (kk % RD) * 2048); else dma16_b(kk < 28 ? sw2 : swo, vw2b, so);
    };
    auto wdma = [&](int kk) { wdmah(kk, 0); wdmah(kk, 1); };
    {
      const v4u swq = make_srd(wqkv, DM * 3 * DM * 2);
      const unsigned vx0 = (lane >> 3) * 1024 + (((lane & 7) ^ g) * 16), vx1 = (7 + (lane >> 3)) * 1024 + (((lane & 7) ^ (4 + g)) * 16);
      const unsigned vwq = (lane >> 2) * (3 * DM * 2) + p4 * 16;
      auto piece = [&](int s, int i) {
        const unsigned buf = lds0 + (s % 3) * STG;
        const int kc = (s + rot) & 7;
        if (i < 4) {
          const unsigned so = (128 * r + 32 * m + 16 * (i >> 1)) * 1024 + kc * 128;
          if (i & 1) dma16_b(sx, vx1, so); else dma16_a(sx, vx0, so, buf + m * 4096 + (i >> 1) * 2048);
        } else if (i < 7) dma16(swq, vwq, (64 * kc + 16 * m) * (3 * DM * 2) + (512 * (i - 4) + 32 * j) * 2, buf + 16384 + ((i - 4) * 4 + m) * 1024);
        else w1dma(0, s, buf + 28672, i - 7);
      };
#pragma unroll
      for (int i = 0; i < 18; ++i) { piece(i / 9, i % 9); if (i == 8) __builtin_amdgcn_s_barrier(); }
      __builtin_amdgcn_s_setprio(PRIO_G1);
      hsetup(0);
      f32x4 cs[2] = {};
      v4u xf[4], wf[4][2];
      hac[0] = (f32x16){}; hac[1] = (f32x16){};
      static_for<0, 8>([&](auto sc) {
        constexpr int s = sc.value;
        constexpr unsigned buf = (s % 3) * STG;
        auto fill = [&](int i) {
          if constexpr (s < 6) piece(s + 2, i);
          if constexpr (s == 6) { if (i == 0) { stat_load(); bias_load(0); } else wdmah((i - 1) >> 1, (i - 1) & 1); }
          if constexpr (s == 7) { if (heavy) { w1dma(1, i, lds0 + L_WA + i * 8192, 0); w1dma(1, i, 0, 1); } }
          SBAR();
        };
        auto rd = [&](int p) { xf[p] = lds128(buf + xo[p]); wf[p][0] = ldtr2(buf + rk[p][0], 512); wf[p][1] = ldtr2(buf + rk[p][1], 512); SBAR(); };
        if constexpr (s < 7) wait_vm<9>(); else { wait_vm1<8>(st); PIN(bias0); PIN(bias1); }
        bar();
        STAMP_AT(10 + s);
        rd(0);
        if constexpr (s < 7) fill(0);
        rd(1);
        if constexpr (s < 7) fill(1);
        if constexpr (s == 2) get_tag();
#pragma unroll
        for (int p = 0; p < 4; ++p) {
#pragma unroll
          for (int t = 0; t < 2; ++t) {
            hac[t] = MFMA32(wf[p][t], xf[p], hac[t]);
            if (p == 0) { cs[t] = MFMA16(sel, wf[p][t], cs[t]); PIN(cs[t]); }
            SBAR();
            if (p < 2 && t == 0) rd(p + 2);
            if (s == 7) fill(2 * p + t); else if (2 * p + t < 7) fill(2 + 2 * p + t);
          }
          if (s == 7 && p == 1) {
            cswr(0, cs[0]); cswr(1, cs[1]);
            bar();
            STAMP_AT(1);
          }
        }
        if constexpr (s == 7) {
          const f32x4 c0 = csrd(0), c1 = csrd(1);
          row_stats(); make_xaug();
          hac[0] = MFMA32(waug(c0, bias0), xaug, hac[0]);
          hac[1] = MFMA32(waug(c1, bias1), xaug, hac[1]);
          SBAR();
        }
      });
    }
    STAMP_AT(18);
    if (heavy) { wait_vm<8>(); arrive_nowait(lds0 + L_CNT + 16); }
    hgelu(std::integral_constant<int, 0>{}, [&]() {
      wait_vm<2>();
      if (heavy) arrive_nowait(lds0 + L_CNT + 20);
    });
    __builtin_amdgcn_s_setprio(0);
    STAMP_AT(19);
    unsigned rb2[2];
#pragma unroll
    for (int c = 0; c < 2; ++c) rb2[c] = lds0 + L_WG + m * PART + h * 512 + q4 * 128 + ((c ^ (q4 >> 1)) * 64) + (g & 1) * 32 + p4 * 8;
    v4u wq[36][2];
    auto wrd = [&](int kk) {
#pragma unroll
      for (int c = 0; c < 2; ++c) wq[kk][c] = ldtr2(rb2[c] + (kk % RD) * 2048, 1024);
    };
    auto wgot = [&](int kk) { PIN(wq[kk][0]); PIN(wq[kk][1]); };
    const int ftile = 2 * rt + ((lane >> 2) & 1);
    v4u af[28][2], of[8][2];
    v2u fl;
    const unsigned a0 = WS_AF + (2 * rt * AFT + 28 * m) * 1024, o0 = WS_AF + (2 * rt * AFT + 112 + 8 * m) * 1024;
    for (int kk = 0; kk < 4; ++kk) wrd(kk);
    for (int kk = 4; kk < 8; ++kk) wdma(kk);
    for (int kk = 0; kk < 4; ++kk) wgot(kk);
    wait_vm<8>();
    if (lane == 0) __hip_atomic_store(flag(FL_H0, T, j), tag, RLX_AGENT);
    STAMP_AT(2);
    for (int kk = 8; kk < 12; ++kk) wdma(kk);
    static_for<1, 3>([&](auto ic) {
      constexpr int i = ic.value;
      wait_vm<(i == 1) ? 9 : 8>();
      for (int kk = 4 * i; kk < 4 * i + 4; ++kk) wrd(kk);
      for (int kk = 4 * i; kk < 4 * i + 4; ++kk) wgot(kk);
      if constexpr (i == 2) fl = ld8_sc1_async(sws, flag_off(FL_H0, ftile, 4 * m + p4));
      for (int kk = 4 * i; kk < 4 * i + 4; ++kk) wdma(kk + RD);
    });
    STAMP_AT(3);
    wait_vmf<8>(fl);
    if (!__all(fl[0] == taglo && fl[1] == tag32)) poll_flags(flag(FL_H0, ftile, 4 * m + p4), tag);
    STAMP_AT(4);
#pragma unroll
    for (int kk = 0; kk < AW; ++kk) {
      af[kk][0] = ld16_sc1_async(sws, lane * 16, a0 + kmap(kk) * 1024);
      af[kk][1] = ld16_sc1_async(sws, lane * 16, a0 + (AFT + kmap(kk)) * 1024);
    }
    __builtin_amdgcn_s_setprio(PRIO_G2);
    f32x16 c2[2][2] = {};
    auto mm2 = [&](int kk, const v4u (&a)[2], auto&& fill) {
      SBAR();
#pragma unroll
      for (int i = 0; i < 4; ++i) { c2[i >> 1][i & 1] = MFMA32(wq[kk][i & 1], a[i >> 1], c2[i >> 1][i & 1]); SBAR(); fill(i); SBAR(); }
    };
    static_for<0, 28>([&](auto kc) {
      constexpr int kk = kc.value;
      wait_vm2<(kk < AW) ? 2 * (AW - 1 - kk) + g2_range(0, kk) : g2_range(kk - AW + 1, kk)>(af[kk][0], af[kk][1]);
      if constexpr (kk == H1_CK) {
        STAMP_AT(20);
        wait_vmf<g2_range(H1_LD, H1_CK) - 1>(fl);
        if (!__all(fl[0] == taglo && fl[1] == tag32)) poll_flags(flag(FL_H1, ftile, 4 * m + p4), tag);
        STAMP_AT(21);
      }
      if constexpr (kk == H1_LD) fl = ld8_sc1_async(sws, flag_off(FL_H1, ftile, 4 * m + p4));
      if constexpr (kk == O_LD) fl = ld8_sc1_async(sws, flag_off(FL_O0, ftile, 4 * m + p4));
      constexpr bool dw = kk >= 1 && kk - 1 + NR + RD < 36;
      if constexpr (dw) wgot(kk - 1 + NR);
      mm2(kk, af[kk], [&](int i) {
        if constexpr (dw) { if (i < 2) wdmah(kk - 1 + NR + RD, i); }
        if constexpr (kk + NR < 28) { if (i == 1) wrd(kk + NR); }
        if constexpr (kk == 27) { if (i == 0) wrd(28); }
        if constexpr (kk + AW < 28) { if (i >= 2) af[kk + AW][i - 2] = ld16_sc1_async(sws, lane * 16, a0 + (AFT * (i - 2) + kmap(kk + AW)) * 1024); }
      });
      if constexpr (kk == O_CK) {
        wait_vmf<g2_range(O_LD, O_CK) - 1>(fl);
        if (!__all(fl[0] == taglo && fl[1] == tag32)) poll_flags(flag(FL_O0, ftile, 4 * m + p4), tag);
        res_loads();
#pragma unroll
        for (int i = 0; i < 4; ++i) {
          of[i][0] = ld16_sc1_async(sws, lane * 16, o0 + omap(i) * 1024);
          of[i][1] = ld16_sc1_async(sws, lane * 16, o0 + (AFT + omap(i)) * 1024);
        }
      }
    });
    static_for<0, 8>([&](auto kc) {
      constexpr int kk = kc.value;
      if (kk < 7) wrd(28 + kk + 1);
      if constexpr (kk == 1) {
        STAMP_AT(5);
        poll_flags(flag(FL_O1, ftile, 4 * m + p4), tag);
        STAMP_AT(6);
#pragma unroll
        for (int i = 4; i < 8; ++i) {
          of[i][0] = ld16_sc1_async(sws, lane * 16, o0 + omap(i) * 1024);
          of[i][1] = ld16_sc1_async(sws, lane * 16, o0 + (AFT + omap(i)) * 1024);
        }
      }
      wait_vm2<(kk == 0) ? 6 : 14 - 2 * kk>(of[kk][0], of[kk][1]);
      mm2(28 + kk, of[kk], [&](int) {});
    });
    PIN(xres); PIN(bres);
    asm volatile("" ::: "memory");
#pragma unroll
    for (int mt = 0; mt < 2; ++mt)
#pragma unroll
      for (int c = 0; c < 2; ++c)
#pragma unroll
        for (int qd = 0; qd < 4; ++qd) {
          const f32x4 v = {c2[mt][c][4 * qd], c2[mt][c][4 * qd + 1], c2[mt][c][4 * qd + 2], c2[mt][c][4 * qd + 3]};
          sts128(lds0 + L_WG + m * PART + ((mt * 8 + c * 4 + qd) * 66 + lane) * 16, __builtin_bit_cast(v4u, v));
        }
    STAMP_AT(7);
  } else {
    __builtin_amdgcn_s_setprio(1);
    lane_setup();
    v4u xr[2];
    xr[0] = ld16_async(sx, lane * 16, (128 * r + 8 * j + 2 * m) * 1024); xr[1] = ld16_async(sx, lane * 16, (128 * r + 8 * j + 2 * m + 1) * 1024);
    unsigned warm;
    {
      u64 pc; asm volatile("s_getpc_b64 %0" : "=s"(pc));
      warm = ld2_async(make_srd((const void*)(pc & ~127ull), CODEWARM), lane < 3 ? lane * 128 : 0x40000000u, (idx * 4 + m) * 384);
    }
    __builtin_amdgcn_s_barrier();
    v2u ep = ld8_sc1_async(sws, m == 0 ? WS_EPOCH : 0x40000000u);
    float sval;
    auto make_stats = [&]() {
      wait_vm2<1>(xr[0], xr[1]);
      PIN(warm);
      float piv[2], a[4];
#pragma unroll
      for (int u = 0; u < 2; ++u) {
        piv[u] = rdl(bflo(xr[u][0]), 0);
        float s1 = 0.f, s2 = 0.f;
#pragma unroll
        for (int i = 0; i < 8; ++i) { const float d = ((i & 1) ? bfhi(xr[u][i >> 1]) : bflo(xr[u][i >> 1])) - piv[u]; s1 += d; s2 = __builtin_fmaf(d, d, s2); }
        a[2 * u] = s1; a[2 * u + 1] = s2;
      }
#pragma unroll
      for (int i = 0; i < 4; ++i) a[i] += dpp<0xB1>(a[i]);
#pragma unroll
      for (int i = 0; i < 4; ++i) a[i] += dpp<0x4E>(a[i]);
      float t = p4 == 0 ? a[0] : p4 == 1 ? a[1] : p4 == 2 ? a[2] : a[3];
      t += dpp<0x114>(t); t += dpp<0x118>(t);
      const auto s16 = __builtin_amdgcn_permlane16_swap(asu(t), asu(t), false, false);
      t = asf(s16[0]) + asf(s16[1]);
      const auto s32 = __builtin_amdgcn_permlane32_swap(asu(t), asu(t), false, false);
      t = (asf(s32[0]) + asf(s32[1])) * (1.f / DM);
      const float e1 = dpp<0xA0>(t);
      sval = (p4 & 1) ? __builtin_fmaxf(__builtin_fmaf(-e1, e1, t), 0.f) + 1e-5f : t + (p4 ? piv[1] : piv[0]);
      PIN(sval);
    };
    const unsigned acnt = lds0 + L_CNT;
    const unsigned vpos = 2048 + (g & 1) * 512 + h * 256 + (lane & 15) * 16;
    v4u qf[2], am0, am1;
    {
      v4u xf[32];
      f32x16 acc[3] = {};
      f32x4 cs[3] = {};
      v4u wf[4][3];
      unsigned wb[4];
#pragma unroll
      for (int p = 0; p < 4; ++p) wb[p] = 16384 + ((p + m) & 3) * 1024 + (8 * h + q4) * 64 + (g & 1) * 32 + p4 * 8;
      auto rd = [&](int ks, int t) {
        const unsigned buf = lds0 + ((ks >> 2) % 3) * STG;
        if (t == 0) xf[ks] = lds128(buf + xo[ks & 3]);
        wf[ks & 3][t] = ldtr2(buf + wb[ks & 3] + t * 4096, 256);
      };
      auto mm = [&](int ks, int t) {
        acc[t] = t == 2 ? MFMA32(xf[ks], wf[ks & 3][t], acc[t]) : MFMA32(wf[ks & 3][t], xf[ks], acc[t]);
        if ((ks & 3) == 0) { cs[t] = MFMA16(sel, wf[ks & 3][t], cs[t]); PIN(cs[t]); }
        SBAR();
      };
      make_stats();
      bar();
      STAMP_AT(10);
#pragma unroll
      for (int t = 0; t < 6; ++t) rd(t / 3, t % 3);
      SBAR();
      static_for<0, 8>([&](auto sc) {
        constexpr int s = sc.value;
#pragma unroll
        for (int p = 0; p < 2; ++p) {
#pragma unroll
          for (int t = 0; t < 3; ++t) { mm(4 * s + p, t); rd(4 * s + p + 2, t); SBAR(); }
          if (s == 7 && p == 0) { cswr(4, cs[0]); cswr(5, cs[1]); cswr(6, cs[2]); SBAR(); }
        }
        if constexpr (s == 0) {
          wait_vmf<0>(ep);
          if (m == 0 && lane < 10)
            *(LAS unsigned*)(size_t)(lds0 + L_CNT + lane * 4) = lane == 8 ? ep[0] * 0x9E3779B1u + 0x7F4A7C15u : lane == 9 ? ep[1] * 0x85EBCA77u + 0x165667B1u : 0u;
        }
        if constexpr (s == 1) {
          get_tag();
          if ((lane >> 2) == 3) {
            const v2u gr = {asu(sval), tag32};
            __builtin_amdgcn_raw_buffer_store_b64(gr, rws, WS_STATS + (128 * r + 8 * j + 2 * m) * 16 + p4 * 8, 0, 16);
          }
        }
        if constexpr (s == 5) stat_load();
        if constexpr (s == 6) {
          wait_vm1<0>(st);
          row_stats();
        }
        mm(4 * s + 2, 0); mm(4 * s + 2, 1);
        bar();
        if constexpr (s < 7) {
          STAMP_AT(11 + s);
          mm(4 * s + 2, 2);
          rd(4 * s + 4, 0); rd(4 * s + 4, 1); rd(4 * s + 4, 2); SBAR();
#pragma unroll
          for (int t = 0; t < 3; ++t) { mm(4 * s + 3, t); rd(4 * s + 5, t); SBAR(); }
        }
      });
      STAMP_AT(1);
      __builtin_amdgcn_s_setprio(3);
      const f32x4 c5 = csrd(5), c6 = csrd(6), c4 = csrd(4);
      SBAR();
      mm(30, 2); mm(31, 1);
      make_xaug();
      SBAR();
      mm(31, 2); mm(31, 0);
      if (h == 0) *(LAS float*)(size_t)(lds0 + L_RS + (m * 32 + r32) * 4) = rstd;
      acc[1] = MFMA32(waug(c5, 0u), xaug, acc[1]);
      acc[2] = MFMA32(xaug, waug(c6, 0u), acc[2]);
      acc[0] = MFMA32(waug(c4, 0u), xaug, acc[0]);
      SBAR();
      f32x4 rs[4];
#pragma unroll
      for (int qd = 0; qd < 4; ++qd) rs[qd] = __builtin_bit_cast(f32x4, lds128(lds0 + L_RS + (m * 32 + 8 * qd + 4 * h) * 4));
      const unsigned kvo = WS_KV + (j * 64 + T) * 4096;
#pragma unroll
      for (int e = 0; e < 16; ++e) acc[1][e] = MUL1(acc[1][e], rstd);
#pragma unroll
      for (int e = 0; e < 2; ++e) {
        const v4u f = pack8(acc[1], e);
        st16_sc1(f, sws, lane * 16, kvo + e * 1024);
        sts128(lds0 + L_KV + (8 + m) * KVT + e * 1024 + lane * 16, f);
      }
#pragma unroll
      for (int qd = 0; qd < 4; ++qd)
#pragma unroll
        for (int e = 0; e < 4; ++e) acc[2][4 * qd + e] = MUL1(acc[2][4 * qd + e], rs[qd][e]);
#pragma unroll
      for (int s = 0; s < 2; ++s) {
        const v4u f = pack8(acc[2], s);
        st16_sc1(f, sws, lane * 16, kvo + 2048 + s * 1024);
        sts128(lds0 + L_KV + (8 + m) * KVT + s * VB + vpos, f);
      }
      SBAR();
      const float qs = rstd * (0.25f * 1.4426950408889634f);
#pragma unroll
      for (int e = 0; e < 16; ++e) acc[0][e] = MUL1(acc[0][e], qs);
      qf[0] = pack8(acc[0], 0); qf[1] = pack8(acc[0], 1);
      PIN(qf[0]); PIN(qf[1]);
      STAMP_AT(2);
      const int T0 = 4 * r - 8 + 2 * m;
      const bool live = T0 >= 0;
      auto kv_load = [&](v4u (&kv)[8], int i0, int i1) {
        const unsigned src = WS_KV + (j * 64 + (live ? T0 : 0)) * 4096, vo = live ? lane * 16 : h * 512;
#pragma unroll
        for (int i = i0; i < i1; ++i) kv[i] = ld16_sc1_async(sws, vo, src + (live ? i : (i & 3)) * 1024);
      };
      auto kv_stage = [&](v4u (&kv)[8]) {
        const unsigned on = (lane < 2 || live) ? one2 : 0u;
        if (lane < 6) sts128(lds0 + L_KV + (lane < 2 ? 8 + m : 2 * m + ((lane - 2) >> 1)) * KVT + 2048 + (lane & 1) * VB + 1024, (v4u){on, on, on, on});
        const v4u zz = {};
#pragma unroll
        for (int i = 0; i < 2; ++i) {
          sts128(lds0 + L_KV + (2 * m + i) * KVT + lane * 16, kv[4 * i]);
          sts128(lds0 + L_KV + (2 * m + i) * KVT + 1024 + lane * 16, kv[4 * i + 1]);
          sts128(lds0 + L_KV + (2 * m + i) * KVT + vpos, live ? kv[4 * i + 2] : zz);
          sts128(lds0 + L_KV + (2 * m + i) * KVT + VB + vpos, live ? kv[4 * i + 3] : zz);
        }
        arrive_nowait(acnt);
      };
      auto kv_wait = [&](v4u (&kv)[8]) {
        wait_vm<0>();
#pragma unroll
        for (int i = 0; i < 8; ++i) PIN(kv[i]);
      };
      if (heavy) {
        bias_load(1);
        hsetup(1);
        await(acnt + 16, 4);
        STAMP_AT(3);
        v2u fl;
        v4u kv[8];
        hmm(std::integral_constant<int, 1>{}, xf, [&](auto kc) {
          constexpr int ks = kc.value;
          if constexpr (ks == KVPUB) {
            wait_vm<0>();
            PIN(bias0); PIN(bias1);
            if (lane == 0) __hip_atomic_store(flag(FL_KV, T, j), tag, RLX_AGENT);
          }
          if constexpr (ks == 24) fl = ld8_sc1_async(sws, flag_off(FL_KV, live ? T0 + (lane & 1) : 0, j));
        });
        STAMP_AT(18);
        wait_vmf<0>(fl);
        if (!__all(fl[0] == taglo && fl[1] == tag32)) poll_flags(flag(FL_KV, live ? T0 + (lane & 1) : 0, j), tag);
        kv_load(kv, 0, 8);
        hgelu(std::integral_constant<int, 1>{}, [&]() {});
        STAMP_AT(4);
        wait_vm<4>();
#pragma unroll
        for (int i = 0; i < 8; ++i) PIN(kv[i]);
        kv_stage(kv);
      } else {
        wait_vm<0>();
        if (lane == 0) __hip_atomic_store(flag(FL_KV, T, j), tag, RLX_AGENT);
        v4u kv[8];
        poll_flags(flag(FL_KV, live ? T0 + (lane & 1) : 0, j), tag);
        kv_load(kv, 0, 8); kv_wait(kv); kv_stage(kv);
      }
    }
    STAMP_AT(19);
    const unsigned kb = lds0 + L_KV + m * KVT + lane * 16;
    unsigned vb[2];
#pragma unroll
    for (int e = 0; e < 2; ++e) { vb[e] = lds0 + L_KV + m * KVT + 2048 + ((g & 1) ? 1024 : e * 512 + h * 256 + (lane & 15) * 16); PIN(vb[e]); }
    {
      const int dq = r32 - 4 * h;
#pragma unroll
      for (int d = 0; d < 8; ++d) { const int row = 2 * (d & 1) + 8 * (d >> 1); (d < 4 ? am0 : am1)[d & 3] = row > dq ? 0xffffffffu : row == dq ? 0xffff0000u : 0u; }
      PIN(am0); PIN(am1);
    }
    await(acnt, heavy ? 8 : 4);
    __builtin_amdgcn_s_setprio(PRIO_ATT);
    STAMP_AT(5);
    const f32x16 zero = {};
    auto o_store = [&](f32x16& o, int e) {
      const float rl = __builtin_amdgcn_rcpf(o[8]);
#pragma unroll
      for (int u = 0; u < 8; ++u) o[u] = MUL1(o[u], rl);
      st16_sc1(pack8(o, 0), sws, lane * 16, WS_AF + (T * AFT + 112 + 2 * j + e) * 1024);
    };
    int redo = 0;
    {
      f32x16 o[2], sa, sb, su;
      bool pend = false;
      v4u kn, ku, va, vc, vn0, vn1, vu0, vu1, p0, p1, pu;
      auto kaddr = [&](int n) { return kb + ((n & 7) + 1) * KVT + (n >> 3) * 1024; };
      auto vaddr = [&](int n) { return vb[n >> 3] + ((n & 7) + 1) * KVT; };
      kn = lds128(kaddr(0)); vn0 = lds128(vaddr(0)); vn1 = lds128(vaddr(0) + VB);
      sa = MFMA32(kn, qf[0], zero);
      kn = lds128(kaddr(1));
      SBAR();
      auto step = [&](auto nc, f32x16& sc, f32x16& sn) {
        constexpr int n = nc.value, e = n >> 3, i = n & 7;
        if constexpr (n < 15) sn = MFMA32(kn, qf[(n + 1) >> 3], zero);
        if constexpr (n < 14) kn = lds128(kaddr(n + 2));
        if constexpr (i == 5) ku = lds128(kb + e * 1024);
        v4u v0 = vn0, v1 = vn1;
        if constexpr (n < 15) { vn0 = lds128(vaddr(n + 1)); vn1 = lds128(vaddr(n + 1) + VB); }
        if constexpr (i == 6) { vu0 = lds128(vb[e]); vu1 = lds128(vb[e] + VB); }
        SBAR();
        if constexpr (i == 7) {
          static_for<0, 16>([&](auto u) { sc[u.value] = upper<u.value>() ? su[u.value] : sc[u.value]; });
          PIN(sc); SBAR();
        }
#pragma unroll
        for (int u = 0; u < 6; ++u) sc[u] = EXP2(sc[u]);
        PIN(sc); SBAR();
        if constexpr (n == 8) { o[0] = MFMA32(vc, p1, o[0]); SBAR(); }
        else if constexpr (n > 0) { o[e] = MFMA32(vc, p1, o[e]); SBAR(); }
        sc[6] = EXP2(sc[6]); sc[7] = EXP2(sc[7]);
        p0 = pack8(sc, 0);
        sc[8] = EXP2(sc[8]); sc[9] = EXP2(sc[9]);
        if constexpr (i == 7) { va = p0 & am0; p0 &= ~am0; PIN(va); }
        PIN(sc); PIN(p0); SBAR();
        if constexpr (n == 8) { o[0] = MFMA32(vu1, pu, o[0]); SBAR(); }
        o[e] = MFMA32(v0, p0, i == 0 ? zero : o[e]);
        SBAR();
        if constexpr (i == 6) { su = MFMA32(ku, qf[e], zero); SBAR(); }
#pragma unroll
        for (int u = 10; u < 13; ++u) sc[u] = EXP2(sc[u]);
        if constexpr (i == 7) { PIN(sc); SBAR(); o[e] = MFMA32(vu0, va, o[e]); SBAR(); }
#pragma unroll
        for (int u = 13; u < 16; ++u) sc[u] = EXP2(sc[u]);
        p1 = pack8(sc, 1); vc = v1;
        if constexpr (i == 7) { pu = p1 & am1; p1 &= ~am1; PIN(pu); }
        PIN(p1); SBAR();
        if constexpr (n == 8) {
          if (__all(o[0][8] >= 0x1p-30f && o[0][8] <= 0x1p30f)) o_store(o[0], 0); else redo = 1;
          SBAR();
        }
        if constexpr (i == 2) pend = !redo;
        if constexpr (i >= 2) {
          if (pend && (i == 7 || (__builtin_amdgcn_s_getreg(7 | (3 << 11)) & 15) == 0)) {
            wait_vm<0>();
            if (lane == 0) __hip_atomic_store(flag(FL_H1 + e, T, j), tag, RLX_AGENT);
            pend = false;
          }
        }
      };
      static_for<0, 8>([&](auto hc) {
        step(std::integral_constant<int, 2 * hc.value>{}, sa, sb);
        step(std::integral_constant<int, 2 * hc.value + 1>{}, sb, sa);
      });
      o[1] = MFMA32(vc, p1, o[1]);
      o[1] = MFMA32(vu1, pu, o[1]);
      if (__all(o[1][8] >= 0x1p-30f && o[1][8] <= 0x1p30f)) o_store(o[1], 1); else redo |= 2;
    }
    if (redo) {
#pragma unroll 1
      for (int e = 0; e < 2; ++e) {
        if (!((redo >> e) & 1)) continue;
        const v4u q = e ? qf[1] : qf[0];
        const unsigned vbe = e ? vb[1] : vb[0];
        auto merged = [&](const f32x16& c) {
          f32x16 sl = MFMA32(lds128(kb + 8 * KVT + e * 1024), q, c);
          const f32x16 su = MFMA32(lds128(kb + e * 1024), q, c);
          static_for<0, 16>([&](auto u) { sl[u.value] = upper<u.value>() ? su[u.value] : sl[u.value]; });
          return sl;
        };
        float mx = -INFINITY;
#pragma unroll 1
        for (int i = 0; i < 8; ++i) {
          const f32x16 sc = i ? MFMA32(lds128(kb + i * KVT + e * 1024), q, zero) : merged(zero);
#pragma unroll
          for (int u = 0; u < 16; u += 2) mx = max3(mx, sc[u], sc[u + 1]);
        }
        const auto sw = __builtin_amdgcn_permlane32_swap(asu(mx), asu(mx), false, false);
        mx = __builtin_fmaxf(asf(sw[0]), asf(sw[1]));
        f32x16 negm, o;
#pragma unroll
        for (int u = 0; u < 16; ++u) negm[u] = -mx;
        {
          f32x16 sc = merged(negm);
#pragma unroll
          for (int u = 0; u < 16; ++u) sc[u] = EXP2(sc[u]);
          const v4u p0 = pack8(sc, 0), p1 = pack8(sc, 1);
          o = MFMA32(lds128(vbe), p0 & am0, zero);
          o = MFMA32(lds128(vbe + VB), p1 & am1, o);
          o = MFMA32(lds128(vbe + 8 * KVT), p0 & ~am0, o);
          o = MFMA32(lds128(vbe + 8 * KVT + VB), p1 & ~am1, o);
        }
#pragma unroll 1
        for (int i = 1; i < 8; ++i) {
          f32x16 sc = MFMA32(lds128(kb + i * KVT + e * 1024), q, negm);
#pragma unroll
          for (int u = 0; u < 16; ++u) sc[u] = EXP2(sc[u]);
          o = MFMA32(lds128(vbe + i * KVT), pack8(sc, 0), o);
          o = MFMA32(lds128(vbe + i * KVT + VB), pack8(sc, 1), o);
        }
        o_store(o, e);
      }
      wait_vm<0>();
      if (lane == 0) __hip_atomic_store(flag(FL_O0, T, j), tag, RLX_AGENT);
    }
    STAMP_AT(6);
    wait_vm<0>();
    if (lane == 0) __hip_atomic_store(flag(FL_O1, T, j), tag, RLX_AGENT);
    STAMP_AT(7);
    res_loads();
    wait_vm2<0>(xres, bres);
    if (blockIdx.x == 0 && wave == 7) {
#pragma unroll 1
      for (int i = 0; i < 4; ++i) poll_flags(flag(FL_KV, 4 * (4 * i + g), lane & 15), tag);
      if (lane == 0) __hip_atomic_store((GAS u64*)(ws + WS_EPOCH), tag, RLX_AGENT);
    }
  }
  bar();
  {
    int t2 = threadIdx.x; PIN(t2);
    const int row = t2 >> 3, ch = t2 & 7, orow = 64 * rt + row, ocol = 64 * ct + ch * 8;
    const unsigned a = lds0 + L_WG + (((row >> 5) * 8 + ch) * 66 + (row & 31)) * 16;
    f32x4 lo = {}, hi = {};
#pragma unroll
    for (int w = 0; w < 4; ++w) {
      lo += __builtin_bit_cast(f32x4, lds128(a + w * PART));
      hi += __builtin_bit_cast(f32x4, lds128(a + w * PART + 512));
    }
    v4u o;
    o[0] = pk2(lo[0] + bflo(xres[0]) + bflo(bres[0]), lo[1] + bfhi(xres[0]) + bfhi(bres[0]));
    o[1] = pk2(lo[2] + bflo(xres[1]) + bflo(bres[1]), lo[3] + bfhi(xres[1]) + bfhi(bres[1]));
    o[2] = pk2(hi[0] + bflo(xres[2]) + bflo(bres[2]), hi[1] + bfhi(xres[2]) + bfhi(bres[2]));
    o[3] = pk2(hi[2] + bflo(xres[3]) + bflo(bres[3]), hi[3] + bfhi(xres[3]) + bfhi(bres[3]));
    *(GAS v4u*)((char*)out + (size_t)orow * 1024 + ocol * 2) = o;
  }
  STAMP_AT(8);
}

extern "C" void kernel_launch(void* const* d_in, const int* in_sizes, int n_in, void* d_out, int out_size, void* d_ws, size_t ws_size,
                              hipStream_t stream) {
  (void)in_sizes; (void)n_in; (void)out_size; (void)ws_size;
  (void)hipFuncSetAttribute((const void*)fused_block, hipFuncAttributeMaxDynamicSharedMemorySize, LDS_BYTES);
  fused_block<<<256, 512, LDS_BYTES, stream>>>(d_in[0], d_in[1], d_in[2], d_in[4], d_in[5], d_in[6], d_in[7], d_out, (char*)d_ws);
}
```

```cpp
#define MIDWARM 1
#include <hip/hip_runtime.h>
#include <hip/hip_bf16.h>
#include <stdint.h>
#include <type_traits>

typedef unsigned v4u __attribute__((ext_vector_type(4)));
typedef unsigned v2u __attribute__((ext_vector_type(2)));
typedef float f32x2 __attribute__((ext_vector_type(2)));
typedef float f32x4 __attribute__((ext_vector_type(4)));
typedef float f32x16 __attribute__((ext_vector_type(16)));
typedef short bf16x8 __attribute__((ext_vector_type(8)));
typedef short s16x4 __attribute__((ext_vector_type(4)));
typedef __bf16 bf16x2_t __attribute__((ext_vector_type(2)));
typedef unsigned long long u64;
#define GAS __attribute__((address_space(1)))
#define LAS __attribute__((address_space(3)))
#define DI __device__ __forceinline__
#define RLX_AGENT __ATOMIC_RELAXED, __HIP_MEMORY_SCOPE_AGENT

constexpr int SEQ = 2048, DM = 512, FF = 1792;
constexpr unsigned WS_EPOCH = 0;
constexpr unsigned WS_STATS = 32768;
constexpr unsigned WS_FLAG = 65536;
constexpr unsigned WS_KV = WS_FLAG + 5 * 1024 * 128;
constexpr int AFT = 145;
constexpr unsigned WS_AF = WS_KV + 16 * 64 * 4096;
constexpr unsigned WS_END = WS_AF + 64 * AFT * 1024;
constexpr unsigned WS_STAMP = WS_END;
enum { FL_KV = 0, FL_H0 = 1, FL_H1 = 2, FL_O0 = 3, FL_O1 = 4 };
constexpr int STG = 36864;
constexpr int L_WA = 0;
constexpr int L_WG = 86016;
constexpr int VB = 1040;
constexpr int KVT = 2048 + 2 * VB;
constexpr int L_KV = 65536 - 8 * KVT;
constexpr int RD = 8, NR = 12;
constexpr int PART = 16 * 66 * 16;
constexpr int L_CS = L_WG + 4 * PART;
constexpr int L_RS = L_CS + 3584;
constexpr int L_CNT = L_RS + 512;
constexpr int LDS_BYTES = L_CNT + 64;
static_assert(L_KV + 12 * KVT <= L_WG && L_WG >= 2 * STG && L_CS >= 3 * STG && PART >= RD * 2048 && LDS_BYTES <= 163840, "lds map");
#define AWIN 6
constexpr int AW = AWIN;
constexpr unsigned SPIN_CAP = 1u << 23;

#define MFMA32(a, b, c) __builtin_amdgcn_mfma_f32_32x32x16_bf16(__builtin_bit_cast(bf16x8, a), __builtin_bit_cast(bf16x8, b), c, 0, 0, 0)
#define MFMA16(a, b, c) __builtin_amdgcn_mfma_f32_16x16x32_bf16(__builtin_bit_cast(bf16x8, a), __builtin_bit_cast(bf16x8, b), c, 0, 0, 0)
#define SBAR() __builtin_amdgcn_sched_barrier(0)

#define STAMP_AT(i) do { } while (0)

DI float asf(unsigned u) { return __builtin_bit_cast(float, u); }
DI unsigned asu(float f) { return __builtin_bit_cast(unsigned, f); }
DI unsigned pk2(float lo, float hi) { return __builtin_bit_cast(unsigned, __builtin_convertvector((f32x2){lo, hi}, bf16x2_t)); }
DI float bflo(unsigned u) { return __builtin_bit_cast(float, u << 16); }
DI float bfhi(unsigned u) { return __builtin_bit_cast(float, u & 0xffff0000u); }
DI v4u make_srd(const void* p, unsigned bytes) { const u64 a = (u64)p; return (v4u){(unsigned)a, (unsigned)(a >> 32) & 0xffffu, bytes, 0x00020000u}; }
DI void dma16(v4u srd, unsigned voff, unsigned soff, unsigned lds) {
  asm volatile("s_mov_b32 m0, %3\n\ts_nop 0\n\tbuffer_load_dwordx4 %0, %1, %2 offen lds" :: "v"(voff), "s"(srd), "s"(soff), "s"(lds) : "memory");
}
DI void dma16_a(v4u srd, unsigned voff, unsigned soff, unsigned lds) { dma16(srd, voff, soff, lds); }
DI void dma16_b(v4u srd, unsigned voff1, unsigned soff) {
  asm volatile("buffer_load_dwordx4 %0, %1, %2 offen offset:1024 lds" :: "v"(voff1), "s"(srd), "s"(soff) : "memory");
}
DI v4u ld16_sc1_async(v4u srd, unsigned voff, unsigned soff) {
  v4u r; asm volatile("buffer_load_dwordx4 %0, %1, %2, %3 offen sc1" : "=v"(r) : "v"(voff), "s"(srd), "s"(soff) : "memory"); return r;
}
DI v4u ld16_async(v4u srd, unsigned voff, unsigned soff) {
  v4u r; asm volatile("buffer_load_dwordx4 %0, %1, %2, %3 offen" : "=v"(r) : "v"(voff), "s"(srd), "s"(soff) : "memory"); return r;
}
DI unsigned ld2_async(v4u srd, unsigned voff, unsigned soff) {
  unsigned r; asm volatile("buffer_load_ushort %0, %1, %2, %3 offen" : "=v"(r) : "v"(voff), "s"(srd), "s"(soff) : "memory"); return r;
}
DI void st16_sc1(v4u v, v4u srd, unsigned voff, unsigned soff) {
  asm volatile("buffer_store_dwordx4 %0, %1, %2, %3 offen sc1\n\ts_nop 1" :: "v"(v), "v"(voff), "s"(srd), "s"(soff) : "memory");
}
template <int N> DI void wait_vm1(v4u& a) { asm volatile("s_waitcnt vmcnt(%1)" : "+v"(a) : "i"(N) : "memory"); }
template <int N> DI void wait_vm2(v4u& a, v4u& b) { asm volatile("s_waitcnt vmcnt(%2)" : "+v"(a), "+v"(b) : "i"(N) : "memory"); }
template <int N> DI void wait_vm3(v4u& a, v4u& b, v4u& c) { asm volatile("s_waitcnt vmcnt(%3)" : "+v"(a), "+v"(b), "+v"(c) : "i"(N) : "memory"); }
template <class V> DI void pin(V& v) { asm volatile("" : "+v"(v)); }
template <int N> DI void wait_vm() { asm volatile("s_waitcnt vmcnt(%0)" :: "i"(N) : "memory"); }
DI void bar() { asm volatile("s_waitcnt lgkmcnt(0)\n\ts_barrier" ::: "memory"); }
DI v4u lds128(unsigned a) { return *(const LAS v4u*)(size_t)a; }
DI void sts128(unsigned a, v4u v) { *(LAS v4u*)(size_t)a = v; }
DI v2u ldtr(unsigned a) { return __builtin_bit_cast(v2u, __builtin_amdgcn_ds_read_tr16_b64_v4i16((LAS s16x4*)(size_t)a)); }
DI v4u ldtr2(unsigned a, int second) { const v2u lo = ldtr(a), hi = ldtr(a + second); return (v4u){lo.x, lo.y, hi.x, hi.y}; }
template <int CTRL> DI float dpp(float v) { return __builtin_bit_cast(float, __builtin_amdgcn_update_dpp(0, __builtin_bit_cast(int, v), CTRL, 0xf, 0xf, true)); }
DI float rdl(float v, int l) { return __builtin_bit_cast(float, __builtin_amdgcn_readlane(__builtin_bit_cast(int, v), l)); }
DI v4u pack8(const f32x16& a, int s) { return (v4u){pk2(a[8 * s], a[8 * s + 1]), pk2(a[8 * s + 2], a[8 * s + 3]), pk2(a[8 * s + 4], a[8 * s + 5]), pk2(a[8 * s + 6], a[8 * s + 7])}; }
DI void poll_flags(const GAS u64* p, u64 tag) {
#pragma unroll 1
  for (unsigned spin = 0; spin < SPIN_CAP; ++spin) {
    const u64 v = __hip_atomic_load(p, RLX_AGENT);
    if (__all(v == tag)) break;
    __builtin_amdgcn_s_sleep(2);
  }
  asm volatile("" ::: "memory");
}
constexpr u64 upper_mask(int reg) {
  u64 m = 0;
  for (int l = 0; l < 64; ++l) { const int row = (reg & 3) + 8 * (reg >> 2) + 4 * (l >> 5); if (row > (l & 31)) m |= 1ull << l; }
  return m;
}
template <int REG> DI bool upper() { return __builtin_amdgcn_inverse_ballot_w64(upper_mask(REG)); }
template <int I, int N, class Fn> DI void static_for(Fn&& f) { if constexpr (I < N) { f(std::integral_constant<int, I>{}); static_for<I + 1, N>(f); } }
DI float max3(float a, float b, float c) { return __builtin_fmaxf(__builtin_fmaxf(a, b), c); }

constexpr int H1_CK = 16 - AW, H1_LD = H1_CK - 3, O_CK = 28 - AW, O_LD = O_CK - 3;
constexpr int g2_issued(int t) { return ((t >= 1 && t - 1 + NR + RD < 36) ? 2 : 0) + ((t + AW < 28) ? 2 : 0) + (t == H1_LD) + (t == O_LD) + (t == O_CK ? 10 : 0); }
constexpr int g2_range(int a, int b) { int n = 0; for (int t = a; t < b; ++t) n += g2_issued(t); return n; }
#define GELU_PK(P) (P == 1)
#define PRIO_G2 2
#define PRIO_ATT 3
#define PRIO_G1 2
#define ROT r
#define CODEWARM 39000
#define KVPUB 6
#define EXP2(v) __builtin_amdgcn_exp2f(v)
#define PIN(v) pin(v)
extern "C" __device__ float mul_unpacked(float, float) __asm("llvm.amdgcn.fmul.legacy");
#define MUL1(a, b) mul_unpacked(a, b)
#define FMA1(a, b, c) __builtin_amdgcn_div_fmasf(a, b, c, false)
DI v2u ld8_sc1_async(v4u srd, unsigned voff) {
  v2u r; asm volatile("buffer_load_dwordx2 %0, %1, %2, 0 offen sc1" : "=v"(r) : "v"(voff), "s"(srd) : "memory"); return r;
}
template <int N> DI void wait_vmf(v2u& a) { asm volatile("s_waitcnt vmcnt(%1)" : "+v"(a) : "i"(N) : "memory"); }
DI void arrive(unsigned cnt, int lane) {
  asm volatile("s_waitcnt lgkmcnt(0)" ::: "memory");
  if (lane == 0) __hip_atomic_fetch_add((LAS unsigned*)(size_t)cnt, 1u, __ATOMIC_RELAXED, __HIP_MEMORY_SCOPE_WORKGROUP);
}
DI void arrive_nowait(unsigned cnt) {
  asm volatile("s_mov_b64 exec, 1\n\tds_add_u32 %0, %1\n\ts_mov_b64 exec, -1" :: "v"(cnt), "v"(1u) : "memory");
}
DI unsigned peek(unsigned cnt) { return __hip_atomic_load((LAS unsigned*)(size_t)cnt, __ATOMIC_RELAXED, __HIP_MEMORY_SCOPE_WORKGROUP); }
DI void await(unsigned cnt, unsigned n) {
#pragma unroll 1
  for (unsigned spin = 0; spin < SPIN_CAP * 32; ++spin) {
    if (__builtin_amdgcn_readfirstlane(peek(cnt)) >= n) break;
    __builtin_amdgcn_s_sleep(1);
  }
  asm volatile("" ::: "memory");
}

__global__ __launch_bounds__(512, 2) void fused_block(const void* __restrict__ x, const void* __restrict__ wqkv, const void* __restrict__ wo,
                                                      const void* __restrict__ w1, const void* __restrict__ b1, const void* __restrict__ w2,
                                                      const void* __restrict__ b2, void* __restrict__ out, char* __restrict__ ws) {
  constexpr unsigned lds0 = 0;
  const int wave = __builtin_amdgcn_readfirstlane(threadIdx.x >> 6);
  const int m = wave & 3;
  const bool isG = wave < 4;
  const int xcd = blockIdx.x & 7, idx = blockIdx.x >> 3;
  const int r = (xcd >> 2) * 8 + (idx >> 2), j = (xcd & 3) * 4 + (idx & 3);
  const int rt = (xcd >> 1) * 8 + (idx >> 2), ct = (xcd & 1) * 4 + (idx & 3);
  int lane = threadIdx.x & 63, r32, h, g, q4, p4;
  STAMP_AT(0);
  asm volatile("" :: "s"(x), "s"(wqkv), "s"(wo), "s"(w1), "s"(b1), "s"(w2), "s"(b2), "s"(out), "s"(ws));

  const v4u sx = make_srd(x, SEQ * DM * 2), sws = make_srd(ws, WS_END);
  const __amdgpu_buffer_rsrc_t rws = __builtin_amdgcn_make_buffer_rsrc(ws, 0, WS_END, 0x00020000);
  const unsigned one2 = 0x3f803f80u;
  const int T = 4 * r + m;
  const int rot = ROT;
  auto flag_off = [&](int kind, int tile, int jj) { return WS_FLAG + ((kind * 64 + tile) * 16 + jj) * 128; };
  auto flag = [&](int kind, int tile, int jj) { return (GAS u64*)(ws + flag_off(kind, tile, jj)); };
  u64 tag;
  unsigned taglo, tag32;
  auto get_tag = [&]() {
    const v2u tg = *(const LAS v2u*)(size_t)(lds0 + L_CNT + 32);
    taglo = __builtin_amdgcn_readfirstlane(tg[0]); tag32 = __builtin_amdgcn_readfirstlane(tg[1]);
    tag = ((u64)tag32 << 32) | taglo;
  };
  v4u xres, bres, xaug, st;
  float mean, var, rstd, sd;
  v4u sel;
  unsigned xo[4], swz;
  auto lane_setup = [&]() {
    lane = threadIdx.x & 63; PIN(lane);
    r32 = lane & 31; h = lane >> 5; g = lane >> 4; q4 = (lane >> 2) & 3; p4 = lane & 3;
    const unsigned selv = ((lane & 15) == (g & 1)) ? one2 : 0u;
    sel = (v4u){selv, selv, selv, selv};
#pragma unroll
    for (int p = 0; p < 4; ++p) xo[p] = (32 * m + r32) * 128 + (((2 * ((p + m) & 3) + h) ^ ((r32 >> 1) & 7)) * 16);
    swz = ((((lane >> 2) & 1) ^ (g & 1)) * 64) + p4 * 16;
  };

  auto stat_load = [&]() { st = ld16_sc1_async(sws, WS_STATS + (128 * r + 32 * m + r32) * 16, 0); };
  auto res_loads = [&]() {
    int t2 = threadIdx.x; PIN(t2);
    const int row = 64 * rt + (t2 >> 3), col = 64 * ct + (t2 & 7) * 8;
    xres = ld16_async(sx, row * 1024 + col * 2, 0);
    bres = ld16_async(make_srd(b2, DM * 2), col * 2, 0);
  };
  auto row_stats = [&]() {
    if (!__all(st[1] == tag32 && st[3] == tag32)) {
#pragma unroll 1
      for (unsigned spin = 0; spin < SPIN_CAP; ++spin) {
        st = ld16_sc1_async(sws, WS_STATS + (128 * r + 32 * m + r32) * 16, 0);
        wait_vm1<0>(st);
        if (__all(st[1] == tag32 && st[3] == tag32)) break;
      }
    }
    mean = asf(st[0]); var = asf(st[2]);
    PIN(mean); PIN(var);
  };
  auto make_xaug = [&]() {
    PIN(mean); PIN(var);
    rstd = __builtin_amdgcn_rsqf(var); sd = var * rstd;
    const unsigned hi = pk2(-mean, sd);
    const unsigned lo = pk2(-mean - bflo(hi), FMA1(bfhi(hi), -1.f, sd));
    xaug = (v4u){(hi & 0xffffu) | (hi << 16), (lo & 0xffffu) | (hi & 0xffff0000u), lo >> 16, 0u};
    if (h) xaug = (v4u){0u, 0u, 0u, 0u};
    PIN(xaug);
  };
  auto cswr = [&](int tile, f32x4 c) {
    if (lane < 16) { LAS float* p = (LAS float*)(size_t)(lds0 + L_CS + (tile * 32 + lane) * 16 + m * 4); p[0] = c[0]; p[64] = c[1]; }
  };
  auto csrd = [&](int tile) { return __builtin_bit_cast(f32x4, lds128(lds0 + L_CS + (tile * 32 + r32) * 16)); };
  auto waug = [&](f32x4 c4, unsigned b16) {
    const float c = ((c4[0] + c4[1]) + c4[2]) + c4[3];
    const unsigned hi = pk2(c, 0.f);
    const unsigned lo = pk2(c - bflo(hi), 0.f);
    return (v4u){hi | (lo << 16), hi | (b16 << 16), b16, 0u};
  };

  const v4u sw1 = make_srd(w1, DM * FF * 2);
  const int hcol0 = 448 * (j >> 2) + 128 * (j & 3);
  const bool heavy = (j & 3) != 3;
  unsigned bias0, bias1;
  auto bias_load = [&](int P) {
    bias0 = ld2_async(make_srd(b1, FF * 2), r32 * 2, (hcol0 + 64 * P) * 2);
    bias1 = ld2_async(make_srd(b1, FF * 2), r32 * 2, (hcol0 + 64 * P + 32) * 2);
  };
  auto w1dma = [&](int P, int c, unsigned dst, int half) {
    const unsigned vw1 = (lane >> 3) * (FF * 2) + swz, vw1b = vw1 + 8 * (FF * 2) - 1024;
    const unsigned so = (64 * ((c + rot) & 7) + 16 * m) * (FF * 2) + (hcol0 + 64 * P) * 2;
    if (half == 0) dma16_a(sw1, vw1, so, dst + m * 2048); else dma16_b(sw1, vw1b, so);
  };
  f32x16 hac[2];
  unsigned rk[4][2];
  auto hsetup = [&](int P) {
#pragma unroll
    for (int p = 0; p < 4; ++p)
#pragma unroll
      for (int c = 0; c < 2; ++c) {
        rk[p][c] = lds0 + (P ? L_WA : 28672) + ((p + m) & 3) * 2048 + h * 1024 + q4 * 128 + ((c ^ (q4 >> 1)) * 64) + (g & 1) * 32 + p4 * 8;
        PIN(rk[p][c]);
      }
  };
  auto hmm = [&](auto pc, const v4u (&xf)[32], auto&& hook) {
    constexpr int P = pc.value;
    constexpr int D = 4;
    const unsigned cnt = lds0 + L_CNT;
    f32x4 cs[2] = {}, c0, c1;
    v4u wf[D][2], au0, au1;
    unsigned seen = 0;
    hac[0] = (f32x16){}; hac[1] = (f32x16){};
    auto rd = [&](int ks, int t) { wf[ks % D][t] = ldtr2(rk[ks & 3][t] + (ks >> 2) * 8192, 512); };
#pragma unroll
    for (int ks = 0; ks < D; ++ks) { rd(ks, 0); rd(ks, 1); }
    SBAR();
    static_for<0, 32>([&](auto kc) {
      constexpr int ks = kc.value;
      hook(kc);
      if constexpr (ks == 10) seen = peek(cnt + 20);
      if constexpr (ks == 29) seen = peek(cnt);
#pragma unroll
      for (int t = 0; t < 2; ++t) {
        hac[t] = MFMA32(wf[ks % D][t], xf[ks], hac[t]);
        if ((ks & 3) == 0 && ks < 28) { cs[t] = MFMA16(sel, wf[ks % D][t], cs[t]); PIN(cs[t]); }
        if (ks == 26) { cs[t] = MFMA16(sel, wf[28 % D][t], cs[t]); PIN(cs[t]); }
        SBAR();
        if constexpr (ks + D < 32) rd(ks + D, t);
        if constexpr (ks == 31) { if (t == 0) { au0 = waug(c0, bias0); PIN(au0); } else { au1 = waug(c1, bias1); PIN(au1); } }
        SBAR();
      }
      if constexpr (ks == 11 || ks == 30) {
        PIN(hac[0]); PIN(hac[1]);
        if (__builtin_amdgcn_readfirstlane(seen) < 4) await(ks == 11 ? cnt + 20 : cnt, 4);
        asm volatile("" ::: "memory");
        if constexpr (ks == 30) { c0 = csrd(2 * P); c1 = csrd(2 * P + 1); }
      }
      if constexpr (ks == 27) { cswr(2 * P, cs[0]); cswr(2 * P + 1, cs[1]); arrive_nowait(cnt); }
      SBAR();
    });
    hac[0] = MFMA32(au0, xaug, hac[0]);
    hac[1] = MFMA32(au1, xaug, hac[1]);
    SBAR();
  };
  auto hgelu = [&](auto pc, auto&& mid) {
    constexpr int P = pc.value;
    constexpr bool PK = GELU_PK(P);
    const float C0 = 2.3022081986f, C1 = C0 * 0.044715f;
    const float ga = -C1 * rstd * rstd * rstd, gb = -C0 * rstd;
    auto mul = [&](f32x2 a, f32x2 b) { return PK ? a * b : (f32x2){MUL1(a[0], b[0]), MUL1(a[1], b[1])}; };
    auto fma = [&](f32x2 a, float b, float c) {
      return PK ? __builtin_elementwise_fma(a, (f32x2){b, b}, (f32x2){c, c}) : (f32x2){FMA1(a[0], b, c), FMA1(a[1], b, c)}; };
#pragma unroll
    for (int t = 0; t < 2; ++t) {
      f32x2 v[8], z[8];
#pragma unroll
      for (int e = 0; e < 8; ++e) { v[e] = (f32x2){hac[t][2 * e], hac[t][2 * e + 1]}; z[e] = mul(v[e], v[e]); }
      SBAR();
#pragma unroll
      for (int e = 0; e < 8; ++e) z[e] = fma(z[e], ga, gb);
      SBAR();
#pragma unroll
      for (int e = 0; e < 8; ++e) z[e] = mul(z[e], v[e]);
      SBAR();
#pragma unroll
      for (int e = 0; e < 8; ++e) z[e] = (f32x2){EXP2(z[e][0]), EXP2(z[e][1])};
      SBAR();
#pragma unroll
      for (int e = 0; e < 8; ++e) z[e] = fma(z[e], sd, sd);
      SBAR();
#pragma unroll
      for (int e = 0; e < 8; ++e) z[e] = (f32x2){__builtin_amdgcn_rcpf(z[e][0]), __builtin_amdgcn_rcpf(z[e][1])};
      SBAR();
#pragma unroll
      for (int e = 0; e < 8; ++e) z[e] = mul(z[e], v[e]);
      SBAR();
#pragma unroll
      for (int s = 0; s < 2; ++s) {
        const v4u f = {pk2(z[4 * s][0], z[4 * s][1]), pk2(z[4 * s + 1][0], z[4 * s + 1][1]), pk2(z[4 * s + 2][0], z[4 * s + 2][1]), pk2(z[4 * s + 3][0], z[4 * s + 3][1])};
        st16_sc1(f, sws, lane * 16, WS_AF + (T * AFT + (hcol0 >> 4) + 4 * P + 2 * t + s) * 1024);
      }
      if (t == 0) mid();
      SBAR();
    }
  };

  if (isG) {
    lane_setup();
    const v4u sw2 = make_srd(w2, FF * DM * 2), swo = make_srd(wo, DM * DM * 2);
    const unsigned vw2 = (lane >> 3) * 1024 + swz, vw2b = vw2 + 7 * 1024;
    const int ro3 = rt % 3;
    auto kmap = [&](int kk) {
      if (kk < 16) return 8 * (((kk >> 2) + rt) & 3) + (kk & 3);
      const int p = ((kk - 16) >> 2) + ro3;
      return 8 * (p >= 3 ? p - 3 : p) + 4 + (kk & 3);
    };
    auto omap = [&](int i) { return 2 * ((i + rt) & 3) + (i >> 2); };
    auto wdmah = [&](int kk, int half) {
      const unsigned so = (kk < 28 ? 448 * m + 16 * kmap(kk) : 128 * m + 16 * omap(kk - 28)) * 1024 + 128 * ct;
      if (half == 0) dma16_a(kk < 28 ? sw2 : swo, vw2, so, lds0 + L_WG + m * PART + (kk % RD) * 2048); else dma16_b(kk < 28 ? sw2 : swo, vw2b, so);
    };
    auto wdma = [&](int kk) { wdmah(kk, 0); wdmah(kk, 1); };
    {
      const v4u swq = make_srd(wqkv, DM * 3 * DM * 2);
      const unsigned vx0 = (lane >> 3) * 1024 + (((lane & 7) ^ g) * 16), vx1 = (7 + (lane >> 3)) * 1024 + (((lane & 7) ^ (4 + g)) * 16);
      const unsigned vwq = (lane >> 2) * (3 * DM * 2) + p4 * 16;
      auto piece = [&](int s, int i) {
        const unsigned buf = lds0 + (s % 3) * STG;
        const int kc = (s + rot) & 7;
        if (i < 4) {
          const unsigned so = (128 * r + 32 * m + 16 * (i >> 1)) * 1024 + kc * 128;
          if (i & 1) dma16_b(sx, vx1, so); else dma16_a(sx, vx0, so, buf + m * 4096 + (i >> 1) * 2048);
        } else if (i < 7) dma16(swq, vwq, (64 * kc + 16 * m) * (3 * DM * 2) + (512 * (i - 4) + 32 * j) * 2, buf + 16384 + ((i - 4) * 4 + m) * 1024);
        else w1dma(0, s, buf + 28672, i - 7);
      };
#pragma unroll
      for (int i = 0; i < 18; ++i) { piece(i / 9, i % 9); if (i == 8) __builtin_amdgcn_s_barrier(); }
      __builtin_amdgcn_s_setprio(PRIO_G1);
      hsetup(0);
      f32x4 cs[2] = {};
      v4u xf[4], wf[4][2];
      hac[0] = (f32x16){}; hac[1] = (f32x16){};
      static_for<0, 8>([&](auto sc) {
        constexpr int s = sc.value;
        constexpr unsigned buf = (s % 3) * STG;
        auto fill = [&](int i) {
          if constexpr (s < 6) piece(s + 2, i);
          if constexpr (s == 6) { if (i == 0) { stat_load(); bias_load(0); } else wdmah((i - 1) >> 1, (i - 1) & 1); }
          if constexpr (s == 7) { if (heavy) { w1dma(1, i, lds0 + L_WA + i * 8192, 0); w1dma(1, i, 0, 1); } }
          SBAR();
        };
        auto rd = [&](int p) { xf[p] = lds128(buf + xo[p]); wf[p][0] = ldtr2(buf + rk[p][0], 512); wf[p][1] = ldtr2(buf + rk[p][1], 512); SBAR(); };
        if constexpr (s < 7) wait_vm<9>(); else { wait_vm1<8>(st); PIN(bias0); PIN(bias1); }
        bar();
        STAMP_AT(10 + s);
        rd(0);
        if constexpr (s < 7) fill(0);
        rd(1);
        if constexpr (s < 7) fill(1);
        if constexpr (s == 2) get_tag();
#pragma unroll
        for (int p = 0; p < 4; ++p) {
#pragma unroll
          for (int t = 0; t < 2; ++t) {
            hac[t] = MFMA32(wf[p][t], xf[p], hac[t]);
            if (p == 0) { cs[t] = MFMA16(sel, wf[p][t], cs[t]); PIN(cs[t]); }
            SBAR();
            if (p < 2 && t == 0) rd(p + 2);
            if (s == 7) fill(2 * p + t); else if (2 * p + t < 7) fill(2 + 2 * p + t);
          }
          if (s == 7 && p == 1) {
            cswr(0, cs[0]); cswr(1, cs[1]);
            bar();
            STAMP_AT(1);
          }
        }
        if constexpr (s == 7) {
          const f32x4 c0 = csrd(0), c1 = csrd(1);
          row_stats(); make_xaug();
          hac[0] = MFMA32(waug(c0, bias0), xaug, hac[0]);
          hac[1] = MFMA32(waug(c1, bias1), xaug, hac[1]);
          SBAR();
        }
      });
    }
    STAMP_AT(18);
    if (heavy) { wait_vm<8>(); arrive_nowait(lds0 + L_CNT + 16); }
    hgelu(std::integral_constant<int, 0>{}, [&]() {
      wait_vm<2>();
      if (heavy) arrive_nowait(lds0 + L_CNT + 20);
    });
    __builtin_amdgcn_s_setprio(0);
    STAMP_AT(19);
    unsigned rb2[2];
#pragma unroll
    for (int c = 0; c < 2; ++c) rb2[c] = lds0 + L_WG + m * PART + h * 512 + q4 * 128 + ((c ^ (q4 >> 1)) * 64) + (g & 1) * 32 + p4 * 8;
    v4u wq[36][2];
    auto wrd = [&](int kk) {
#pragma unroll
      for (int c = 0; c < 2; ++c) wq[kk][c] = ldtr2(rb2[c] + (kk % RD) * 2048, 1024);
    };
    auto wgot = [&](int kk) { PIN(wq[kk][0]); PIN(wq[kk][1]); };
    const int ftile = 2 * rt + ((lane >> 2) & 1);
    v4u af[28][2], of[8][2];
    v2u fl;
    const unsigned a0 = WS_AF + (2 * rt * AFT + 28 * m) * 1024, o0 = WS_AF + (2 * rt * AFT + 112 + 8 * m) * 1024;
    for (int kk = 0; kk < 4; ++kk) wrd(kk);
    for (int kk = 4; kk < 8; ++kk) wdma(kk);
    for (int kk = 0; kk < 4; ++kk) wgot(kk);
    wait_vm<8>();
    if (lane == 0) __hip_atomic_store(flag(FL_H0, T, j), tag, RLX_AGENT);
    STAMP_AT(2);
    for (int kk = 8; kk < 12; ++kk) wdma(kk);
    static_for<1, 3>([&](auto ic) {
      constexpr int i = ic.value;
      wait_vm<(i == 1) ? 9 : 8>();
      for (int kk = 4 * i; kk < 4 * i + 4; ++kk) wrd(kk);
      for (int kk = 4 * i; kk < 4 * i + 4; ++kk) wgot(kk);
      if constexpr (i == 2) fl = ld8_sc1_async(sws, flag_off(FL_H0, ftile, 4 * m + p4));
      for (int kk = 4 * i; kk < 4 * i + 4; ++kk) wdma(kk + RD);
    });
    STAMP_AT(3);
    wait_vmf<8>(fl);
    if (!__all(fl[0] == taglo && fl[1] == tag32)) poll_flags(flag(FL_H0, ftile, 4 * m + p4), tag);
    STAMP_AT(4);
#pragma unroll
    for (int kk = 0; kk < AW; ++kk) {
      af[kk][0] = ld16_sc1_async(sws, lane * 16, a0 + kmap(kk) * 1024);
      af[kk][1] = ld16_sc1_async(sws, lane * 16, a0 + (AFT + kmap(kk)) * 1024);
    }
    __builtin_amdgcn_s_setprio(PRIO_G2);
    f32x16 c2[2][2] = {};
    auto mm2 = [&](int kk, const v4u (&a)[2], auto&& fill) {
      SBAR();
#pragma unroll
      for (int i = 0; i < 4; ++i) { c2[i >> 1][i & 1] = MFMA32(wq[kk][i & 1], a[i >> 1], c2[i >> 1][i & 1]); SBAR(); fill(i); SBAR(); }
    };
    static_for<0, 28>([&](auto kc) {
      constexpr int kk = kc.value;
      wait_vm2<(kk < AW) ? 2 * (AW - 1 - kk) + g2_range(0, kk) : g2_range(kk - AW + 1, kk)>(af[kk][0], af[kk][1]);
      if constexpr (kk == H1_CK) {
        STAMP_AT(20);
        wait_vmf<g2_range(H1_LD, H1_CK) - 1>(fl);
        if (!__all(fl[0] == taglo && fl[1] == tag32)) poll_flags(flag(FL_H1, ftile, 4 * m + p4), tag);
        STAMP_AT(21);
      }
      if constexpr (kk == H1_LD) fl = ld8_sc1_async(sws, flag_off(FL_H1, ftile, 4 * m + p4));
      if constexpr (kk == O_LD) fl = ld8_sc1_async(sws, flag_off(FL_O0, ftile, 4 * m + p4));
      constexpr bool dw = kk >= 1 && kk - 1 + NR + RD < 36;
      if constexpr (dw) wgot(kk - 1 + NR);
      mm2(kk, af[kk], [&](int i) {
        if constexpr (dw) { if (i < 2) wdmah(kk - 1 + NR + RD, i); }
        if constexpr (kk + NR < 28) { if (i == 1) wrd(kk + NR); }
        if constexpr (kk == 27) { if (i == 0) wrd(28); }
        if constexpr (kk + AW < 28) { if (i >= 2) af[kk + AW][i - 2] = ld16_sc1_async(sws, lane * 16, a0 + (AFT * (i - 2) + kmap(kk + AW)) * 1024); }
      });
      if constexpr (kk == O_CK) {
        wait_vmf<g2_range(O_LD, O_CK) - 1>(fl);
        if (!__all(fl[0] == taglo && fl[1] == tag32)) poll_flags(flag(FL_O0, ftile, 4 * m + p4), tag);
        res_loads();
#pragma unroll
        for (int i = 0; i < 4; ++i) {
          of[i][0] = ld16_sc1_async(sws, lane * 16, o0 + omap(i) * 1024);
          of[i][1] = ld16_sc1_async(sws, lane * 16, o0 + (AFT + omap(i)) * 1024);
        }
      }
    });
    static_for<0, 8>([&](auto kc) {
      constexpr int kk = kc.value;
      if (kk < 7) wrd(28 + kk + 1);
      if constexpr (kk == 1) {
        STAMP_AT(5);
        poll_flags(flag(FL_O1, ftile, 4 * m + p4), tag);
        STAMP_AT(6);
#pragma unroll
        for (int i = 4; i < 8; ++i) {
          of[i][0] = ld16_sc1_async(sws, lane * 16, o0 + omap(i) * 1024);
          of[i][1] = ld16_sc1_async(sws, lane * 16, o0 + (AFT + omap(i)) * 1024);
        }
      }
      wait_vm2<(kk == 0) ? 6 : 14 - 2 * kk>(of[kk][0], of[kk][1]);
      mm2(28 + kk, of[kk], [&](int) {});
    });
    PIN(xres); PIN(bres);
    asm volatile("" ::: "memory");
#pragma unroll
    for (int mt = 0; mt < 2; ++mt)
#pragma unroll
      for (int c = 0; c < 2; ++c)
#pragma unroll
        for (int qd = 0; qd < 4; ++qd) {
          const f32x4 v = {c2[mt][c][4 * qd], c2[mt][c][4 * qd + 1], c2[mt][c][4 * qd + 2], c2[mt][c][4 * qd + 3]};
          sts128(lds0 + L_WG + m * PART + ((mt * 8 + c * 4 + qd) * 66 + lane) * 16, __builtin_bit_cast(v4u, v));
        }
    STAMP_AT(7);
  } else {
    __builtin_amdgcn_s_setprio(1);
    lane_setup();
    v4u xr[2];
    xr[0] = ld16_async(sx, lane * 16, (128 * r + 8 * j + 2 * m) * 1024); xr[1] = ld16_async(sx, lane * 16, (128 * r + 8 * j + 2 * m + 1) * 1024);
    unsigned warm;
    {
      u64 pc; asm volatile("s_getpc_b64 %0" : "=s"(pc));
      warm = ld2_async(make_srd((const void*)(pc & ~127ull), CODEWARM), lane < 3 ? lane * 128 : 0x40000000u, (idx * 4 + m) * 384);
    }
    __builtin_amdgcn_s_barrier();
    v2u ep = ld8_sc1_async(sws, m == 0 ? WS_EPOCH : 0x40000000u);
    float sval;
    const int wi = blockIdx.x * 4 + m;
    const u64 wbase = wi < 448 ? (u64)w1 + wi * 4096 : wi < 896 ? (u64)w2 + (wi - 448) * 4096 : (u64)wo + (wi - 896) * 4096;
    auto make_stats = [&]() {
      wait_vm2<1>(xr[0], xr[1]);
      PIN(warm);
      float piv[2], a[4];
#pragma unroll
      for (int u = 0; u < 2; ++u) {
        piv[u] = rdl(bflo(xr[u][0]), 0);
        float s1 = 0.f, s2 = 0.f;
#pragma unroll
        for (int i = 0; i < 8; ++i) { const float d = ((i & 1) ? bfhi(xr[u][i >> 1]) : bflo(xr[u][i >> 1])) - piv[u]; s1 += d; s2 = __builtin_fmaf(d, d, s2); }
        a[2 * u] = s1; a[2 * u + 1] = s2;
      }
#pragma unroll
      for (int i = 0; i < 4; ++i) a[i] += dpp<0xB1>(a[i]);
#pragma unroll
      for (int i = 0; i < 4; ++i) a[i] += dpp<0x4E>(a[i]);
      float t = p4 == 0 ? a[0] : p4 == 1 ? a[1] : p4 == 2 ? a[2] : a[3];
      t += dpp<0x114>(t); t += dpp<0x118>(t);
      const auto s16 = __builtin_amdgcn_permlane16_swap(asu(t), asu(t), false, false);
      t = asf(s16[0]) + asf(s16[1]);
      const auto s32 = __builtin_amdgcn_permlane32_swap(asu(t), asu(t), false, false);
      t = (asf(s32[0]) + asf(s32[1])) * (1.f / DM);
      const float e1 = dpp<0xA0>(t);
      sval = (p4 & 1) ? __builtin_fmaxf(__builtin_fmaf(-e1, e1, t), 0.f) + 1e-5f : t + (p4 ? piv[1] : piv[0]);
      PIN(sval);
    };
    const unsigned acnt = lds0 + L_CNT;
    const unsigned vpos = 2048 + (g & 1) * 512 + h * 256 + (lane & 15) * 16;
    v4u qf[2], am0, am1;
    {
      v4u xf[32];
      f32x16 acc[3] = {};
      f32x4 cs[3] = {};
      v4u wf[4][3];
      unsigned wb[4];
#pragma unroll
      for (int p = 0; p < 4; ++p) wb[p] = 16384 + ((p + m) & 3) * 1024 + (8 * h + q4) * 64 + (g & 1) * 32 + p4 * 8;
      auto rd = [&](int ks, int t) {
        const unsigned buf = lds0 + ((ks >> 2) % 3) * STG;
        if (t == 0) xf[ks] = lds128(buf + xo[ks & 3]);
        wf[ks & 3][t] = ldtr2(buf + wb[ks & 3] + t * 4096, 256);
      };
      auto mm = [&](int ks, int t) {
        acc[t] = t == 2 ? MFMA32(xf[ks], wf[ks & 3][t], acc[t]) : MFMA32(wf[ks & 3][t], xf[ks], acc[t]);
        if ((ks & 3) == 0) { cs[t] = MFMA16(sel, wf[ks & 3][t], cs[t]); PIN(cs[t]); }
        SBAR();
      };
      make_stats();
      bar();
      STAMP_AT(10);
#pragma unroll
      for (int t = 0; t < 6; ++t) rd(t / 3, t % 3);
      SBAR();
      static_for<0, 8>([&](auto sc) {
        constexpr int s = sc.value;
#pragma unroll
        for (int p = 0; p < 2; ++p) {
#pragma unroll
          for (int t = 0; t < 3; ++t) { mm(4 * s + p, t); rd(4 * s + p + 2, t); SBAR(); }
          if (s == 7 && p == 0) { cswr(4, cs[0]); cswr(5, cs[1]); cswr(6, cs[2]); SBAR(); }
        }
        if constexpr (s == 0) {
          wait_vmf<0>(ep);
          if (m == 0 && lane < 10)
            *(LAS unsigned*)(size_t)(lds0 + L_CNT + lane * 4) = lane == 8 ? ep[0] * 0x9E3779B1u + 0x7F4A7C15u : lane == 9 ? ep[1] * 0x85EBCA77u + 0x165667B1u : 0u;
        }
        if constexpr (s == 1) {
          get_tag();
          if ((lane >> 2) == 3) {
            const v2u gr = {asu(sval), tag32};
            __builtin_amdgcn_raw_buffer_store_b64(gr, rws, WS_STATS + (128 * r + 8 * j + 2 * m) * 16 + p4 * 8, 0, 16);
          }
        }
        if constexpr (s == MIDWARM) {
          warm = ld2_async(make_srd((const void*)wbase, 4096), lane * 128, 0);
        }
        if constexpr (s == 5) stat_load();
        if constexpr (s == 6) {
          wait_vm1<0>(st);
          PIN(warm);

          row_stats();
        }
        mm(4 * s + 2, 0); mm(4 * s + 2, 1);
        bar();
        if constexpr (s < 7) {
          STAMP_AT(11 + s);
          mm(4 * s + 2, 2);
          rd(4 * s + 4, 0); rd(4 * s + 4, 1); rd(4 * s + 4, 2); SBAR();
#pragma unroll
          for (int t = 0; t < 3; ++t) { mm(4 * s + 3, t); rd(4 * s + 5, t); SBAR(); }
        }
      });
      STAMP_AT(1);
      __builtin_amdgcn_s_setprio(3);
      const f32x4 c5 = csrd(5), c6 = csrd(6), c4 = csrd(4);
      SBAR();
      mm(30, 2); mm(31, 1);
      make_xaug();
      SBAR();
      mm(31, 2); mm(31, 0);
      if (h == 0) *(LAS float*)(size_t)(lds0 + L_RS + (m * 32 + r32) * 4) = rstd;
      acc[1] = MFMA32(waug(c5, 0u), xaug, acc[1]);
      acc[2] = MFMA32(xaug, waug(c6, 0u), acc[2]);
      acc[0] = MFMA32(waug(c4, 0u), xaug, acc[0]);
      SBAR();
      f32x4 rs[4];
#pragma unroll
      for (int qd = 0; qd < 4; ++qd) rs[qd] = __builtin_bit_cast(f32x4, lds128(lds0 + L_RS + (m * 32 + 8 * qd + 4 * h) * 4));
      const unsigned kvo = WS_KV + (j * 64 + T) * 4096;
#pragma unroll
      for (int e = 0; e < 16; ++e) acc[1][e] = MUL1(acc[1][e], rstd);
#pragma unroll
      for (int e = 0; e < 2; ++e) {
        const v4u f = pack8(acc[1], e);
        st16_sc1(f, sws, lane * 16, kvo + e * 1024);
        sts128(lds0 + L_KV + (8 + m) * KVT + e * 1024 + lane * 16, f);
      }
#pragma unroll
      for (int qd = 0; qd < 4; ++qd)
#pragma unroll
        for (int e = 0; e < 4; ++e) acc[2][4 * qd + e] = MUL1(acc[2][4 * qd + e], rs[qd][e]);
#pragma unroll
      for (int s = 0; s < 2; ++s) {
        const v4u f = pack8(acc[2], s);
        st16_sc1(f, sws, lane * 16, kvo + 2048 + s * 1024);
        sts128(lds0 + L_KV + (8 + m) * KVT + s * VB + vpos, f);
      }
      SBAR();
      const float qs = rstd * (0.25f * 1.4426950408889634f);
#pragma unroll
      for (int e = 0; e < 16; ++e) acc[0][e] = MUL1(acc[0][e], qs);
      qf[0] = pack8(acc[0], 0); qf[1] = pack8(acc[0], 1);
      PIN(qf[0]); PIN(qf[1]);
      STAMP_AT(2);
      const int T0 = 4 * r - 8 + 2 * m;
      const bool live = T0 >= 0;
      auto kv_load = [&](v4u (&kv)[8], int i0, int i1) {
        const unsigned src = WS_KV + (j * 64 + (live ? T0 : 0)) * 4096, vo = live ? lane * 16 : h * 512;
#pragma unroll
        for (int i = i0; i < i1; ++i) kv[i] = ld16_sc1_async(sws, vo, src + (live ? i : (i & 3)) * 1024);
      };
      auto kv_stage = [&](v4u (&kv)[8]) {
        const unsigned on = (lane < 2 || live) ? one2 : 0u;
        if (lane < 6) sts128(lds0 + L_KV + (lane < 2 ? 8 + m : 2 * m + ((lane - 2) >> 1)) * KVT + 2048 + (lane & 1) * VB + 1024, (v4u){on, on, on, on});
        const v4u zz = {};
#pragma unroll
        for (int i = 0; i < 2; ++i) {
          sts128(lds0 + L_KV + (2 * m + i) * KVT + lane * 16, kv[4 * i]);
          sts128(lds0 + L_KV + (2 * m + i) * KVT + 1024 + lane * 16, kv[4 * i + 1]);
          sts128(lds0 + L_KV + (2 * m + i) * KVT + vpos, live ? kv[4 * i + 2] : zz);
          sts128(lds0 + L_KV + (2 * m + i) * KVT + VB + vpos, live ? kv[4 * i + 3] : zz);
        }
        arrive_nowait(acnt);
      };
      auto kv_wait = [&](v4u (&kv)[8]) {
        wait_vm<0>();
#pragma unroll
        for (int i = 0; i < 8; ++i) PIN(kv[i]);
      };
      if (heavy) {
        bias_load(1);
        hsetup(1);
        await(acnt + 16, 4);
        STAMP_AT(3);
        v2u fl;
        v4u kv[8];
        hmm(std::integral_constant<int, 1>{}, xf, [&](auto kc) {
          constexpr int ks = kc.value;
          if constexpr (ks == KVPUB) {
            wait_vm<0>();
            PIN(bias0); PIN(bias1);
            if (lane == 0) __hip_atomic_store(flag(FL_KV, T, j), tag, RLX_AGENT);
          }
          if constexpr (ks == 24) fl = ld8_sc1_async(sws, flag_off(FL_KV, live ? T0 + (lane & 1) : 0, j));
        });
        STAMP_AT(18);
        wait_vmf<0>(fl);
        if (!__all(fl[0] == taglo && fl[1] == tag32)) poll_flags(flag(FL_KV, live ? T0 + (lane & 1) : 0, j), tag);
        kv_load(kv, 0, 8);
        hgelu(std::integral_constant<int, 1>{}, [&]() {});
        STAMP_AT(4);
        wait_vm<4>();
#pragma unroll
        for (int i = 0; i < 8; ++i) PIN(kv[i]);
        kv_stage(kv);
      } else {
        wait_vm<0>();
        if (lane == 0) __hip_atomic_store(flag(FL_KV, T, j), tag, RLX_AGENT);
        v4u kv[8];
        poll_flags(flag(FL_KV, live ? T0 + (lane & 1) : 0, j), tag);
        kv_load(kv, 0, 8); kv_wait(kv); kv_stage(kv);
      }
    }
    STAMP_AT(19);
    const unsigned kb = lds0 + L_KV + m * KVT + lane * 16;
    unsigned vb[2];
#pragma unroll
    for (int e = 0; e < 2; ++e) { vb[e] = lds0 + L_KV + m * KVT + 2048 + ((g & 1) ? 1024 : e * 512 + h * 256 + (lane & 15) * 16); PIN(vb[e]); }
    {
      const int dq = r32 - 4 * h;
#pragma unroll
      for (int d = 0; d < 8; ++d) { const int row = 2 * (d & 1) + 8 * (d >> 1); (d < 4 ? am0 : am1)[d & 3] = row > dq ? 0xffffffffu : row == dq ? 0xffff0000u : 0u; }
      PIN(am0); PIN(am1);
    }
    await(acnt, heavy ? 8 : 4);
    __builtin_amdgcn_s_setprio(PRIO_ATT);
    STAMP_AT(5);
    const f32x16 zero = {};
    auto o_store = [&](f32x16& o, int e) {
      const float rl = __builtin_amdgcn_rcpf(o[8]);
#pragma unroll
      for (int u = 0; u < 8; ++u) o[u] = MUL1(o[u], rl);
      st16_sc1(pack8(o, 0), sws, lane * 16, WS_AF + (T * AFT + 112 + 2 * j + e) * 1024);
    };
    int redo = 0;
    {
      f32x16 o[2], sa, sb, su;
      bool pend = false;
      v4u kn, ku, va, vc, vn0, vn1, vu0, vu1, p0, p1, pu;
      auto kaddr = [&](int n) { return kb + ((n & 7) + 1) * KVT + (n >> 3) * 1024; };
      auto vaddr = [&](int n) { return vb[n >> 3] + ((n & 7) + 1) * KVT; };
      kn = lds128(kaddr(0)); vn0 = lds128(vaddr(0)); vn1 = lds128(vaddr(0) + VB);
      sa = MFMA32(kn, qf[0], zero);
      kn = lds128(kaddr(1));
      SBAR();
      auto step = [&](auto nc, f32x16& sc, f32x16& sn) {
        constexpr int n = nc.value, e = n >> 3, i = n & 7;
        if constexpr (n < 15) sn = MFMA32(kn, qf[(n + 1) >> 3], zero);
        if constexpr (n < 14) kn = lds128(kaddr(n + 2));
        if constexpr (i == 5) ku = lds128(kb + e * 1024);
        v4u v0 = vn0, v1 = vn1;
        if constexpr (n < 15) { vn0 = lds128(vaddr(n + 1)); vn1 = lds128(vaddr(n + 1) + VB); }
        if constexpr (i == 6) { vu0 = lds128(vb[e]); vu1 = lds128(vb[e] + VB); }
        SBAR();
        if constexpr (i == 7) {
          static_for<0, 16>([&](auto u) { sc[u.value] = upper<u.value>() ? su[u.value] : sc[u.value]; });
          PIN(sc); SBAR();
        }
#pragma unroll
        for (int u = 0; u < 6; ++u) sc[u] = EXP2(sc[u]);
        PIN(sc); SBAR();
        if constexpr (n == 8) { o[0] = MFMA32(vc, p1, o[0]); SBAR(); }
        else if constexpr (n > 0) { o[e] = MFMA32(vc, p1, o[e]); SBAR(); }
        sc[6] = EXP2(sc[6]); sc[7] = EXP2(sc[7]);
        p0 = pack8(sc, 0);
        sc[8] = EXP2(sc[8]); sc[9] = EXP2(sc[9]);
        if constexpr (i == 7) { va = p0 & am0; p0 &= ~am0; PIN(va); }
        PIN(sc); PIN(p0); SBAR();
        if constexpr (n == 8) { o[0] = MFMA32(vu1, pu, o[0]); SBAR(); }
        o[e] = MFMA32(v0, p0, i == 0 ? zero : o[e]);
        SBAR();
        if constexpr (i == 6) { su = MFMA32(ku, qf[e], zero); SBAR(); }
#pragma unroll
        for (int u = 10; u < 13; ++u) sc[u] = EXP2(sc[u]);
        if constexpr (i == 7) { PIN(sc); SBAR(); o[e] = MFMA32(vu0, va, o[e]); SBAR(); }
#pragma unroll
        for (int u = 13; u < 16; ++u) sc[u] = EXP2(sc[u]);
        p1 = pack8(sc, 1); vc = v1;
        if constexpr (i == 7) { pu = p1 & am1; p1 &= ~am1; PIN(pu); }
        PIN(p1); SBAR();
        if constexpr (n == 8) {
          if (__all(o[0][8] >= 0x1p-30f && o[0][8] <= 0x1p30f)) o_store(o[0], 0); else redo = 1;
          SBAR();
        }
        if constexpr (i == 2) pend = !redo;
        if constexpr (i >= 2) {
          if (pend && (i == 7 || (__builtin_amdgcn_s_getreg(7 | (3 << 11)) & 15) == 0)) {
            wait_vm<0>();
            if (lane == 0) __hip_atomic_store(flag(FL_H1 + e, T, j), tag, RLX_AGENT);
            pend = false;
          }
        }
      };
      static_for<0, 8>([&](auto hc) {
        step(std::integral_constant<int, 2 * hc.value>{}, sa, sb);
        step(std::integral_constant<int, 2 * hc.value + 1>{}, sb, sa);
      });
      o[1] = MFMA32(vc, p1, o[1]);
      o[1] = MFMA32(vu1, pu, o[1]);
      if (__all(o[1][8] >= 0x1p-30f && o[1][8] <= 0x1p30f)) o_store(o[1], 1); else redo |= 2;
    }
    if (redo) {
#pragma unroll 1
      for (int e = 0; e < 2; ++e) {
        if (!((redo >> e) & 1)) continue;
        const v4u q = e ? qf[1] : qf[0];
        const unsigned vbe = e ? vb[1] : vb[0];
        auto merged = [&](const f32x16& c) {
          f32x16 sl = MFMA32(lds128(kb + 8 * KVT + e * 1024), q, c);
          const f32x16 su = MFMA32(lds128(kb + e * 1024), q, c);
          static_for<0, 16>([&](auto u) { sl[u.value] = upper<u.value>() ? su[u.value] : sl[u.value]; });
          return sl;
        };
        float mx = -INFINITY;
#pragma unroll 1
        for (int i = 0; i < 8; ++i) {
          const f32x16 sc = i ? MFMA32(lds128(kb + i * KVT + e * 1024), q, zero) : merged(zero);
#pragma unroll
          for (int u = 0; u < 16; u += 2) mx = max3(mx, sc[u], sc[u + 1]);
        }
        const auto sw = __builtin_amdgcn_permlane32_swap(asu(mx), asu(mx), false, false);
        mx = __builtin_fmaxf(asf(sw[0]), asf(sw[1]));
        f32x16 negm, o;
#pragma unroll
        for (int u = 0; u < 16; ++u) negm[u] = -mx;
        {
          f32x16 sc = merged(negm);
#pragma unroll
          for (int u = 0; u < 16; ++u) sc[u] = EXP2(sc[u]);
          const v4u p0 = pack8(sc, 0), p1 = pack8(sc, 1);
          o = MFMA32(lds128(vbe), p0 & am0, zero);
          o = MFMA32(lds128(vbe + VB), p1 & am1, o);
          o = MFMA32(lds128(vbe + 8 * KVT), p0 & ~am0, o);
          o = MFMA32(lds128(vbe + 8 * KVT + VB), p1 & ~am1, o);
        }
#pragma unroll 1
        for (int i = 1; i < 8; ++i) {
          f32x16 sc = MFMA32(lds128(kb + i * KVT + e * 1024), q, negm);
#pragma unroll
          for (int u = 0; u < 16; ++u) sc[u] = EXP2(sc[u]);
          o = MFMA32(lds128(vbe + i * KVT), pack8(sc, 0), o);
          o = MFMA32(lds128(vbe + i * KVT + VB), pack8(sc, 1), o);
        }
        o_store(o, e);
      }
      wait_vm<0>();
      if (lane == 0) __hip_atomic_store(flag(FL_O0, T, j), tag, RLX_AGENT);
    }
    STAMP_AT(6);
    wait_vm<0>();
    if (lane == 0) __hip_atomic_store(flag(FL_O1, T, j), tag, RLX_AGENT);
    STAMP_AT(7);
    res_loads();
    wait_vm2<0>(xres, bres);
    if (blockIdx.x == 0 && wave == 7) {
#pragma unroll 1
      for (int i = 0; i < 4; ++i) poll_flags(flag(FL_KV, 4 * (4 * i + g), lane & 15), tag);
      if (lane == 0) __hip_atomic_store((GAS u64*)(ws + WS_EPOCH), tag, RLX_AGENT);
    }
  }
  bar();
  {
    int t2 = threadIdx.x; PIN(t2);
    const int row = t2 >> 3, ch = t2 & 7, orow = 64 * rt + row, ocol = 64 * ct + ch * 8;
    const unsigned a = lds0 + L_WG + (((row >> 5) * 8 + ch) * 66 + (row & 31)) * 16;
    f32x4 lo = {}, hi = {};
#pragma unroll
    for (int w = 0; w < 4; ++w) {
      lo += __builtin_bit_cast(f32x4, lds128(a + w * PART));
      hi += __builtin_bit_cast(f32x4, lds128(a + w * PART + 512));
    }
    v4u o;
    o[0] = pk2(lo[0] + bflo(xres[0]) + bflo(bres[0]), lo[1] + bfhi(xres[0]) + bfhi(bres[0]));
    o[1] = pk2(lo[2] + bflo(xres[1]) + bflo(bres[1]), lo[3] + bfhi(xres[1]) + bfhi(bres[1]));
    o[2] = pk2(hi[0] + bflo(xres[2]) + bflo(bres[2]), hi[1] + bfhi(xres[2]) + bfhi(bres[2]));
    o[3] = pk2(hi[2] + bflo(xres[3]) + bflo(bres[3]), hi[3] + bfhi(xres[3]) + bfhi(bres[3]));
    *(GAS v4u*)((char*)out + (size_t)orow * 1024 + ocol * 2) = o;
  }
  STAMP_AT(8);
}

extern "C" void kernel_launch(void* const* d_in, const int* in_sizes, int n_in, void* d_out, int out_size, void* d_ws, size_t ws_size,
                              hipStream_t stream) {
  (void)in_sizes; (void)n_in; (void)out_size; (void)ws_size;
  (void)hipFuncSetAttribute((const void*)fused_block, hipFuncAttributeMaxDynamicSharedMemorySize, LDS_BYTES);
  fused_block<<<256, 512, LDS_BYTES, stream>>>(d_in[0], d_in[1], d_in[2], d_in[4], d_in[5], d_in[6], d_in[7], d_out, (char*)d_ws);
}
```

```cpp
#define PROBE 10
#define PROLE 0
#include <hip/hip_runtime.h>
#include <hip/hip_bf16.h>
#include <stdint.h>
#include <type_traits>

typedef unsigned v4u __attribute__((ext_vector_type(4)));
typedef unsigned v2u __attribute__((ext_vector_type(2)));
typedef float f32x2 __attribute__((ext_vector_type(2)));
typedef float f32x4 __attribute__((ext_vector_type(4)));
typedef float f32x16 __attribute__((ext_vector_type(16)));
typedef short bf16x8 __attribute__((ext_vector_type(8)));
typedef short s16x4 __attribute__((ext_vector_type(4)));
typedef __bf16 bf16x2_t __attribute__((ext_vector_type(2)));
typedef unsigned long long u64;
#define GAS __attribute__((address_space(1)))
#define LAS __attribute__((address_space(3)))
#define DI __device__ __forceinline__
#define RLX_AGENT __ATOMIC_RELAXED, __HIP_MEMORY_SCOPE_AGENT

constexpr int SEQ = 2048, DM = 512, FF = 1792;
constexpr unsigned WS_EPOCH = 0;
constexpr unsigned WS_STATS = 32768;
constexpr unsigned WS_FLAG = 65536;
constexpr unsigned WS_KV = WS_FLAG + 5 * 1024 * 128;
constexpr int AFT = 145;
constexpr unsigned WS_AF = WS_KV + 16 * 64 * 4096;
constexpr unsigned WS_END = WS_AF + 64 * AFT * 1024;
constexpr unsigned WS_STAMP = WS_END;
enum { FL_KV = 0, FL_H0 = 1, FL_H1 = 2, FL_O0 = 3, FL_O1 = 4 };
constexpr int STG = 36864;
constexpr int L_WA = 0;
constexpr int L_WG = 86016;
constexpr int VB = 1040;
constexpr int KVT = 2048 + 2 * VB;
constexpr int L_KV = 65536 - 8 * KVT;
constexpr int RD = 8, NR = 12;
constexpr int PART = 16 * 66 * 16;
constexpr int L_CS = L_WG + 4 * PART;
constexpr int L_RS = L_CS + 3584;
constexpr int L_CNT = L_RS + 512;
constexpr int LDS_BYTES = L_CNT + 64;
static_assert(L_KV + 12 * KVT <= L_WG && L_WG >= 2 * STG && L_CS >= 3 * STG && PART >= RD * 2048 && LDS_BYTES <= 163840, "lds map");
#define AWIN 6
constexpr int AW = AWIN;
constexpr unsigned SPIN_CAP = 1u << 23;

#define MFMA32(a, b, c) __builtin_amdgcn_mfma_f32_32x32x16_bf16(__builtin_bit_cast(bf16x8, a), __builtin_bit_cast(bf16x8, b), c, 0, 0, 0)
#define MFMA16(a, b, c) __builtin_amdgcn_mfma_f32_16x16x32_bf16(__builtin_bit_cast(bf16x8, a), __builtin_bit_cast(bf16x8, b), c, 0, 0, 0)
#define SBAR() __builtin_amdgcn_sched_barrier(0)

#define STAMP_AT(i) do { if ((i) == PROBE && (isG ? 0 : 1) == PROLE) tstart = now_t(); } while (0)

DI u64 now_t() { u64 t; asm volatile("s_memrealtime %0\n\ts_waitcnt lgkmcnt(0)" : "=s"(t) :: "memory"); return t; }
DI float asf(unsigned u) { return __builtin_bit_cast(float, u); }
DI unsigned asu(float f) { return __builtin_bit_cast(unsigned, f); }
DI unsigned pk2(float lo, float hi) { return __builtin_bit_cast(unsigned, __builtin_convertvector((f32x2){lo, hi}, bf16x2_t)); }
DI float bflo(unsigned u) { return __builtin_bit_cast(float, u << 16); }
DI float bfhi(unsigned u) { return __builtin_bit_cast(float, u & 0xffff0000u); }
DI v4u make_srd(const void* p, unsigned bytes) { const u64 a = (u64)p; return (v4u){(unsigned)a, (unsigned)(a >> 32) & 0xffffu, bytes, 0x00020000u}; }
DI void dma16(v4u srd, unsigned voff, unsigned soff, unsigned lds) {
  asm volatile("s_mov_b32 m0, %3\n\ts_nop 0\n\tbuffer_load_dwordx4 %0, %1, %2 offen lds" :: "v"(voff), "s"(srd), "s"(soff), "s"(lds) : "memory");
}
DI void dma16_a(v4u srd, unsigned voff, unsigned soff, unsigned lds) { dma16(srd, voff, soff, lds); }
DI void dma16_b(v4u srd, unsigned voff1, unsigned soff) {
  asm volatile("buffer_load_dwordx4 %0, %1, %2 offen offset:1024 lds" :: "v"(voff1), "s"(srd), "s"(soff) : "memory");
}
DI v4u ld16_sc1_async(v4u srd, unsigned voff, unsigned soff) {
  v4u r; asm volatile("buffer_load_dwordx4 %0, %1, %2, %3 offen sc1" : "=v"(r) : "v"(voff), "s"(srd), "s"(soff) : "memory"); return r;
}
DI v4u ld16_async(v4u srd, unsigned voff, unsigned soff) {
  v4u r; asm volatile("buffer_load_dwordx4 %0, %1, %2, %3 offen" : "=v"(r) : "v"(voff), "s"(srd), "s"(soff) : "memory"); return r;
}
DI unsigned ld2_async(v4u srd, unsigned voff, unsigned soff) {
  unsigned r; asm volatile("buffer_load_ushort %0, %1, %2, %3 offen" : "=v"(r) : "v"(voff), "s"(srd), "s"(soff) : "memory"); return r;
}
DI void st16_sc1(v4u v, v4u srd, unsigned voff, unsigned soff) {
  asm volatile("buffer_store_dwordx4 %0, %1, %2, %3 offen sc1\n\ts_nop 1" :: "v"(v), "v"(voff), "s"(srd), "s"(soff) : "memory");
}
template <int N> DI void wait_vm1(v4u& a) { asm volatile("s_waitcnt vmcnt(%1)" : "+v"(a) : "i"(N) : "memory"); }
template <int N> DI void wait_vm2(v4u& a, v4u& b) { asm volatile("s_waitcnt vmcnt(%2)" : "+v"(a), "+v"(b) : "i"(N) : "memory"); }
template <int N> DI void wait_vm3(v4u& a, v4u& b, v4u& c) { asm volatile("s_waitcnt vmcnt(%3)" : "+v"(a), "+v"(b), "+v"(c) : "i"(N) : "memory"); }
template <class V> DI void pin(V& v) { asm volatile("" : "+v"(v)); }
template <int N> DI void wait_vm() { asm volatile("s_waitcnt vmcnt(%0)" :: "i"(N) : "memory"); }
DI void bar() { asm volatile("s_waitcnt lgkmcnt(0)\n\ts_barrier" ::: "memory"); }
DI v4u lds128(unsigned a) { return *(const LAS v4u*)(size_t)a; }
DI void sts128(unsigned a, v4u v) { *(LAS v4u*)(size_t)a = v; }
DI v2u ldtr(unsigned a) { return __builtin_bit_cast(v2u, __builtin_amdgcn_ds_read_tr16_b64_v4i16((LAS s16x4*)(size_t)a)); }
DI v4u ldtr2(unsigned a, int second) { const v2u lo = ldtr(a), hi = ldtr(a + second); return (v4u){lo.x, lo.y, hi.x, hi.y}; }
template <int CTRL> DI float dpp(float v) { return __builtin_bit_cast(float, __builtin_amdgcn_update_dpp(0, __builtin_bit_cast(int, v), CTRL, 0xf, 0xf, true)); }
DI float rdl(float v, int l) { return __builtin_bit_cast(float, __builtin_amdgcn_readlane(__builtin_bit_cast(int, v), l)); }
DI v4u pack8(const f32x16& a, int s) { return (v4u){pk2(a[8 * s], a[8 * s + 1]), pk2(a[8 * s + 2], a[8 * s + 3]), pk2(a[8 * s + 4], a[8 * s + 5]), pk2(a[8 * s + 6], a[8 * s + 7])}; }
DI void poll_flags(const GAS u64* p, u64 tag) {
#pragma unroll 1
  for (unsigned spin = 0; spin < SPIN_CAP; ++spin) {
    const u64 v = __hip_atomic_load(p, RLX_AGENT);
    if (__all(v == tag)) break;
    __builtin_amdgcn_s_sleep(2);
  }
  asm volatile("" ::: "memory");
}
constexpr u64 upper_mask(int reg) {
  u64 m = 0;
  for (int l = 0; l < 64; ++l) { const int row = (reg & 3) + 8 * (reg >> 2) + 4 * (l >> 5); if (row > (l & 31)) m |= 1ull << l; }
  return m;
}
template <int REG> DI bool upper() { return __builtin_amdgcn_inverse_ballot_w64(upper_mask(REG)); }
template <int I, int N, class Fn> DI void static_for(Fn&& f) { if constexpr (I < N) { f(std::integral_constant<int, I>{}); static_for<I + 1, N>(f); } }
DI float max3(float a, float b, float c) { return __builtin_fmaxf(__builtin_fmaxf(a, b), c); }

constexpr int H1_CK = 16 - AW, H1_LD = H1_CK - 3, O_CK = 28 - AW, O_LD = O_CK - 3;
constexpr int g2_issued(int t) { return ((t >= 1 && t - 1 + NR + RD < 36) ? 2 : 0) + ((t + AW < 28) ? 2 : 0) + (t == H1_LD) + (t == O_LD) + (t == O_CK ? 10 : 0); }
constexpr int g2_range(int a, int b) { int n = 0; for (int t = a; t < b; ++t) n += g2_issued(t); return n; }
#define GELU_PK(P) (P == 1)
#define PRIO_G2 2
#define PRIO_ATT 3
#define PRIO_G1 2
#define ROT r
#define CODEWARM 39000
#define MIDWARM 1
#define KVPUB 6
#define EXP2(v) __builtin_amdgcn_exp2f(v)
#define PIN(v) pin(v)
extern "C" __device__ float mul_unpacked(float, float) __asm("llvm.amdgcn.fmul.legacy");
#define MUL1(a, b) mul_unpacked(a, b)
#define FMA1(a, b, c) __builtin_amdgcn_div_fmasf(a, b, c, false)
DI v2u ld8_sc1_async(v4u srd, unsigned voff) {
  v2u r; asm volatile("buffer_load_dwordx2 %0, %1, %2, 0 offen sc1" : "=v"(r) : "v"(voff), "s"(srd) : "memory"); return r;
}
template <int N> DI void wait_vmf(v2u& a) { asm volatile("s_waitcnt vmcnt(%1)" : "+v"(a) : "i"(N) : "memory"); }
DI void arrive(unsigned cnt, int lane) {
  asm volatile("s_waitcnt lgkmcnt(0)" ::: "memory");
  if (lane == 0) __hip_atomic_fetch_add((LAS unsigned*)(size_t)cnt, 1u, __ATOMIC_RELAXED, __HIP_MEMORY_SCOPE_WORKGROUP);
}
DI void arrive_nowait(unsigned cnt) {
  asm volatile("s_mov_b64 exec, 1\n\tds_add_u32 %0, %1\n\ts_mov_b64 exec, -1" :: "v"(cnt), "v"(1u) : "memory");
}
DI unsigned peek(unsigned cnt) { return __hip_atomic_load((LAS unsigned*)(size_t)cnt, __ATOMIC_RELAXED, __HIP_MEMORY_SCOPE_WORKGROUP); }
DI void await(unsigned cnt, unsigned n) {
#pragma unroll 1
  for (unsigned spin = 0; spin < SPIN_CAP * 32; ++spin) {
    if (__builtin_amdgcn_readfirstlane(peek(cnt)) >= n) break;
    __builtin_amdgcn_s_sleep(1);
  }
  asm volatile("" ::: "memory");
}

__global__ __launch_bounds__(512, 2) void fused_block(const void* __restrict__ x, const void* __restrict__ wqkv, const void* __restrict__ wo,
                                                      const void* __restrict__ w1, const void* __restrict__ b1, const void* __restrict__ w2,
                                                      const void* __restrict__ b2, void* __restrict__ out, char* __restrict__ ws) {
  constexpr unsigned lds0 = 0;
  const int wave = __builtin_amdgcn_readfirstlane(threadIdx.x >> 6);
  const int m = wave & 3;
  const bool isG = wave < 4;
  const int xcd = blockIdx.x & 7, idx = blockIdx.x >> 3;
  const int r = (xcd >> 2) * 8 + (idx >> 2), j = (xcd & 3) * 4 + (idx & 3);
  const int rt = (xcd >> 1) * 8 + (idx >> 2), ct = (xcd & 1) * 4 + (idx & 3);
  int lane = threadIdx.x & 63, r32, h, g, q4, p4;
  u64 tstart = 0;
  STAMP_AT(0);
  asm volatile("" :: "s"(x), "s"(wqkv), "s"(wo), "s"(w1), "s"(b1), "s"(w2), "s"(b2), "s"(out), "s"(ws));

  const v4u sx = make_srd(x, SEQ * DM * 2), sws = make_srd(ws, WS_END);
  const __amdgpu_buffer_rsrc_t rws = __builtin_amdgcn_make_buffer_rsrc(ws, 0, WS_END, 0x00020000);
  const unsigned one2 = 0x3f803f80u;
  const int T = 4 * r + m;
  const int rot = ROT;
  auto flag_off = [&](int kind, int tile, int jj) { return WS_FLAG + ((kind * 64 + tile) * 16 + jj) * 128; };
  auto flag = [&](int kind, int tile, int jj) { return (GAS u64*)(ws + flag_off(kind, tile, jj)); };
  u64 tag;
  unsigned taglo, tag32;
  auto get_tag = [&]() {
    const v2u tg = *(const LAS v2u*)(size_t)(lds0 + L_CNT + 32);
    taglo = __builtin_amdgcn_readfirstlane(tg[0]); tag32 = __builtin_amdgcn_readfirstlane(tg[1]);
    tag = ((u64)tag32 << 32) | taglo;
  };
  v4u xres, bres, xaug, st;
  float mean, var, rstd, sd;
  v4u sel;
  unsigned xo[4], swz;
  auto lane_setup = [&]() {
    lane = threadIdx.x & 63; PIN(lane);
    r32 = lane & 31; h = lane >> 5; g = lane >> 4; q4 = (lane >> 2) & 3; p4 = lane & 3;
    const unsigned selv = ((lane & 15) == (g & 1)) ? one2 : 0u;
    sel = (v4u){selv, selv, selv, selv};
#pragma unroll
    for (int p = 0; p < 4; ++p) xo[p] = (32 * m + r32) * 128 + (((2 * ((p + m) & 3) + h) ^ ((r32 >> 1) & 7)) * 16);
    swz = ((((lane >> 2) & 1) ^ (g & 1)) * 64) + p4 * 16;
  };

  auto stat_load = [&]() { st = ld16_sc1_async(sws, WS_STATS + (128 * r + 32 * m + r32) * 16, 0); };
  auto res_loads = [&]() {
    int t2 = threadIdx.x; PIN(t2);
    const int row = 64 * rt + (t2 >> 3), col = 64 * ct + (t2 & 7) * 8;
    xres = ld16_async(sx, row * 1024 + col * 2, 0);
    bres = ld16_async(make_srd(b2, DM * 2), col * 2, 0);
  };
  auto row_stats = [&]() {
    if (!__all(st[1] == tag32 && st[3] == tag32)) {
#pragma unroll 1
      for (unsigned spin = 0; spin < SPIN_CAP; ++spin) {
        st = ld16_sc1_async(sws, WS_STATS + (128 * r + 32 * m + r32) * 16, 0);
        wait_vm1<0>(st);
        if (__all(st[1] == tag32 && st[3] == tag32)) break;
      }
    }
    mean = asf(st[0]); var = asf(st[2]);
    PIN(mean); PIN(var);
  };
  auto make_xaug = [&]() {
    PIN(mean); PIN(var);
    rstd = __builtin_amdgcn_rsqf(var); sd = var * rstd;
    const unsigned hi = pk2(-mean, sd);
    const unsigned lo = pk2(-mean - bflo(hi), FMA1(bfhi(hi), -1.f, sd));
    xaug = (v4u){(hi & 0xffffu) | (hi << 16), (lo & 0xffffu) | (hi & 0xffff0000u), lo >> 16, 0u};
    if (h) xaug = (v4u){0u, 0u, 0u, 0u};
    PIN(xaug);
  };
  auto cswr = [&](int tile, f32x4 c) {
    if (lane < 16) { LAS float* p = (LAS float*)(size_t)(lds0 + L_CS + (tile * 32 + lane) * 16 + m * 4); p[0] = c[0]; p[64] = c[1]; }
  };
  auto csrd = [&](int tile) { return __builtin_bit_cast(f32x4, lds128(lds0 + L_CS + (tile * 32 + r32) * 16)); };
  auto waug = [&](f32x4 c4, unsigned b16) {
    const float c = ((c4[0] + c4[1]) + c4[2]) + c4[3];
    const unsigned hi = pk2(c, 0.f);
    const unsigned lo = pk2(c - bflo(hi), 0.f);
    return (v4u){hi | (lo << 16), hi | (b16 << 16), b16, 0u};
  };

  const v4u sw1 = make_srd(w1, DM * FF * 2);
  const int hcol0 = 448 * (j >> 2) + 128 * (j & 3);
  const bool heavy = (j & 3) != 3;
  unsigned bias0, bias1;
  auto bias_load = [&](int P) {
    bias0 = ld2_async(make_srd(b1, FF * 2), r32 * 2, (hcol0 + 64 * P) * 2);
    bias1 = ld2_async(make_srd(b1, FF * 2), r32 * 2, (hcol0 + 64 * P + 32) * 2);
  };
  auto w1dma = [&](int P, int c, unsigned dst, int half) {
    const unsigned vw1 = (lane >> 3) * (FF * 2) + swz, vw1b = vw1 + 8 * (FF * 2) - 1024;
    const unsigned so = (64 * ((c + rot) & 7) + 16 * m) * (FF * 2) + (hcol0 + 64 * P) * 2;
    if (half == 0) dma16_a(sw1, vw1, so, dst + m * 2048); else dma16_b(sw1, vw1b, so);
  };
  f32x16 hac[2];
  unsigned rk[4][2];
  auto hsetup = [&](int P) {
#pragma unroll
    for (int p = 0; p < 4; ++p)
#pragma unroll
      for (int c = 0; c < 2; ++c) {
        rk[p][c] = lds0 + (P ? L_WA : 28672) + ((p + m) & 3) * 2048 + h * 1024 + q4 * 128 + ((c ^ (q4 >> 1)) * 64) + (g & 1) * 32 + p4 * 8;
        PIN(rk[p][c]);
      }
  };
  auto hmm = [&](auto pc, const v4u (&xf)[32], auto&& hook) {
    constexpr int P = pc.value;
    constexpr int D = 4;
    const unsigned cnt = lds0 + L_CNT;
    f32x4 cs[2] = {}, c0, c1;
    v4u wf[D][2], au0, au1;
    unsigned seen = 0;
    hac[0] = (f32x16){}; hac[1] = (f32x16){};
    auto rd = [&](int ks, int t) { wf[ks % D][t] = ldtr2(rk[ks & 3][t] + (ks >> 2) * 8192, 512); };
#pragma unroll
    for (int ks = 0; ks < D; ++ks) { rd(ks, 0); rd(ks, 1); }
    SBAR();
    static_for<0, 32>([&](auto kc) {
      constexpr int ks = kc.value;
      hook(kc);
      if constexpr (ks == 10) seen = peek(cnt + 20);
      if constexpr (ks == 29) seen = peek(cnt);
#pragma unroll
      for (int t = 0; t < 2; ++t) {
        hac[t] = MFMA32(wf[ks % D][t], xf[ks], hac[t]);
        if ((ks & 3) == 0 && ks < 28) { cs[t] = MFMA16(sel, wf[ks % D][t], cs[t]); PIN(cs[t]); }
        if (ks == 26) { cs[t] = MFMA16(sel, wf[28 % D][t], cs[t]); PIN(cs[t]); }
        SBAR();
        if constexpr (ks + D < 32) rd(ks + D, t);
        if constexpr (ks == 31) { if (t == 0) { au0 = waug(c0, bias0); PIN(au0); } else { au1 = waug(c1, bias1); PIN(au1); } }
        SBAR();
      }
      if constexpr (ks == 11 || ks == 30) {
        PIN(hac[0]); PIN(hac[1]);
        if (__builtin_amdgcn_readfirstlane(seen) < 4) await(ks == 11 ? cnt + 20 : cnt, 4);
        asm volatile("" ::: "memory");
        if constexpr (ks == 30) { c0 = csrd(2 * P); c1 = csrd(2 * P + 1); }
      }
      if constexpr (ks == 27) { cswr(2 * P, cs[0]); cswr(2 * P + 1, cs[1]); arrive_nowait(cnt); }
      SBAR();
    });
    hac[0] = MFMA32(au0, xaug, hac[0]);
    hac[1] = MFMA32(au1, xaug, hac[1]);
    SBAR();
  };
  auto hgelu = [&](auto pc, auto&& mid) {
    constexpr int P = pc.value;
    constexpr bool PK = GELU_PK(P);
    const float C0 = 2.3022081986f, C1 = C0 * 0.044715f;
    const float ga = -C1 * rstd * rstd * rstd, gb = -C0 * rstd;
    auto mul = [&](f32x2 a, f32x2 b) { return PK ? a * b : (f32x2){MUL1(a[0], b[0]), MUL1(a[1], b[1])}; };
    auto fma = [&](f32x2 a, float b, float c) {
      return PK ? __builtin_elementwise_fma(a, (f32x2){b, b}, (f32x2){c, c}) : (f32x2){FMA1(a[0], b, c), FMA1(a[1], b, c)}; };
#pragma unroll
    for (int t = 0; t < 2; ++t) {
      f32x2 v[8], z[8];
#pragma unroll
      for (int e = 0; e < 8; ++e) { v[e] = (f32x2){hac[t][2 * e], hac[t][2 * e + 1]}; z[e] = mul(v[e], v[e]); }
      SBAR();
#pragma unroll
      for (int e = 0; e < 8; ++e) z[e] = fma(z[e], ga, gb);
      SBAR();
#pragma unroll
      for (int e = 0; e < 8; ++e) z[e] = mul(z[e], v[e]);
      SBAR();
#pragma unroll
      for (int e = 0; e < 8; ++e) z[e] = (f32x2){EXP2(z[e][0]), EXP2(z[e][1])};
      SBAR();
#pragma unroll
      for (int e = 0; e < 8; ++e) z[e] = fma(z[e], sd, sd);
      SBAR();
#pragma unroll
      for (int e = 0; e < 8; ++e) z[e] = (f32x2){__builtin_amdgcn_rcpf(z[e][0]), __builtin_amdgcn_rcpf(z[e][1])};
      SBAR();
#pragma unroll
      for (int e = 0; e < 8; ++e) z[e] = mul(z[e], v[e]);
      SBAR();
#pragma unroll
      for (int s = 0; s < 2; ++s) {
        const v4u f = {pk2(z[4 * s][0], z[4 * s][1]), pk2(z[4 * s + 1][0], z[4 * s + 1][1]), pk2(z[4 * s + 2][0], z[4 * s + 2][1]), pk2(z[4 * s + 3][0], z[4 * s + 3][1])};
        st16_sc1(f, sws, lane * 16, WS_AF + (T * AFT + (hcol0 >> 4) + 4 * P + 2 * t + s) * 1024);
      }
      if (t == 0) mid();
      SBAR();
    }
  };

  if (isG) {
    lane_setup();
    const v4u sw2 = make_srd(w2, FF * DM * 2), swo = make_srd(wo, DM * DM * 2);
    const unsigned vw2 = (lane >> 3) * 1024 + swz, vw2b = vw2 + 7 * 1024;
    const int ro3 = rt % 3;
    auto kmap = [&](int kk) {
      if (kk < 16) return 8 * (((kk >> 2) + rt) & 3) + (kk & 3);
      const int p = ((kk - 16) >> 2) + ro3;
      return 8 * (p >= 3 ? p - 3 : p) + 4 + (kk & 3);
    };
    auto omap = [&](int i) { return 2 * ((i + rt) & 3) + (i >> 2); };
    auto wdmah = [&](int kk, int half) {
      const unsigned so = (kk < 28 ? 448 * m + 16 * kmap(kk) : 128 * m + 16 * omap(kk - 28)) * 1024 + 128 * ct;
      if (half == 0) dma16_a(kk < 28 ? sw2 : swo, vw2, so, lds0 + L_WG + m * PART + (kk % RD) * 2048); else dma16_b(kk < 28 ? sw2 : swo, vw2b, so);
    };
    auto wdma = [&](int kk) { wdmah(kk, 0); wdmah(kk, 1); };
    {
      const v4u swq = make_srd(wqkv, DM * 3 * DM * 2);
      const unsigned vx0 = (lane >> 3) * 1024 + (((lane & 7) ^ g) * 16), vx1 = (7 + (lane >> 3)) * 1024 + (((lane & 7) ^ (4 + g)) * 16);
      const unsigned vwq = (lane >> 2) * (3 * DM * 2) + p4 * 16;
      auto piece = [&](int s, int i) {
        const unsigned buf = lds0 + (s % 3) * STG;
        const int kc = (s + rot) & 7;
        if (i < 4) {
          const unsigned so = (128 * r + 32 * m + 16 * (i >> 1)) * 1024 + kc * 128;
          if (i & 1) dma16_b(sx, vx1, so); else dma16_a(sx, vx0, so, buf + m * 4096 + (i >> 1) * 2048);
        } else if (i < 7) dma16(swq, vwq, (64 * kc + 16 * m) * (3 * DM * 2) + (512 * (i - 4) + 32 * j) * 2, buf + 16384 + ((i - 4) * 4 + m) * 1024);
        else w1dma(0, s, buf + 28672, i - 7);
      };
#pragma unroll
      for (int i = 0; i < 18; ++i) { piece(i / 9, i % 9); if (i == 8) __builtin_amdgcn_s_barrier(); }
      __builtin_amdgcn_s_setprio(PRIO_G1);
      hsetup(0);
      f32x4 cs[2] = {};
      v4u xf[4], wf[4][2];
      hac[0] = (f32x16){}; hac[1] = (f32x16){};
      static_for<0, 8>([&](auto sc) {
        constexpr int s = sc.value;
        constexpr unsigned buf = (s % 3) * STG;
        auto fill = [&](int i) {
          if constexpr (s < 6) piece(s + 2, i);
          if constexpr (s == 6) { if (i == 0) { stat_load(); bias_load(0); } else wdmah((i - 1) >> 1, (i - 1) & 1); }
          if constexpr (s == 7) { if (heavy) { w1dma(1, i, lds0 + L_WA + i * 8192, 0); w1dma(1, i, 0, 1); } }
          SBAR();
        };
        auto rd = [&](int p) { xf[p] = lds128(buf + xo[p]); wf[p][0] = ldtr2(buf + rk[p][0], 512); wf[p][1] = ldtr2(buf + rk[p][1], 512); SBAR(); };
        if constexpr (s < 7) wait_vm<9>(); else { wait_vm1<8>(st); PIN(bias0); PIN(bias1); }
        bar();
        STAMP_AT(10 + s);
        rd(0);
        if constexpr (s < 7) fill(0);
        rd(1);
        if constexpr (s < 7) fill(1);
        if constexpr (s == 2) get_tag();
#pragma unroll
        for (int p = 0; p < 4; ++p) {
#pragma unroll
          for (int t = 0; t < 2; ++t) {
            hac[t] = MFMA32(wf[p][t], xf[p], hac[t]);
            if (p == 0) { cs[t] = MFMA16(sel, wf[p][t], cs[t]); PIN(cs[t]); }
            SBAR();
            if (p < 2 && t == 0) rd(p + 2);
            if (s == 7) fill(2 * p + t); else if (2 * p + t < 7) fill(2 + 2 * p + t);
          }
          if (s == 7 && p == 1) {
            cswr(0, cs[0]); cswr(1, cs[1]);
            bar();
            STAMP_AT(1);
          }
        }
        if constexpr (s == 7) {
          const f32x4 c0 = csrd(0), c1 = csrd(1);
          row_stats(); make_xaug();
          hac[0] = MFMA32(waug(c0, bias0), xaug, hac[0]);
          hac[1] = MFMA32(waug(c1, bias1), xaug, hac[1]);
          SBAR();
        }
      });
    }
    STAMP_AT(18);
    if (heavy) { wait_vm<8>(); arrive_nowait(lds0 + L_CNT + 16); }
    hgelu(std::integral_constant<int, 0>{}, [&]() {
      wait_vm<2>();
      if (heavy) arrive_nowait(lds0 + L_CNT + 20);
    });
    __builtin_amdgcn_s_setprio(0);
    STAMP_AT(19);
    unsigned rb2[2];
#pragma unroll
    for (int c = 0; c < 2; ++c) rb2[c] = lds0 + L_WG + m * PART + h * 512 + q4 * 128 + ((c ^ (q4 >> 1)) * 64) + (g & 1) * 32 + p4 * 8;
    v4u wq[36][2];
    auto wrd = [&](int kk) {
#pragma unroll
      for (int c = 0; c < 2; ++c) wq[kk][c] = ldtr2(rb2[c] + (kk % RD) * 2048, 1024);
    };
    auto wgot = [&](int kk) { PIN(wq[kk][0]); PIN(wq[kk][1]); };
    const int ftile = 2 * rt + ((lane >> 2) & 1);
    v4u af[28][2], of[8][2];
    v2u fl;
    const unsigned a0 = WS_AF + (2 * rt * AFT + 28 * m) * 1024, o0 = WS_AF + (2 * rt * AFT + 112 + 8 * m) * 1024;
    for (int kk = 0; kk < 4; ++kk) wrd(kk);
    for (int kk = 4; kk < 8; ++kk) wdma(kk);
    for (int kk = 0; kk < 4; ++kk) wgot(kk);
    wait_vm<8>();
    if (lane == 0) __hip_atomic_store(flag(FL_H0, T, j), tag, RLX_AGENT);
    STAMP_AT(2);
    for (int kk = 8; kk < 12; ++kk) wdma(kk);
    static_for<1, 3>([&](auto ic) {
      constexpr int i = ic.value;
      wait_vm<(i == 1) ? 9 : 8>();
      for (int kk = 4 * i; kk < 4 * i + 4; ++kk) wrd(kk);
      for (int kk = 4 * i; kk < 4 * i + 4; ++kk) wgot(kk);
      if constexpr (i == 2) fl = ld8_sc1_async(sws, flag_off(FL_H0, ftile, 4 * m + p4));
      for (int kk = 4 * i; kk < 4 * i + 4; ++kk) wdma(kk + RD);
    });
    STAMP_AT(3);
    wait_vmf<8>(fl);
    if (!__all(fl[0] == taglo && fl[1] == tag32)) poll_flags(flag(FL_H0, ftile, 4 * m + p4), tag);
    STAMP_AT(4);
#pragma unroll
    for (int kk = 0; kk < AW; ++kk) {
      af[kk][0] = ld16_sc1_async(sws, lane * 16, a0 + kmap(kk) * 1024);
      af[kk][1] = ld16_sc1_async(sws, lane * 16, a0 + (AFT + kmap(kk)) * 1024);
    }
    __builtin_amdgcn_s_setprio(PRIO_G2);
    f32x16 c2[2][2] = {};
    auto mm2 = [&](int kk, const v4u (&a)[2], auto&& fill) {
      SBAR();
#pragma unroll
      for (int i = 0; i < 4; ++i) { c2[i >> 1][i & 1] = MFMA32(wq[kk][i & 1], a[i >> 1], c2[i >> 1][i & 1]); SBAR(); fill(i); SBAR(); }
    };
    static_for<0, 28>([&](auto kc) {
      constexpr int kk = kc.value;
      wait_vm2<(kk < AW) ? 2 * (AW - 1 - kk) + g2_range(0, kk) : g2_range(kk - AW + 1, kk)>(af[kk][0], af[kk][1]);
      if constexpr (kk == H1_CK) {
        STAMP_AT(20);
        wait_vmf<g2_range(H1_LD, H1_CK) - 1>(fl);
        if (!__all(fl[0] == taglo && fl[1] == tag32)) poll_flags(flag(FL_H1, ftile, 4 * m + p4), tag);
        STAMP_AT(21);
      }
      if constexpr (kk == H1_LD) fl = ld8_sc1_async(sws, flag_off(FL_H1, ftile, 4 * m + p4));
      if constexpr (kk == O_LD) fl = ld8_sc1_async(sws, flag_off(FL_O0, ftile, 4 * m + p4));
      constexpr bool dw = kk >= 1 && kk - 1 + NR + RD < 36;
      if constexpr (dw) wgot(kk - 1 + NR);
      mm2(kk, af[kk], [&](int i) {
        if constexpr (dw) { if (i < 2) wdmah(kk - 1 + NR + RD, i); }
        if constexpr (kk + NR < 28) { if (i == 1) wrd(kk + NR); }
        if constexpr (kk == 27) { if (i == 0) wrd(28); }
        if constexpr (kk + AW < 28) { if (i >= 2) af[kk + AW][i - 2] = ld16_sc1_async(sws, lane * 16, a0 + (AFT * (i - 2) + kmap(kk + AW)) * 1024); }
      });
      if constexpr (kk == O_CK) {
        wait_vmf<g2_range(O_LD, O_CK) - 1>(fl);
        if (!__all(fl[0] == taglo && fl[1] == tag32)) poll_flags(flag(FL_O0, ftile, 4 * m + p4), tag);
        res_loads();
#pragma unroll
        for (int i = 0; i < 4; ++i) {
          of[i][0] = ld16_sc1_async(sws, lane * 16, o0 + omap(i) * 1024);
          of[i][1] = ld16_sc1_async(sws, lane * 16, o0 + (AFT + omap(i)) * 1024);
        }
      }
    });
    static_for<0, 8>([&](auto kc) {
      constexpr int kk = kc.value;
      if (kk < 7) wrd(28 + kk + 1);
      if constexpr (kk == 1) {
        STAMP_AT(5);
        poll_flags(flag(FL_O1, ftile, 4 * m + p4), tag);
        STAMP_AT(6);
#pragma unroll
        for (int i = 4; i < 8; ++i) {
          of[i][0] = ld16_sc1_async(sws, lane * 16, o0 + omap(i) * 1024);
          of[i][1] = ld16_sc1_async(sws, lane * 16, o0 + (AFT + omap(i)) * 1024);
        }
      }
      wait_vm2<(kk == 0) ? 6 : 14 - 2 * kk>(of[kk][0], of[kk][1]);
      mm2(28 + kk, of[kk], [&](int) {});
    });
    PIN(xres); PIN(bres);
    asm volatile("" ::: "memory");
#pragma unroll
    for (int mt = 0; mt < 2; ++mt)
#pragma unroll
      for (int c = 0; c < 2; ++c)
#pragma unroll
        for (int qd = 0; qd < 4; ++qd) {
          const f32x4 v = {c2[mt][c][4 * qd], c2[mt][c][4 * qd + 1], c2[mt][c][4 * qd + 2], c2[mt][c][4 * qd + 3]};
          sts128(lds0 + L_WG + m * PART + ((mt * 8 + c * 4 + qd) * 66 + lane) * 16, __builtin_bit_cast(v4u, v));
        }
    STAMP_AT(7);
  } else {
    __builtin_amdgcn_s_setprio(1);
    lane_setup();
    v4u xr[2];
    xr[0] = ld16_async(sx, lane * 16, (128 * r + 8 * j + 2 * m) * 1024); xr[1] = ld16_async(sx, lane * 16, (128 * r + 8 * j + 2 * m + 1) * 1024);
    unsigned warm;
    {
      u64 pc; asm volatile("s_getpc_b64 %0" : "=s"(pc));
      warm = ld2_async(make_srd((const void*)(pc & ~127ull), CODEWARM), lane < 3 ? lane * 128 : 0x40000000u, (idx * 4 + m) * 384);
    }
    __builtin_amdgcn_s_barrier();
    v2u ep = ld8_sc1_async(sws, m == 0 ? WS_EPOCH : 0x40000000u);
    float sval;
    const int wi = blockIdx.x * 4 + m;
    const u64 wbase = wi < 448 ? (u64)w1 + wi * 4096 : wi < 896 ? (u64)w2 + (wi - 448) * 4096 : (u64)wo + (wi - 896) * 4096;
    auto make_stats = [&]() {
      wait_vm2<1>(xr[0], xr[1]);
      PIN(warm);
      float piv[2], a[4];
#pragma unroll
      for (int u = 0; u < 2; ++u) {
        piv[u] = rdl(bflo(xr[u][0]), 0);
        float s1 = 0.f, s2 = 0.f;
#pragma unroll
        for (int i = 0; i < 8; ++i) { const float d = ((i & 1) ? bfhi(xr[u][i >> 1]) : bflo(xr[u][i >> 1])) - piv[u]; s1 += d; s2 = __builtin_fmaf(d, d, s2); }
        a[2 * u] = s1; a[2 * u + 1] = s2;
      }
#pragma unroll
      for (int i = 0; i < 4; ++i) a[i] += dpp<0xB1>(a[i]);
#pragma unroll
      for (int i = 0; i < 4; ++i) a[i] += dpp<0x4E>(a[i]);
      float t = p4 == 0 ? a[0] : p4 == 1 ? a[1] : p4 == 2 ? a[2] : a[3];
      t += dpp<0x114>(t); t += dpp<0x118>(t);
      const auto s16 = __builtin_amdgcn_permlane16_swap(asu(t), asu(t), false, false);
      t = asf(s16[0]) + asf(s16[1]);
      const auto s32 = __builtin_amdgcn_permlane32_swap(asu(t), asu(t), false, false);
      t = (asf(s32[0]) + asf(s32[1])) * (1.f / DM);
      const float e1 = dpp<0xA0>(t);
      sval = (p4 & 1) ? __builtin_fmaxf(__builtin_fmaf(-e1, e1, t), 0.f) + 1e-5f : t + (p4 ? piv[1] : piv[0]);
      PIN(sval);
    };
    const unsigned acnt = lds0 + L_CNT;
    const unsigned vpos = 2048 + (g & 1) * 512 + h * 256 + (lane & 15) * 16;
    v4u qf[2], am0, am1;
    {
      v4u xf[32];
      f32x16 acc[3] = {};
      f32x4 cs[3] = {};
      v4u wf[4][3];
      unsigned wb[4];
#pragma unroll
      for (int p = 0; p < 4; ++p) wb[p] = 16384 + ((p + m) & 3) * 1024 + (8 * h + q4) * 64 + (g & 1) * 32 + p4 * 8;
      auto rd = [&](int ks, int t) {
        const unsigned buf = lds0 + ((ks >> 2) % 3) * STG;
        if (t == 0) xf[ks] = lds128(buf + xo[ks & 3]);
        wf[ks & 3][t] = ldtr2(buf + wb[ks & 3] + t * 4096, 256);
      };
      auto mm = [&](int ks, int t) {
        acc[t] = t == 2 ? MFMA32(xf[ks], wf[ks & 3][t], acc[t]) : MFMA32(wf[ks & 3][t], xf[ks], acc[t]);
        if ((ks & 3) == 0) { cs[t] = MFMA16(sel, wf[ks & 3][t], cs[t]); PIN(cs[t]); }
        SBAR();
      };
      make_stats();
      bar();
      STAMP_AT(10);
#pragma unroll
      for (int t = 0; t < 6; ++t) rd(t / 3, t % 3);
      SBAR();
      static_for<0, 8>([&](auto sc) {
        constexpr int s = sc.value;
#pragma unroll
        for (int p = 0; p < 2; ++p) {
#pragma unroll
          for (int t = 0; t < 3; ++t) { mm(4 * s + p, t); rd(4 * s + p + 2, t); SBAR(); }
          if (s == 7 && p == 0) { cswr(4, cs[0]); cswr(5, cs[1]); cswr(6, cs[2]); SBAR(); }
        }
        if constexpr (s == 0) {
          wait_vmf<0>(ep);
          if (m == 0 && lane < 10)
            *(LAS unsigned*)(size_t)(lds0 + L_CNT + lane * 4) = lane == 8 ? ep[0] * 0x9E3779B1u + 0x7F4A7C15u : lane == 9 ? ep[1] * 0x85EBCA77u + 0x165667B1u : 0u;
        }
        if constexpr (s == 1) {
          get_tag();
          if ((lane >> 2) == 3) {
            const v2u gr = {asu(sval), tag32};
            __builtin_amdgcn_raw_buffer_store_b64(gr, rws, WS_STATS + (128 * r + 8 * j + 2 * m) * 16 + p4 * 8, 0, 16);
          }
        }
        if constexpr (s == MIDWARM) {
          warm = ld2_async(make_srd((const void*)wbase, 4096), lane * 128, 0);
        }
        if constexpr (s == 5) stat_load();
        if constexpr (s == 6) {
          wait_vm1<0>(st);
          PIN(warm);

          row_stats();
        }
        mm(4 * s + 2, 0); mm(4 * s + 2, 1);
        bar();
        if constexpr (s < 7) {
          STAMP_AT(11 + s);
          mm(4 * s + 2, 2);
          rd(4 * s + 4, 0); rd(4 * s + 4, 1); rd(4 * s + 4, 2); SBAR();
#pragma unroll
          for (int t = 0; t < 3; ++t) { mm(4 * s + 3, t); rd(4 * s + 5, t); SBAR(); }
        }
      });
      STAMP_AT(1);
      __builtin_amdgcn_s_setprio(3);
      const f32x4 c5 = csrd(5), c6 = csrd(6), c4 = csrd(4);
      SBAR();
      mm(30, 2); mm(31, 1);
      make_xaug();
      SBAR();
      mm(31, 2); mm(31, 0);
      if (h == 0) *(LAS float*)(size_t)(lds0 + L_RS + (m * 32 + r32) * 4) = rstd;
      acc[1] = MFMA32(waug(c5, 0u), xaug, acc[1]);
      acc[2] = MFMA32(xaug, waug(c6, 0u), acc[2]);
      acc[0] = MFMA32(waug(c4, 0u), xaug, acc[0]);
      SBAR();
      f32x4 rs[4];
#pragma unroll
      for (int qd = 0; qd < 4; ++qd) rs[qd] = __builtin_bit_cast(f32x4, lds128(lds0 + L_RS + (m * 32 + 8 * qd + 4 * h) * 4));
      const unsigned kvo = WS_KV + (j * 64 + T) * 4096;
#pragma unroll
      for (int e = 0; e < 16; ++e) acc[1][e] = MUL1(acc[1][e], rstd);
#pragma unroll
      for (int e = 0; e < 2; ++e) {
        const v4u f = pack8(acc[1], e);
        st16_sc1(f, sws, lane * 16, kvo + e * 1024);
        sts128(lds0 + L_KV + (8 + m) * KVT + e * 1024 + lane * 16, f);
      }
#pragma unroll
      for (int qd = 0; qd < 4; ++qd)
#pragma unroll
        for (int e = 0; e < 4; ++e) acc[2][4 * qd + e] = MUL1(acc[2][4 * qd + e], rs[qd][e]);
#pragma unroll
      for (int s = 0; s < 2; ++s) {
        const v4u f = pack8(acc[2], s);
        st16_sc1(f, sws, lane * 16, kvo + 2048 + s * 1024);
        sts128(lds0 + L_KV + (8 + m) * KVT + s * VB + vpos, f);
      }
      SBAR();
      const float qs = rstd * (0.25f * 1.4426950408889634f);
#pragma unroll
      for (int e = 0; e < 16; ++e) acc[0][e] = MUL1(acc[0][e], qs);
      qf[0] = pack8(acc[0], 0); qf[1] = pack8(acc[0], 1);
      PIN(qf[0]); PIN(qf[1]);
      STAMP_AT(2);
      const int T0 = 4 * r - 8 + 2 * m;
      const bool live = T0 >= 0;
      auto kv_load = [&](v4u (&kv)[8], int i0, int i1) {
        const unsigned src = WS_KV + (j * 64 + (live ? T0 : 0)) * 4096, vo = live ? lane * 16 : h * 512;
#pragma unroll
        for (int i = i0; i < i1; ++i) kv[i] = ld16_sc1_async(sws, vo, src + (live ? i : (i & 3)) * 1024);
      };
      auto kv_stage = [&](v4u (&kv)[8]) {
        const unsigned on = (lane < 2 || live) ? one2 : 0u;
        if (lane < 6) sts128(lds0 + L_KV + (lane < 2 ? 8 + m : 2 * m + ((lane - 2) >> 1)) * KVT + 2048 + (lane & 1) * VB + 1024, (v4u){on, on, on, on});
        const v4u zz = {};
#pragma unroll
        for (int i = 0; i < 2; ++i) {
          sts128(lds0 + L_KV + (2 * m + i) * KVT + lane * 16, kv[4 * i]);
          sts128(lds0 + L_KV + (2 * m + i) * KVT + 1024 + lane * 16, kv[4 * i + 1]);
          sts128(lds0 + L_KV + (2 * m + i) * KVT + vpos, live ? kv[4 * i + 2] : zz);
          sts128(lds0 + L_KV + (2 * m + i) * KVT + VB + vpos, live ? kv[4 * i + 3] : zz);
        }
        arrive_nowait(acnt);
      };
      auto kv_wait = [&](v4u (&kv)[8]) {
        wait_vm<0>();
#pragma unroll
        for (int i = 0; i < 8; ++i) PIN(kv[i]);
      };
      if (heavy) {
        bias_load(1);
        hsetup(1);
        await(acnt + 16, 4);
        STAMP_AT(3);
        v2u fl;
        v4u kv[8];
        hmm(std::integral_constant<int, 1>{}, xf, [&](auto kc) {
          constexpr int ks = kc.value;
          if constexpr (ks == KVPUB) {
            wait_vm<0>();
            PIN(bias0); PIN(bias1);
            if (lane == 0) __hip_atomic_store(flag(FL_KV, T, j), tag, RLX_AGENT);
          }
          if constexpr (ks == 24) fl = ld8_sc1_async(sws, flag_off(FL_KV, live ? T0 + (lane & 1) : 0, j));
        });
        STAMP_AT(18);
        wait_vmf<0>(fl);
        if (!__all(fl[0] == taglo && fl[1] == tag32)) poll_flags(flag(FL_KV, live ? T0 + (lane & 1) : 0, j), tag);
        kv_load(kv, 0, 8);
        hgelu(std::integral_constant<int, 1>{}, [&]() {});
        STAMP_AT(4);
        wait_vm<4>();
#pragma unroll
        for (int i = 0; i < 8; ++i) PIN(kv[i]);
        kv_stage(kv);
      } else {
        wait_vm<0>();
        if (lane == 0) __hip_atomic_store(flag(FL_KV, T, j), tag, RLX_AGENT);
        v4u kv[8];
        poll_flags(flag(FL_KV, live ? T0 + (lane & 1) : 0, j), tag);
        kv_load(kv, 0, 8); kv_wait(kv); kv_stage(kv);
      }
    }
    STAMP_AT(19);
    const unsigned kb = lds0 + L_KV + m * KVT + lane * 16;
    unsigned vb[2];
#pragma unroll
    for (int e = 0; e < 2; ++e) { vb[e] = lds0 + L_KV + m * KVT + 2048 + ((g & 1) ? 1024 : e * 512 + h * 256 + (lane & 15) * 16); PIN(vb[e]); }
    {
      const int dq = r32 - 4 * h;
#pragma unroll
      for (int d = 0; d < 8; ++d) { const int row = 2 * (d & 1) + 8 * (d >> 1); (d < 4 ? am0 : am1)[d & 3] = row > dq ? 0xffffffffu : row == dq ? 0xffff0000u : 0u; }
      PIN(am0); PIN(am1);
    }
    await(acnt, heavy ? 8 : 4);
    __builtin_amdgcn_s_setprio(PRIO_ATT);
    STAMP_AT(5);
    const f32x16 zero = {};
    auto o_store = [&](f32x16& o, int e) {
      const float rl = __builtin_amdgcn_rcpf(o[8]);
#pragma unroll
      for (int u = 0; u < 8; ++u) o[u] = MUL1(o[u], rl);
      st16_sc1(pack8(o, 0), sws, lane * 16, WS_AF + (T * AFT + 112 + 2 * j + e) * 1024);
    };
    int redo = 0;
    {
      f32x16 o[2], sa, sb, su;
      bool pend = false;
      v4u kn, ku, va, vc, vn0, vn1, vu0, vu1, p0, p1, pu;
      auto kaddr = [&](int n) { return kb + ((n & 7) + 1) * KVT + (n >> 3) * 1024; };
      auto vaddr = [&](int n) { return vb[n >> 3] + ((n & 7) + 1) * KVT; };
      kn = lds128(kaddr(0)); vn0 = lds128(vaddr(0)); vn1 = lds128(vaddr(0) + VB);
      sa = MFMA32(kn, qf[0], zero);
      kn = lds128(kaddr(1));
      SBAR();
      auto step = [&](auto nc, f32x16& sc, f32x16& sn) {
        constexpr int n = nc.value, e = n >> 3, i = n & 7;
        if constexpr (n < 15) sn = MFMA32(kn, qf[(n + 1) >> 3], zero);
        if constexpr (n < 14) kn = lds128(kaddr(n + 2));
        if constexpr (i == 5) ku = lds128(kb + e * 1024);
        v4u v0 = vn0, v1 = vn1;
        if constexpr (n < 15) { vn0 = lds128(vaddr(n + 1)); vn1 = lds128(vaddr(n + 1) + VB); }
        if constexpr (i == 6) { vu0 = lds128(vb[e]); vu1 = lds128(vb[e] + VB); }
        SBAR();
        if constexpr (i == 7) {
          static_for<0, 16>([&](auto u) { sc[u.value] = upper<u.value>() ? su[u.value] : sc[u.value]; });
          PIN(sc); SBAR();
        }
#pragma unroll
        for (int u = 0; u < 6; ++u) sc[u] = EXP2(sc[u]);
        PIN(sc); SBAR();
        if constexpr (n == 8) { o[0] = MFMA32(vc, p1, o[0]); SBAR(); }
        else if constexpr (n > 0) { o[e] = MFMA32(vc, p1, o[e]); SBAR(); }
        sc[6] = EXP2(sc[6]); sc[7] = EXP2(sc[7]);
        p0 = pack8(sc, 0);
        sc[8] = EXP2(sc[8]); sc[9] = EXP2(sc[9]);
        if constexpr (i == 7) { va = p0 & am0; p0 &= ~am0; PIN(va); }
        PIN(sc); PIN(p0); SBAR();
        if constexpr (n == 8) { o[0] = MFMA32(vu1, pu, o[0]); SBAR(); }
        o[e] = MFMA32(v0, p0, i == 0 ? zero : o[e]);
        SBAR();
        if constexpr (i == 6) { su = MFMA32(ku, qf[e], zero); SBAR(); }
#pragma unroll
        for (int u = 10; u < 13; ++u) sc[u] = EXP2(sc[u]);
        if constexpr (i == 7) { PIN(sc); SBAR(); o[e] = MFMA32(vu0, va, o[e]); SBAR(); }
#pragma unroll
        for (int u = 13; u < 16; ++u) sc[u] = EXP2(sc[u]);
        p1 = pack8(sc, 1); vc = v1;
        if constexpr (i == 7) { pu = p1 & am1; p1 &= ~am1; PIN(pu); }
        PIN(p1); SBAR();
        if constexpr (n == 8) {
          if (__all(o[0][8] >= 0x1p-30f && o[0][8] <= 0x1p30f)) o_store(o[0], 0); else redo = 1;
          SBAR();
        }
        if constexpr (i == 2) pend = !redo;
        if constexpr (i >= 2) {
          if (pend && (i == 7 || (__builtin_amdgcn_s_getreg(7 | (3 << 11)) & 15) == 0)) {
            wait_vm<0>();
            if (lane == 0) __hip_atomic_store(flag(FL_H1 + e, T, j), tag, RLX_AGENT);
            pend = false;
          }
        }
      };
      static_for<0, 8>([&](auto hc) {
        step(std::integral_constant<int, 2 * hc.value>{}, sa, sb);
        step(std::integral_constant<int, 2 * hc.value + 1>{}, sb, sa);
      });
      o[1] = MFMA32(vc, p1, o[1]);
      o[1] = MFMA32(vu1, pu, o[1]);
      if (__all(o[1][8] >= 0x1p-30f && o[1][8] <= 0x1p30f)) o_store(o[1], 1); else redo |= 2;
    }
    if (redo) {
#pragma unroll 1
      for (int e = 0; e < 2; ++e) {
        if (!((redo >> e) & 1)) continue;
        const v4u q = e ? qf[1] : qf[0];
        const unsigned vbe = e ? vb[1] : vb[0];
        auto merged = [&](const f32x16& c) {
          f32x16 sl = MFMA32(lds128(kb + 8 * KVT + e * 1024), q, c);
          const f32x16 su = MFMA32(lds128(kb + e * 1024), q, c);
          static_for<0, 16>([&](auto u) { sl[u.value] = upper<u.value>() ? su[u.value] : sl[u.value]; });
          return sl;
        };
        float mx = -INFINITY;
#pragma unroll 1
        for (int i = 0; i < 8; ++i) {
          const f32x16 sc = i ? MFMA32(lds128(kb + i * KVT + e * 1024), q, zero) : merged(zero);
#pragma unroll
          for (int u = 0; u < 16; u += 2) mx = max3(mx, sc[u], sc[u + 1]);
        }
        const auto sw = __builtin_amdgcn_permlane32_swap(asu(mx), asu(mx), false, false);
        mx = __builtin_fmaxf(asf(sw[0]), asf(sw[1]));
        f32x16 negm, o;
#pragma unroll
        for (int u = 0; u < 16; ++u) negm[u] = -mx;
        {
          f32x16 sc = merged(negm);
#pragma unroll
          for (int u = 0; u < 16; ++u) sc[u] = EXP2(sc[u]);
          const v4u p0 = pack8(sc, 0), p1 = pack8(sc, 1);
          o = MFMA32(lds128(vbe), p0 & am0, zero);
          o = MFMA32(lds128(vbe + VB), p1 & am1, o);
          o = MFMA32(lds128(vbe + 8 * KVT), p0 & ~am0, o);
          o = MFMA32(lds128(vbe + 8 * KVT + VB), p1 & ~am1, o);
        }
#pragma unroll 1
        for (int i = 1; i < 8; ++i) {
          f32x16 sc = MFMA32(lds128(kb + i * KVT + e * 1024), q, negm);
#pragma unroll
          for (int u = 0; u < 16; ++u) sc[u] = EXP2(sc[u]);
          o = MFMA32(lds128(vbe + i * KVT), pack8(sc, 0), o);
          o = MFMA32(lds128(vbe + i * KVT + VB), pack8(sc, 1), o);
        }
        o_store(o, e);
      }
      wait_vm<0>();
      if (lane == 0) __hip_atomic_store(flag(FL_O0, T, j), tag, RLX_AGENT);
    }
    STAMP_AT(6);
    wait_vm<0>();
    if (lane == 0) __hip_atomic_store(flag(FL_O1, T, j), tag, RLX_AGENT);
    STAMP_AT(7);
    res_loads();
    wait_vm2<0>(xres, bres);
    if (blockIdx.x == 0 && wave == 7) {
#pragma unroll 1
      for (int i = 0; i < 4; ++i) poll_flags(flag(FL_KV, 4 * (4 * i + g), lane & 15), tag);
      if (lane == 0) __hip_atomic_store((GAS u64*)(ws + WS_EPOCH), tag, RLX_AGENT);
    }
  }
  bar();
  {
    int t2 = threadIdx.x; PIN(t2);
    const int row = t2 >> 3, ch = t2 & 7, orow = 64 * rt + row, ocol = 64 * ct + ch * 8;
    const unsigned a = lds0 + L_WG + (((row >> 5) * 8 + ch) * 66 + (row & 31)) * 16;
    f32x4 lo = {}, hi = {};
#pragma unroll
    for (int w = 0; w < 4; ++w) {
      lo += __builtin_bit_cast(f32x4, lds128(a + w * PART));
      hi += __builtin_bit_cast(f32x4, lds128(a + w * PART + 512));
    }
    v4u o;
    o[0] = pk2(lo[0] + bflo(xres[0]) + bflo(bres[0]), lo[1] + bfhi(xres[0]) + bfhi(bres[0]));
    o[1] = pk2(lo[2] + bflo(xres[1]) + bflo(bres[1]), lo[3] + bfhi(xres[1]) + bfhi(bres[1]));
    o[2] = pk2(hi[0] + bflo(xres[2]) + bflo(bres[2]), hi[1] + bfhi(xres[2]) + bfhi(bres[2]));
    o[3] = pk2(hi[2] + bflo(xres[3]) + bflo(bres[3]), hi[3] + bfhi(xres[3]) + bfhi(bres[3]));
    *(GAS v4u*)((char*)out + (size_t)orow * 1024 + ocol * 2) = o;
  }
  STAMP_AT(8);
  if (tstart) while (now_t() - tstart < 4000) __builtin_amdgcn_s_sleep(1);
}

extern "C" void kernel_launch(void* const* d_in, const int* in_sizes, int n_in, void* d_out, int out_size, void* d_ws, size_t ws_size,
                              hipStream_t stream) {
  (void)in_sizes; (void)n_in; (void)out_size; (void)ws_size;
  (void)hipFuncSetAttribute((const void*)fused_block, hipFuncAttributeMaxDynamicSharedMemorySize, LDS_BYTES);
  fused_block<<<256, 512, LDS_BYTES, stream>>>(d_in[0], d_in[1], d_in[2], d_in[4], d_in[5], d_in[6], d_in[7], d_out, (char*)d_ws);
}
```

```cpp
#include <hip/hip_runtime.h>
#include <hip/hip_bf16.h>
#include <stdint.h>
#include <type_traits>

typedef unsigned v4u __attribute__((ext_vector_type(4)));
typedef unsigned v2u __attribute__((ext_vector_type(2)));
typedef float f32x2 __attribute__((ext_vector_type(2)));
typedef float f32x4 __attribute__((ext_vector_type(4)));
typedef float f32x16 __attribute__((ext_vector_type(16)));
typedef short bf16x8 __attribute__((ext_vector_type(8)));
typedef short s16x4 __attribute__((ext_vector_type(4)));
typedef __bf16 bf16x2_t __attribute__((ext_vector_type(2)));
typedef unsigned long long u64;
#define GAS __attribute__((address_space(1)))
#define LAS __attribute__((address_space(3)))
#define DI __device__ __forceinline__
#define RLX_AGENT __ATOMIC_RELAXED, __HIP_MEMORY_SCOPE_AGENT

constexpr int SEQ = 2048, DM = 512, FF = 1792;
constexpr unsigned WS_EPOCH = 0;
constexpr unsigned WS_STATS = 32768;
constexpr unsigned WS_FLAG = 65536;
constexpr unsigned WS_KV = WS_FLAG + 5 * 1024 * 128;
constexpr int AFT = 145;
constexpr unsigned WS_AF = WS_KV + 16 * 64 * 4096;
constexpr unsigned WS_END = WS_AF + 64 * AFT * 1024;
constexpr unsigned WS_STAMP = WS_END;
enum { FL_KV = 0, FL_H0 = 1, FL_H1 = 2, FL_O0 = 3, FL_O1 = 4 };
constexpr int STG = 36864;
constexpr int L_WA = 0;
constexpr int L_WG = 86016;
constexpr int VB = 1040;
constexpr int KVT = 2048 + 2 * VB;
constexpr int L_KV = 65536 - 8 * KVT;
constexpr int RD = 8, NR = 12;
constexpr int PART = 16 * 66 * 16;
constexpr int L_CS = L_WG + 4 * PART;
constexpr int L_RS = L_CS + 3584;
constexpr int L_CNT = L_RS + 512;
constexpr int LDS_BYTES = L_CNT + 64;
static_assert(L_KV + 12 * KVT <= L_WG && L_WG >= 2 * STG && L_CS >= 3 * STG && PART >= RD * 2048 && LDS_BYTES <= 163840, "lds map");
#define AWIN 6
constexpr int AW = AWIN;
constexpr unsigned SPIN_CAP = 1u << 23;

#define MFMA32(a, b, c) __builtin_amdgcn_mfma_f32_32x32x16_bf16(__builtin_bit_cast(bf16x8, a), __builtin_bit_cast(bf16x8, b), c, 0, 0, 0)
#define MFMA16(a, b, c) __builtin_amdgcn_mfma_f32_16x16x32_bf16(__builtin_bit_cast(bf16x8, a), __builtin_bit_cast(bf16x8, b), c, 0, 0, 0)
#define SBAR() __builtin_amdgcn_sched_barrier(0)

#define STAMP_AT(i) do { } while (0)

DI float asf(unsigned u) { return __builtin_bit_cast(float, u); }
DI unsigned asu(float f) { return __builtin_bit_cast(unsigned, f); }
DI unsigned pk2(float lo, float hi) { return __builtin_bit_cast(unsigned, __builtin_convertvector((f32x2){lo, hi}, bf16x2_t)); }
DI float bflo(unsigned u) { return __builtin_bit_cast(float, u << 16); }
DI float bfhi(unsigned u) { return __builtin_bit_cast(float, u & 0xffff0000u); }
DI v4u make_srd(const void* p, unsigned bytes) { const u64 a = (u64)p; return (v4u){(unsigned)a, (unsigned)(a >> 32) & 0xffffu, bytes, 0x00020000u}; }
DI void dma16(v4u srd, unsigned voff, unsigned soff, unsigned lds) {
  asm volatile("s_mov_b32 m0, %3\n\ts_nop 0\n\tbuffer_load_dwordx4 %0, %1, %2 offen lds" :: "v"(voff), "s"(srd), "s"(soff), "s"(lds) : "memory");
}
DI void dma16_a(v4u srd, unsigned voff, unsigned soff, unsigned lds) { dma16(srd, voff, soff, lds); }
DI void dma16_b(v4u srd, unsigned voff1, unsigned soff) {
  asm volatile("buffer_load_dwordx4 %0, %1, %2 offen offset:1024 lds" :: "v"(voff1), "s"(srd), "s"(soff) : "memory");
}
DI v4u ld16_sc1_async(v4u srd, unsigned voff, unsigned soff) {
  v4u r; asm volatile("buffer_load_dwordx4 %0, %1, %2, %3 offen sc1" : "=v"(r) : "v"(voff), "s"(srd), "s"(soff) : "memory"); return r;
}
DI v4u ld16_async(v4u srd, unsigned voff, unsigned soff) {
  v4u r; asm volatile("buffer_load_dwordx4 %0, %1, %2, %3 offen" : "=v"(r) : "v"(voff), "s"(srd), "s"(soff) : "memory"); return r;
}
DI unsigned ld2_async(v4u srd, unsigned voff, unsigned soff) {
  unsigned r; asm volatile("buffer_load_ushort %0, %1, %2, %3 offen" : "=v"(r) : "v"(voff), "s"(srd), "s"(soff) : "memory"); return r;
}
DI void st16_sc1(v4u v, v4u srd, unsigned voff, unsigned soff) {
  asm volatile("buffer_store_dwordx4 %0, %1, %2, %3 offen sc1\n\ts_nop 1" :: "v"(v), "v"(voff), "s"(srd), "s"(soff) : "memory");
}
template <int N> DI void wait_vm1(v4u& a) { asm volatile("s_waitcnt vmcnt(%1)" : "+v"(a) : "i"(N) : "memory"); }
template <int N> DI void wait_vm2(v4u& a, v4u& b) { asm volatile("s_waitcnt vmcnt(%2)" : "+v"(a), "+v"(b) : "i"(N) : "memory"); }
template <int N> DI void wait_vm3(v4u& a, v4u& b, v4u& c) { asm volatile("s_waitcnt vmcnt(%3)" : "+v"(a), "+v"(b), "+v"(c) : "i"(N) : "memory"); }
template <class V> DI void pin(V& v) { asm volatile("" : "+v"(v)); }
template <int N> DI void wait_vm() { asm volatile("s_waitcnt vmcnt(%0)" :: "i"(N) : "memory"); }
DI void bar() { asm volatile("s_waitcnt lgkmcnt(0)\n\ts_barrier" ::: "memory"); }
DI v4u lds128(unsigned a) { return *(const LAS v4u*)(size_t)a; }
DI void sts128(unsigned a, v4u v) { *(LAS v4u*)(size_t)a = v; }
DI v2u ldtr(unsigned a) { return __builtin_bit_cast(v2u, __builtin_amdgcn_ds_read_tr16_b64_v4i16((LAS s16x4*)(size_t)a)); }
DI v4u ldtr2(unsigned a, int second) { const v2u lo = ldtr(a), hi = ldtr(a + second); return (v4u){lo.x, lo.y, hi.x, hi.y}; }
template <int CTRL> DI float dpp(float v) { return __builtin_bit_cast(float, __builtin_amdgcn_update_dpp(0, __builtin_bit_cast(int, v), CTRL, 0xf, 0xf, true)); }
DI float rdl(float v, int l) { return __builtin_bit_cast(float, __builtin_amdgcn_readlane(__builtin_bit_cast(int, v), l)); }
DI v4u pack8(const f32x16& a, int s) { return (v4u){pk2(a[8 * s], a[8 * s + 1]), pk2(a[8 * s + 2], a[8 * s + 3]), pk2(a[8 * s + 4], a[8 * s + 5]), pk2(a[8 * s + 6], a[8 * s + 7])}; }
DI void poll_flags(const GAS u64* p, u64 tag) {
#pragma unroll 1
  for (unsigned spin = 0; spin < SPIN_CAP; ++spin) {
    const u64 v = __hip_atomic_load(p, RLX_AGENT);
    if (__all(v == tag)) break;
    __builtin_amdgcn_s_sleep(2);
  }
  asm volatile("" ::: "memory");
}
constexpr u64 upper_mask(int reg) {
  u64 m = 0;
  for (int l = 0; l < 64; ++l) { const int row = (reg & 3) + 8 * (reg >> 2) + 4 * (l >> 5); if (row > (l & 31)) m |= 1ull << l; }
  return m;
}
template <int REG> DI bool upper() { return __builtin_amdgcn_inverse_ballot_w64(upper_mask(REG)); }
template <int I, int N, class Fn> DI void static_for(Fn&& f) { if constexpr (I < N) { f(std::integral_constant<int, I>{}); static_for<I + 1, N>(f); } }
DI float max3(float a, float b, float c) { return __builtin_fmaxf(__builtin_fmaxf(a, b), c); }

constexpr int H1_CK = 16 - AW, H1_LD = H1_CK - 3, O_CK = 28 - AW, O_LD = O_CK - 3;
constexpr int g2_issued(int t) { return ((t >= 1 && t - 1 + NR + RD < 36) ? 2 : 0) + ((t + AW < 28) ? 2 : 0) + (t == H1_LD) + (t == O_LD) + (t == O_CK ? 10 : 0); }
constexpr int g2_range(int a, int b) { int n = 0; for (int t = a; t < b; ++t) n += g2_issued(t); return n; }
#define GELU_PK(P) (P == 1)
#define PRIO_G2 2
#define PRIO_ATT 3
#define PRIO_G1 2
#define ROT r
#define CODEWARM 39000
#define MIDWARM 1
#define KVMID 0
#define KVPUB 2
#define EXP2(v) __builtin_amdgcn_exp2f(v)
#define PIN(v) pin(v)
extern "C" __device__ float mul_unpacked(float, float) __asm("llvm.amdgcn.fmul.legacy");
#define MUL1(a, b) mul_unpacked(a, b)
#define FMA1(a, b, c) __builtin_amdgcn_div_fmasf(a, b, c, false)
DI v2u ld8_sc1_async(v4u srd, unsigned voff) {
  v2u r; asm volatile("buffer_load_dwordx2 %0, %1, %2, 0 offen sc1" : "=v"(r) : "v"(voff), "s"(srd) : "memory"); return r;
}
template <int N> DI void wait_vmf(v2u& a) { asm volatile("s_waitcnt vmcnt(%1)" : "+v"(a) : "i"(N) : "memory"); }
DI bool drained() { return (__builtin_amdgcn_s_getreg(7 | (3 << 11)) & 15) == 0; }
DI void arrive(unsigned cnt, int lane) {
  asm volatile("s_waitcnt lgkmcnt(0)" ::: "memory");
  if (lane == 0) __hip_atomic_fetch_add((LAS unsigned*)(size_t)cnt, 1u, __ATOMIC_RELAXED, __HIP_MEMORY_SCOPE_WORKGROUP);
}
DI void arrive_nowait(unsigned cnt) {
  asm volatile("s_mov_b64 exec, 1\n\tds_add_u32 %0, %1\n\ts_mov_b64 exec, -1" :: "v"(cnt), "v"(1u) : "memory");
}
DI unsigned peek(unsigned cnt) { return __hip_atomic_load((LAS unsigned*)(size_t)cnt, __ATOMIC_RELAXED, __HIP_MEMORY_SCOPE_WORKGROUP); }
DI void await(unsigned cnt, unsigned n) {
#pragma unroll 1
  for (unsigned spin = 0; spin < SPIN_CAP * 32; ++spin) {
    if (__builtin_amdgcn_readfirstlane(peek(cnt)) >= n) break;
    __builtin_amdgcn_s_sleep(1);
  }
  asm volatile("" ::: "memory");
}

__global__ __launch_bounds__(512, 2) void fused_block(const void* __restrict__ x, const void* __restrict__ wqkv, const void* __restrict__ wo,
                                                      const void* __restrict__ w1, const void* __restrict__ b1, const void* __restrict__ w2,
                                                      const void* __restrict__ b2, void* __restrict__ out, char* __restrict__ ws) {
  constexpr unsigned lds0 = 0;
  const int wave = __builtin_amdgcn_readfirstlane(threadIdx.x >> 6);
  const int m = wave & 3;
  const bool isG = wave < 4;
  const int xcd = blockIdx.x & 7, idx = blockIdx.x >> 3;
  const int r = (xcd >> 2) * 8 + (idx >> 2), j = (xcd & 3) * 4 + (idx & 3);
  const int rt = (xcd >> 1) * 8 + (idx >> 2), ct = (xcd & 1) * 4 + (idx & 3);
  int lane = threadIdx.x & 63, r32, h, g, q4, p4;
  STAMP_AT(0);
  asm volatile("" :: "s"(x), "s"(wqkv), "s"(wo), "s"(w1), "s"(b1), "s"(w2), "s"(b2), "s"(out), "s"(ws));

  const v4u sx = make_srd(x, SEQ * DM * 2), sws = make_srd(ws, WS_END);
  const __amdgpu_buffer_rsrc_t rws = __builtin_amdgcn_make_buffer_rsrc(ws, 0, WS_END, 0x00020000);
  const unsigned one2 = 0x3f803f80u;
  const int T = 4 * r + m;
  const int rot = ROT;
  auto flag_off = [&](int kind, int tile, int jj) { return WS_FLAG + ((kind * 64 + tile) * 16 + jj) * 128; };
  auto flag = [&](int kind, int tile, int jj) { return (GAS u64*)(ws + flag_off(kind, tile, jj)); };
  u64 tag;
  unsigned taglo, tag32;
  auto get_tag = [&]() {
    const v2u tg = *(const LAS v2u*)(size_t)(lds0 + L_CNT + 32);
    taglo = __builtin_amdgcn_readfirstlane(tg[0]); tag32 = __builtin_amdgcn_readfirstlane(tg[1]);
    tag = ((u64)tag32 << 32) | taglo;
  };
  v4u xres, bres, xaug, st;
  float mean, var, rstd, sd;
  v4u sel;
  unsigned xo[4], swz;
  auto lane_setup = [&]() {
    lane = threadIdx.x & 63; PIN(lane);
    r32 = lane & 31; h = lane >> 5; g = lane >> 4; q4 = (lane >> 2) & 3; p4 = lane & 3;
    const unsigned selv = ((lane & 15) == (g & 1)) ? one2 : 0u;
    sel = (v4u){selv, selv, selv, selv};
#pragma unroll
    for (int p = 0; p < 4; ++p) xo[p] = (32 * m + r32) * 128 + (((2 * ((p + m) & 3) + h) ^ ((r32 >> 1) & 7)) * 16);
    swz = ((((lane >> 2) & 1) ^ (g & 1)) * 64) + p4 * 16;
  };

  auto stat_load = [&]() { st = ld16_sc1_async(sws, WS_STATS + (128 * r + 32 * m + r32) * 16, 0); };
  auto res_loads = [&]() {
    int t2 = threadIdx.x; PIN(t2);
    const int row = 64 * rt + (t2 >> 3), col = 64 * ct + (t2 & 7) * 8;
    xres = ld16_async(sx, row * 1024 + col * 2, 0);
    bres = ld16_async(make_srd(b2, DM * 2), col * 2, 0);
  };
  auto row_stats = [&]() {
    if (!__all(st[1] == tag32 && st[3] == tag32)) {
#pragma unroll 1
      for (unsigned spin = 0; spin < SPIN_CAP; ++spin) {
        st = ld16_sc1_async(sws, WS_STATS + (128 * r + 32 * m + r32) * 16, 0);
        wait_vm1<0>(st);
        if (__all(st[1] == tag32 && st[3] == tag32)) break;
      }
    }
    mean = asf(st[0]); var = asf(st[2]);
    PIN(mean); PIN(var);
  };
  auto make_xaug = [&]() {
    PIN(mean); PIN(var);
    rstd = __builtin_amdgcn_rsqf(var); sd = var * rstd;
    const unsigned hi = pk2(-mean, sd);
    const unsigned lo = pk2(-mean - bflo(hi), FMA1(bfhi(hi), -1.f, sd));
    xaug = (v4u){(hi & 0xffffu) | (hi << 16), (lo & 0xffffu) | (hi & 0xffff0000u), lo >> 16, 0u};
    if (h) xaug = (v4u){0u, 0u, 0u, 0u};
    PIN(xaug);
  };
  auto cswr = [&](int tile, f32x4 c) {
    if (lane < 16) { LAS float* p = (LAS float*)(size_t)(lds0 + L_CS + (tile * 32 + lane) * 16 + m * 4); p[0] = c[0]; p[64] = c[1]; }
  };
  auto csrd = [&](int tile) { return __builtin_bit_cast(f32x4, lds128(lds0 + L_CS + (tile * 32 + r32) * 16)); };
  auto waug = [&](f32x4 c4, unsigned b16) {
    const float c = ((c4[0] + c4[1]) + c4[2]) + c4[3];
    const unsigned hi = pk2(c, 0.f);
    const unsigned lo = pk2(c - bflo(hi), 0.f);
    return (v4u){hi | (lo << 16), hi | (b16 << 16), b16, 0u};
  };

  const v4u sw1 = make_srd(w1, DM * FF * 2);
  const int hcol0 = 448 * (j >> 2) + 128 * (j & 3);
  const bool heavy = (j & 3) != 3;
  unsigned bias0, bias1;
  auto bias_load = [&](int P) {
    bias0 = ld2_async(make_srd(b1, FF * 2), r32 * 2, (hcol0 + 64 * P) * 2);
    bias1 = ld2_async(make_srd(b1, FF * 2), r32 * 2, (hcol0 + 64 * P + 32) * 2);
  };
  auto w1dma = [&](int P, int c, unsigned dst, int half) {
    const unsigned vw1 = (lane >> 3) * (FF * 2) + swz, vw1b = vw1 + 8 * (FF * 2) - 1024;
    const unsigned so = (64 * ((c + rot) & 7) + 16 * m) * (FF * 2) + (hcol0 + 64 * P) * 2;
    if (half == 0) dma16_a(sw1, vw1, so, dst + m * 2048); else dma16_b(sw1, vw1b, so);
  };
  f32x16 hac[2];
  unsigned rk[4][2];
  auto hsetup = [&](int P) {
#pragma unroll
    for (int p = 0; p < 4; ++p)
#pragma unroll
      for (int c = 0; c < 2; ++c) {
        rk[p][c] = lds0 + (P ? L_WA : 28672) + ((p + m) & 3) * 2048 + h * 1024 + q4 * 128 + ((c ^ (q4 >> 1)) * 64) + (g & 1) * 32 + p4 * 8;
        PIN(rk[p][c]);
      }
  };
  auto hmm = [&](auto pc, const v4u (&xf)[32], auto&& hook) {
    constexpr int P = pc.value;
    constexpr int D = 4;
    const unsigned cnt = lds0 + L_CNT;
    f32x4 cs[2] = {}, c0, c1;
    v4u wf[D][2], au0, au1;
    unsigned seen = 0;
    hac[0] = (f32x16){}; hac[1] = (f32x16){};
    auto rd = [&](int ks, int t) { wf[ks % D][t] = ldtr2(rk[ks & 3][t] + (ks >> 2) * 8192, 512); };
#pragma unroll
    for (int ks = 0; ks < D; ++ks) { rd(ks, 0); rd(ks, 1); }
    SBAR();
    static_for<0, 32>([&](auto kc) {
      constexpr int ks = kc.value;
      hook(kc);
      if constexpr (ks == 10) seen = peek(cnt + 20);
      if constexpr (ks == 29) seen = peek(cnt);
#pragma unroll
      for (int t = 0; t < 2; ++t) {
        hac[t] = MFMA32(wf[ks % D][t], xf[ks], hac[t]);
        if ((ks & 3) == 0 && ks < 28) { cs[t] = MFMA16(sel, wf[ks % D][t], cs[t]); PIN(cs[t]); }
        if (ks == 26) { cs[t] = MFMA16(sel, wf[28 % D][t], cs[t]); PIN(cs[t]); }
        SBAR();
        if constexpr (ks + D < 32) rd(ks + D, t);
        if constexpr (ks == 31) { if (t == 0) { au0 = waug(c0, bias0); PIN(au0); } else { au1 = waug(c1, bias1); PIN(au1); } }
        SBAR();
      }
      if constexpr (ks == 11 || ks == 30) {
        PIN(hac[0]); PIN(hac[1]);
        if (__builtin_amdgcn_readfirstlane(seen) < 4) await(ks == 11 ? cnt + 20 : cnt, 4);
        asm volatile("" ::: "memory");
        if constexpr (ks == 30) { c0 = csrd(2 * P); c1 = csrd(2 * P + 1); }
      }
      if constexpr (ks == 27) { cswr(2 * P, cs[0]); cswr(2 * P + 1, cs[1]); arrive_nowait(cnt); }
      SBAR();
    });
    hac[0] = MFMA32(au0, xaug, hac[0]);
    hac[1] = MFMA32(au1, xaug, hac[1]);
    SBAR();
  };
  auto hgelu = [&](auto pc, auto&& mid) {
    constexpr int P = pc.value;
    constexpr bool PK = GELU_PK(P);
    const float C0 = 2.3022081986f, C1 = C0 * 0.044715f;
    const float ga = -C1 * rstd * rstd * rstd, gb = -C0 * rstd;
    auto mul = [&](f32x2 a, f32x2 b) { return PK ? a * b : (f32x2){MUL1(a[0], b[0]), MUL1(a[1], b[1])}; };
    auto fma = [&](f32x2 a, float b, float c) {
      return PK ? __builtin_elementwise_fma(a, (f32x2){b, b}, (f32x2){c, c}) : (f32x2){FMA1(a[0], b, c), FMA1(a[1], b, c)}; };
#pragma unroll
    for (int t = 0; t < 2; ++t) {
      f32x2 v[8], z[8];
#pragma unroll
      for (int e = 0; e < 8; ++e) { v[e] = (f32x2){hac[t][2 * e], hac[t][2 * e + 1]}; z[e] = mul(v[e], v[e]); }
      SBAR();
#pragma unroll
      for (int e = 0; e < 8; ++e) z[e] = fma(z[e], ga, gb);
      SBAR();
#pragma unroll
      for (int e = 0; e < 8; ++e) z[e] = mul(z[e], v[e]);
      SBAR();
#pragma unroll
      for (int e = 0; e < 8; ++e) z[e] = (f32x2){EXP2(z[e][0]), EXP2(z[e][1])};
      SBAR();
#pragma unroll
      for (int e = 0; e < 8; ++e) z[e] = fma(z[e], sd, sd);
      SBAR();
#pragma unroll
      for (int e = 0; e < 8; ++e) z[e] = (f32x2){__builtin_amdgcn_rcpf(z[e][0]), __builtin_amdgcn_rcpf(z[e][1])};
      SBAR();
#pragma unroll
      for (int e = 0; e < 8; ++e) z[e] = mul(z[e], v[e]);
      SBAR();
#pragma unroll
      for (int s = 0; s < 2; ++s) {
        const v4u f = {pk2(z[4 * s][0], z[4 * s][1]), pk2(z[4 * s + 1][0], z[4 * s + 1][1]), pk2(z[4 * s + 2][0], z[4 * s + 2][1]), pk2(z[4 * s + 3][0], z[4 * s + 3][1])};
        st16_sc1(f, sws, lane * 16, WS_AF + (T * AFT + (hcol0 >> 4) + 4 * P + 2 * t + s) * 1024);
      }
      if (t == 0) mid();
      SBAR();
    }
  };

  if (isG) {
    lane_setup();
    const v4u sw2 = make_srd(w2, FF * DM * 2), swo = make_srd(wo, DM * DM * 2);
    const unsigned vw2 = (lane >> 3) * 1024 + swz, vw2b = vw2 + 7 * 1024;
    const int ro3 = rt % 3;
    auto kmap = [&](int kk) {
      if (kk < 16) return 8 * (((kk >> 2) + rt) & 3) + (kk & 3);
      const int p = ((kk - 16) >> 2) + ro3;
      return 8 * (p >= 3 ? p - 3 : p) + 4 + (kk & 3);
    };
    auto omap = [&](int i) { return 2 * ((i + rt) & 3) + (i >> 2); };
    auto wdmah = [&](int kk, int half) {
      const unsigned so = (kk < 28 ? 448 * m + 16 * kmap(kk) : 128 * m + 16 * omap(kk - 28)) * 1024 + 128 * ct;
      if (half == 0) dma16_a(kk < 28 ? sw2 : swo, vw2, so, lds0 + L_WG + m * PART + (kk % RD) * 2048); else dma16_b(kk < 28 ? sw2 : swo, vw2b, so);
    };
    auto wdma = [&](int kk) { wdmah(kk, 0); wdmah(kk, 1); };
    {
      const v4u swq = make_srd(wqkv, DM * 3 * DM * 2);
      const unsigned vx0 = (lane >> 3) * 1024 + (((lane & 7) ^ g) * 16), vx1 = (7 + (lane >> 3)) * 1024 + (((lane & 7) ^ (4 + g)) * 16);
      const unsigned vwq = (lane >> 2) * (3 * DM * 2) + p4 * 16;
      auto piece = [&](int s, int i) {
        const unsigned buf = lds0 + (s % 3) * STG;
        const int kc = (s + rot) & 7;
        if (i < 4) {
          const unsigned so = (128 * r + 32 * m + 16 * (i >> 1)) * 1024 + kc * 128;
          if (i & 1) dma16_b(sx, vx1, so); else dma16_a(sx, vx0, so, buf + m * 4096 + (i >> 1) * 2048);
        } else if (i < 7) dma16(swq, vwq, (64 * kc + 16 * m) * (3 * DM * 2) + (512 * (i - 4) + 32 * j) * 2, buf + 16384 + ((i - 4) * 4 + m) * 1024);
        else w1dma(0, s, buf + 28672, i - 7);
      };
#pragma unroll
      for (int i = 0; i < 18; ++i) { piece(i / 9, i % 9); if (i == 8) __builtin_amdgcn_s_barrier(); }
      __builtin_amdgcn_s_setprio(PRIO_G1);
      hsetup(0);
      f32x4 cs[2] = {};
      v4u xf[4], wf[4][2];
      hac[0] = (f32x16){}; hac[1] = (f32x16){};
      static_for<0, 8>([&](auto sc) {
        constexpr int s = sc.value;
        constexpr unsigned buf = (s % 3) * STG;
        auto fill = [&](int i) {
          if constexpr (s < 6) piece(s + 2, i);
          if constexpr (s == 6) { if (i == 0) { stat_load(); bias_load(0); } else wdmah((i - 1) >> 1, (i - 1) & 1); }
          if constexpr (s == 7) { if (heavy) { w1dma(1, i, lds0 + L_WA + i * 8192, 0); w1dma(1, i, 0, 1); } }
          SBAR();
        };
        auto rd = [&](int p) { xf[p] = lds128(buf + xo[p]); wf[p][0] = ldtr2(buf + rk[p][0], 512); wf[p][1] = ldtr2(buf + rk[p][1], 512); SBAR(); };
        if constexpr (s < 7) wait_vm<9>(); else { wait_vm1<8>(st); PIN(bias0); PIN(bias1); }
        bar();
        STAMP_AT(10 + s);
        rd(0);
        if constexpr (s < 7) fill(0);
        rd(1);
        if constexpr (s < 7) fill(1);
        if constexpr (s == 2) get_tag();
#pragma unroll
        for (int p = 0; p < 4; ++p) {
#pragma unroll
          for (int t = 0; t < 2; ++t) {
            hac[t] = MFMA32(wf[p][t], xf[p], hac[t]);
            if (p == 0) { cs[t] = MFMA16(sel, wf[p][t], cs[t]); PIN(cs[t]); }
            SBAR();
            if (p < 2 && t == 0) rd(p + 2);
            if (s == 7) fill(2 * p + t); else if (2 * p + t < 7) fill(2 + 2 * p + t);
          }
          if (s == 7 && p == 1) {
            cswr(0, cs[0]); cswr(1, cs[1]);
            bar();
            STAMP_AT(1);
          }
        }
        if constexpr (s == 7) {
          const f32x4 c0 = csrd(0), c1 = csrd(1);
          row_stats(); make_xaug();
          hac[0] = MFMA32(waug(c0, bias0), xaug, hac[0]);
          hac[1] = MFMA32(waug(c1, bias1), xaug, hac[1]);
          SBAR();
        }
      });
    }
    STAMP_AT(18);
    if (heavy) { wait_vm<8>(); arrive_nowait(lds0 + L_CNT + 16); }
    hgelu(std::integral_constant<int, 0>{}, [&]() {
      wait_vm<2>();
      if (heavy) arrive_nowait(lds0 + L_CNT + 20);
    });
    __builtin_amdgcn_s_setprio(0);
    STAMP_AT(19);
    unsigned rb2[2];
#pragma unroll
    for (int c = 0; c < 2; ++c) rb2[c] = lds0 + L_WG + m * PART + h * 512 + q4 * 128 + ((c ^ (q4 >> 1)) * 64) + (g & 1) * 32 + p4 * 8;
    v4u wq[36][2];
    auto wrd = [&](int kk) {
#pragma unroll
      for (int c = 0; c < 2; ++c) wq[kk][c] = ldtr2(rb2[c] + (kk % RD) * 2048, 1024);
    };
    auto wgot = [&](int kk) { PIN(wq[kk][0]); PIN(wq[kk][1]); };
    const int ftile = 2 * rt + ((lane >> 2) & 1);
    v4u af[28][2], of[8][2];
    v2u fl;
    const unsigned a0 = WS_AF + (2 * rt * AFT + 28 * m) * 1024, o0 = WS_AF + (2 * rt * AFT + 112 + 8 * m) * 1024;
    for (int kk = 0; kk < 4; ++kk) wrd(kk);
    for (int kk = 4; kk < 8; ++kk) wdma(kk);
    for (int kk = 0; kk < 4; ++kk) wgot(kk);
    wait_vm<8>();
    if (lane == 0) __hip_atomic_store(flag(FL_H0, T, j), tag, RLX_AGENT);
    STAMP_AT(2);
    for (int kk = 8; kk < 12; ++kk) wdma(kk);
    static_for<1, 3>([&](auto ic) {
      constexpr int i = ic.value;
      wait_vm<(i == 1) ? 9 : 8>();
      for (int kk = 4 * i; kk < 4 * i + 4; ++kk) wrd(kk);
      for (int kk = 4 * i; kk < 4 * i + 4; ++kk) wgot(kk);
      if constexpr (i == 2) fl = ld8_sc1_async(sws, flag_off(FL_H0, ftile, 4 * m + p4));
      for (int kk = 4 * i; kk < 4 * i + 4; ++kk) wdma(kk + RD);
    });
    STAMP_AT(3);
    wait_vmf<8>(fl);
    if (!__all(fl[0] == taglo && fl[1] == tag32)) poll_flags(flag(FL_H0, ftile, 4 * m + p4), tag);
    STAMP_AT(4);
#pragma unroll
    for (int kk = 0; kk < AW; ++kk) {
      af[kk][0] = ld16_sc1_async(sws, lane * 16, a0 + kmap(kk) * 1024);
      af[kk][1] = ld16_sc1_async(sws, lane * 16, a0 + (AFT + kmap(kk)) * 1024);
    }
    __builtin_amdgcn_s_setprio(PRIO_G2);
    f32x16 c2[2][2] = {};
    auto mm2 = [&](int kk, const v4u (&a)[2], auto&& fill) {
      SBAR();
#pragma unroll
      for (int i = 0; i < 4; ++i) { c2[i >> 1][i & 1] = MFMA32(wq[kk][i & 1], a[i >> 1], c2[i >> 1][i & 1]); SBAR(); fill(i); SBAR(); }
    };
    static_for<0, 28>([&](auto kc) {
      constexpr int kk = kc.value;
      wait_vm2<(kk < AW) ? 2 * (AW - 1 - kk) + g2_range(0, kk) : g2_range(kk - AW + 1, kk)>(af[kk][0], af[kk][1]);
      if constexpr (kk == H1_CK) {
        STAMP_AT(20);
        wait_vmf<g2_range(H1_LD, H1_CK) - 1>(fl);
        if (!__all(fl[0] == taglo && fl[1] == tag32)) poll_flags(flag(FL_H1, ftile, 4 * m + p4), tag);
        STAMP_AT(21);
      }
      if constexpr (kk == H1_LD) fl = ld8_sc1_async(sws, flag_off(FL_H1, ftile, 4 * m + p4));
      if constexpr (kk == O_LD) fl = ld8_sc1_async(sws, flag_off(FL_O0, ftile, 4 * m + p4));
      constexpr bool dw = kk >= 1 && kk - 1 + NR + RD < 36;
      if constexpr (dw) wgot(kk - 1 + NR);
      mm2(kk, af[kk], [&](int i) {
        if constexpr (dw) { if (i < 2) wdmah(kk - 1 + NR + RD, i); }
        if constexpr (kk + NR < 28) { if (i == 1) wrd(kk + NR); }
        if constexpr (kk == 27) { if (i == 0) wrd(28); }
        if constexpr (kk + AW < 28) { if (i >= 2) af[kk + AW][i - 2] = ld16_sc1_async(sws, lane * 16, a0 + (AFT * (i - 2) + kmap(kk + AW)) * 1024); }
      });
      if constexpr (kk == O_CK) {
        wait_vmf<g2_range(O_LD, O_CK) - 1>(fl);
        if (!__all(fl[0] == taglo && fl[1] == tag32)) poll_flags(flag(FL_O0, ftile, 4 * m + p4), tag);
        res_loads();
#pragma unroll
        for (int i = 0; i < 4; ++i) {
          of[i][0] = ld16_sc1_async(sws, lane * 16, o0 + omap(i) * 1024);
          of[i][1] = ld16_sc1_async(sws, lane * 16, o0 + (AFT + omap(i)) * 1024);
        }
      }
    });
    static_for<0, 8>([&](auto kc) {
      constexpr int kk = kc.value;
      if (kk < 7) wrd(28 + kk + 1);
      if constexpr (kk == 1) {
        STAMP_AT(5);
        poll_flags(flag(FL_O1, ftile, 4 * m + p4), tag);
        STAMP_AT(6);
#pragma unroll
        for (int i = 4; i < 8; ++i) {
          of[i][0] = ld16_sc1_async(sws, lane * 16, o0 + omap(i) * 1024);
          of[i][1] = ld16_sc1_async(sws, lane * 16, o0 + (AFT + omap(i)) * 1024);
        }
      }
      wait_vm2<(kk == 0) ? 6 : 14 - 2 * kk>(of[kk][0], of[kk][1]);
      mm2(28 + kk, of[kk], [&](int) {});
    });
    PIN(xres); PIN(bres);
    asm volatile("" ::: "memory");
#pragma unroll
    for (int mt = 0; mt < 2; ++mt)
#pragma unroll
      for (int c = 0; c < 2; ++c)
#pragma unroll
        for (int qd = 0; qd < 4; ++qd) {
          const f32x4 v = {c2[mt][c][4 * qd], c2[mt][c][4 * qd + 1], c2[mt][c][4 * qd + 2], c2[mt][c][4 * qd + 3]};
          sts128(lds0 + L_WG + m * PART + ((mt * 8 + c * 4 + qd) * 66 + lane) * 16, __builtin_bit_cast(v4u, v));
        }
    STAMP_AT(7);
  } else {
    __builtin_amdgcn_s_setprio(1);
    lane_setup();
    v4u xr[2];
    xr[0] = ld16_async(sx, lane * 16, (128 * r + 8 * j + 2 * m) * 1024); xr[1] = ld16_async(sx, lane * 16, (128 * r + 8 * j + 2 * m + 1) * 1024);
    unsigned warm;
    {
      u64 pc; asm volatile("s_getpc_b64 %0" : "=s"(pc));
      warm = ld2_async(make_srd((const void*)(pc & ~127ull), CODEWARM), lane < 3 ? lane * 128 : 0x40000000u, (idx * 4 + m) * 384);
    }
    __builtin_amdgcn_s_barrier();
    v2u ep = ld8_sc1_async(sws, m == 0 ? WS_EPOCH : 0x40000000u);
    float sval;
    const int wi = blockIdx.x * 4 + m;
    const u64 wbase = wi < 448 ? (u64)w1 + wi * 4096 : wi < 896 ? (u64)w2 + (wi - 448) * 4096 : (u64)wo + (wi - 896) * 4096;
    auto make_stats = [&]() {
      wait_vm2<1>(xr[0], xr[1]);
      PIN(warm);
      float piv[2], a[4];
#pragma unroll
      for (int u = 0; u < 2; ++u) {
        piv[u] = rdl(bflo(xr[u][0]), 0);
        float s1 = 0.f, s2 = 0.f;
#pragma unroll
        for (int i = 0; i < 8; ++i) { const float d = ((i & 1) ? bfhi(xr[u][i >> 1]) : bflo(xr[u][i >> 1])) - piv[u]; s1 += d; s2 = __builtin_fmaf(d, d, s2); }
        a[2 * u] = s1; a[2 * u + 1] = s2;
      }
#pragma unroll
      for (int i = 0; i < 4; ++i) a[i] += dpp<0xB1>(a[i]);
#pragma unroll
      for (int i = 0; i < 4; ++i) a[i] += dpp<0x4E>(a[i]);
      float t = p4 == 0 ? a[0] : p4 == 1 ? a[1] : p4 == 2 ? a[2] : a[3];
      t += dpp<0x114>(t); t += dpp<0x118>(t);
      const auto s16 = __builtin_amdgcn_permlane16_swap(asu(t), asu(t), false, false);
      t = asf(s16[0]) + asf(s16[1]);
      const auto s32 = __builtin_amdgcn_permlane32_swap(asu(t), asu(t), false, false);
      t = (asf(s32[0]) + asf(s32[1])) * (1.f / DM);
      const float e1 = dpp<0xA0>(t);
      sval = (p4 & 1) ? __builtin_fmaxf(__builtin_fmaf(-e1, e1, t), 0.f) + 1e-5f : t + (p4 ? piv[1] : piv[0]);
      PIN(sval);
    };
    const unsigned acnt = lds0 + L_CNT;
    const unsigned vpos = 2048 + (g & 1) * 512 + h * 256 + (lane & 15) * 16;
    v4u qf[2], am0, am1;
    {
      v4u xf[32];
      f32x16 acc[3] = {};
      f32x4 cs[3] = {};
      v4u wf[4][3];
      unsigned wb[4];
#pragma unroll
      for (int p = 0; p < 4; ++p) wb[p] = 16384 + ((p + m) & 3) * 1024 + (8 * h + q4) * 64 + (g & 1) * 32 + p4 * 8;
      auto rd = [&](int ks, int t) {
        const unsigned buf = lds0 + ((ks >> 2) % 3) * STG;
        if (t == 0) xf[ks] = lds128(buf + xo[ks & 3]);
        wf[ks & 3][t] = ldtr2(buf + wb[ks & 3] + t * 4096, 256);
      };
      auto mm = [&](int ks, int t) {
        acc[t] = t == 2 ? MFMA32(xf[ks], wf[ks & 3][t], acc[t]) : MFMA32(wf[ks & 3][t], xf[ks], acc[t]);
        if ((ks & 3) == 0) { cs[t] = MFMA16(sel, wf[ks & 3][t], cs[t]); PIN(cs[t]); }
        SBAR();
      };
      make_stats();
      bar();
      STAMP_AT(10);
#pragma unroll
      for (int t = 0; t < 6; ++t) rd(t / 3, t % 3);
      SBAR();
      static_for<0, 8>([&](auto sc) {
        constexpr int s = sc.value;
#pragma unroll
        for (int p = 0; p < 2; ++p) {
#pragma unroll
          for (int t = 0; t < 3; ++t) { mm(4 * s + p, t); rd(4 * s + p + 2, t); SBAR(); }
          if (s == 7 && p == 0) { cswr(4, cs[0]); cswr(5, cs[1]); cswr(6, cs[2]); SBAR(); }
        }
        if constexpr (s == 0) {
          wait_vmf<0>(ep);
          if (m == 0 && lane < 10)
            *(LAS unsigned*)(size_t)(lds0 + L_CNT + lane * 4) = lane == 8 ? ep[0] * 0x9E3779B1u + 0x7F4A7C15u : lane == 9 ? ep[1] * 0x85EBCA77u + 0x165667B1u : 0u;
        }
        if constexpr (s == 1) {
          get_tag();
          if ((lane >> 2) == 3) {
            const v2u gr = {asu(sval), tag32};
            __builtin_amdgcn_raw_buffer_store_b64(gr, rws, WS_STATS + (128 * r + 8 * j + 2 * m) * 16 + p4 * 8, 0, 16);
          }
        }
        if constexpr (s == MIDWARM) {
          warm = ld2_async(make_srd((const void*)wbase, 4096), lane * 128, 0);
        }
        if constexpr (s == 5) stat_load();
        if constexpr (s == 6) {
          wait_vm1<0>(st);
          PIN(warm);

          row_stats();
        }
        mm(4 * s + 2, 0); mm(4 * s + 2, 1);
        bar();
        if constexpr (s < 7) {
          STAMP_AT(11 + s);
          mm(4 * s + 2, 2);
          rd(4 * s + 4, 0); rd(4 * s + 4, 1); rd(4 * s + 4, 2); SBAR();
#pragma unroll
          for (int t = 0; t < 3; ++t) { mm(4 * s + 3, t); rd(4 * s + 5, t); SBAR(); }
        }
      });
      STAMP_AT(1);
      __builtin_amdgcn_s_setprio(3);
      const f32x4 c5 = csrd(5), c6 = csrd(6), c4 = csrd(4);
      SBAR();
      mm(30, 2); mm(31, 1);
      make_xaug();
      SBAR();
      mm(31, 2); mm(31, 0);
      if (h == 0) *(LAS float*)(size_t)(lds0 + L_RS + (m * 32 + r32) * 4) = rstd;
      acc[1] = MFMA32(waug(c5, 0u), xaug, acc[1]);
      acc[2] = MFMA32(xaug, waug(c6, 0u), acc[2]);
      acc[0] = MFMA32(waug(c4, 0u), xaug, acc[0]);
      SBAR();
      f32x4 rs[4];
#pragma unroll
      for (int qd = 0; qd < 4; ++qd) rs[qd] = __builtin_bit_cast(f32x4, lds128(lds0 + L_RS + (m * 32 + 8 * qd + 4 * h) * 4));
      const unsigned kvo = WS_KV + (j * 64 + T) * 4096;
#pragma unroll
      for (int e = 0; e < 16; ++e) acc[1][e] = MUL1(acc[1][e], rstd);
#pragma unroll
      for (int e = 0; e < 2; ++e) {
        const v4u f = pack8(acc[1], e);
        st16_sc1(f, sws, lane * 16, kvo + e * 1024);
        sts128(lds0 + L_KV + (8 + m) * KVT + e * 1024 + lane * 16, f);
      }
#pragma unroll
      for (int qd = 0; qd < 4; ++qd)
#pragma unroll
        for (int e = 0; e < 4; ++e) acc[2][4 * qd + e] = MUL1(acc[2][4 * qd + e], rs[qd][e]);
#pragma unroll
      for (int s = 0; s < 2; ++s) {
        const v4u f = pack8(acc[2], s);
        st16_sc1(f, sws, lane * 16, kvo + 2048 + s * 1024);
        sts128(lds0 + L_KV + (8 + m) * KVT + s * VB + vpos, f);
      }
      SBAR();
      const float qs = rstd * (0.25f * 1.4426950408889634f);
#pragma unroll
      for (int e = 0; e < 16; ++e) acc[0][e] = MUL1(acc[0][e], qs);
      qf[0] = pack8(acc[0], 0); qf[1] = pack8(acc[0], 1);
      PIN(qf[0]); PIN(qf[1]);
      STAMP_AT(2);
      const int T0 = 4 * r - 8 + 2 * m;
      const bool live = T0 >= 0;
      auto kv_load = [&](v4u (&kv)[8], int i0, int i1) {
        const unsigned src = WS_KV + (j * 64 + (live ? T0 : 0)) * 4096, vo = live ? lane * 16 : h * 512;
#pragma unroll
        for (int i = i0; i < i1; ++i) kv[i] = ld16_sc1_async(sws, vo, src + (live ? i : (i & 3)) * 1024);
      };
      auto kv_stage = [&](v4u (&kv)[8]) {
        const unsigned on = (lane < 2 || live) ? one2 : 0u;
        if (lane < 6) sts128(lds0 + L_KV + (lane < 2 ? 8 + m : 2 * m + ((lane - 2) >> 1)) * KVT + 2048 + (lane & 1) * VB + 1024, (v4u){on, on, on, on});
        const v4u zz = {};
#pragma unroll
        for (int i = 0; i < 2; ++i) {
          sts128(lds0 + L_KV + (2 * m + i) * KVT + lane * 16, kv[4 * i]);
          sts128(lds0 + L_KV + (2 * m + i) * KVT + 1024 + lane * 16, kv[4 * i + 1]);
          sts128(lds0 + L_KV + (2 * m + i) * KVT + vpos, live ? kv[4 * i + 2] : zz);
          sts128(lds0 + L_KV + (2 * m + i) * KVT + VB + vpos, live ? kv[4 * i + 3] : zz);
        }
        arrive_nowait(acnt);
      };
      auto kv_wait = [&](v4u (&kv)[8]) {
        wait_vm<0>();
#pragma unroll
        for (int i = 0; i < 8; ++i) PIN(kv[i]);
      };
      if (heavy) {
        bias_load(1);
        hsetup(1);
        await(acnt + 16, 4);
        STAMP_AT(3);
        v2u fl;
        v4u kv[8];
        bool pend = true;
        hmm(std::integral_constant<int, 1>{}, xf, [&](auto kc) {
          constexpr int ks = kc.value;
          if constexpr (ks >= KVPUB && ks <= 22 && (ks & 1) == 0) {
            if (pend && (ks == 22 || drained())) {
              wait_vm<0>();
              if (lane == 0) __hip_atomic_store(flag(FL_KV, T, j), tag, RLX_AGENT);
              pend = false;
            }
          }
          if constexpr (ks == 23) { PIN(bias0); PIN(bias1); }
          if constexpr (ks == (KVMID ? 31 : 24)) fl = ld8_sc1_async(sws, flag_off(FL_KV, live ? T0 + (lane & 1) : 0, j));
        });
        STAMP_AT(18);
        auto kv_fetch = [&]() {
          wait_vmf<KVMID ? 2 : 0>(fl);
          if (!__all(fl[0] == taglo && fl[1] == tag32)) poll_flags(flag(FL_KV, live ? T0 + (lane & 1) : 0, j), tag);
          kv_load(kv, 0, 8);
        };
        if (!KVMID) kv_fetch();
        hgelu(std::integral_constant<int, 1>{}, [&]() { if (KVMID) kv_fetch(); });
        STAMP_AT(4);
        wait_vm<KVMID ? 2 : 4>();
#pragma unroll
        for (int i = 0; i < 8; ++i) PIN(kv[i]);
        kv_stage(kv);
      } else {
        wait_vm<0>();
        if (lane == 0) __hip_atomic_store(flag(FL_KV, T, j), tag, RLX_AGENT);
        v4u kv[8];
        poll_flags(flag(FL_KV, live ? T0 + (lane & 1) : 0, j), tag);
        kv_load(kv, 0, 8); kv_wait(kv); kv_stage(kv);
      }
    }
    STAMP_AT(19);
    const unsigned kb = lds0 + L_KV + m * KVT + lane * 16;
    unsigned vb[2];
#pragma unroll
    for (int e = 0; e < 2; ++e) { vb[e] = lds0 + L_KV + m * KVT + 2048 + ((g & 1) ? 1024 : e * 512 + h * 256 + (lane & 15) * 16); PIN(vb[e]); }
    {
      const int dq = r32 - 4 * h;
#pragma unroll
      for (int d = 0; d < 8; ++d) { const int row = 2 * (d & 1) + 8 * (d >> 1); (d < 4 ? am0 : am1)[d & 3] = row > dq ? 0xffffffffu : row == dq ? 0xffff0000u : 0u; }
      PIN(am0); PIN(am1);
    }
    await(acnt, heavy ? 8 : 4);
    __builtin_amdgcn_s_setprio(PRIO_ATT);
    STAMP_AT(5);
    const f32x16 zero = {};
    auto o_store = [&](f32x16& o, int e) {
      const float rl = __builtin_amdgcn_rcpf(o[8]);
#pragma unroll
      for (int u = 0; u < 8; ++u) o[u] = MUL1(o[u], rl);
      st16_sc1(pack8(o, 0), sws, lane * 16, WS_AF + (T * AFT + 112 + 2 * j + e) * 1024);
    };
    int redo = 0;
    {
      f32x16 o[2], sa, sb, su;
      bool pend = false;
      v4u kn, ku, va, vc, vn0, vn1, vu0, vu1, p0, p1, pu;
      auto kaddr = [&](int n) { return kb + ((n & 7) + 1) * KVT + (n >> 3) * 1024; };
      auto vaddr = [&](int n) { return vb[n >> 3] + ((n & 7) + 1) * KVT; };
      kn = lds128(kaddr(0)); vn0 = lds128(vaddr(0)); vn1 = lds128(vaddr(0) + VB);
      sa = MFMA32(kn, qf[0], zero);
      kn = lds128(kaddr(1));
      SBAR();
      auto step = [&](auto nc, f32x16& sc, f32x16& sn) {
        constexpr int n = nc.value, e = n >> 3, i = n & 7;
        if constexpr (n < 15) sn = MFMA32(kn, qf[(n + 1) >> 3], zero);
        if constexpr (n < 14) kn = lds128(kaddr(n + 2));
        if constexpr (i == 5) ku = lds128(kb + e * 1024);
        v4u v0 = vn0, v1 = vn1;
        if constexpr (n < 15) { vn0 = lds128(vaddr(n + 1)); vn1 = lds128(vaddr(n + 1) + VB); }
        if constexpr (i == 6) { vu0 = lds128(vb[e]); vu1 = lds128(vb[e] + VB); }
        SBAR();
        if constexpr (i == 7) {
          static_for<0, 16>([&](auto u) { sc[u.value] = upper<u.value>() ? su[u.value] : sc[u.value]; });
          PIN(sc); SBAR();
        }
#pragma unroll
        for (int u = 0; u < 6; ++u) sc[u] = EXP2(sc[u]);
        PIN(sc); SBAR();
        if constexpr (n == 8) { o[0] = MFMA32(vc, p1, o[0]); SBAR(); }
        else if constexpr (n > 0) { o[e] = MFMA32(vc, p1, o[e]); SBAR(); }
        sc[6] = EXP2(sc[6]); sc[7] = EXP2(sc[7]);
        p0 = pack8(sc, 0);
        sc[8] = EXP2(sc[8]); sc[9] = EXP2(sc[9]);
        if constexpr (i == 7) { va = p0 & am0; p0 &= ~am0; PIN(va); }
        PIN(sc); PIN(p0); SBAR();
        if constexpr (n == 8) { o[0] = MFMA32(vu1, pu, o[0]); SBAR(); }
        o[e] = MFMA32(v0, p0, i == 0 ? zero : o[e]);
        SBAR();
        if constexpr (i == 6) { su = MFMA32(ku, qf[e], zero); SBAR(); }
#pragma unroll
        for (int u = 10; u < 13; ++u) sc[u] = EXP2(sc[u]);
        if constexpr (i == 7) { PIN(sc); SBAR(); o[e] = MFMA32(vu0, va, o[e]); SBAR(); }
#pragma unroll
        for (int u = 13; u < 16; ++u) sc[u] = EXP2(sc[u]);
        p1 = pack8(sc, 1); vc = v1;
        if constexpr (i == 7) { pu = p1 & am1; p1 &= ~am1; PIN(pu); }
        PIN(p1); SBAR();
        if constexpr (n == 8) {
          if (__all(o[0][8] >= 0x1p-30f && o[0][8] <= 0x1p30f)) o_store(o[0], 0); else redo = 1;
          SBAR();
        }
        if constexpr (i == 2) pend = !redo;
        if constexpr (i >= 2) {
          if (pend && (i == 7 || drained())) {
            wait_vm<0>();
            if (lane == 0) __hip_atomic_store(flag(FL_H1 + e, T, j), tag, RLX_AGENT);
            pend = false;
          }
        }
      };
      static_for<0, 8>([&](auto hc) {
        step(std::integral_constant<int, 2 * hc.value>{}, sa, sb);
        step(std::integral_constant<int, 2 * hc.value + 1>{}, sb, sa);
      });
      o[1] = MFMA32(vc, p1, o[1]);
      o[1] = MFMA32(vu1, pu, o[1]);
      if (__all(o[1][8] >= 0x1p-30f && o[1][8] <= 0x1p30f)) o_store(o[1], 1); else redo |= 2;
    }
    if (redo) {
#pragma unroll 1
      for (int e = 0; e < 2; ++e) {
        if (!((redo >> e) & 1)) continue;
        const v4u q = e ? qf[1] : qf[0];
        const unsigned vbe = e ? vb[1] : vb[0];
        auto merged = [&](const f32x16& c) {
          f32x16 sl = MFMA32(lds128(kb + 8 * KVT + e * 1024), q, c);
          const f32x16 su = MFMA32(lds128(kb + e * 1024), q, c);
          static_for<0, 16>([&](auto u) { sl[u.value] = upper<u.value>() ? su[u.value] : sl[u.value]; });
          return sl;
        };
        float mx = -INFINITY;
#pragma unroll 1
        for (int i = 0; i < 8; ++i) {
          const f32x16 sc = i ? MFMA32(lds128(kb + i * KVT + e * 1024), q, zero) : merged(zero);
#pragma unroll
          for (int u = 0; u < 16; u += 2) mx = max3(mx, sc[u], sc[u + 1]);
        }
        const auto sw = __builtin_amdgcn_permlane32_swap(asu(mx), asu(mx), false, false);
        mx = __builtin_fmaxf(asf(sw[0]), asf(sw[1]));
        f32x16 negm, o;
#pragma unroll
        for (int u = 0; u < 16; ++u) negm[u] = -mx;
        {
          f32x16 sc = merged(negm);
#pragma unroll
          for (int u = 0; u < 16; ++u) sc[u] = EXP2(sc[u]);
          const v4u p0 = pack8(sc, 0), p1 = pack8(sc, 1);
          o = MFMA32(lds128(vbe), p0 & am0, zero);
          o = MFMA32(lds128(vbe + VB), p1 & am1, o);
          o = MFMA32(lds128(vbe + 8 * KVT), p0 & ~am0, o);
          o = MFMA32(lds128(vbe + 8 * KVT + VB), p1 & ~am1, o);
        }
#pragma unroll 1
        for (int i = 1; i < 8; ++i) {
          f32x16 sc = MFMA32(lds128(kb + i * KVT + e * 1024), q, negm);
#pragma unroll
          for (int u = 0; u < 16; ++u) sc[u] = EXP2(sc[u]);
          o = MFMA32(lds128(vbe + i * KVT), pack8(sc, 0), o);
          o = MFMA32(lds128(vbe + i * KVT + VB), pack8(sc, 1), o);
        }
        o_store(o, e);
      }
      wait_vm<0>();
      if (lane == 0) __hip_atomic_store(flag(FL_O0, T, j), tag, RLX_AGENT);
    }
    STAMP_AT(6);
    wait_vm<0>();
    if (lane == 0) __hip_atomic_store(flag(FL_O1, T, j), tag, RLX_AGENT);
    STAMP_AT(7);
    res_loads();
    wait_vm2<0>(xres, bres);
    if (blockIdx.x == 0 && wave == 7) {
#pragma unroll 1
      for (int i = 0; i < 4; ++i) poll_flags(flag(FL_KV, 4 * (4 * i + g), lane & 15), tag);
      if (lane == 0) __hip_atomic_store((GAS u64*)(ws + WS_EPOCH), tag, RLX_AGENT);
    }
  }
  bar();
  {
    int t2 = threadIdx.x; PIN(t2);
    const int row = t2 >> 3, ch = t2 & 7, orow = 64 * rt + row, ocol = 64 * ct + ch * 8;
    const unsigned a = lds0 + L_WG + (((row >> 5) * 8 + ch) * 66 + (row & 31)) * 16;
    f32x4 lo = {}, hi = {};
#pragma unroll
    for (int w = 0; w < 4; ++w) {
      lo += __builtin_bit_cast(f32x4, lds128(a + w * PART));
      hi += __builtin_bit_cast(f32x4, lds128(a + w * PART + 512));
    }
    v4u o;
    o[0] = pk2(lo[0] + bflo(xres[0]) + bflo(bres[0]), lo[1] + bfhi(xres[0]) + bfhi(bres[0]));
    o[1] = pk2(lo[2] + bflo(xres[1]) + bflo(bres[1]), lo[3] + bfhi(xres[1]) + bfhi(bres[1]));
    o[2] = pk2(hi[0] + bflo(xres[2]) + bflo(bres[2]), hi[1] + bfhi(xres[2]) + bfhi(bres[2]));
    o[3] = pk2(hi[2] + bflo(xres[3]) + bflo(bres[3]), hi[3] + bfhi(xres[3]) + bfhi(bres[3]));
    *(GAS v4u*)((char*)out + (size_t)orow * 1024 + ocol * 2) = o;
  }
  STAMP_AT(8);
}

extern "C" void kernel_launch(void* const* d_in, const int* in_sizes, int n_in, void* d_out, int out_size, void* d_ws, size_t ws_size,
                              hipStream_t stream) {
  (void)in_sizes; (void)n_in; (void)out_size; (void)ws_size;
  (void)hipFuncSetAttribute((const void*)fused_block, hipFuncAttributeMaxDynamicSharedMemorySize, LDS_BYTES);
  fused_block<<<256, 512, LDS_BYTES, stream>>>(d_in[0], d_in[1], d_in[2], d_in[4], d_in[5], d_in[6], d_in[7], d_out, (char*)d_ws);
}
```

```cpp
#include <hip/hip_runtime.h>
#include <hip/hip_bf16.h>
#include <stdint.h>
#include <type_traits>

typedef unsigned v4u __attribute__((ext_vector_type(4)));
typedef unsigned v2u __attribute__((ext_vector_type(2)));
typedef float f32x2 __attribute__((ext_vector_type(2)));
typedef float f32x4 __attribute__((ext_vector_type(4)));
typedef float f32x16 __attribute__((ext_vector_type(16)));
typedef short bf16x8 __attribute__((ext_vector_type(8)));
typedef short s16x4 __attribute__((ext_vector_type(4)));
typedef __bf16 bf16x2_t __attribute__((ext_vector_type(2)));
typedef unsigned long long u64;
#define GAS __attribute__((address_space(1)))
#define LAS __attribute__((address_space(3)))
#define DI __device__ __forceinline__
#define RLX_AGENT __ATOMIC_RELAXED, __HIP_MEMORY_SCOPE_AGENT

constexpr int SEQ = 2048, DM = 512, FF = 1792;
constexpr unsigned WS_EPOCH = 0;
constexpr unsigned WS_STATS = 32768;
constexpr unsigned WS_FLAG = 65536;
constexpr unsigned WS_KV = WS_FLAG + 5 * 1024 * 128;
constexpr int AFT = 145;
constexpr unsigned WS_AF = WS_KV + 16 * 64 * 4096;
constexpr unsigned WS_END = WS_AF + 64 * AFT * 1024;
constexpr unsigned WS_STAMP = WS_END;
enum { FL_KV = 0, FL_H0 = 1, FL_H1 = 2, FL_O0 = 3, FL_O1 = 4 };
constexpr int STG = 36864;
constexpr int L_WA = 0;
constexpr int L_WG = 86016;
constexpr int VB = 1040;
constexpr int KVT = 2048 + 2 * VB;
constexpr int L_KV = 65536 - 8 * KVT;
constexpr int RD = 8, NR = 12;
constexpr int PART = 16 * 66 * 16;
constexpr int L_CS = L_WG + 4 * PART;
constexpr int L_RS = L_CS + 3584;
constexpr int L_CNT = L_RS + 512;
constexpr int LDS_BYTES = L_CNT + 64;
static_assert(L_KV + 12 * KVT <= L_WG && L_WG >= 2 * STG && L_CS >= 3 * STG && PART >= RD * 2048 && LDS_BYTES <= 163840, "lds map");
#define AWIN 6
constexpr int AW = AWIN;
constexpr unsigned SPIN_CAP = 1u << 23;

#define MFMA32(a, b, c) __builtin_amdgcn_mfma_f32_32x32x16_bf16(__builtin_bit_cast(bf16x8, a), __builtin_bit_cast(bf16x8, b), c, 0, 0, 0)
#define MFMA16(a, b, c) __builtin_amdgcn_mfma_f32_16x16x32_bf16(__builtin_bit_cast(bf16x8, a), __builtin_bit_cast(bf16x8, b), c, 0, 0, 0)
#define SBAR() __builtin_amdgcn_sched_barrier(0)

#define STAMP_AT(i) do { } while (0)

DI float asf(unsigned u) { return __builtin_bit_cast(float, u); }
DI unsigned asu(float f) { return __builtin_bit_cast(unsigned, f); }
DI unsigned pk2(float lo, float hi) { return __builtin_bit_cast(unsigned, __builtin_convertvector((f32x2){lo, hi}, bf16x2_t)); }
DI float bflo(unsigned u) { return __builtin_bit_cast(float, u << 16); }
DI float bfhi(unsigned u) { return __builtin_bit_cast(float, u & 0xffff0000u); }
DI v4u make_srd(const void* p, unsigned bytes) { const u64 a = (u64)p; return (v4u){(unsigned)a, (unsigned)(a >> 32) & 0xffffu, bytes, 0x00020000u}; }
DI void dma16(v4u srd, unsigned voff, unsigned soff, unsigned lds) {
  asm volatile("s_mov_b32 m0, %3\n\ts_nop 0\n\tbuffer_load_dwordx4 %0, %1, %2 offen lds" :: "v"(voff), "s"(srd), "s"(soff), "s"(lds) : "memory");
}
DI void dma16_a(v4u srd, unsigned voff, unsigned soff, unsigned lds) { dma16(srd, voff, soff, lds); }
DI void dma16_b(v4u srd, unsigned voff1, unsigned soff) {
  asm volatile("buffer_load_dwordx4 %0, %1, %2 offen offset:1024 lds" :: "v"(voff1), "s"(srd), "s"(soff) : "memory");
}
DI v4u ld16_sc1_async(v4u srd, unsigned voff, unsigned soff) {
  v4u r; asm volatile("buffer_load_dwordx4 %0, %1, %2, %3 offen sc1" : "=v"(r) : "v"(voff), "s"(srd), "s"(soff) : "memory"); return r;
}
DI v4u ld16_async(v4u srd, unsigned voff, unsigned soff) {
  v4u r; asm volatile("buffer_load_dwordx4 %0, %1, %2, %3 offen" : "=v"(r) : "v"(voff), "s"(srd), "s"(soff) : "memory"); return r;
}
DI unsigned ld2_async(v4u srd, unsigned voff, unsigned soff) {
  unsigned r; asm volatile("buffer_load_ushort %0, %1, %2, %3 offen" : "=v"(r) : "v"(voff), "s"(srd), "s"(soff) : "memory"); return r;
}
DI void st16_sc1(v4u v, v4u srd, unsigned voff, unsigned soff) {
  asm volatile("buffer_store_dwordx4 %0, %1, %2, %3 offen sc1\n\ts_nop 1" :: "v"(v), "v"(voff), "s"(srd), "s"(soff) : "memory");
}
template <int N> DI void wait_vm1(v4u& a) { asm volatile("s_waitcnt vmcnt(%1)" : "+v"(a) : "i"(N) : "memory"); }
template <int N> DI void wait_vm2(v4u& a, v4u& b) { asm volatile("s_waitcnt vmcnt(%2)" : "+v"(a), "+v"(b) : "i"(N) : "memory"); }
template <int N> DI void wait_vm3(v4u& a, v4u& b, v4u& c) { asm volatile("s_waitcnt vmcnt(%3)" : "+v"(a), "+v"(b), "+v"(c) : "i"(N) : "memory"); }
template <class V> DI void pin(V& v) { asm volatile("" : "+v"(v)); }
template <int N> DI void wait_vm() { asm volatile("s_waitcnt vmcnt(%0)" :: "i"(N) : "memory"); }
DI void bar() { asm volatile("s_waitcnt lgkmcnt(0)\n\ts_barrier" ::: "memory"); }
DI v4u lds128(unsigned a) { return *(const LAS v4u*)(size_t)a; }
DI void sts128(unsigned a, v4u v) { *(LAS v4u*)(size_t)a = v; }
DI v2u ldtr(unsigned a) { return __builtin_bit_cast(v2u, __builtin_amdgcn_ds_read_tr16_b64_v4i16((LAS s16x4*)(size_t)a)); }
DI v4u ldtr2(unsigned a, int second) { const v2u lo = ldtr(a), hi = ldtr(a + second); return (v4u){lo.x, lo.y, hi.x, hi.y}; }
template <int CTRL> DI float dpp(float v) { return __builtin_bit_cast(float, __builtin_amdgcn_update_dpp(0, __builtin_bit_cast(int, v), CTRL, 0xf, 0xf, true)); }
DI float rdl(float v, int l) { return __builtin_bit_cast(float, __builtin_amdgcn_readlane(__builtin_bit_cast(int, v), l)); }
DI v4u pack8(const f32x16& a, int s) { return (v4u){pk2(a[8 * s], a[8 * s + 1]), pk2(a[8 * s + 2], a[8 * s + 3]), pk2(a[8 * s + 4], a[8 * s + 5]), pk2(a[8 * s + 6], a[8 * s + 7])}; }
DI void poll_flags(const GAS u64* p, u64 tag) {
#pragma unroll 1
  for (unsigned spin = 0; spin < SPIN_CAP; ++spin) {
    const u64 v = __hip_atomic_load(p, RLX_AGENT);
    if (__all(v == tag)) break;
    __builtin_amdgcn_s_sleep(2);
  }
  asm volatile("" ::: "memory");
}
constexpr u64 upper_mask(int reg) {
  u64 m = 0;
  for (int l = 0; l < 64; ++l) { const int row = (reg & 3) + 8 * (reg >> 2) + 4 * (l >> 5); if (row > (l & 31)) m |= 1ull << l; }
  return m;
}
template <int REG> DI bool upper() { return __builtin_amdgcn_inverse_ballot_w64(upper_mask(REG)); }
template <int I, int N, class Fn> DI void static_for(Fn&& f) { if constexpr (I < N) { f(std::integral_constant<int, I>{}); static_for<I + 1, N>(f); } }
DI float max3(float a, float b, float c) { return __builtin_fmaxf(__builtin_fmaxf(a, b), c); }

constexpr int H1_CK = 16 - AW, H1_LD = H1_CK - 3, O_CK = 28 - AW, O_LD = O_CK - 3;
constexpr int g2_issued(int t) { return ((t >= 1 && t - 1 + NR + RD < 36) ? 2 : 0) + ((t + AW < 28) ? 2 : 0) + (t == H1_LD) + (t == O_LD) + (t == O_CK ? 10 : 0); }
constexpr int g2_range(int a, int b) { int n = 0; for (int t = a; t < b; ++t) n += g2_issued(t); return n; }
#define GELU_PK(P) (P == 1)
#define PRIO_G2 2
#define PRIO_ATT 3
#define PRIO_G1 2
#define ROT r
#define CODEWARM 39000
#define MIDWARM 1
#define KVPUB 6
#define EXP2(v) __builtin_amdgcn_exp2f(v)
#define PIN(v) pin(v)
extern "C" __device__ float mul_unpacked(float, float) __asm("llvm.amdgcn.fmul.legacy");
#define MUL1(a, b) mul_unpacked(a, b)
#define FMA1(a, b, c) __builtin_amdgcn_div_fmasf(a, b, c, false)
DI v2u ld8_sc1_async(v4u srd, unsigned voff) {
  v2u r; asm volatile("buffer_load_dwordx2 %0, %1, %2, 0 offen sc1" : "=v"(r) : "v"(voff), "s"(srd) : "memory"); return r;
}
template <int N> DI void wait_vmf(v2u& a) { asm volatile("s_waitcnt vmcnt(%1)" : "+v"(a) : "i"(N) : "memory"); }
DI void arrive(unsigned cnt, int lane) {
  asm volatile("s_waitcnt lgkmcnt(0)" ::: "memory");
  if (lane == 0) __hip_atomic_fetch_add((LAS unsigned*)(size_t)cnt, 1u, __ATOMIC_RELAXED, __HIP_MEMORY_SCOPE_WORKGROUP);
}
DI void arrive_nowait(unsigned cnt) {
  asm volatile("s_mov_b64 exec, 1\n\tds_add_u32 %0, %1\n\ts_mov_b64 exec, -1" :: "v"(cnt), "v"(1u) : "memory");
}
DI unsigned peek(unsigned cnt) { return __hip_atomic_load((LAS unsigned*)(size_t)cnt, __ATOMIC_RELAXED, __HIP_MEMORY_SCOPE_WORKGROUP); }
DI void await(unsigned cnt, unsigned n) {
#pragma unroll 1
  for (unsigned spin = 0; spin < SPIN_CAP * 32; ++spin) {
    if (__builtin_amdgcn_readfirstlane(peek(cnt)) >= n) break;
    __builtin_amdgcn_s_sleep(1);
  }
  asm volatile("" ::: "memory");
}

__global__ __launch_bounds__(512, 2) void fused_block(const void* __restrict__ x, const void* __restrict__ wqkv, const void* __restrict__ wo,
                                                      const void* __restrict__ w1, const void* __restrict__ b1, const void* __restrict__ w2,
                                                      const void* __restrict__ b2, void* __restrict__ out, char* __restrict__ ws) {
  constexpr unsigned lds0 = 0;
  const int wave = __builtin_amdgcn_readfirstlane(threadIdx.x >> 6);
  const int m = wave & 3;
  const bool isG = wave < 4;
  const int xcd = blockIdx.x & 7, idx = blockIdx.x >> 3;
  const int r = (xcd >> 2) * 8 + (idx >> 2), j = (xcd & 3) * 4 + (idx & 3);
  const int rt = (xcd >> 1) * 8 + (idx >> 2), ct = (xcd & 1) * 4 + (idx & 3);
  int lane = threadIdx.x & 63, r32, h, g, q4, p4;
  STAMP_AT(0);
  asm volatile("" :: "s"(x), "s"(wqkv), "s"(wo), "s"(w1), "s"(b1), "s"(w2), "s"(b2), "s"(out), "s"(ws));

  const v4u sx = make_srd(x, SEQ * DM * 2), sws = make_srd(ws, WS_END);
  const __amdgpu_buffer_rsrc_t rws = __builtin_amdgcn_make_buffer_rsrc(ws, 0, WS_END, 0x00020000);
  const unsigned one2 = 0x3f803f80u;
  const int T = 4 * r + m;
  const int rot = ROT;
  auto flag_off = [&](int kind, int tile, int jj) { return WS_FLAG + ((kind * 64 + tile) * 16 + jj) * 128; };
  auto flag = [&](int kind, int tile, int jj) { return (GAS u64*)(ws + flag_off(kind, tile, jj)); };
  u64 tag;
  unsigned taglo, tag32;
  auto get_tag = [&]() {
    const v2u tg = *(const LAS v2u*)(size_t)(lds0 + L_CNT + 32);
    taglo = __builtin_amdgcn_readfirstlane(tg[0]); tag32 = __builtin_amdgcn_readfirstlane(tg[1]);
    tag = ((u64)tag32 << 32) | taglo;
  };
  v4u xres, bres, xaug, st;
  float mean, var, rstd, sd;
  v4u sel;
  unsigned xo[4], swz;
  auto lane_setup = [&]() {
    lane = threadIdx.x & 63; PIN(lane);
    r32 = lane & 31; h = lane >> 5; g = lane >> 4; q4 = (lane >> 2) & 3; p4 = lane & 3;
    const unsigned selv = ((lane & 15) == (g & 1)) ? one2 : 0u;
    sel = (v4u){selv, selv, selv, selv};
#pragma unroll
    for (int p = 0; p < 4; ++p) xo[p] = (32 * m + r32) * 128 + (((2 * ((p + m) & 3) + h) ^ ((r32 >> 1) & 7)) * 16);
    swz = ((((lane >> 2) & 1) ^ (g & 1)) * 64) + p4 * 16;
  };

  auto stat_load = [&]() { st = ld16_sc1_async(sws, WS_STATS + (128 * r + 32 * m + r32) * 16, 0); };
  auto res_loads = [&]() {
    int t2 = threadIdx.x; PIN(t2);
    const int row = 64 * rt + (t2 >> 3), col = 64 * ct + (t2 & 7) * 8;
    xres = ld16_async(sx, row * 1024 + col * 2, 0);
    bres = ld16_async(make_srd(b2, DM * 2), col * 2, 0);
  };
  auto row_stats = [&]() {
    if (!__all(st[1] == tag32 && st[3] == tag32)) {
#pragma unroll 1
      for (unsigned spin = 0; spin < SPIN_CAP; ++spin) {
        st = ld16_sc1_async(sws, WS_STATS + (128 * r + 32 * m + r32) * 16, 0);
        wait_vm1<0>(st);
        if (__all(st[1] == tag32 && st[3] == tag32)) break;
      }
    }
    mean = asf(st[0]); var = asf(st[2]);
    PIN(mean); PIN(var);
  };
  auto make_xaug = [&]() {
    PIN(mean); PIN(var);
    rstd = __builtin_amdgcn_rsqf(var); sd = var * rstd;
    const unsigned hi = pk2(-mean, sd);
    const unsigned lo = pk2(-mean - bflo(hi), FMA1(bfhi(hi), -1.f, sd));
    xaug = (v4u){(hi & 0xffffu) | (hi << 16), (lo & 0xffffu) | (hi & 0xffff0000u), lo >> 16, 0u};
    if (h) xaug = (v4u){0u, 0u, 0u, 0u};
    PIN(xaug);
  };
  auto cswr = [&](int tile, f32x4 c) {
    if (lane < 16) { LAS float* p = (LAS float*)(size_t)(lds0 + L_CS + (tile * 32 + lane) * 16 + m * 4); p[0] = c[0]; p[64] = c[1]; }
  };
  auto csrd = [&](int tile) { return __builtin_bit_cast(f32x4, lds128(lds0 + L_CS + (tile * 32 + r32) * 16)); };
  auto waug = [&](f32x4 c4, unsigned b16) {
    const float c = ((c4[0] + c4[1]) + c4[2]) + c4[3];
    const unsigned hi = pk2(c, 0.f);
    const unsigned lo = pk2(c - bflo(hi), 0.f);
    return (v4u){hi | (lo << 16), hi | (b16 << 16), b16, 0u};
  };

  const v4u sw1 = make_srd(w1, DM * FF * 2);
  const int hcol0 = 448 * (j >> 2) + 128 * (j & 3);
  const bool heavy = (j & 3) != 3;
  unsigned bias0, bias1;
  auto bias_load = [&](int P) {
    bias0 = ld2_async(make_srd(b1, FF * 2), r32 * 2, (hcol0 + 64 * P) * 2);
    bias1 = ld2_async(make_srd(b1, FF * 2), r32 * 2, (hcol0 + 64 * P + 32) * 2);
  };
  auto w1dma = [&](int P, int c, unsigned dst, int half) {
    const unsigned vw1 = (lane >> 3) * (FF * 2) + swz, vw1b = vw1 + 8 * (FF * 2) - 1024;
    const unsigned so = (64 * ((c + rot) & 7) + 16 * m) * (FF * 2) + (hcol0 + 64 * P) * 2;
    if (half == 0) dma16_a(sw1, vw1, so, dst + m * 2048); else dma16_b(sw1, vw1b, so);
  };
  f32x16 hac[2];
  unsigned rk[4][2];
  auto hsetup = [&](int P) {
#pragma unroll
    for (int p = 0; p < 4; ++p)
#pragma unroll
      for (int c = 0; c < 2; ++c) {
        rk[p][c] = lds0 + (P ? L_WA : 28672) + ((p + m) & 3) * 2048 + h * 1024 + q4 * 128 + ((c ^ (q4 >> 1)) * 64) + (g & 1) * 32 + p4 * 8;
        PIN(rk[p][c]);
      }
  };
  auto hmm = [&](auto pc, const v4u (&xf)[32], auto&& hook) {
    constexpr int P = pc.value;
    constexpr int D = 4;
    const unsigned cnt = lds0 + L_CNT;
    f32x4 cs[2] = {}, c0, c1;
    v4u wf[D][2], au0, au1;
    unsigned seen = 0;
    hac[0] = (f32x16){}; hac[1] = (f32x16){};
    auto rd = [&](int ks, int t) { wf[ks % D][t] = ldtr2(rk[ks & 3][t] + (ks >> 2) * 8192, 512); };
#pragma unroll
    for (int ks = 0; ks < D; ++ks) { rd(ks, 0); rd(ks, 1); }
    SBAR();
    static_for<0, 32>([&](auto kc) {
      constexpr int ks = kc.value;
      hook(kc);
      if constexpr (ks == 10) seen = peek(cnt + 20);
      if constexpr (ks == 29) seen = peek(cnt);
#pragma unroll
      for (int t = 0; t < 2; ++t) {
        hac[t] = MFMA32(wf[ks % D][t], xf[ks], hac[t]);
        if ((ks & 3) == 0 && ks < 28) { cs[t] = MFMA16(sel, wf[ks % D][t], cs[t]); PIN(cs[t]); }
        if (ks == 26) { cs[t] = MFMA16(sel, wf[28 % D][t], cs[t]); PIN(cs[t]); }
        SBAR();
        if constexpr (ks + D < 32) rd(ks + D, t);
        if constexpr (ks == 31) { if (t == 0) { au0 = waug(c0, bias0); PIN(au0); } else { au1 = waug(c1, bias1); PIN(au1); } }
        SBAR();
      }
      if constexpr (ks == 11 || ks == 30) {
        PIN(hac[0]); PIN(hac[1]);
        if (__builtin_amdgcn_readfirstlane(seen) < 4) await(ks == 11 ? cnt + 20 : cnt, 4);
        asm volatile("" ::: "memory");
        if constexpr (ks == 30) { c0 = csrd(2 * P); c1 = csrd(2 * P + 1); }
      }
      if constexpr (ks == 27) { cswr(2 * P, cs[0]); cswr(2 * P + 1, cs[1]); arrive_nowait(cnt); }
      SBAR();
    });
    hac[0] = MFMA32(au0, xaug, hac[0]);
    hac[1] = MFMA32(au1, xaug, hac[1]);
    SBAR();
  };
  auto hgelu = [&](auto pc, auto&& mid) {
    constexpr int P = pc.value;
    constexpr bool PK = GELU_PK(P);
    const float C0 = 2.3022081986f, C1 = C0 * 0.044715f;
    const float ga = -C1 * rstd * rstd * rstd, gb = -C0 * rstd;
    auto mul = [&](f32x2 a, f32x2 b) { return PK ? a * b : (f32x2){MUL1(a[0], b[0]), MUL1(a[1], b[1])}; };
    auto fma = [&](f32x2 a, float b, float c) {
      return PK ? __builtin_elementwise_fma(a, (f32x2){b, b}, (f32x2){c, c}) : (f32x2){FMA1(a[0], b, c), FMA1(a[1], b, c)}; };
#pragma unroll
    for (int t = 0; t < 2; ++t) {
      f32x2 v[8], z[8];
#pragma unroll
      for (int e = 0; e < 8; ++e) { v[e] = (f32x2){hac[t][2 * e], hac[t][2 * e + 1]}; z[e] = mul(v[e], v[e]); }
      SBAR();
#pragma unroll
      for (int e = 0; e < 8; ++e) z[e] = fma(z[e], ga, gb);
      SBAR();
#pragma unroll
      for (int e = 0; e < 8; ++e) z[e] = mul(z[e], v[e]);
      SBAR();
#pragma unroll
      for (int e = 0; e < 8; ++e) z[e] = (f32x2){EXP2(z[e][0]), EXP2(z[e][1])};
      SBAR();
#pragma unroll
      for (int e = 0; e < 8; ++e) z[e] = fma(z[e], sd, sd);
      SBAR();
#pragma unroll
      for (int e = 0; e < 8; ++e) z[e] = (f32x2){__builtin_amdgcn_rcpf(z[e][0]), __builtin_amdgcn_rcpf(z[e][1])};
      SBAR();
#pragma unroll
      for (int e = 0; e < 8; ++e) z[e] = mul(z[e], v[e]);
      SBAR();
#pragma unroll
      for (int s = 0; s < 2; ++s) {
        const v4u f = {pk2(z[4 * s][0], z[4 * s][1]), pk2(z[4 * s + 1][0], z[4 * s + 1][1]), pk2(z[4 * s + 2][0], z[4 * s + 2][1]), pk2(z[4 * s + 3][0], z[4 * s + 3][1])};
        st16_sc1(f, sws, lane * 16, WS_AF + (T * AFT + (hcol0 >> 4) + 4 * P + 2 * t + s) * 1024);
      }
      if (t == 0) mid();
      SBAR();
    }
  };

  if (isG) {
    lane_setup();
    const v4u sw2 = make_srd(w2, FF * DM * 2), swo = make_srd(wo, DM * DM * 2);
    const unsigned vw2 = (lane >> 3) * 1024 + swz, vw2b = vw2 + 7 * 1024;
    const int ro3 = rt % 3;
    auto kmap = [&](int kk) {
      if (kk < 16) return 8 * (((kk >> 2) + rt) & 3) + (kk & 3);
      const int p = ((kk - 16) >> 2) + ro3;
      return 8 * (p >= 3 ? p - 3 : p) + 4 + (kk & 3);
    };
    auto omap = [&](int i) { return 2 * ((i + rt) & 3) + (i >> 2); };
    auto wdmah = [&](int kk, int half) {
      const unsigned so = (kk < 28 ? 448 * m + 16 * kmap(kk) : 128 * m + 16 * omap(kk - 28)) * 1024 + 128 * ct;
      if (half == 0) dma16_a(kk < 28 ? sw2 : swo, vw2, so, lds0 + L_WG + m * PART + (kk % RD) * 2048); else dma16_b(kk < 28 ? sw2 : swo, vw2b, so);
    };
    auto wdma = [&](int kk) { wdmah(kk, 0); wdmah(kk, 1); };
    {
      const v4u swq = make_srd(wqkv, DM * 3 * DM * 2);
      const unsigned vx0 = (lane >> 3) * 1024 + (((lane & 7) ^ g) * 16), vx1 = (7 + (lane >> 3)) * 1024 + (((lane & 7) ^ (4 + g)) * 16);
      const unsigned vwq = (lane >> 2) * (3 * DM * 2) + p4 * 16;
      auto piece = [&](int s, int i) {
        const unsigned buf = lds0 + (s % 3) * STG;
        const int kc = (s + rot) & 7;
        if (i < 4) {
          const unsigned so = (128 * r + 32 * m + 16 * (i >> 1)) * 1024 + kc * 128;
          if (i & 1) dma16_b(sx, vx1, so); else dma16_a(sx, vx0, so, buf + m * 4096 + (i >> 1) * 2048);
        } else if (i < 7) dma16(swq, vwq, (64 * kc + 16 * m) * (3 * DM * 2) + (512 * (i - 4) + 32 * j) * 2, buf + 16384 + ((i - 4) * 4 + m) * 1024);
        else w1dma(0, s, buf + 28672, i - 7);
      };
#pragma unroll
      for (int i = 0; i < 18; ++i) { piece(i / 9, i % 9); if (i == 8) __builtin_amdgcn_s_barrier(); }
      __builtin_amdgcn_s_setprio(PRIO_G1);
      hsetup(0);
      f32x4 cs[2] = {};
      v4u xf[4], wf[4][2];
      hac[0] = (f32x16){}; hac[1] = (f32x16){};
      static_for<0, 8>([&](auto sc) {
        constexpr int s = sc.value;
        constexpr unsigned buf = (s % 3) * STG;
        auto fill = [&](int i) {
          if constexpr (s < 6) piece(s + 2, i);
          if constexpr (s == 6) { if (i == 0) { stat_load(); bias_load(0); } else wdmah((i - 1) >> 1, (i - 1) & 1); }
          if constexpr (s == 7) { if (heavy) { w1dma(1, i, lds0 + L_WA + i * 8192, 0); w1dma(1, i, 0, 1); } }
          SBAR();
        };
        auto rd = [&](int p) { xf[p] = lds128(buf + xo[p]); wf[p][0] = ldtr2(buf + rk[p][0], 512); wf[p][1] = ldtr2(buf + rk[p][1], 512); SBAR(); };
        if constexpr (s < 7) wait_vm<9>(); else { wait_vm1<8>(st); PIN(bias0); PIN(bias1); }
        bar();
        STAMP_AT(10 + s);
        rd(0);
        if constexpr (s < 7) fill(0);
        rd(1);
        if constexpr (s < 7) fill(1);
        if constexpr (s == 2) get_tag();
#pragma unroll
        for (int p = 0; p < 4; ++p) {
#pragma unroll
          for (int t = 0; t < 2; ++t) {
            hac[t] = MFMA32(wf[p][t], xf[p], hac[t]);
            if (p == 0) { cs[t] = MFMA16(sel, wf[p][t], cs[t]); PIN(cs[t]); }
            SBAR();
            if (p < 2 && t == 0) rd(p + 2);
            if (s == 7) fill(2 * p + t); else if (2 * p + t < 7) fill(2 + 2 * p + t);
          }
          if (s == 7 && p == 1) {
            cswr(0, cs[0]); cswr(1, cs[1]);
            bar();
            STAMP_AT(1);
          }
        }
        if constexpr (s == 7) {
          const f32x4 c0 = csrd(0), c1 = csrd(1);
          row_stats(); make_xaug();
          hac[0] = MFMA32(waug(c0, bias0), xaug, hac[0]);
          hac[1] = MFMA32(waug(c1, bias1), xaug, hac[1]);
          SBAR();
        }
      });
    }
    STAMP_AT(18);
    if (heavy) { wait_vm<8>(); arrive_nowait(lds0 + L_CNT + 16); }
    hgelu(std::integral_constant<int, 0>{}, [&]() {
      wait_vm<2>();
      if (heavy) arrive_nowait(lds0 + L_CNT + 20);
    });
    __builtin_amdgcn_s_setprio(0);
    STAMP_AT(19);
    unsigned rb2[2];
#pragma unroll
    for (int c = 0; c < 2; ++c) rb2[c] = lds0 + L_WG + m * PART + h * 512 + q4 * 128 + ((c ^ (q4 >> 1)) * 64) + (g & 1) * 32 + p4 * 8;
    v4u wq[36][2];
    auto wrd = [&](int kk) {
#pragma unroll
      for (int c = 0; c < 2; ++c) wq[kk][c] = ldtr2(rb2[c] + (kk % RD) * 2048, 1024);
    };
    auto wgot = [&](int kk) { PIN(wq[kk][0]); PIN(wq[kk][1]); };
    const int ftile = 2 * rt + ((lane >> 2) & 1);
    v4u af[28][2], of[8][2];
    v2u fl;
    const unsigned a0 = WS_AF + (2 * rt * AFT + 28 * m) * 1024, o0 = WS_AF + (2 * rt * AFT + 112 + 8 * m) * 1024;
    for (int kk = 0; kk < 4; ++kk) wrd(kk);
    for (int kk = 4; kk < 8; ++kk) wdma(kk);
    for (int kk = 0; kk < 4; ++kk) wgot(kk);
    wait_vm<8>();
    if (lane == 0) __hip_atomic_store(flag(FL_H0, T, j), tag, RLX_AGENT);
    STAMP_AT(2);
    for (int kk = 8; kk < 12; ++kk) wdma(kk);
    static_for<1, 3>([&](auto ic) {
      constexpr int i = ic.value;
      wait_vm<(i == 1) ? 9 : 8>();
      for (int kk = 4 * i; kk < 4 * i + 4; ++kk) wrd(kk);
      for (int kk = 4 * i; kk < 4 * i + 4; ++kk) wgot(kk);
      if constexpr (i == 2) fl = ld8_sc1_async(sws, flag_off(FL_H0, ftile, 4 * m + p4));
      for (int kk = 4 * i; kk < 4 * i + 4; ++kk) wdma(kk + RD);
    });
    STAMP_AT(3);
    wait_vmf<8>(fl);
    if (!__all(fl[0] == taglo && fl[1] == tag32)) poll_flags(flag(FL_H0, ftile, 4 * m + p4), tag);
    STAMP_AT(4);
#pragma unroll
    for (int kk = 0; kk < AW; ++kk) {
      af[kk][0] = ld16_sc1_async(sws, lane * 16, a0 + kmap(kk) * 1024);
      af[kk][1] = ld16_sc1_async(sws, lane * 16, a0 + (AFT + kmap(kk)) * 1024);
    }
    __builtin_amdgcn_s_setprio(PRIO_G2);
    f32x16 c2[2][2] = {};
    auto mm2 = [&](int kk, const v4u (&a)[2], auto&& fill) {
      SBAR();
#pragma unroll
      for (int i = 0; i < 4; ++i) { c2[i >> 1][i & 1] = MFMA32(wq[kk][i & 1], a[i >> 1], c2[i >> 1][i & 1]); SBAR(); fill(i); SBAR(); }
    };
    static_for<0, 28>([&](auto kc) {
      constexpr int kk = kc.value;
      wait_vm2<(kk < AW) ? 2 * (AW - 1 - kk) + g2_range(0, kk) : g2_range(kk - AW + 1, kk)>(af[kk][0], af[kk][1]);
      if constexpr (kk == H1_CK) {
        STAMP_AT(20);
        wait_vmf<g2_range(H1_LD, H1_CK) - 1>(fl);
        if (!__all(fl[0] == taglo && fl[1] == tag32)) poll_flags(flag(FL_H1, ftile, 4 * m + p4), tag);
        STAMP_AT(21);
      }
      if constexpr (kk == H1_LD) fl = ld8_sc1_async(sws, flag_off(FL_H1, ftile, 4 * m + p4));
      if constexpr (kk == O_LD) fl = ld8_sc1_async(sws, flag_off(FL_O0, ftile, 4 * m + p4));
      constexpr bool dw = kk >= 1 && kk - 1 + NR + RD < 36;
      if constexpr (dw) wgot(kk - 1 + NR);
      mm2(kk, af[kk], [&](int i) {
        if constexpr (dw) { if (i < 2) wdmah(kk - 1 + NR + RD, i); }
        if constexpr (kk + NR < 28) { if (i == 1) wrd(kk + NR); }
        if constexpr (kk == 27) { if (i == 0) wrd(28); }
        if constexpr (kk + AW < 28) { if (i >= 2) af[kk + AW][i - 2] = ld16_sc1_async(sws, lane * 16, a0 + (AFT * (i - 2) + kmap(kk + AW)) * 1024); }
      });
      if constexpr (kk == O_CK) {
        wait_vmf<g2_range(O_LD, O_CK) - 1>(fl);
        if (!__all(fl[0] == taglo && fl[1] == tag32)) poll_flags(flag(FL_O0, ftile, 4 * m + p4), tag);
        res_loads();
#pragma unroll
        for (int i = 0; i < 4; ++i) {
          of[i][0] = ld16_sc1_async(sws, lane * 16, o0 + omap(i) * 1024);
          of[i][1] = ld16_sc1_async(sws, lane * 16, o0 + (AFT + omap(i)) * 1024);
        }
      }
    });
    static_for<0, 8>([&](auto kc) {
      constexpr int kk = kc.value;
      if (kk < 7) wrd(28 + kk + 1);
      if constexpr (kk == 1) {
        STAMP_AT(5);
        poll_flags(flag(FL_O1, ftile, 4 * m + p4), tag);
        STAMP_AT(6);
#pragma unroll
        for (int i = 4; i < 8; ++i) {
          of[i][0] = ld16_sc1_async(sws, lane * 16, o0 + omap(i) * 1024);
          of[i][1] = ld16_sc1_async(sws, lane * 16, o0 + (AFT + omap(i)) * 1024);
        }
      }
      wait_vm2<(kk == 0) ? 6 : 14 - 2 * kk>(of[kk][0], of[kk][1]);
      mm2(28 + kk, of[kk], [&](int) {});
    });
    PIN(xres); PIN(bres);
    asm volatile("" ::: "memory");
#pragma unroll
    for (int mt = 0; mt < 2; ++mt)
#pragma unroll
      for (int c = 0; c < 2; ++c)
#pragma unroll
        for (int qd = 0; qd < 4; ++qd) {
          const f32x4 v = {c2[mt][c][4 * qd], c2[mt][c][4 * qd + 1], c2[mt][c][4 * qd + 2], c2[mt][c][4 * qd + 3]};
          sts128(lds0 + L_WG + m * PART + ((mt * 8 + c * 4 + qd) * 66 + lane) * 16, __builtin_bit_cast(v4u, v));
        }
    STAMP_AT(7);
  } else {
    __builtin_amdgcn_s_setprio(1);
    lane_setup();
    v4u xr[2];
    xr[0] = ld16_async(sx, lane * 16, (128 * r + 8 * j + 2 * m) * 1024); xr[1] = ld16_async(sx, lane * 16, (128 * r + 8 * j + 2 * m + 1) * 1024);
    unsigned warm;
    {
      u64 pc; asm volatile("s_getpc_b64 %0" : "=s"(pc));
      warm = ld2_async(make_srd((const void*)(pc & ~127ull), CODEWARM), lane < 3 ? lane * 128 : 0x40000000u, (idx * 4 + m) * 384);
    }
    __builtin_amdgcn_s_barrier();
    v2u ep = ld8_sc1_async(sws, m == 0 ? WS_EPOCH : 0x40000000u);
    float sval;
    const int wi = blockIdx.x * 4 + m;
    const u64 wbase = wi < 448 ? (u64)w1 + wi * 4096 : wi < 896 ? (u64)w2 + (wi - 448) * 4096 : (u64)wo + (wi - 896) * 4096;
    auto make_stats = [&]() {
      wait_vm2<1>(xr[0], xr[1]);
      PIN(warm);
      float piv[2], a[4];
#pragma unroll
      for (int u = 0; u < 2; ++u) {
        piv[u] = rdl(bflo(xr[u][0]), 0);
        float s1 = 0.f, s2 = 0.f;
#pragma unroll
        for (int i = 0; i < 8; ++i) { const float d = ((i & 1) ? bfhi(xr[u][i >> 1]) : bflo(xr[u][i >> 1])) - piv[u]; s1 += d; s2 = __builtin_fmaf(d, d, s2); }
        a[2 * u] = s1; a[2 * u + 1] = s2;
      }
#pragma unroll
      for (int i = 0; i < 4; ++i) a[i] += dpp<0xB1>(a[i]);
#pragma unroll
      for (int i = 0; i < 4; ++i) a[i] += dpp<0x4E>(a[i]);
      float t = p4 == 0 ? a[0] : p4 == 1 ? a[1] : p4 == 2 ? a[2] : a[3];
      t += dpp<0x114>(t); t += dpp<0x118>(t);
      const auto s16 = __builtin_amdgcn_permlane16_swap(asu(t), asu(t), false, false);
      t = asf(s16[0]) + asf(s16[1]);
      const auto s32 = __builtin_amdgcn_permlane32_swap(asu(t), asu(t), false, false);
      t = (asf(s32[0]) + asf(s32[1])) * (1.f / DM);
      const float e1 = dpp<0xA0>(t);
      sval = (p4 & 1) ? __builtin_fmaxf(__builtin_fmaf(-e1, e1, t), 0.f) + 1e-5f : t + (p4 ? piv[1] : piv[0]);
      PIN(sval);
    };
    const unsigned acnt = lds0 + L_CNT;
    const unsigned vpos = 2048 + (g & 1) * 512 + h * 256 + (lane & 15) * 16;
    v4u qf[2], am0, am1;
    {
      v4u xf[32];
      f32x16 acc[3] = {};
      f32x4 cs[3] = {};
      v4u wf[4][3];
      unsigned wb[4];
#pragma unroll
      for (int p = 0; p < 4; ++p) wb[p] = 16384 + ((p + m) & 3) * 1024 + (8 * h + q4) * 64 + (g & 1) * 32 + p4 * 8;
      auto rd = [&](int ks, int t) {
        const unsigned buf = lds0 + ((ks >> 2) % 3) * STG;
        if (t == 0) xf[ks] = lds128(buf + xo[ks & 3]);
        wf[ks & 3][t] = ldtr2(buf + wb[ks & 3] + t * 4096, 256);
      };
      auto mm = [&](int ks, int t) {
        acc[t] = t == 2 ? MFMA32(xf[ks], wf[ks & 3][t], acc[t]) : MFMA32(wf[ks & 3][t], xf[ks], acc[t]);
        if ((ks & 3) == 0) { cs[t] = MFMA16(sel, wf[ks & 3][t], cs[t]); PIN(cs[t]); }
        SBAR();
      };
      make_stats();
      bar();
      STAMP_AT(10);
#pragma unroll
      for (int t = 0; t < 6; ++t) rd(t / 3, t % 3);
      SBAR();
      static_for<0, 8>([&](auto sc) {
        constexpr int s = sc.value;
#pragma unroll
        for (int p = 0; p < 2; ++p) {
#pragma unroll
          for (int t = 0; t < 3; ++t) { mm(4 * s + p, t); rd(4 * s + p + 2, t); SBAR(); }
          if (s == 7 && p == 0) { cswr(4, cs[0]); cswr(5, cs[1]); cswr(6, cs[2]); SBAR(); }
        }
        if constexpr (s == 0) {
          wait_vmf<0>(ep);
          if (m == 0 && lane < 10)
            *(LAS unsigned*)(size_t)(lds0 + L_CNT + lane * 4) = lane == 8 ? ep[0] * 0x9E3779B1u + 0x7F4A7C15u : lane == 9 ? ep[1] * 0x85EBCA77u + 0x165667B1u : 0u;
        }
        if constexpr (s == 1) {
          get_tag();
          if ((lane >> 2) == 3) {
            const v2u gr = {asu(sval), tag32};
            __builtin_amdgcn_raw_buffer_store_b64(gr, rws, WS_STATS + (128 * r + 8 * j + 2 * m) * 16 + p4 * 8, 0, 16);
          }
        }
        if constexpr (s == MIDWARM) {
          warm = ld2_async(make_srd((const void*)wbase, 4096), lane * 128, 0);
        }
        if constexpr (s == 5) stat_load();
        if constexpr (s == 6) {
          wait_vm1<0>(st);
          PIN(warm);

          row_stats();
        }
        mm(4 * s + 2, 0); mm(4 * s + 2, 1);
        bar();
        if constexpr (s < 7) {
          STAMP_AT(11 + s);
          mm(4 * s + 2, 2);
          rd(4 * s + 4, 0); rd(4 * s + 4, 1); rd(4 * s + 4, 2); SBAR();
#pragma unroll
          for (int t = 0; t < 3; ++t) { mm(4 * s + 3, t); rd(4 * s + 5, t); SBAR(); }
        }
      });
      STAMP_AT(1);
      __builtin_amdgcn_s_setprio(3);
      const f32x4 c5 = csrd(5), c6 = csrd(6), c4 = csrd(4);
      SBAR();
      mm(30, 2); mm(31, 1);
      make_xaug();
      SBAR();
      mm(31, 2); mm(31, 0);
      if (h == 0) *(LAS float*)(size_t)(lds0 + L_RS + (m * 32 + r32) * 4) = rstd;
      acc[1] = MFMA32(waug(c5, 0u), xaug, acc[1]);
      acc[2] = MFMA32(xaug, waug(c6, 0u), acc[2]);
      acc[0] = MFMA32(waug(c4, 0u), xaug, acc[0]);
      SBAR();
      f32x4 rs[4];
#pragma unroll
      for (int qd = 0; qd < 4; ++qd) rs[qd] = __builtin_bit_cast(f32x4, lds128(lds0 + L_RS + (m * 32 + 8 * qd + 4 * h) * 4));
      const unsigned kvo = WS_KV + (j * 64 + T) * 4096;
#pragma unroll
      for (int e = 0; e < 16; ++e) acc[1][e] = MUL1(acc[1][e], rstd);
#pragma unroll
      for (int e = 0; e < 2; ++e) {
        const v4u f = pack8(acc[1], e);
        st16_sc1(f, sws, lane * 16, kvo + e * 1024);
        sts128(lds0 + L_KV + (8 + m) * KVT + e * 1024 + lane * 16, f);
      }
#pragma unroll
      for (int qd = 0; qd < 4; ++qd)
#pragma unroll
        for (int e = 0; e < 4; ++e) acc[2][4 * qd + e] = MUL1(acc[2][4 * qd + e], rs[qd][e]);
#pragma unroll
      for (int s = 0; s < 2; ++s) {
        const v4u f = pack8(acc[2], s);
        st16_sc1(f, sws, lane * 16, kvo + 2048 + s * 1024);
        sts128(lds0 + L_KV + (8 + m) * KVT + s * VB + vpos, f);
      }
      SBAR();
      const float qs = rstd * (0.25f * 1.4426950408889634f);
#pragma unroll
      for (int e = 0; e < 16; ++e) acc[0][e] = MUL1(acc[0][e], qs);
      qf[0] = pack8(acc[0], 0); qf[1] = pack8(acc[0], 1);
      PIN(qf[0]); PIN(qf[1]);
      STAMP_AT(2);
      const int T0 = 4 * r - 8 + 2 * m;
      const bool live = T0 >= 0;
      auto kv_load = [&](v4u (&kv)[8], int i0, int i1) {
        const unsigned src = WS_KV + (j * 64 + (live ? T0 : 0)) * 4096, vo = live ? lane * 16 : h * 512;
#pragma unroll
        for (int i = i0; i < i1; ++i) kv[i] = ld16_sc1_async(sws, vo, src + (live ? i : (i & 3)) * 1024);
      };
      auto kv_stage = [&](v4u (&kv)[8]) {
        const unsigned on = (lane < 2 || live) ? one2 : 0u;
        if (lane < 6) sts128(lds0 + L_KV + (lane < 2 ? 8 + m : 2 * m + ((lane - 2) >> 1)) * KVT + 2048 + (lane & 1) * VB + 1024, (v4u){on, on, on, on});
        const v4u zz = {};
#pragma unroll
        for (int i = 0; i < 2; ++i) {
          sts128(lds0 + L_KV + (2 * m + i) * KVT + lane * 16, kv[4 * i]);
          sts128(lds0 + L_KV + (2 * m + i) * KVT + 1024 + lane * 16, kv[4 * i + 1]);
          sts128(lds0 + L_KV + (2 * m + i) * KVT + vpos, live ? kv[4 * i + 2] : zz);
          sts128(lds0 + L_KV + (2 * m + i) * KVT + VB + vpos, live ? kv[4 * i + 3] : zz);
        }
        arrive_nowait(acnt);
      };
      auto kv_wait = [&](v4u (&kv)[8]) {
        wait_vm<0>();
#pragma unroll
        for (int i = 0; i < 8; ++i) PIN(kv[i]);
      };
      if (heavy) {
        bias_load(1);
        hsetup(1);
        await(acnt + 16, 4);
        STAMP_AT(3);
        v2u fl;
        v4u kv[8];
        hmm(std::integral_constant<int, 1>{}, xf, [&](auto kc) {
          constexpr int ks = kc.value;
          if constexpr (ks == KVPUB) {
            wait_vm<0>();
            PIN(bias0); PIN(bias1);
            if (lane == 0) __hip_atomic_store(flag(FL_KV, T, j), tag, RLX_AGENT);
          }
          if constexpr (ks == 24) fl = ld8_sc1_async(sws, flag_off(FL_KV, live ? T0 + (lane & 1) : 0, j));
        });
        STAMP_AT(18);
        wait_vmf<0>(fl);
        if (!__all(fl[0] == taglo && fl[1] == tag32)) poll_flags(flag(FL_KV, live ? T0 + (lane & 1) : 0, j), tag);
        kv_load(kv, 0, 8);
        hgelu(std::integral_constant<int, 1>{}, [&]() {});
        STAMP_AT(4);
        wait_vm<4>();
#pragma unroll
        for (int i = 0; i < 8; ++i) PIN(kv[i]);
        kv_stage(kv);
      } else {
        wait_vm<0>();
        if (lane == 0) __hip_atomic_store(flag(FL_KV, T, j), tag, RLX_AGENT);
        v4u kv[8];
        poll_flags(flag(FL_KV, live ? T0 + (lane & 1) : 0, j), tag);
        kv_load(kv, 0, 8); kv_wait(kv); kv_stage(kv);
      }
    }
    STAMP_AT(19);
    const unsigned kb = lds0 + L_KV + m * KVT + lane * 16;
    unsigned vb[2];
#pragma unroll
    for (int e = 0; e < 2; ++e) { vb[e] = lds0 + L_KV + m * KVT + 2048 + ((g & 1) ? 1024 : e * 512 + h * 256 + (lane & 15) * 16); PIN(vb[e]); }
    {
      const int dq = r32 - 4 * h;
#pragma unroll
      for (int d = 0; d < 8; ++d) { const int row = 2 * (d & 1) + 8 * (d >> 1); (d < 4 ? am0 : am1)[d & 3] = row > dq ? 0xffffffffu : row == dq ? 0xffff0000u : 0u; }
      PIN(am0); PIN(am1);
    }
    await(acnt, heavy ? 8 : 4);
    __builtin_amdgcn_s_setprio(PRIO_ATT);
    STAMP_AT(5);
    const f32x16 zero = {};
    auto o_store = [&](f32x16& o, int e) {
      const float rl = __builtin_amdgcn_rcpf(o[8]);
#pragma unroll
      for (int u = 0; u < 8; ++u) o[u] = MUL1(o[u], rl);
      st16_sc1(pack8(o, 0), sws, lane * 16, WS_AF + (T * AFT + 112 + 2 * j + e) * 1024);
    };
    int redo = 0;
    {
      f32x16 o[2], sa, sb, su;
      bool pend = false;
      v4u kn, ku, va, vc, vn0, vn1, vu0, vu1, p0, p1, pu;
      auto kaddr = [&](int n) { return kb + ((n & 7) + 1) * KVT + (n >> 3) * 1024; };
      auto vaddr = [&](int n) { return vb[n >> 3] + ((n & 7) + 1) * KVT; };
      kn = lds128(kaddr(0)); vn0 = lds128(vaddr(0)); vn1 = lds128(vaddr(0) + VB);
      sa = MFMA32(kn, qf[0], zero);
      kn = lds128(kaddr(1));
      SBAR();
      auto step = [&](auto nc, f32x16& sc, f32x16& sn) {
        constexpr int n = nc.value, e = n >> 3, i = n & 7;
        if constexpr (n < 15) sn = MFMA32(kn, qf[(n + 1) >> 3], zero);
        if constexpr (n < 14) kn = lds128(kaddr(n + 2));
        if constexpr (i == 5) ku = lds128(kb + e * 1024);
        v4u v0 = vn0, v1 = vn1;
        if constexpr (n < 15) { vn0 = lds128(vaddr(n + 1)); vn1 = lds128(vaddr(n + 1) + VB); }
        if constexpr (i == 6) { vu0 = lds128(vb[e]); vu1 = lds128(vb[e] + VB); }
        SBAR();
        if constexpr (i == 7) {
          static_for<0, 16>([&](auto u) { sc[u.value] = upper<u.value>() ? su[u.value] : sc[u.value]; });
          PIN(sc); SBAR();
        }
#pragma unroll
        for (int u = 0; u < 6; ++u) sc[u] = EXP2(sc[u]);
        PIN(sc); SBAR();
        if constexpr (n == 8) { o[0] = MFMA32(vc, p1, o[0]); SBAR(); }
        else if constexpr (n > 0) { o[e] = MFMA32(vc, p1, o[e]); SBAR(); }
        sc[6] = EXP2(sc[6]); sc[7] = EXP2(sc[7]);
        p0 = pack8(sc, 0);
        sc[8] = EXP2(sc[8]); sc[9] = EXP2(sc[9]);
        if constexpr (i == 7) { va = p0 & am0; p0 &= ~am0; PIN(va); }
        PIN(sc); PIN(p0); SBAR();
        if constexpr (n == 8) { o[0] = MFMA32(vu1, pu, o[0]); SBAR(); }
        o[e] = MFMA32(v0, p0, i == 0 ? zero : o[e]);
        SBAR();
        if constexpr (i == 6) { su = MFMA32(ku, qf[e], zero); SBAR(); }
#pragma unroll
        for (int u = 10; u < 13; ++u) sc[u] = EXP2(sc[u]);
        if constexpr (i == 7) { PIN(sc); SBAR(); o[e] = MFMA32(vu0, va, o[e]); SBAR(); }
#pragma unroll
        for (int u = 13; u < 16; ++u) sc[u] = EXP2(sc[u]);
        p1 = pack8(sc, 1); vc = v1;
        if constexpr (i == 7) { pu = p1 & am1; p1 &= ~am1; PIN(pu); }
        PIN(p1); SBAR();
        if constexpr (n == 8) {
          if (__all(o[0][8] >= 0x1p-30f && o[0][8] <= 0x1p30f)) o_store(o[0], 0); else redo = 1;
          SBAR();
        }
        if constexpr (i == 2) pend = !redo;
        if constexpr (i >= 2) {
          if (pend && (i == 7 || (__builtin_amdgcn_s_getreg(7 | (3 << 11)) & 15) == 0)) {
            wait_vm<0>();
            if (lane == 0) __hip_atomic_store(flag(FL_H1 + e, T, j), tag, RLX_AGENT);
            pend = false;
          }
        }
      };
      static_for<0, 8>([&](auto hc) {
        step(std::integral_constant<int, 2 * hc.value>{}, sa, sb);
        step(std::integral_constant<int, 2 * hc.value + 1>{}, sb, sa);
      });
      o[1] = MFMA32(vc, p1, o[1]);
      o[1] = MFMA32(vu1, pu, o[1]);
      if (__all(o[1][8] >= 0x1p-30f && o[1][8] <= 0x1p30f)) o_store(o[1], 1); else redo |= 2;
    }
    if (redo) {
#pragma unroll 1
      for (int e = 0; e < 2; ++e) {
        if (!((redo >> e) & 1)) continue;
        const v4u q = e ? qf[1] : qf[0];
        const unsigned vbe = e ? vb[1] : vb[0];
        auto merged = [&](const f32x16& c) {
          f32x16 sl = MFMA32(lds128(kb + 8 * KVT + e * 1024), q, c);
          const f32x16 su = MFMA32(lds128(kb + e * 1024), q, c);
          static_for<0, 16>([&](auto u) { sl[u.value] = upper<u.value>() ? su[u.value] : sl[u.value]; });
          return sl;
        };
        float mx = -INFINITY;
#pragma unroll 1
        for (int i = 0; i < 8; ++i) {
          const f32x16 sc = i ? MFMA32(lds128(kb + i * KVT + e * 1024), q, zero) : merged(zero);
#pragma unroll
          for (int u = 0; u < 16; u += 2) mx = max3(mx, sc[u], sc[u + 1]);
        }
        const auto sw = __builtin_amdgcn_permlane32_swap(asu(mx), asu(mx), false, false);
        mx = __builtin_fmaxf(asf(sw[0]), asf(sw[1]));
        f32x16 negm, o;
#pragma unroll
        for (int u = 0; u < 16; ++u) negm[u] = -mx;
        {
          f32x16 sc = merged(negm);
#pragma unroll
          for (int u = 0; u < 16; ++u) sc[u] = EXP2(sc[u]);
          const v4u p0 = pack8(sc, 0), p1 = pack8(sc, 1);
          o = MFMA32(lds128(vbe), p0 & am0, zero);
          o = MFMA32(lds128(vbe + VB), p1 & am1, o);
          o = MFMA32(lds128(vbe + 8 * KVT), p0 & ~am0, o);
          o = MFMA32(lds128(vbe + 8 * KVT + VB), p1 & ~am1, o);
        }
#pragma unroll 1
        for (int i = 1; i < 8; ++i) {
          f32x16 sc = MFMA32(lds128(kb + i * KVT + e * 1024), q, negm);
#pragma unroll
          for (int u = 0; u < 16; ++u) sc[u] = EXP2(sc[u]);
          o = MFMA32(lds128(vbe + i * KVT), pack8(sc, 0), o);
          o = MFMA32(lds128(vbe + i * KVT + VB), pack8(sc, 1), o);
        }
        o_store(o, e);
      }
      wait_vm<0>();
      if (lane == 0) __hip_atomic_store(flag(FL_O0, T, j), tag, RLX_AGENT);
    }
    STAMP_AT(6);
    wait_vm<0>();
    if (lane == 0) __hip_atomic_store(flag(FL_O1, T, j), tag, RLX_AGENT);
    STAMP_AT(7);
    res_loads();
    wait_vm2<0>(xres, bres);
    if (blockIdx.x == 0 && wave == 7) {
#pragma unroll 1
      for (int i = 0; i < 4; ++i) poll_flags(flag(FL_KV, 4 * (4 * i + g), lane & 15), tag);
      if (lane == 0) __hip_atomic_store((GAS u64*)(ws + WS_EPOCH), tag, RLX_AGENT);
    }
  }
  bar();
  {
    int t2 = threadIdx.x; PIN(t2);
    const int row = t2 >> 3, ch = t2 & 7, orow = 64 * rt + row, ocol = 64 * ct + ch * 8;
    const unsigned a = lds0 + L_WG + (((row >> 5) * 8 + ch) * 66 + (row & 31)) * 16;
    f32x4 lo = {}, hi = {};
#pragma unroll
    for (int w = 0; w < 4; ++w) {
      lo += __builtin_bit_cast(f32x4, lds128(a + w * PART));
      hi += __builtin_bit_cast(f32x4, lds128(a + w * PART + 512));
    }
    v4u o;
    o[0] = pk2(lo[0] + bflo(xres[0]) + bflo(bres[0]), lo[1] + bfhi(xres[0]) + bfhi(bres[0]));
    o[1] = pk2(lo[2] + bflo(xres[1]) + bflo(bres[1]), lo[3] + bfhi(xres[1]) + bfhi(bres[1]));
    o[2] = pk2(hi[0] + bflo(xres[2]) + bflo(bres[2]), hi[1] + bfhi(xres[2]) + bfhi(bres[2]));
    o[3] = pk2(hi[2] + bflo(xres[3]) + bflo(bres[3]), hi[3] + bfhi(xres[3]) + bfhi(bres[3]));
    *(GAS v4u*)((char*)out + (size_t)orow * 1024 + ocol * 2) = o;
  }
  STAMP_AT(8);
}

extern "C" void kernel_launch(void* const* d_in, const int* in_sizes, int n_in, void* d_out, int out_size, void* d_ws, size_t ws_size,
                              hipStream_t stream) {
  (void)in_sizes; (void)n_in; (void)out_size; (void)ws_size;
  (void)hipFuncSetAttribute((const void*)fused_block, hipFuncAttributeMaxDynamicSharedMemorySize, LDS_BYTES);
  fused_block<<<256, 512, LDS_BYTES, stream>>>(d_in[0], d_in[1], d_in[2], d_in[4], d_in[5], d_in[6], d_in[7], d_out, (char*)d_ws);
}
```

```cpp
#include <hip/hip_runtime.h>
#include <hip/hip_bf16.h>
#include <stdint.h>
#include <type_traits>

typedef unsigned v4u __attribute__((ext_vector_type(4)));
typedef unsigned v2u __attribute__((ext_vector_type(2)));
typedef float f32x2 __attribute__((ext_vector_type(2)));
typedef float f32x4 __attribute__((ext_vector_type(4)));
typedef float f32x16 __attribute__((ext_vector_type(16)));
typedef short bf16x8 __attribute__((ext_vector_type(8)));
typedef short s16x4 __attribute__((ext_vector_type(4)));
typedef __bf16 bf16x2_t __attribute__((ext_vector_type(2)));
typedef unsigned long long u64;
#define GAS __attribute__((address_space(1)))
#define LAS __attribute__((address_space(3)))
#define DI __device__ __forceinline__
#define RLX_AGENT __ATOMIC_RELAXED, __HIP_MEMORY_SCOPE_AGENT

constexpr int SEQ = 2048, DM = 512, FF = 1792;
constexpr unsigned WS_EPOCH = 0;
constexpr unsigned WS_STATS = 32768;
constexpr unsigned WS_FLAG = 65536;
constexpr unsigned WS_KV = WS_FLAG + 5 * 1024 * 128;
constexpr int AFT = 145;
constexpr unsigned WS_AF = WS_KV + 16 * 64 * 4096;
constexpr unsigned WS_END = WS_AF + 64 * AFT * 1024;
constexpr unsigned WS_STAMP = WS_END;
enum { FL_KV = 0, FL_H0 = 1, FL_H1 = 2, FL_O0 = 3, FL_O1 = 4 };
constexpr int STG = 36864;
constexpr int L_WA = 0;
constexpr int L_WG = 86016;
constexpr int VB = 1040;
constexpr int KVT = 2048 + 2 * VB;
constexpr int L_KV = 32768;
constexpr int RD = 8, NR = 12;
constexpr int PART = 16 * 66 * 16;
constexpr int L_CS = L_WG + 4 * PART;
constexpr int L_RS = L_CS + 3584;
constexpr int L_CNT = L_RS + 512;
constexpr int LDS_BYTES = L_CNT + 64;
static_assert(L_KV + 8 * KVT >= 65536 && L_KV + 12 * KVT <= L_WG && L_WG >= 2 * STG && L_CS >= 3 * STG && PART >= RD * 2048 && LDS_BYTES <= 163840, "lds map");
#define AWIN 6
constexpr int AW = AWIN;
constexpr unsigned SPIN_CAP = 1u << 23;

#define MFMA32(a, b, c) __builtin_amdgcn_mfma_f32_32x32x16_bf16(__builtin_bit_cast(bf16x8, a), __builtin_bit_cast(bf16x8, b), c, 0, 0, 0)
#define MFMA16(a, b, c) __builtin_amdgcn_mfma_f32_16x16x32_bf16(__builtin_bit_cast(bf16x8, a), __builtin_bit_cast(bf16x8, b), c, 0, 0, 0)
#define SBAR() __builtin_amdgcn_sched_barrier(0)

#define STAMP_AT(i) do { } while (0)

DI float asf(unsigned u) { return __builtin_bit_cast(float, u); }
DI unsigned asu(float f) { return __builtin_bit_cast(unsigned, f); }
DI unsigned pk2(float lo, float hi) { return __builtin_bit_cast(unsigned, __builtin_convertvector((f32x2){lo, hi}, bf16x2_t)); }
DI float bflo(unsigned u) { return __builtin_bit_cast(float, u << 16); }
DI float bfhi(unsigned u) { return __builtin_bit_cast(float, u & 0xffff0000u); }
DI v4u make_srd(const void* p, unsigned bytes) { const u64 a = (u64)p; return (v4u){(unsigned)a, (unsigned)(a >> 32) & 0xffffu, bytes, 0x00020000u}; }
DI void dma16(v4u srd, unsigned voff, unsigned soff, unsigned lds) {
  asm volatile("s_mov_b32 m0, %3\n\ts_nop 0\n\tbuffer_load_dwordx4 %0, %1, %2 offen lds" :: "v"(voff), "s"(srd), "s"(soff), "s"(lds) : "memory");
}
DI void dma16_a(v4u srd, unsigned voff, unsigned soff, unsigned lds) { dma16(srd, voff, soff, lds); }
DI void dma16_b(v4u srd, unsigned voff1, unsigned soff) {
  asm volatile("buffer_load_dwordx4 %0, %1, %2 offen offset:1024 lds" :: "v"(voff1), "s"(srd), "s"(soff) : "memory");
}
DI v4u ld16_sc1_async(v4u srd, unsigned voff, unsigned soff) {
  v4u r; asm volatile("buffer_load_dwordx4 %0, %1, %2, %3 offen sc1" : "=v"(r) : "v"(voff), "s"(srd), "s"(soff) : "memory"); return r;
}
DI v4u ld16_async(v4u srd, unsigned voff, unsigned soff) {
  v4u r; asm volatile("buffer_load_dwordx4 %0, %1, %2, %3 offen" : "=v"(r) : "v"(voff), "s"(srd), "s"(soff) : "memory"); return r;
}
DI unsigned ld2_async(v4u srd, unsigned voff, unsigned soff) {
  unsigned r; asm volatile("buffer_load_ushort %0, %1, %2, %3 offen" : "=v"(r) : "v"(voff), "s"(srd), "s"(soff) : "memory"); return r;
}
DI void st16_sc1(v4u v, v4u srd, unsigned voff, unsigned soff) {
  asm volatile("buffer_store_dwordx4 %0, %1, %2, %3 offen sc1\n\ts_nop 1" :: "v"(v), "v"(voff), "s"(srd), "s"(soff) : "memory");
}
template <int N> DI void wait_vm1(v4u& a) { asm volatile("s_waitcnt vmcnt(%1)" : "+v"(a) : "i"(N) : "memory"); }
template <int N> DI void wait_vm2(v4u& a, v4u& b) { asm volatile("s_waitcnt vmcnt(%2)" : "+v"(a), "+v"(b) : "i"(N) : "memory"); }
template <int N> DI void wait_vm3(v4u& a, v4u& b, v4u& c) { asm volatile("s_waitcnt vmcnt(%3)" : "+v"(a), "+v"(b), "+v"(c) : "i"(N) : "memory"); }
template <class V> DI void pin(V& v) { asm volatile("" : "+v"(v)); }
template <int N> DI void wait_vm() { asm volatile("s_waitcnt vmcnt(%0)" :: "i"(N) : "memory"); }
DI void bar() { asm volatile("s_waitcnt lgkmcnt(0)\n\ts_barrier" ::: "memory"); }
DI v4u lds128(unsigned a) { return *(const LAS v4u*)(size_t)a; }
DI void sts128(unsigned a, v4u v) { *(LAS v4u*)(size_t)a = v; }
DI v2u ldtr(unsigned a) { return __builtin_bit_cast(v2u, __builtin_amdgcn_ds_read_tr16_b64_v4i16((LAS s16x4*)(size_t)a)); }
DI v4u ldtr2(unsigned a, int second) { const v2u lo = ldtr(a), hi = ldtr(a + second); return (v4u){lo.x, lo.y, hi.x, hi.y}; }
template <int CTRL> DI float dpp(float v) { return __builtin_bit_cast(float, __builtin_amdgcn_update_dpp(0, __builtin_bit_cast(int, v), CTRL, 0xf, 0xf, true)); }
DI float rdl(float v, int l) { return __builtin_bit_cast(float, __builtin_amdgcn_readlane(__builtin_bit_cast(int, v), l)); }
DI v4u pack8(const f32x16& a, int s) { return (v4u){pk2(a[8 * s], a[8 * s + 1]), pk2(a[8 * s + 2], a[8 * s + 3]), pk2(a[8 * s + 4], a[8 * s + 5]), pk2(a[8 * s + 6], a[8 * s + 7])}; }
DI void poll_flags(const GAS u64* p, u64 tag) {
#pragma unroll 1
  for (unsigned spin = 0; spin < SPIN_CAP; ++spin) {
    const u64 v = __hip_atomic_load(p, RLX_AGENT);
    if (__all(v == tag)) break;
    __builtin_amdgcn_s_sleep(2);
  }
  asm volatile("" ::: "memory");
}
constexpr u64 upper_mask(int reg) {
  u64 m = 0;
  for (int l = 0; l < 64; ++l) { const int row = (reg & 3) + 8 * (reg >> 2) + 4 * (l >> 5); if (row > (l & 31)) m |= 1ull << l; }
  return m;
}
template <int REG> DI bool upper() { return __builtin_amdgcn_inverse_ballot_w64(upper_mask(REG)); }
template <int I, int N, class Fn> DI void static_for(Fn&& f) { if constexpr (I < N) { f(std::integral_constant<int, I>{}); static_for<I + 1, N>(f); } }
DI float max3(float a, float b, float c) { return __builtin_fmaxf(__builtin_fmaxf(a, b), c); }

constexpr int NX = 4;
constexpr bool isx(int kk) { return kk >= NR + RD && kk < NR + RD + NX; }
constexpr int ringpos(int kk) { return (kk < NR + RD ? kk : kk - NX) % RD; }
constexpr int refill_of(int kk) { const int n = isx(kk) ? 99 : kk < NR + RD ? kk + RD + NX : kk + RD; return n < 36 ? n : -1; }
constexpr int H1_CK = 16 - AW, H1_LD = H1_CK - 3, O_CK = 28 - AW, O_LD = O_CK - 3;
constexpr int g2_issued(int t) { return ((t >= 1 && refill_of(t - 1 + NR) >= 0) ? 2 : 0) + ((t + AW < 28) ? 2 : 0) + (t == H1_LD) + (t == O_LD) + (t == O_CK ? 10 : 0); }
constexpr int g2_range(int a, int b) { int n = 0; for (int t = a; t < b; ++t) n += g2_issued(t); return n; }
#define GELU_PK(P) (P == 1)
#define PRIO_G2 2
#define PRIO_ATT 3
#define PRIO_G1 2
#define ROT r
#define CODEWARM 39000
#define MIDWARM 1
#define KVPUB 6
#define EXP2(v) __builtin_amdgcn_exp2f(v)
#define PIN(v) pin(v)
extern "C" __device__ float mul_unpacked(float, float) __asm("llvm.amdgcn.fmul.legacy");
#define MUL1(a, b) mul_unpacked(a, b)
#define FMA1(a, b, c) __builtin_amdgcn_div_fmasf(a, b, c, false)
DI v2u ld8_sc1_async(v4u srd, unsigned voff) {
  v2u r; asm volatile("buffer_load_dwordx2 %0, %1, %2, 0 offen sc1" : "=v"(r) : "v"(voff), "s"(srd) : "memory"); return r;
}
template <int N> DI void wait_vmf(v2u& a) { asm volatile("s_waitcnt vmcnt(%1)" : "+v"(a) : "i"(N) : "memory"); }
template <int N> DI bool complete(const v4u* f) {
  unsigned mx = 0;
#pragma unroll
  for (int i = 0; i < N; ++i) { mx = max(max(mx, f[i][0]), f[i][1]); mx = max(max(mx, f[i][2]), f[i][3]); }
  return __all(mx != 0xffffffffu);
}
DI void arrive(unsigned cnt, int lane) {
  asm volatile("s_waitcnt lgkmcnt(0)" ::: "memory");
  if (lane == 0) __hip_atomic_fetch_add((LAS unsigned*)(size_t)cnt, 1u, __ATOMIC_RELAXED, __HIP_MEMORY_SCOPE_WORKGROUP);
}
DI void arrive_nowait(unsigned cnt) {
  asm volatile("s_mov_b64 exec, 1\n\tds_add_u32 %0, %1\n\ts_mov_b64 exec, -1" :: "v"(cnt), "v"(1u) : "memory");
}
DI unsigned peek(unsigned cnt) { return __hip_atomic_load((LAS unsigned*)(size_t)cnt, __ATOMIC_RELAXED, __HIP_MEMORY_SCOPE_WORKGROUP); }
DI void await(unsigned cnt, unsigned n) {
#pragma unroll 1
  for (unsigned spin = 0; spin < SPIN_CAP * 32; ++spin) {
    if (__builtin_amdgcn_readfirstlane(peek(cnt)) >= n) break;
    __builtin_amdgcn_s_sleep(1);
  }
  asm volatile("" ::: "memory");
}

__global__ __launch_bounds__(512, 2) void fused_block(const void* __restrict__ x, const void* __restrict__ wqkv, const void* __restrict__ wo,
                                                      const void* __restrict__ w1, const void* __restrict__ b1, const void* __restrict__ w2,
                                                      const void* __restrict__ b2, void* __restrict__ out, char* __restrict__ ws) {
  constexpr unsigned lds0 = 0;
  const int wave = __builtin_amdgcn_readfirstlane(threadIdx.x >> 6);
  const int m = wave & 3;
  const bool isG = wave < 4;
  const int xcd = blockIdx.x & 7, idx = blockIdx.x >> 3;
  const int r = (xcd >> 2) * 8 + (idx >> 2), j = (xcd & 3) * 4 + (idx & 3);
  const int rt = (xcd >> 1) * 8 + (idx >> 2), ct = (xcd & 1) * 4 + (idx & 3);
  int lane = threadIdx.x & 63, r32, h, g, q4, p4;
  STAMP_AT(0);
  asm volatile("" :: "s"(x), "s"(wqkv), "s"(wo), "s"(w1), "s"(b1), "s"(w2), "s"(b2), "s"(out), "s"(ws));

  const v4u sx = make_srd(x, SEQ * DM * 2), sws = make_srd(ws, WS_END);
  const __amdgpu_buffer_rsrc_t rws = __builtin_amdgcn_make_buffer_rsrc(ws, 0, WS_END, 0x00020000);
  const unsigned one2 = 0x3f803f80u;
  const int T = 4 * r + m;
  const int rot = ROT;
  auto flag_off = [&](int kind, int tile, int jj) { return WS_FLAG + ((kind * 64 + tile) * 16 + jj) * 128; };
  auto flag = [&](int kind, int tile, int jj) { return (GAS u64*)(ws + flag_off(kind, tile, jj)); };
  u64 tag;
  unsigned taglo, tag32;
  auto get_tag = [&]() {
    const v2u tg = *(const LAS v2u*)(size_t)(lds0 + L_CNT + 32);
    taglo = __builtin_amdgcn_readfirstlane(tg[0]); tag32 = __builtin_amdgcn_readfirstlane(tg[1]);
    tag = ((u64)tag32 << 32) | taglo;
  };
  v4u xres, bres, xaug, st;
  float mean, var, rstd, sd;
  v4u sel;
  unsigned xo[4], swz;
  auto lane_setup = [&]() {
    lane = threadIdx.x & 63; PIN(lane);
    r32 = lane & 31; h = lane >> 5; g = lane >> 4; q4 = (lane >> 2) & 3; p4 = lane & 3;
    const unsigned selv = ((lane & 15) == (g & 1)) ? one2 : 0u;
    sel = (v4u){selv, selv, selv, selv};
#pragma unroll
    for (int p = 0; p < 4; ++p) xo[p] = (32 * m + r32) * 128 + (((2 * ((p + m) & 3) + h) ^ ((r32 >> 1) & 7)) * 16);
    swz = ((((lane >> 2) & 1) ^ (g & 1)) * 64) + p4 * 16;
  };

  auto stat_load = [&]() { st = ld16_sc1_async(sws, WS_STATS + (128 * r + 32 * m + r32) * 16, 0); };
  auto res_loads = [&]() {
    int t2 = threadIdx.x; PIN(t2);
    const int row = 64 * rt + (t2 >> 3), col = 64 * ct + (t2 & 7) * 8;
    xres = ld16_async(sx, row * 1024 + col * 2, 0);
    bres = ld16_async(make_srd(b2, DM * 2), col * 2, 0);
  };
  auto row_stats = [&]() {
    if (!__all(st[1] == tag32 && st[3] == tag32)) {
#pragma unroll 1
      for (unsigned spin = 0; spin < SPIN_CAP; ++spin) {
        st = ld16_sc1_async(sws, WS_STATS + (128 * r + 32 * m + r32) * 16, 0);
        wait_vm1<0>(st);
        if (__all(st[1] == tag32 && st[3] == tag32)) break;
      }
    }
    mean = asf(st[0]); var = asf(st[2]);
    PIN(mean); PIN(var);
  };
  auto make_xaug = [&]() {
    PIN(mean); PIN(var);
    rstd = __builtin_amdgcn_rsqf(var); sd = var * rstd;
    const unsigned hi = pk2(-mean, sd);
    const unsigned lo = pk2(-mean - bflo(hi), FMA1(bfhi(hi), -1.f, sd));
    xaug = (v4u){(hi & 0xffffu) | (hi << 16), (lo & 0xffffu) | (hi & 0xffff0000u), lo >> 16, 0u};
    if (h) xaug = (v4u){0u, 0u, 0u, 0u};
    PIN(xaug);
  };
  auto cswr = [&](int tile, f32x4 c) {
    if (lane < 16) { LAS float* p = (LAS float*)(size_t)(lds0 + L_CS + (tile * 32 + lane) * 16 + m * 4); p[0] = c[0]; p[64] = c[1]; }
  };
  auto csrd = [&](int tile) { return __builtin_bit_cast(f32x4, lds128(lds0 + L_CS + (tile * 32 + r32) * 16)); };
  auto waug = [&](f32x4 c4, unsigned b16) {
    const float c = ((c4[0] + c4[1]) + c4[2]) + c4[3];
    const unsigned hi = pk2(c, 0.f);
    const unsigned lo = pk2(c - bflo(hi), 0.f);
    return (v4u){hi | (lo << 16), hi | (b16 << 16), b16, 0u};
  };

  const v4u sw1 = make_srd(w1, DM * FF * 2);
  const int hcol0 = 448 * (j >> 2) + 128 * (j & 3);
  const bool heavy = (j & 3) != 3;
  unsigned bias0, bias1;
  auto bias_load = [&](int P) {
    bias0 = ld2_async(make_srd(b1, FF * 2), r32 * 2, (hcol0 + 64 * P) * 2);
    bias1 = ld2_async(make_srd(b1, FF * 2), r32 * 2, (hcol0 + 64 * P + 32) * 2);
  };
  auto w1dma = [&](int P, int c, unsigned dst, int half) {
    const unsigned vw1 = (lane >> 3) * (FF * 2) + swz, vw1b = vw1 + 8 * (FF * 2) - 1024;
    const unsigned so = (64 * ((c + rot) & 7) + 16 * m) * (FF * 2) + (hcol0 + 64 * P) * 2;
    if (half == 0) dma16_a(sw1, vw1, so, dst + m * 2048); else dma16_b(sw1, vw1b, so);
  };
  f32x16 hac[2];
  unsigned rk[4][2];
  auto hsetup = [&](int P) {
#pragma unroll
    for (int p = 0; p < 4; ++p)
#pragma unroll
      for (int c = 0; c < 2; ++c) {
        rk[p][c] = lds0 + (P ? L_WA : 28672) + ((p + m) & 3) * 2048 + h * 1024 + q4 * 128 + ((c ^ (q4 >> 1)) * 64) + (g & 1) * 32 + p4 * 8;
        PIN(rk[p][c]);
      }
  };
  auto hmm = [&](auto pc, const v4u (&xf)[32], auto&& hook) {
    constexpr int P = pc.value;
    constexpr int D = 4;
    const unsigned cnt = lds0 + L_CNT;
    f32x4 cs[2] = {}, c0, c1;
    v4u wf[D][2], au0, au1;
    unsigned seen = 0;
    hac[0] = (f32x16){}; hac[1] = (f32x16){};
    auto rd = [&](int ks, int t) { wf[ks % D][t] = ldtr2(rk[ks & 3][t] + (ks >> 2) * 8192, 512); };
#pragma unroll
    for (int ks = 0; ks < D; ++ks) { rd(ks, 0); rd(ks, 1); }
    SBAR();
    static_for<0, 32>([&](auto kc) {
      constexpr int ks = kc.value;
      hook(kc);
      if constexpr (ks == 10) seen = peek(cnt + 20);
      if constexpr (ks == 29) seen = peek(cnt);
#pragma unroll
      for (int t = 0; t < 2; ++t) {
        hac[t] = MFMA32(wf[ks % D][t], xf[ks], hac[t]);
        if ((ks & 3) == 0 && ks < 28) { cs[t] = MFMA16(sel, wf[ks % D][t], cs[t]); PIN(cs[t]); }
        if (ks == 26) { cs[t] = MFMA16(sel, wf[28 % D][t], cs[t]); PIN(cs[t]); }
        SBAR();
        if constexpr (ks + D < 32) rd(ks + D, t);
        if constexpr (ks == 31) { if (t == 0) { au0 = waug(c0, bias0); PIN(au0); } else { au1 = waug(c1, bias1); PIN(au1); } }
        SBAR();
      }
      if constexpr (ks == 11 || ks == 30) {
        PIN(hac[0]); PIN(hac[1]);
        if (__builtin_amdgcn_readfirstlane(seen) < 4) await(ks == 11 ? cnt + 20 : cnt, 4);
        asm volatile("" ::: "memory");
        if constexpr (ks == 30) { c0 = csrd(2 * P); c1 = csrd(2 * P + 1); }
      }
      if constexpr (ks == 27) { cswr(2 * P, cs[0]); cswr(2 * P + 1, cs[1]); arrive_nowait(cnt); }
      SBAR();
    });
    hac[0] = MFMA32(au0, xaug, hac[0]);
    hac[1] = MFMA32(au1, xaug, hac[1]);
    SBAR();
  };
  auto hgelu = [&](auto pc, auto&& mid) {
    constexpr int P = pc.value;
    constexpr bool PK = GELU_PK(P);
    const float C0 = 2.3022081986f, C1 = C0 * 0.044715f;
    const float ga = -C1 * rstd * rstd * rstd, gb = -C0 * rstd;
    auto mul = [&](f32x2 a, f32x2 b) { return PK ? a * b : (f32x2){MUL1(a[0], b[0]), MUL1(a[1], b[1])}; };
    auto fma = [&](f32x2 a, float b, float c) {
      return PK ? __builtin_elementwise_fma(a, (f32x2){b, b}, (f32x2){c, c}) : (f32x2){FMA1(a[0], b, c), FMA1(a[1], b, c)}; };
#pragma unroll
    for (int t = 0; t < 2; ++t) {
      f32x2 v[8], z[8];
#pragma unroll
      for (int e = 0; e < 8; ++e) { v[e] = (f32x2){hac[t][2 * e], hac[t][2 * e + 1]}; z[e] = mul(v[e], v[e]); }
      SBAR();
#pragma unroll
      for (int e = 0; e < 8; ++e) z[e] = fma(z[e], ga, gb);
      SBAR();
#pragma unroll
      for (int e = 0; e < 8; ++e) z[e] = mul(z[e], v[e]);
      SBAR();
#pragma unroll
      for (int e = 0; e < 8; ++e) z[e] = (f32x2){EXP2(z[e][0]), EXP2(z[e][1])};
      SBAR();
#pragma unroll
      for (int e = 0; e < 8; ++e) z[e] = fma(z[e], sd, sd);
      SBAR();
#pragma unroll
      for (int e = 0; e < 8; ++e) z[e] = (f32x2){__builtin_amdgcn_rcpf(z[e][0]), __builtin_amdgcn_rcpf(z[e][1])};
      SBAR();
#pragma unroll
      for (int e = 0; e < 8; ++e) z[e] = mul(z[e], v[e]);
      SBAR();
#pragma unroll
      for (int s = 0; s < 2; ++s) {
        const v4u f = {pk2(z[4 * s][0], z[4 * s][1]), pk2(z[4 * s + 1][0], z[4 * s + 1][1]), pk2(z[4 * s + 2][0], z[4 * s + 2][1]), pk2(z[4 * s + 3][0], z[4 * s + 3][1])};
        st16_sc1(f, sws, lane * 16, WS_AF + (T * AFT + (hcol0 >> 4) + 4 * P + 2 * t + s) * 1024);
      }
      if (t == 0) mid();
      SBAR();
    }
  };

  if (isG) {
    lane_setup();
    const v4u sw2 = make_srd(w2, FF * DM * 2), swo = make_srd(wo, DM * DM * 2);
    const unsigned vw2 = (lane >> 3) * 1024 + swz, vw2b = vw2 + 7 * 1024;
    const int ro3 = rt % 3;
    auto kmap = [&](int kk) {
      if (kk < 16) return 8 * (((kk >> 2) + rt) & 3) + (kk & 3);
      const int p = ((kk - 16) >> 2) + ro3;
      return 8 * (p >= 3 ? p - 3 : p) + 4 + (kk & 3);
    };
    auto omap = [&](int i) { return 2 * ((i + rt) & 3) + (i >> 2); };
    auto wdmah = [&](int kk, int half) {
      const unsigned so = (kk < 28 ? 448 * m + 16 * kmap(kk) : 128 * m + 16 * omap(kk - 28)) * 1024 + 128 * ct;
      if (half == 0) dma16_a(kk < 28 ? sw2 : swo, vw2, so, lds0 + (isx(kk) ? L_WA + m * 8192 + (kk - NR - RD) * 2048 : L_WG + m * PART + ringpos(kk) * 2048)); else dma16_b(kk < 28 ? sw2 : swo, vw2b, so);
    };
    auto wdma = [&](int kk) { wdmah(kk, 0); wdmah(kk, 1); };
    {
      const v4u swq = make_srd(wqkv, DM * 3 * DM * 2);
      const unsigned vx0 = (lane >> 3) * 1024 + (((lane & 7) ^ g) * 16), vx1 = (7 + (lane >> 3)) * 1024 + (((lane & 7) ^ (4 + g)) * 16);
      const unsigned vwq = (lane >> 2) * (3 * DM * 2) + p4 * 16;
      auto piece = [&](int s, int i) {
        const unsigned buf = lds0 + (s % 3) * STG;
        const int kc = (s + rot) & 7;
        if (i < 4) {
          const unsigned so = (128 * r + 32 * m + 16 * (i >> 1)) * 1024 + kc * 128;
          if (i & 1) dma16_b(sx, vx1, so); else dma16_a(sx, vx0, so, buf + m * 4096 + (i >> 1) * 2048);
        } else if (i < 7) dma16(swq, vwq, (64 * kc + 16 * m) * (3 * DM * 2) + (512 * (i - 4) + 32 * j) * 2, buf + 16384 + ((i - 4) * 4 + m) * 1024);
        else w1dma(0, s, buf + 28672, i - 7);
      };
#pragma unroll
      for (int i = 0; i < 18; ++i) { piece(i / 9, i % 9); if (i == 8) __builtin_amdgcn_s_barrier(); }
      __builtin_amdgcn_s_setprio(PRIO_G1);
      hsetup(0);
      f32x4 cs[2] = {};
      v4u xf[4], wf[4][2];
      hac[0] = (f32x16){}; hac[1] = (f32x16){};
      static_for<0, 8>([&](auto sc) {
        constexpr int s = sc.value;
        constexpr unsigned buf = (s % 3) * STG;
        auto fill = [&](int i) {
          if constexpr (s < 6) piece(s + 2, i);
          if constexpr (s == 6) { if (i == 0) { stat_load(); bias_load(0); } else wdmah((i - 1) >> 1, (i - 1) & 1); }
          if constexpr (s == 7) { if (heavy) { w1dma(1, i, lds0 + L_WA + i * 8192, 0); w1dma(1, i, 0, 1); } }
          SBAR();
        };
        auto rd = [&](int p) { xf[p] = lds128(buf + xo[p]); wf[p][0] = ldtr2(buf + rk[p][0], 512); wf[p][1] = ldtr2(buf + rk[p][1], 512); SBAR(); };
        if constexpr (s < 7) wait_vm<9>(); else { wait_vm1<8>(st); PIN(bias0); PIN(bias1); }
        bar();
        STAMP_AT(10 + s);
        rd(0);
        if constexpr (s < 7) fill(0);
        rd(1);
        if constexpr (s < 7) fill(1);
        if constexpr (s == 2) get_tag();
#pragma unroll
        for (int p = 0; p < 4; ++p) {
#pragma unroll
          for (int t = 0; t < 2; ++t) {
            hac[t] = MFMA32(wf[p][t], xf[p], hac[t]);
            if (p == 0) { cs[t] = MFMA16(sel, wf[p][t], cs[t]); PIN(cs[t]); }
            SBAR();
            if (p < 2 && t == 0) rd(p + 2);
            if (s == 7) fill(2 * p + t); else if (2 * p + t < 7) fill(2 + 2 * p + t);
          }
          if (s == 7 && p == 1) {
            cswr(0, cs[0]); cswr(1, cs[1]);
            bar();
            STAMP_AT(1);
          }
        }
        if constexpr (s == 7) {
          const f32x4 c0 = csrd(0), c1 = csrd(1);
          row_stats(); make_xaug();
          hac[0] = MFMA32(waug(c0, bias0), xaug, hac[0]);
          hac[1] = MFMA32(waug(c1, bias1), xaug, hac[1]);
          SBAR();
        }
      });
    }
    STAMP_AT(18);
    if (heavy) { wait_vm<8>(); arrive_nowait(lds0 + L_CNT + 16); }
    hgelu(std::integral_constant<int, 0>{}, [&]() {
      wait_vm<2>();
      if (heavy) arrive_nowait(lds0 + L_CNT + 20);
    });
    __builtin_amdgcn_s_setprio(0);
    STAMP_AT(19);
    unsigned rb2[2];
#pragma unroll
    for (int c = 0; c < 2; ++c) rb2[c] = lds0 + L_WG + m * PART + h * 512 + q4 * 128 + ((c ^ (q4 >> 1)) * 64) + (g & 1) * 32 + p4 * 8;
    v4u wq[36][2];
    auto wrd = [&](int kk) {
#pragma unroll
      for (int c = 0; c < 2; ++c)
        wq[kk][c] = isx(kk) ? ldtr2(rb2[c] - (L_WG - L_WA) - m * (PART - 8192) + (kk - NR - RD) * 2048, 1024) : ldtr2(rb2[c] + ringpos(kk) * 2048, 1024);
    };
    auto wgot = [&](int kk) { PIN(wq[kk][0]); PIN(wq[kk][1]); };
    const int ftile = 2 * rt + ((lane >> 2) & 1);
    v4u af[28][2], of[8][2];
    v2u fl;
    const unsigned a0 = WS_AF + (2 * rt * AFT + 28 * m) * 1024, o0 = WS_AF + (2 * rt * AFT + 112 + 8 * m) * 1024;
    for (int kk = 0; kk < 4; ++kk) wrd(kk);
    for (int kk = 4; kk < 8; ++kk) wdma(kk);
    for (int kk = 0; kk < 4; ++kk) wgot(kk);
    wait_vm<8>();
    if (lane == 0) __hip_atomic_store(flag(FL_H0, T, j), tag, RLX_AGENT);
    STAMP_AT(2);
    for (int kk = 8; kk < 12; ++kk) wdma(kk);
    static_for<1, 3>([&](auto ic) {
      constexpr int i = ic.value;
      wait_vm<(i == 1) ? 9 : 8>();
      for (int kk = 4 * i; kk < 4 * i + 4; ++kk) wrd(kk);
      for (int kk = 4 * i; kk < 4 * i + 4; ++kk) wgot(kk);
      if constexpr (i == 2) fl = ld8_sc1_async(sws, flag_off(FL_H0, ftile, 4 * m + p4));
      for (int kk = 4 * i; kk < 4 * i + 4; ++kk) wdma(kk + RD);
    });
    if (heavy) await(lds0 + L_CNT, 4);
    for (int kk = NR + RD; kk < NR + RD + NX; ++kk) wdma(kk);
    STAMP_AT(3);
    wait_vmf<8 + 2 * NX>(fl);
    if (!__all(fl[0] == taglo && fl[1] == tag32)) poll_flags(flag(FL_H0, ftile, 4 * m + p4), tag);
    STAMP_AT(4);
#pragma unroll
    for (int kk = 0; kk < AW; ++kk) {
      af[kk][0] = ld16_sc1_async(sws, lane * 16, a0 + kmap(kk) * 1024);
      af[kk][1] = ld16_sc1_async(sws, lane * 16, a0 + (AFT + kmap(kk)) * 1024);
    }
    __builtin_amdgcn_s_setprio(PRIO_G2);
    f32x16 c2[2][2] = {};
    auto mm2 = [&](int kk, const v4u (&a)[2], auto&& fill) {
      SBAR();
#pragma unroll
      for (int i = 0; i < 4; ++i) { c2[i >> 1][i & 1] = MFMA32(wq[kk][i & 1], a[i >> 1], c2[i >> 1][i & 1]); SBAR(); fill(i); SBAR(); }
    };
    static_for<0, 28>([&](auto kc) {
      constexpr int kk = kc.value;
      wait_vm2<(kk < AW) ? 2 * (AW - 1 - kk) + g2_range(0, kk) : g2_range(kk - AW + 1, kk)>(af[kk][0], af[kk][1]);
      if constexpr (kk == H1_CK) {
        STAMP_AT(20);
        wait_vmf<g2_range(H1_LD, H1_CK) - 1>(fl);
        if (!__all(fl[0] == taglo && fl[1] == tag32)) poll_flags(flag(FL_H1, ftile, 4 * m + p4), tag);
        STAMP_AT(21);
      }
      if constexpr (kk == H1_LD) fl = ld8_sc1_async(sws, flag_off(FL_H1, ftile, 4 * m + p4));
      if constexpr (kk == O_LD) fl = ld8_sc1_async(sws, flag_off(FL_O0, ftile, 4 * m + p4));
      constexpr bool dw = kk >= 1 && refill_of(kk - 1 + NR) >= 0;
      if constexpr (dw) wgot(kk - 1 + NR);
      mm2(kk, af[kk], [&](int i) {
        if constexpr (dw) { if (i < 2) wdmah(refill_of(kk - 1 + NR), i); }
        if constexpr (kk + NR < 28) { if (i == 1) wrd(kk + NR); }
        if constexpr (kk == 27) { if (i == 0) wrd(28); }
        if constexpr (kk + AW < 28) { if (i >= 2) af[kk + AW][i - 2] = ld16_sc1_async(sws, lane * 16, a0 + (AFT * (i - 2) + kmap(kk + AW)) * 1024); }
      });
      if constexpr (kk == O_CK) {
        wait_vmf<g2_range(O_LD, O_CK) - 1>(fl);
        if (!__all(fl[0] == taglo && fl[1] == tag32)) poll_flags(flag(FL_O0, ftile, 4 * m + p4), tag);
        res_loads();
#pragma unroll
        for (int i = 0; i < 4; ++i) {
          of[i][0] = ld16_sc1_async(sws, lane * 16, o0 + omap(i) * 1024);
          of[i][1] = ld16_sc1_async(sws, lane * 16, o0 + (AFT + omap(i)) * 1024);
        }
      }
    });
    auto o1_load = [&]() {
#pragma unroll
      for (int i = 4; i < 8; ++i) {
        of[i][0] = ld16_sc1_async(sws, lane * 16, o0 + omap(i) * 1024);
        of[i][1] = ld16_sc1_async(sws, lane * 16, o0 + (AFT + omap(i)) * 1024);
      }
      fl = ld8_sc1_async(sws, flag_off(FL_O1, ftile, 4 * m + p4));
    };
    static_for<0, 8>([&](auto kc) {
      constexpr int kk = kc.value;
      if (kk < 7) wrd(28 + kk + 1);
      if constexpr (kk == 4) {
        STAMP_AT(5);
        bool up = false;
#pragma unroll 1
        for (unsigned spin = 0; spin < SPIN_CAP; ++spin) {
          wait_vm<0>();
#pragma unroll
          for (int i = 4; i < 8; ++i) { PIN(of[i][0]); PIN(of[i][1]); }
          PIN(fl);
          if (up || complete<8>(&of[4][0])) break;
          up = __all(fl[0] == taglo && fl[1] == tag32);
          o1_load();
        }
        wait_vm<0>();
        STAMP_AT(6);
      }
      if constexpr (kk == 0) o1_load();
      if constexpr (kk < 4) wait_vm2<15 - 2 * kk>(of[kk][0], of[kk][1]);
      mm2(28 + kk, of[kk], [&](int) {});
    });
    PIN(xres); PIN(bres);
    asm volatile("" ::: "memory");
#pragma unroll
    for (int mt = 0; mt < 2; ++mt)
#pragma unroll
      for (int c = 0; c < 2; ++c)
#pragma unroll
        for (int qd = 0; qd < 4; ++qd) {
          const f32x4 v = {c2[mt][c][4 * qd], c2[mt][c][4 * qd + 1], c2[mt][c][4 * qd + 2], c2[mt][c][4 * qd + 3]};
          sts128(lds0 + L_WG + m * PART + ((mt * 8 + c * 4 + qd) * 66 + lane) * 16, __builtin_bit_cast(v4u, v));
        }
    STAMP_AT(7);
  } else {
    __builtin_amdgcn_s_setprio(1);
    lane_setup();
    v4u xr[2];
    xr[0] = ld16_async(sx, lane * 16, (128 * r + 8 * j + 2 * m) * 1024); xr[1] = ld16_async(sx, lane * 16, (128 * r + 8 * j + 2 * m + 1) * 1024);
    unsigned warm;
    {
      u64 pc; asm volatile("s_getpc_b64 %0" : "=s"(pc));
      warm = ld2_async(make_srd((const void*)(pc & ~127ull), CODEWARM), lane < 3 ? lane * 128 : 0x40000000u, (idx * 4 + m) * 384);
    }
    __builtin_amdgcn_s_barrier();
    v2u ep = ld8_sc1_async(sws, m == 0 ? WS_EPOCH : 0x40000000u);
    float sval;
    const int wi = blockIdx.x * 4 + m;
    const u64 wbase = wi < 448 ? (u64)w1 + wi * 4096 : wi < 896 ? (u64)w2 + (wi - 448) * 4096 : (u64)wo + (wi - 896) * 4096;
    auto make_stats = [&]() {
      wait_vm2<1>(xr[0], xr[1]);
      PIN(warm);
      float piv[2], a[4];
#pragma unroll
      for (int u = 0; u < 2; ++u) {
        piv[u] = rdl(bflo(xr[u][0]), 0);
        float s1 = 0.f, s2 = 0.f;
#pragma unroll
        for (int i = 0; i < 8; ++i) { const float d = ((i & 1) ? bfhi(xr[u][i >> 1]) : bflo(xr[u][i >> 1])) - piv[u]; s1 += d; s2 = __builtin_fmaf(d, d, s2); }
        a[2 * u] = s1; a[2 * u + 1] = s2;
      }
#pragma unroll
      for (int i = 0; i < 4; ++i) a[i] += dpp<0xB1>(a[i]);
#pragma unroll
      for (int i = 0; i < 4; ++i) a[i] += dpp<0x4E>(a[i]);
      float t = p4 == 0 ? a[0] : p4 == 1 ? a[1] : p4 == 2 ? a[2] : a[3];
      t += dpp<0x114>(t); t += dpp<0x118>(t);
      const auto s16 = __builtin_amdgcn_permlane16_swap(asu(t), asu(t), false, false);
      t = asf(s16[0]) + asf(s16[1]);
      const auto s32 = __builtin_amdgcn_permlane32_swap(asu(t), asu(t), false, false);
      t = (asf(s32[0]) + asf(s32[1])) * (1.f / DM);
      const float e1 = dpp<0xA0>(t);
      sval = (p4 & 1) ? __builtin_fmaxf(__builtin_fmaf(-e1, e1, t), 0.f) + 1e-5f : t + (p4 ? piv[1] : piv[0]);
      PIN(sval);
    };
    const unsigned acnt = lds0 + L_CNT;
    const unsigned vpos = 2048 + (g & 1) * 512 + h * 256 + (lane & 15) * 16;
    v4u qf[2], am0, am1;
    {
      v4u xf[32];
      f32x16 acc[3] = {};
      f32x4 cs[3] = {};
      v4u wf[4][3];
      unsigned wb[4];
#pragma unroll
      for (int p = 0; p < 4; ++p) wb[p] = 16384 + ((p + m) & 3) * 1024 + (8 * h + q4) * 64 + (g & 1) * 32 + p4 * 8;
      auto rd = [&](int ks, int t) {
        const unsigned buf = lds0 + ((ks >> 2) % 3) * STG;
        if (t == 0) xf[ks] = lds128(buf + xo[ks & 3]);
        wf[ks & 3][t] = ldtr2(buf + wb[ks & 3] + t * 4096, 256);
      };
      auto mm = [&](int ks, int t) {
        acc[t] = t == 2 ? MFMA32(xf[ks], wf[ks & 3][t], acc[t]) : MFMA32(wf[ks & 3][t], xf[ks], acc[t]);
        if ((ks & 3) == 0) { cs[t] = MFMA16(sel, wf[ks & 3][t], cs[t]); PIN(cs[t]); }
        SBAR();
      };
      make_stats();
      bar();
      STAMP_AT(10);
#pragma unroll
      for (int t = 0; t < 6; ++t) rd(t / 3, t % 3);
      SBAR();
      static_for<0, 8>([&](auto sc) {
        constexpr int s = sc.value;
#pragma unroll
        for (int p = 0; p < 2; ++p) {
#pragma unroll
          for (int t = 0; t < 3; ++t) { mm(4 * s + p, t); rd(4 * s + p + 2, t); SBAR(); }
          if (s == 7 && p == 0) { cswr(4, cs[0]); cswr(5, cs[1]); cswr(6, cs[2]); SBAR(); }
        }
        if constexpr (s == 0) {
          wait_vmf<0>(ep);
          if (m == 0 && lane < 10)
            *(LAS unsigned*)(size_t)(lds0 + L_CNT + lane * 4) = lane == 8 ? ep[0] * 0x9E3779B1u + 0x7F4A7C15u : lane == 9 ? ep[1] * 0x85EBCA77u + 0x165667B1u : 0u;
        }
        if constexpr (s == 1) {
          get_tag();
          if ((lane >> 2) == 3) {
            const v2u gr = {asu(sval), tag32};
            __builtin_amdgcn_raw_buffer_store_b64(gr, rws, WS_STATS + (128 * r + 8 * j + 2 * m) * 16 + p4 * 8, 0, 16);
          }
        }
        if constexpr (s == MIDWARM) {
          warm = ld2_async(make_srd((const void*)wbase, 4096), lane * 128, 0);
        }
        if constexpr (s == 5) stat_load();
        if constexpr (s == 6) {
          wait_vm1<0>(st);
          PIN(warm);

          row_stats();
        }
        mm(4 * s + 2, 0); mm(4 * s + 2, 1);
        bar();
        if constexpr (s < 7) {
          STAMP_AT(11 + s);
          mm(4 * s + 2, 2);
          rd(4 * s + 4, 0); rd(4 * s + 4, 1); rd(4 * s + 4, 2); SBAR();
#pragma unroll
          for (int t = 0; t < 3; ++t) { mm(4 * s + 3, t); rd(4 * s + 5, t); SBAR(); }
        }
      });
      STAMP_AT(1);
      __builtin_amdgcn_s_setprio(3);
      const f32x4 c5 = csrd(5), c6 = csrd(6), c4 = csrd(4);
      SBAR();
      mm(30, 2); mm(31, 1);
      make_xaug();
      SBAR();
      mm(31, 2); mm(31, 0);
      if (h == 0) *(LAS float*)(size_t)(lds0 + L_RS + (m * 32 + r32) * 4) = rstd;
      acc[1] = MFMA32(waug(c5, 0u), xaug, acc[1]);
      acc[2] = MFMA32(xaug, waug(c6, 0u), acc[2]);
      acc[0] = MFMA32(waug(c4, 0u), xaug, acc[0]);
      SBAR();
      f32x4 rs[4];
#pragma unroll
      for (int qd = 0; qd < 4; ++qd) rs[qd] = __builtin_bit_cast(f32x4, lds128(lds0 + L_RS + (m * 32 + 8 * qd + 4 * h) * 4));
      const unsigned kvo = WS_KV + (j * 64 + T) * 4096;
#pragma unroll
      for (int e = 0; e < 16; ++e) acc[1][e] = MUL1(acc[1][e], rstd);
#pragma unroll
      for (int e = 0; e < 2; ++e) {
        const v4u f = pack8(acc[1], e);
        st16_sc1(f, sws, lane * 16, kvo + e * 1024);
        sts128(lds0 + L_KV + (8 + m) * KVT + e * 1024 + lane * 16, f);
      }
#pragma unroll
      for (int qd = 0; qd < 4; ++qd)
#pragma unroll
        for (int e = 0; e < 4; ++e) acc[2][4 * qd + e] = MUL1(acc[2][4 * qd + e], rs[qd][e]);
#pragma unroll
      for (int s = 0; s < 2; ++s) {
        const v4u f = pack8(acc[2], s);
        st16_sc1(f, sws, lane * 16, kvo + 2048 + s * 1024);
        sts128(lds0 + L_KV + (8 + m) * KVT + s * VB + vpos, f);
      }
      SBAR();
      st16_sc1((v4u){~0u, ~0u, ~0u, ~0u}, sws, lane * 16, WS_AF + (T * AFT + 112 + 2 * j + 1) * 1024);
      const float qs = rstd * (0.25f * 1.4426950408889634f);
#pragma unroll
      for (int e = 0; e < 16; ++e) acc[0][e] = MUL1(acc[0][e], qs);
      qf[0] = pack8(acc[0], 0); qf[1] = pack8(acc[0], 1);
      PIN(qf[0]); PIN(qf[1]);
      STAMP_AT(2);
      const int T0 = 4 * r - 8 + 2 * m;
      const bool live = T0 >= 0;
      auto kv_load = [&](v4u (&kv)[8], int i0, int i1) {
        const unsigned src = WS_KV + (j * 64 + (live ? T0 : 0)) * 4096, vo = live ? lane * 16 : h * 512;
#pragma unroll
        for (int i = i0; i < i1; ++i) kv[i] = ld16_sc1_async(sws, vo, src + (live ? i : (i & 3)) * 1024);
      };
      auto kv_stage = [&](v4u (&kv)[8]) {
        const unsigned on = (lane < 2 || live) ? one2 : 0u;
        if (lane < 6) sts128(lds0 + L_KV + (lane < 2 ? 8 + m : 2 * m + ((lane - 2) >> 1)) * KVT + 2048 + (lane & 1) * VB + 1024, (v4u){on, on, on, on});
        const v4u zz = {};
#pragma unroll
        for (int i = 0; i < 2; ++i) {
          sts128(lds0 + L_KV + (2 * m + i) * KVT + lane * 16, kv[4 * i]);
          sts128(lds0 + L_KV + (2 * m + i) * KVT + 1024 + lane * 16, kv[4 * i + 1]);
          sts128(lds0 + L_KV + (2 * m + i) * KVT + vpos, live ? kv[4 * i + 2] : zz);
          sts128(lds0 + L_KV + (2 * m + i) * KVT + VB + vpos, live ? kv[4 * i + 3] : zz);
        }
        arrive_nowait(acnt);
      };
      auto kv_wait = [&](v4u (&kv)[8]) {
        wait_vm<0>();
#pragma unroll
        for (int i = 0; i < 8; ++i) PIN(kv[i]);
      };
      if (heavy) {
        bias_load(1);
        hsetup(1);
        await(acnt + 16, 4);
        STAMP_AT(3);
        v2u fl;
        v4u kv[8];
        hmm(std::integral_constant<int, 1>{}, xf, [&](auto kc) {
          constexpr int ks = kc.value;
          if constexpr (ks == KVPUB) {
            wait_vm<0>();
            PIN(bias0); PIN(bias1);
            if (lane == 0) __hip_atomic_store(flag(FL_KV, T, j), tag, RLX_AGENT);
          }
          if constexpr (ks == 24) fl = ld8_sc1_async(sws, flag_off(FL_KV, live ? T0 + (lane & 1) : 0, j));
        });
        STAMP_AT(18);
        wait_vmf<0>(fl);
        if (!__all(fl[0] == taglo && fl[1] == tag32)) poll_flags(flag(FL_KV, live ? T0 + (lane & 1) : 0, j), tag);
        kv_load(kv, 0, 8);
        hgelu(std::integral_constant<int, 1>{}, [&]() {});
        STAMP_AT(4);
        wait_vm<4>();
#pragma unroll
        for (int i = 0; i < 8; ++i) PIN(kv[i]);
        kv_stage(kv);
      } else {
        wait_vm<0>();
        if (lane == 0) __hip_atomic_store(flag(FL_KV, T, j), tag, RLX_AGENT);
        v4u kv[8];
        poll_flags(flag(FL_KV, live ? T0 + (lane & 1) : 0, j), tag);
        kv_load(kv, 0, 8); kv_wait(kv); kv_stage(kv);
      }
    }
    STAMP_AT(19);
    const unsigned kb = lds0 + L_KV + m * KVT + lane * 16;
    unsigned vb[2];
#pragma unroll
    for (int e = 0; e < 2; ++e) { vb[e] = lds0 + L_KV + m * KVT + 2048 + ((g & 1) ? 1024 : e * 512 + h * 256 + (lane & 15) * 16); PIN(vb[e]); }
    {
      const int dq = r32 - 4 * h;
#pragma unroll
      for (int d = 0; d < 8; ++d) { const int row = 2 * (d & 1) + 8 * (d >> 1); (d < 4 ? am0 : am1)[d & 3] = row > dq ? 0xffffffffu : row == dq ? 0xffff0000u : 0u; }
      PIN(am0); PIN(am1);
    }
    await(acnt, heavy ? 8 : 4);
    __builtin_amdgcn_s_setprio(PRIO_ATT);
    STAMP_AT(5);
    const f32x16 zero = {};
    auto o_store = [&](f32x16& o, int e) {
      const float rl = __builtin_amdgcn_rcpf(o[8]);
#pragma unroll
      for (int u = 0; u < 8; ++u) o[u] = MUL1(o[u], rl);
      st16_sc1(pack8(o, 0), sws, lane * 16, WS_AF + (T * AFT + 112 + 2 * j + e) * 1024);
    };
    int redo = 0;
    {
      f32x16 o[2], sa, sb, su;
      bool pend = false;
      v4u kn, ku, va, vc, vn0, vn1, vu0, vu1, p0, p1, pu;
      auto kaddr = [&](int n) { return kb + ((n & 7) + 1) * KVT + (n >> 3) * 1024; };
      auto vaddr = [&](int n) { return vb[n >> 3] + ((n & 7) + 1) * KVT; };
      kn = lds128(kaddr(0)); vn0 = lds128(vaddr(0)); vn1 = lds128(vaddr(0) + VB);
      sa = MFMA32(kn, qf[0], zero);
      kn = lds128(kaddr(1));
      SBAR();
      auto step = [&](auto nc, f32x16& sc, f32x16& sn) {
        constexpr int n = nc.value, e = n >> 3, i = n & 7;
        if constexpr (n < 15) sn = MFMA32(kn, qf[(n + 1) >> 3], zero);
        if constexpr (n < 14) kn = lds128(kaddr(n + 2));
        if constexpr (i == 5) ku = lds128(kb + e * 1024);
        v4u v0 = vn0, v1 = vn1;
        if constexpr (n < 15) { vn0 = lds128(vaddr(n + 1)); vn1 = lds128(vaddr(n + 1) + VB); }
        if constexpr (i == 6) { vu0 = lds128(vb[e]); vu1 = lds128(vb[e] + VB); }
        SBAR();
        if constexpr (i == 7) {
          static_for<0, 16>([&](auto u) { sc[u.value] = upper<u.value>() ? su[u.value] : sc[u.value]; });
          PIN(sc); SBAR();
        }
#pragma unroll
        for (int u = 0; u < 6; ++u) sc[u] = EXP2(sc[u]);
        PIN(sc); SBAR();
        if constexpr (n == 8) { o[0] = MFMA32(vc, p1, o[0]); SBAR(); }
        else if constexpr (n > 0) { o[e] = MFMA32(vc, p1, o[e]); SBAR(); }
        sc[6] = EXP2(sc[6]); sc[7] = EXP2(sc[7]);
        p0 = pack8(sc, 0);
        sc[8] = EXP2(sc[8]); sc[9] = EXP2(sc[9]);
        if constexpr (i == 7) { va = p0 & am0; p0 &= ~am0; PIN(va); }
        PIN(sc); PIN(p0); SBAR();
        if constexpr (n == 8) { o[0] = MFMA32(vu1, pu, o[0]); SBAR(); }
        o[e] = MFMA32(v0, p0, i == 0 ? zero : o[e]);
        SBAR();
        if constexpr (i == 6) { su = MFMA32(ku, qf[e], zero); SBAR(); }
#pragma unroll
        for (int u = 10; u < 13; ++u) sc[u] = EXP2(sc[u]);
        if constexpr (i == 7) { PIN(sc); SBAR(); o[e] = MFMA32(vu0, va, o[e]); SBAR(); }
#pragma unroll
        for (int u = 13; u < 16; ++u) sc[u] = EXP2(sc[u]);
        p1 = pack8(sc, 1); vc = v1;
        if constexpr (i == 7) { pu = p1 & am1; p1 &= ~am1; PIN(pu); }
        PIN(p1); SBAR();
        if constexpr (n == 8) {
          if (__all(o[0][8] >= 0x1p-30f && o[0][8] <= 0x1p30f)) o_store(o[0], 0); else redo = 1;
          SBAR();
        }
        if constexpr (i == 2) pend = !redo;
        if constexpr (i >= 2) {
          if (pend && (i == 7 || (__builtin_amdgcn_s_getreg(7 | (3 << 11)) & 15) == 0)) {
            wait_vm<0>();
            if (lane == 0) __hip_atomic_store(flag(FL_H1 + e, T, j), tag, RLX_AGENT);
            pend = false;
          }
        }
      };
      static_for<0, 8>([&](auto hc) {
        step(std::integral_constant<int, 2 * hc.value>{}, sa, sb);
        step(std::integral_constant<int, 2 * hc.value + 1>{}, sb, sa);
      });
      o[1] = MFMA32(vc, p1, o[1]);
      o[1] = MFMA32(vu1, pu, o[1]);
      if (__all(o[1][8] >= 0x1p-30f && o[1][8] <= 0x1p30f)) o_store(o[1], 1); else redo |= 2;
    }
    if (redo) {
#pragma unroll 1
      for (int e = 0; e < 2; ++e) {
        if (!((redo >> e) & 1)) continue;
        const v4u q = e ? qf[1] : qf[0];
        const unsigned vbe = e ? vb[1] : vb[0];
        auto merged = [&](const f32x16& c) {
          f32x16 sl = MFMA32(lds128(kb + 8 * KVT + e * 1024), q, c);
          const f32x16 su = MFMA32(lds128(kb + e * 1024), q, c);
          static_for<0, 16>([&](auto u) { sl[u.value] = upper<u.value>() ? su[u.value] : sl[u.value]; });
          return sl;
        };
        float mx = -INFINITY;
#pragma unroll 1
        for (int i = 0; i < 8; ++i) {
          const f32x16 sc = i ? MFMA32(lds128(kb + i * KVT + e * 1024), q, zero) : merged(zero);
#pragma unroll
          for (int u = 0; u < 16; u += 2) mx = max3(mx, sc[u], sc[u + 1]);
        }
        const auto sw = __builtin_amdgcn_permlane32_swap(asu(mx), asu(mx), false, false);
        mx = __builtin_fmaxf(asf(sw[0]), asf(sw[1]));
        f32x16 negm, o;
#pragma unroll
        for (int u = 0; u < 16; ++u) negm[u] = -mx;
        {
          f32x16 sc = merged(negm);
#pragma unroll
          for (int u = 0; u < 16; ++u) sc[u] = EXP2(sc[u]);
          const v4u p0 = pack8(sc, 0), p1 = pack8(sc, 1);
          o = MFMA32(lds128(vbe), p0 & am0, zero);
          o = MFMA32(lds128(vbe + VB), p1 & am1, o);
          o = MFMA32(lds128(vbe + 8 * KVT), p0 & ~am0, o);
          o = MFMA32(lds128(vbe + 8 * KVT + VB), p1 & ~am1, o);
        }
#pragma unroll 1
        for (int i = 1; i < 8; ++i) {
          f32x16 sc = MFMA32(lds128(kb + i * KVT + e * 1024), q, negm);
#pragma unroll
          for (int u = 0; u < 16; ++u) sc[u] = EXP2(sc[u]);
          o = MFMA32(lds128(vbe + i * KVT), pack8(sc, 0), o);
          o = MFMA32(lds128(vbe + i * KVT + VB), pack8(sc, 1), o);
        }
        o_store(o, e);
      }
      wait_vm<0>();
      if (lane == 0) __hip_atomic_store(flag(FL_O0, T, j), tag, RLX_AGENT);
    }
    STAMP_AT(6);
    wait_vm<0>();
    if (lane == 0) __hip_atomic_store(flag(FL_O1, T, j), tag, RLX_AGENT);
    STAMP_AT(7);
    res_loads();
    wait_vm2<0>(xres, bres);
    if (blockIdx.x == 0 && wave == 7) {
#pragma unroll 1
      for (int i = 0; i < 4; ++i) poll_flags(flag(FL_KV, 4 * (4 * i + g), lane & 15), tag);
      if (lane == 0) __hip_atomic_store((GAS u64*)(ws + WS_EPOCH), tag, RLX_AGENT);
    }
  }
  bar();
  {
    int t2 = threadIdx.x; PIN(t2);
    const int row = t2 >> 3, ch = t2 & 7, orow = 64 * rt + row, ocol = 64 * ct + ch * 8;
    const unsigned a = lds0 + L_WG + (((row >> 5) * 8 + ch) * 66 + (row & 31)) * 16;
    f32x4 lo = {}, hi = {};
#pragma unroll
    for (int w = 0; w < 4; ++w) {
      lo += __builtin_bit_cast(f32x4, lds128(a + w * PART));
      hi += __builtin_bit_cast(f32x4, lds128(a + w * PART + 512));
    }
    v4u o;
    o[0] = pk2(lo[0] + bflo(xres[0]) + bflo(bres[0]), lo[1] + bfhi(xres[0]) + bfhi(bres[0]));
    o[1] = pk2(lo[2] + bflo(xres[1]) + bflo(bres[1]), lo[3] + bfhi(xres[1]) + bfhi(bres[1]));
    o[2] = pk2(hi[0] + bflo(xres[2]) + bflo(bres[2]), hi[1] + bfhi(xres[2]) + bfhi(bres[2]));
    o[3] = pk2(hi[2] + bflo(xres[3]) + bflo(bres[3]), hi[3] + bfhi(xres[3]) + bfhi(bres[3]));
    *(GAS v4u*)((char*)out + (size_t)orow * 1024 + ocol * 2) = o;
  }
  STAMP_AT(8);
}

extern "C" void kernel_launch(void* const* d_in, const int* in_sizes, int n_in, void* d_out, int out_size, void* d_ws, size_t ws_size,
                              hipStream_t stream) {
  (void)in_sizes; (void)n_in; (void)out_size; (void)ws_size;
  (void)hipFuncSetAttribute((const void*)fused_block, hipFuncAttributeMaxDynamicSharedMemorySize, LDS_BYTES);
  fused_block<<<256, 512, LDS_BYTES, stream>>>(d_in[0], d_in[1], d_in[2], d_in[4], d_in[5], d_in[6], d_in[7], d_out, (char*)d_ws);
}
```

```cpp
#include <hip/hip_runtime.h>
#include <hip/hip_bf16.h>
#include <stdint.h>
#include <type_traits>

typedef unsigned v4u __attribute__((ext_vector_type(4)));
typedef unsigned v2u __attribute__((ext_vector_type(2)));
typedef float f32x2 __attribute__((ext_vector_type(2)));
typedef float f32x4 __attribute__((ext_vector_type(4)));
typedef float f32x16 __attribute__((ext_vector_type(16)));
typedef short bf16x8 __attribute__((ext_vector_type(8)));
typedef short s16x4 __attribute__((ext_vector_type(4)));
typedef __bf16 bf16x2_t __attribute__((ext_vector_type(2)));
typedef unsigned long long u64;
#define GAS __attribute__((address_space(1)))
#define LAS __attribute__((address_space(3)))
#define DI __device__ __forceinline__
#define RLX_AGENT __ATOMIC_RELAXED, __HIP_MEMORY_SCOPE_AGENT

constexpr int SEQ = 2048, DM = 512, FF = 1792;
constexpr unsigned WS_EPOCH = 0;
constexpr unsigned WS_STATS = 32768;
constexpr unsigned WS_FLAG = 65536;
constexpr unsigned WS_KV = WS_FLAG + 5 * 1024 * 128;
constexpr int AFT = 145;
constexpr unsigned WS_AF = WS_KV + 16 * 64 * 4096;
constexpr unsigned WS_END = WS_AF + 64 * AFT * 1024;
constexpr unsigned WS_STAMP = WS_END;
enum { FL_KV = 0, FL_H0 = 1, FL_H1 = 2, FL_O0 = 3, FL_O1 = 4 };
constexpr int STG = 36864;
constexpr int L_WA = 0;
constexpr int L_WG = 86016;
constexpr int VB = 1040;
constexpr int KVT = 2048 + 2 * VB;
constexpr int L_KV = 32768;
constexpr int RD = 8, NR = 12;
constexpr int PART = 16 * 66 * 16;
constexpr int L_CS = L_WG + 4 * PART;
constexpr int L_RS = L_CS + 3584;
constexpr int L_CNT = L_RS + 512;
constexpr int LDS_BYTES = L_CNT + 64;
static_assert(L_KV + 8 * KVT >= 65536 && L_KV + 12 * KVT <= L_WG && L_WG >= 2 * STG && L_CS >= 3 * STG && PART >= RD * 2048 && LDS_BYTES <= 163840, "lds map");
#define AWIN 6
constexpr int AW = AWIN;
constexpr unsigned SPIN_CAP = 1u << 23;

#define MFMA32(a, b, c) __builtin_amdgcn_mfma_f32_32x32x16_bf16(__builtin_bit_cast(bf16x8, a), __builtin_bit_cast(bf16x8, b), c, 0, 0, 0)
#define MFMA16(a, b, c) __builtin_amdgcn_mfma_f32_16x16x32_bf16(__builtin_bit_cast(bf16x8, a), __builtin_bit_cast(bf16x8, b), c, 0, 0, 0)
#define SBAR() __builtin_amdgcn_sched_barrier(0)

#define STAMP_AT(i) do { } while (0)

DI float asf(unsigned u) { return __builtin_bit_cast(float, u); }
DI unsigned asu(float f) { return __builtin_bit_cast(unsigned, f); }
DI unsigned pk2(float lo, float hi) { return __builtin_bit_cast(unsigned, __builtin_convertvector((f32x2){lo, hi}, bf16x2_t)); }
DI float bflo(unsigned u) { return __builtin_bit_cast(float, u << 16); }
DI float bfhi(unsigned u) { return __builtin_bit_cast(float, u & 0xffff0000u); }
DI v4u make_srd(const void* p, unsigned bytes) { const u64 a = (u64)p; return (v4u){(unsigned)a, (unsigned)(a >> 32) & 0xffffu, bytes, 0x00020000u}; }
DI void dma16(v4u srd, unsigned voff, unsigned soff, unsigned lds) {
  asm volatile("s_mov_b32 m0, %3\n\ts_nop 0\n\tbuffer_load_dwordx4 %0, %1, %2 offen lds" :: "v"(voff), "s"(srd), "s"(soff), "s"(lds) : "memory");
}
DI void dma16_a(v4u srd, unsigned voff, unsigned soff, unsigned lds) { dma16(srd, voff, soff, lds); }
DI void dma16_b(v4u srd, unsigned voff1, unsigned soff) {
  asm volatile("buffer_load_dwordx4 %0, %1, %2 offen offset:1024 lds" :: "v"(voff1), "s"(srd), "s"(soff) : "memory");
}
DI v4u ld16_sc1_async(v4u srd, unsigned voff, unsigned soff) {
  v4u r; asm volatile("buffer_load_dwordx4 %0, %1, %2, %3 offen sc1" : "=v"(r) : "v"(voff), "s"(srd), "s"(soff) : "memory"); return r;
}
DI v4u ld16_async(v4u srd, unsigned voff, unsigned soff) {
  v4u r; asm volatile("buffer_load_dwordx4 %0, %1, %2, %3 offen" : "=v"(r) : "v"(voff), "s"(srd), "s"(soff) : "memory"); return r;
}
DI unsigned ld2_async(v4u srd, unsigned voff, unsigned soff) {
  unsigned r; asm volatile("buffer_load_ushort %0, %1, %2, %3 offen" : "=v"(r) : "v"(voff), "s"(srd), "s"(soff) : "memory"); return r;
}
DI void st16_sc1(v4u v, v4u srd, unsigned voff, unsigned soff) {
  asm volatile("buffer_store_dwordx4 %0, %1, %2, %3 offen sc1\n\ts_nop 1" :: "v"(v), "v"(voff), "s"(srd), "s"(soff) : "memory");
}
template <int N> DI void wait_vm1(v4u& a) { asm volatile("s_waitcnt vmcnt(%1)" : "+v"(a) : "i"(N) : "memory"); }
template <int N> DI void wait_vm2(v4u& a, v4u& b) { asm volatile("s_waitcnt vmcnt(%2)" : "+v"(a), "+v"(b) : "i"(N) : "memory"); }
template <int N> DI void wait_vm3(v4u& a, v4u& b, v4u& c) { asm volatile("s_waitcnt vmcnt(%3)" : "+v"(a), "+v"(b), "+v"(c) : "i"(N) : "memory"); }
DI void pin_s(u64& v) { asm volatile("" : "+s"(v)); }
template <class V> DI void pin(V& v) { asm volatile("" : "+v"(v)); }
template <int N> DI void wait_vm() { asm volatile("s_waitcnt vmcnt(%0)" :: "i"(N) : "memory"); }
DI void bar() { asm volatile("s_waitcnt lgkmcnt(0)\n\ts_barrier" ::: "memory"); }
DI v4u lds128(unsigned a) { return *(const LAS v4u*)(size_t)a; }
DI void sts128(unsigned a, v4u v) { *(LAS v4u*)(size_t)a = v; }
DI v2u ldtr(unsigned a) { return __builtin_bit_cast(v2u, __builtin_amdgcn_ds_read_tr16_b64_v4i16((LAS s16x4*)(size_t)a)); }
DI v4u ldtr2(unsigned a, int second) { const v2u lo = ldtr(a), hi = ldtr(a + second); return (v4u){lo.x, lo.y, hi.x, hi.y}; }
template <int CTRL> DI float dpp(float v) { return __builtin_bit_cast(float, __builtin_amdgcn_update_dpp(0, __builtin_bit_cast(int, v), CTRL, 0xf, 0xf, true)); }
DI float rdl(float v, int l) { return __builtin_bit_cast(float, __builtin_amdgcn_readlane(__builtin_bit_cast(int, v), l)); }
DI v4u pack8(const f32x16& a, int s) { return (v4u){pk2(a[8 * s], a[8 * s + 1]), pk2(a[8 * s + 2], a[8 * s + 3]), pk2(a[8 * s + 4], a[8 * s + 5]), pk2(a[8 * s + 6], a[8 * s + 7])}; }
DI void poll_flags(const GAS u64* p, u64 tag) {
#pragma unroll 1
  for (unsigned spin = 0; spin < SPIN_CAP; ++spin) {
    const u64 v = __hip_atomic_load(p, RLX_AGENT);
    if (__all(v == tag)) break;
    __builtin_amdgcn_s_sleep(2);
  }
  asm volatile("" ::: "memory");
}
constexpr u64 upper_mask(int reg) {
  u64 m = 0;
  for (int l = 0; l < 64; ++l) { const int row = (reg & 3) + 8 * (reg >> 2) + 4 * (l >> 5); if (row > (l & 31)) m |= 1ull << l; }
  return m;
}
template <int REG> DI bool upper() { return __builtin_amdgcn_inverse_ballot_w64(upper_mask(REG)); }
template <int I, int N, class Fn> DI void static_for(Fn&& f) { if constexpr (I < N) { f(std::integral_constant<int, I>{}); static_for<I + 1, N>(f); } }
DI float max3(float a, float b, float c) { return __builtin_fmaxf(__builtin_fmaxf(a, b), c); }

constexpr int NX = 4;
constexpr bool isx(int kk) { return kk >= NR + RD && kk < NR + RD + NX; }
constexpr int ringpos(int kk) { return (kk < NR + RD ? kk : kk - NX) % RD; }
constexpr int refill_of(int kk) { const int n = isx(kk) ? 99 : kk < NR + RD ? kk + RD + NX : kk + RD; return n < 36 ? n : -1; }
#define H1CK 14
constexpr int H1_CK = H1CK, H1_LD = H1_CK - 3, NB = 4, O_CK = 23, O_LD = O_CK - 3;
constexpr int w_read(int t) { return t < RD ? t + NR : (t >= 15 && t < 23) ? t + 5 : t == 27 ? 28 : -1; }
constexpr int w_fill(int t) { return t >= 9 && t < 9 + NX ? NR + RD + t - 9 : t >= 1 && w_read(t - 1) >= 0 ? refill_of(w_read(t - 1)) : -1; }
constexpr int a_lo(int t) { return t >= H1_CK && 16 + NB + 2 * (t - H1_CK) < 28 ? 16 + NB + 2 * (t - H1_CK) : -1; }
constexpr int a_hi(int t) { return t + AW < 16 ? t + AW : a_lo(t) >= 0 ? a_lo(t) + 1 : -1; }
constexpr int g2_issued(int t) {
  return (w_fill(t) >= 0 ? 2 : 0) + (a_lo(t) >= 0 ? 2 : 0) + (a_hi(t) >= 0 ? 2 : 0) + (t == H1_LD) + (t == O_LD) + (t == O_CK ? 10 : 0) + (t == H1_CK ? 2 * NB : 0);
}
constexpr int g2_range(int a, int b) { int n = 0; for (int t = a; t < b; ++t) n += g2_issued(t); return n; }
constexpr int a_younger(int k) {
  if (k < AW) return 2 * (AW - 1 - k) + g2_range(0, k);
  if (k >= 16 && k < 16 + NB) return g2_range(H1_CK, k) - 2 * (k - 15);
  for (int t = 0; t < k; ++t) { if (a_hi(t) == k) return g2_range(t + 1, k); if (a_lo(t) == k) return 2 + g2_range(t + 1, k); }
  return 0;
}
constexpr bool sched_ok() { for (int t = 0; t < 28; ++t) if ((w_fill(t) >= 0 && a_lo(t) >= 0) || a_younger(t) > 63) return false; return true; }
static_assert(sched_ok() && H1_CK >= 9 + NX && H1_CK <= 15, "GEMM2 schedule");
#define GELU_PK(P) (P == 1)
#define PRIO_G2 2
#define PRIO_ATT 3
#define PRIO_G1 2
#define ROT r
#define CODEWARM 39000
#define MIDWARM 1
#define KVPUB 6
#define EXP2(v) __builtin_amdgcn_exp2f(v)
#define PIN(v) pin(v)
extern "C" __device__ float mul_unpacked(float, float) __asm("llvm.amdgcn.fmul.legacy");
#define MUL1(a, b) mul_unpacked(a, b)
#define FMA1(a, b, c) __builtin_amdgcn_div_fmasf(a, b, c, false)
DI v2u ld8_sc1_async(v4u srd, unsigned voff) {
  v2u r; asm volatile("buffer_load_dwordx2 %0, %1, %2, 0 offen sc1" : "=v"(r) : "v"(voff), "s"(srd) : "memory"); return r;
}
template <int N> DI void wait_vmf(v2u& a) { asm volatile("s_waitcnt vmcnt(%1)" : "+v"(a) : "i"(N) : "memory"); }
DI void arrive(unsigned cnt, int lane) {
  asm volatile("s_waitcnt lgkmcnt(0)" ::: "memory");
  if (lane == 0) __hip_atomic_fetch_add((LAS unsigned*)(size_t)cnt, 1u, __ATOMIC_RELAXED, __HIP_MEMORY_SCOPE_WORKGROUP);
}
DI void arrive_nowait(unsigned cnt) {
  asm volatile("s_mov_b64 exec, 1\n\tds_add_u32 %0, %1\n\ts_mov_b64 exec, -1" :: "v"(cnt), "v"(1u) : "memory");
}
DI unsigned peek(unsigned cnt) { return __hip_atomic_load((LAS unsigned*)(size_t)cnt, __ATOMIC_RELAXED, __HIP_MEMORY_SCOPE_WORKGROUP); }
DI void await(unsigned cnt, unsigned n) {
#pragma unroll 1
  for (unsigned spin = 0; spin < SPIN_CAP * 32; ++spin) {
    if (__builtin_amdgcn_readfirstlane(peek(cnt)) >= n) break;
    __builtin_amdgcn_s_sleep(1);
  }
  asm volatile("" ::: "memory");
}

__global__ __launch_bounds__(512, 2) void fused_block(const void* __restrict__ x, const void* __restrict__ wqkv, const void* __restrict__ wo,
                                                      const void* __restrict__ w1, const void* __restrict__ b1, const void* __restrict__ w2,
                                                      const void* __restrict__ b2, void* __restrict__ out, char* __restrict__ ws) {
  constexpr unsigned lds0 = 0;
  const int wave = __builtin_amdgcn_readfirstlane(threadIdx.x >> 6);
  const int m = wave & 3;
  const bool isG = wave < 4;
  const int xcd = blockIdx.x & 7, idx = blockIdx.x >> 3;
  const int r = (xcd >> 2) * 8 + (idx >> 2), j = (xcd & 3) * 4 + (idx & 3);
  const int rt = (xcd >> 1) * 8 + (idx >> 2), ct = (xcd & 1) * 4 + (idx & 3);
  int lane = threadIdx.x & 63, r32, h, g, q4, p4;
  STAMP_AT(0);
  asm volatile("" :: "s"(x), "s"(wqkv), "s"(wo), "s"(w1), "s"(b1), "s"(w2), "s"(b2), "s"(out), "s"(ws));

  const v4u sx = make_srd(x, SEQ * DM * 2), sws = make_srd(ws, WS_END);
  const __amdgpu_buffer_rsrc_t rws = __builtin_amdgcn_make_buffer_rsrc(ws, 0, WS_END, 0x00020000);
  const unsigned one2 = 0x3f803f80u;
  const int T = 4 * r + m;
  const int rot = ROT;
  auto flag_off = [&](int kind, int tile, int jj) { return WS_FLAG + ((kind * 64 + tile) * 16 + jj) * 128; };
  auto flag = [&](int kind, int tile, int jj) { return (GAS u64*)(ws + flag_off(kind, tile, jj)); };
  u64 tag;
  unsigned taglo, tag32;
  auto get_tag = [&]() {
    const v2u tg = *(const LAS v2u*)(size_t)(lds0 + L_CNT + 32);
    taglo = __builtin_amdgcn_readfirstlane(tg[0]); tag32 = __builtin_amdgcn_readfirstlane(tg[1]);
    tag = ((u64)tag32 << 32) | taglo;
  };
  v4u xres, bres, xaug, st;
  float mean, var, rstd, sd;
  v4u sel;
  unsigned xo[4], swz;
  auto lane_setup = [&]() {
    lane = threadIdx.x & 63; PIN(lane);
    r32 = lane & 31; h = lane >> 5; g = lane >> 4; q4 = (lane >> 2) & 3; p4 = lane & 3;
    const unsigned selv = ((lane & 15) == (g & 1)) ? one2 : 0u;
    sel = (v4u){selv, selv, selv, selv};
#pragma unroll
    for (int p = 0; p < 4; ++p) xo[p] = (32 * m + r32) * 128 + (((2 * ((p + m) & 3) + h) ^ ((r32 >> 1) & 7)) * 16);
    swz = ((((lane >> 2) & 1) ^ (g & 1)) * 64) + p4 * 16;
  };

  auto stat_load = [&]() { st = ld16_sc1_async(sws, WS_STATS + (128 * r + 32 * m + r32) * 16, 0); };
  auto res_loads = [&]() {
    int t2 = threadIdx.x; PIN(t2);
    const int row = 64 * rt + (t2 >> 3), col = 64 * ct + (t2 & 7) * 8;
    xres = ld16_async(sx, row * 1024 + col * 2, 0);
    bres = ld16_async(make_srd(b2, DM * 2), col * 2, 0);
  };
  auto row_stats = [&]() {
    if (!__all(st[1] == tag32 && st[3] == tag32)) {
#pragma unroll 1
      for (unsigned spin = 0; spin < SPIN_CAP; ++spin) {
        st = ld16_sc1_async(sws, WS_STATS + (128 * r + 32 * m + r32) * 16, 0);
        wait_vm1<0>(st);
        if (__all(st[1] == tag32 && st[3] == tag32)) break;
      }
    }
    mean = asf(st[0]); var = asf(st[2]);
    PIN(mean); PIN(var);
  };
  auto make_xaug = [&]() {
    PIN(mean); PIN(var);
    rstd = __builtin_amdgcn_rsqf(var); sd = var * rstd;
    const unsigned hi = pk2(-mean, sd);
    const unsigned lo = pk2(-mean - bflo(hi), FMA1(bfhi(hi), -1.f, sd));
    xaug = (v4u){(hi & 0xffffu) | (hi << 16), (lo & 0xffffu) | (hi & 0xffff0000u), lo >> 16, 0u};
    if (h) xaug = (v4u){0u, 0u, 0u, 0u};
    PIN(xaug);
  };
  auto cswr = [&](int tile, f32x4 c) {
    if (lane < 16) { LAS float* p = (LAS float*)(size_t)(lds0 + L_CS + (tile * 32 + lane) * 16 + m * 4); p[0] = c[0]; p[64] = c[1]; }
  };
  auto csrd = [&](int tile) { return __builtin_bit_cast(f32x4, lds128(lds0 + L_CS + (tile * 32 + r32) * 16)); };
  auto waug = [&](f32x4 c4, unsigned b16) {
    const float c = ((c4[0] + c4[1]) + c4[2]) + c4[3];
    const unsigned hi = pk2(c, 0.f);
    const unsigned lo = pk2(c - bflo(hi), 0.f);
    return (v4u){hi | (lo << 16), hi | (b16 << 16), b16, 0u};
  };

  const v4u sw1 = make_srd(w1, DM * FF * 2);
  const int hcol0 = 448 * (j >> 2) + 128 * (j & 3);
  const bool heavy = (j & 3) != 3;
  unsigned bias0, bias1;
  auto bias_load = [&](int P) {
    bias0 = ld2_async(make_srd(b1, FF * 2), r32 * 2, (hcol0 + 64 * P) * 2);
    bias1 = ld2_async(make_srd(b1, FF * 2), r32 * 2, (hcol0 + 64 * P + 32) * 2);
  };
  auto w1dma = [&](int P, int c, unsigned dst, int half) {
    const unsigned vw1 = (lane >> 3) * (FF * 2) + swz, vw1b = vw1 + 8 * (FF * 2) - 1024;
    const unsigned so = (64 * ((c + rot) & 7) + 16 * m) * (FF * 2) + (hcol0 + 64 * P) * 2;
    if (half == 0) dma16_a(sw1, vw1, so, dst + m * 2048); else dma16_b(sw1, vw1b, so);
  };
  f32x16 hac[2];
  unsigned rk[4][2];
  auto hsetup = [&](int P) {
#pragma unroll
    for (int p = 0; p < 4; ++p)
#pragma unroll
      for (int c = 0; c < 2; ++c) {
        rk[p][c] = lds0 + (P ? L_WA : 28672) + ((p + m) & 3) * 2048 + h * 1024 + q4 * 128 + ((c ^ (q4 >> 1)) * 64) + (g & 1) * 32 + p4 * 8;
        PIN(rk[p][c]);
      }
  };
  auto hmm = [&](auto pc, const v4u (&xf)[32], auto&& hook) {
    constexpr int P = pc.value;
    constexpr int D = 4;
    const unsigned cnt = lds0 + L_CNT;
    f32x4 cs[2] = {}, c0, c1;
    v4u wf[D][2], au0, au1;
    unsigned seen = 0;
    hac[0] = (f32x16){}; hac[1] = (f32x16){};
    auto rd = [&](int ks, int t) { wf[ks % D][t] = ldtr2(rk[ks & 3][t] + (ks >> 2) * 8192, 512); };
#pragma unroll
    for (int ks = 0; ks < D; ++ks) { rd(ks, 0); rd(ks, 1); }
    SBAR();
    static_for<0, 32>([&](auto kc) {
      constexpr int ks = kc.value;
      hook(kc);
      if constexpr (ks == 10) seen = peek(cnt + 20);
      if constexpr (ks == 29) seen = peek(cnt);
#pragma unroll
      for (int t = 0; t < 2; ++t) {
        hac[t] = MFMA32(wf[ks % D][t], xf[ks], hac[t]);
        if ((ks & 3) == 0 && ks < 28) { cs[t] = MFMA16(sel, wf[ks % D][t], cs[t]); PIN(cs[t]); }
        if (ks == 26) { cs[t] = MFMA16(sel, wf[28 % D][t], cs[t]); PIN(cs[t]); }
        SBAR();
        if constexpr (ks + D < 32) rd(ks + D, t);
        if constexpr (ks == 31) { if (t == 0) { au0 = waug(c0, bias0); PIN(au0); } else { au1 = waug(c1, bias1); PIN(au1); } }
        SBAR();
      }
      if constexpr (ks == 11 || ks == 30) {
        PIN(hac[0]); PIN(hac[1]);
        if (__builtin_amdgcn_readfirstlane(seen) < 4) await(ks == 11 ? cnt + 20 : cnt, 4);
        asm volatile("" ::: "memory");
        if constexpr (ks == 30) { c0 = csrd(2 * P); c1 = csrd(2 * P + 1); }
      }
      if constexpr (ks == 27) { cswr(2 * P, cs[0]); cswr(2 * P + 1, cs[1]); arrive_nowait(cnt); }
      SBAR();
    });
    hac[0] = MFMA32(au0, xaug, hac[0]);
    hac[1] = MFMA32(au1, xaug, hac[1]);
    SBAR();
  };
  auto hgelu = [&](auto pc, auto&& mid) {
    constexpr int P = pc.value;
    constexpr bool PK = GELU_PK(P);
    const float C0 = 2.3022081986f, C1 = C0 * 0.044715f;
    const float ga = -C1 * rstd * rstd * rstd, gb = -C0 * rstd;
    auto mul = [&](f32x2 a, f32x2 b) { return PK ? a * b : (f32x2){MUL1(a[0], b[0]), MUL1(a[1], b[1])}; };
    auto fma = [&](f32x2 a, float b, float c) {
      return PK ? __builtin_elementwise_fma(a, (f32x2){b, b}, (f32x2){c, c}) : (f32x2){FMA1(a[0], b, c), FMA1(a[1], b, c)}; };
#pragma unroll
    for (int t = 0; t < 2; ++t) {
      f32x2 v[8], z[8];
#pragma unroll
      for (int e = 0; e < 8; ++e) { v[e] = (f32x2){hac[t][2 * e], hac[t][2 * e + 1]}; z[e] = mul(v[e], v[e]); }
      SBAR();
#pragma unroll
      for (int e = 0; e < 8; ++e) z[e] = fma(z[e], ga, gb);
      SBAR();
#pragma unroll
      for (int e = 0; e < 8; ++e) z[e] = mul(z[e], v[e]);
      SBAR();
#pragma unroll
      for (int e = 0; e < 8; ++e) z[e] = (f32x2){EXP2(z[e][0]), EXP2(z[e][1])};
      SBAR();
#pragma unroll
      for (int e = 0; e < 8; ++e) z[e] = fma(z[e], sd, sd);
      SBAR();
#pragma unroll
      for (int e = 0; e < 8; ++e) z[e] = (f32x2){__builtin_amdgcn_rcpf(z[e][0]), __builtin_amdgcn_rcpf(z[e][1])};
      SBAR();
#pragma unroll
      for (int e = 0; e < 8; ++e) z[e] = mul(z[e], v[e]);
      SBAR();
#pragma unroll
      for (int s = 0; s < 2; ++s) {
        const v4u f = {pk2(z[4 * s][0], z[4 * s][1]), pk2(z[4 * s + 1][0], z[4 * s + 1][1]), pk2(z[4 * s + 2][0], z[4 * s + 2][1]), pk2(z[4 * s + 3][0], z[4 * s + 3][1])};
        st16_sc1(f, sws, lane * 16, WS_AF + (T * AFT + (hcol0 >> 4) + 4 * P + 2 * t + s) * 1024);
      }
      if (t == 0) mid();
      SBAR();
    }
  };

  if (isG) {
    lane_setup();
    const v4u sw2 = make_srd(w2, FF * DM * 2), swo = make_srd(wo, DM * DM * 2);
    const unsigned vw2 = (lane >> 3) * 1024 + swz, vw2b = vw2 + 7 * 1024;
    const int ro3 = rt % 3;
    auto kmap = [&](int kk) {
      if (kk < 16) return 8 * (((kk >> 2) + rt) & 3) + (kk & 3);
      const int p = ((kk - 16) >> 2) + ro3;
      return 8 * (p >= 3 ? p - 3 : p) + 4 + (kk & 3);
    };
    auto omap = [&](int i) { return 2 * ((i + rt) & 3) + (i >> 2); };
    auto wdmah = [&](int kk, int half) {
      const unsigned so = (kk < 28 ? 448 * m + 16 * kmap(kk) : 128 * m + 16 * omap(kk - 28)) * 1024 + 128 * ct;
      if (half == 0) dma16_a(kk < 28 ? sw2 : swo, vw2, so, lds0 + (isx(kk) ? L_WA + m * 8192 + (kk - NR - RD) * 2048 : L_WG + m * PART + ringpos(kk) * 2048)); else dma16_b(kk < 28 ? sw2 : swo, vw2b, so);
    };
    auto wdma = [&](int kk) { wdmah(kk, 0); wdmah(kk, 1); };
    {
      const v4u swq = make_srd(wqkv, DM * 3 * DM * 2);
      const unsigned vx0 = (lane >> 3) * 1024 + (((lane & 7) ^ g) * 16), vx1 = (7 + (lane >> 3)) * 1024 + (((lane & 7) ^ (4 + g)) * 16);
      const unsigned vwq = (lane >> 2) * (3 * DM * 2) + p4 * 16;
      auto piece = [&](int s, int i) {
        const unsigned buf = lds0 + (s % 3) * STG;
        const int kc = (s + rot) & 7;
        if (i < 4) {
          const unsigned so = (128 * r + 32 * m + 16 * (i >> 1)) * 1024 + kc * 128;
          if (i & 1) dma16_b(sx, vx1, so); else dma16_a(sx, vx0, so, buf + m * 4096 + (i >> 1) * 2048);
        } else if (i < 7) dma16(swq, vwq, (64 * kc + 16 * m) * (3 * DM * 2) + (512 * (i - 4) + 32 * j) * 2, buf + 16384 + ((i - 4) * 4 + m) * 1024);
        else w1dma(0, s, buf + 28672, i - 7);
      };
#pragma unroll
      for (int i = 0; i < 18; ++i) { piece(i / 9, i % 9); if (i == 8) __builtin_amdgcn_s_barrier(); }
      __builtin_amdgcn_s_setprio(PRIO_G1);
      hsetup(0);
      f32x4 cs[2] = {};
      v4u xf[4], wf[4][2];
      hac[0] = (f32x16){}; hac[1] = (f32x16){};
      static_for<0, 8>([&](auto sc) {
        constexpr int s = sc.value;
        constexpr unsigned buf = (s % 3) * STG;
        auto fill = [&](int i) {
          if constexpr (s < 6) piece(s + 2, i);
          if constexpr (s == 6) { if (i == 0) { stat_load(); bias_load(0); } else wdmah((i - 1) >> 1, (i - 1) & 1); }
          if constexpr (s == 7) { if (heavy) { w1dma(1, i, lds0 + L_WA + i * 8192, 0); w1dma(1, i, 0, 1); } }
          SBAR();
        };
        auto rd = [&](int p) { xf[p] = lds128(buf + xo[p]); wf[p][0] = ldtr2(buf + rk[p][0], 512); wf[p][1] = ldtr2(buf + rk[p][1], 512); SBAR(); };
        if constexpr (s < 7) wait_vm<9>(); else { wait_vm1<8>(st); PIN(bias0); PIN(bias1); }
        bar();
        STAMP_AT(10 + s);
        rd(0);
        if constexpr (s < 7) fill(0);
        rd(1);
        if constexpr (s < 7) fill(1);
        if constexpr (s == 2) get_tag();
#pragma unroll
        for (int p = 0; p < 4; ++p) {
#pragma unroll
          for (int t = 0; t < 2; ++t) {
            hac[t] = MFMA32(wf[p][t], xf[p], hac[t]);
            if (p == 0) { cs[t] = MFMA16(sel, wf[p][t], cs[t]); PIN(cs[t]); }
            SBAR();
            if (p < 2 && t == 0) rd(p + 2);
            if (s == 7) fill(2 * p + t); else if (2 * p + t < 7) fill(2 + 2 * p + t);
          }
          if (s == 7 && p == 1) {
            cswr(0, cs[0]); cswr(1, cs[1]);
            bar();
            STAMP_AT(1);
          }
        }
        if constexpr (s == 7) {
          const f32x4 c0 = csrd(0), c1 = csrd(1);
          row_stats(); make_xaug();
          hac[0] = MFMA32(waug(c0, bias0), xaug, hac[0]);
          hac[1] = MFMA32(waug(c1, bias1), xaug, hac[1]);
          SBAR();
        }
      });
    }
    STAMP_AT(18);
    if (heavy) { wait_vm<8>(); arrive_nowait(lds0 + L_CNT + 16); }
    hgelu(std::integral_constant<int, 0>{}, [&]() {
      wait_vm<2>();
      if (heavy) arrive_nowait(lds0 + L_CNT + 20);
    });
    __builtin_amdgcn_s_setprio(0);
    STAMP_AT(19);
    unsigned rb2[2];
#pragma unroll
    for (int c = 0; c < 2; ++c) rb2[c] = lds0 + L_WG + m * PART + h * 512 + q4 * 128 + ((c ^ (q4 >> 1)) * 64) + (g & 1) * 32 + p4 * 8;
    v4u wq[36][2];
    auto wrd = [&](int kk) {
#pragma unroll
      for (int c = 0; c < 2; ++c)
        wq[kk][c] = isx(kk) ? ldtr2(rb2[c] - (L_WG - L_WA) - m * (PART - 8192) + (kk - NR - RD) * 2048, 1024) : ldtr2(rb2[c] + ringpos(kk) * 2048, 1024);
    };
    auto wgot = [&](int kk) { PIN(wq[kk][0]); PIN(wq[kk][1]); };
    const int ftile = 2 * rt + ((lane >> 2) & 1);
    v4u af[28][2], of[8][2];
    v2u fl;
    const unsigned a0 = WS_AF + (2 * rt * AFT + 28 * m) * 1024, o0 = WS_AF + (2 * rt * AFT + 112 + 8 * m) * 1024;
    for (int kk = 0; kk < 4; ++kk) wrd(kk);
    for (int kk = 4; kk < 8; ++kk) wdma(kk);
    for (int kk = 0; kk < 4; ++kk) wgot(kk);
    wait_vm<8>();
    if (lane == 0) __hip_atomic_store(flag(FL_H0, T, j), tag, RLX_AGENT);
    STAMP_AT(2);
    for (int kk = 8; kk < 12; ++kk) wdma(kk);
    static_for<1, 3>([&](auto ic) {
      constexpr int i = ic.value;
      wait_vm<(i == 1) ? 9 : 8>();
      for (int kk = 4 * i; kk < 4 * i + 4; ++kk) wrd(kk);
      for (int kk = 4 * i; kk < 4 * i + 4; ++kk) wgot(kk);
      if constexpr (i == 2) fl = ld8_sc1_async(sws, flag_off(FL_H0, ftile, 4 * m + p4));
      for (int kk = 4 * i; kk < 4 * i + 4; ++kk) wdma(kk + RD);
    });
    STAMP_AT(3);
    wait_vmf<8>(fl);
    if (!__all(fl[0] == taglo && fl[1] == tag32)) poll_flags(flag(FL_H0, ftile, 4 * m + p4), tag);
    STAMP_AT(4);
#pragma unroll
    for (int kk = 0; kk < AW; ++kk) {
      af[kk][0] = ld16_sc1_async(sws, lane * 16, a0 + kmap(kk) * 1024);
      af[kk][1] = ld16_sc1_async(sws, lane * 16, a0 + (AFT + kmap(kk)) * 1024);
    }
    __builtin_amdgcn_s_setprio(PRIO_G2);
    f32x16 c2[2][2] = {};
    auto mm2 = [&](int kk, const v4u (&a)[2], auto&& fill) {
      SBAR();
#pragma unroll
      for (int i = 0; i < 4; ++i) { c2[i >> 1][i & 1] = MFMA32(wq[kk][i & 1], a[i >> 1], c2[i >> 1][i & 1]); SBAR(); fill(i); SBAR(); }
    };
    static_for<0, 28>([&](auto kc) {
      constexpr int kk = kc.value;
      wait_vm2<a_younger(kk)>(af[kk][0], af[kk][1]);
      if constexpr (kk == 9) { if (heavy) await(lds0 + L_CNT, 4); }
      if constexpr (kk == H1_CK) {
        STAMP_AT(20);
        wait_vmf<0>(fl);
        if (!__all(fl[0] == taglo && fl[1] == tag32)) poll_flags(flag(FL_H1, ftile, 4 * m + p4), tag);
        STAMP_AT(21);
#pragma unroll
        for (int i = 0; i < 2 * NB; ++i) af[16 + (i >> 1)][i & 1] = ld16_sc1_async(sws, lane * 16, a0 + (AFT * (i & 1) + kmap(16 + (i >> 1))) * 1024);
      }
      if constexpr (kk == H1_LD) fl = ld8_sc1_async(sws, flag_off(FL_H1, ftile, 4 * m + p4));
      if constexpr (kk == O_LD) fl = ld8_sc1_async(sws, flag_off(FL_O0, ftile, 4 * m + p4));
      if constexpr (w_fill(kk) >= 0 && !isx(w_fill(kk))) wgot(w_read(kk - 1));
      mm2(kk, af[kk], [&](int i) {
        if constexpr (w_fill(kk) >= 0) { if (i < 2) wdmah(w_fill(kk), i); }
        if constexpr (w_read(kk) >= 0) { if (i == 1) wrd(w_read(kk)); }
        if constexpr (a_lo(kk) >= 0) { if (i < 2) af[a_lo(kk)][i] = ld16_sc1_async(sws, lane * 16, a0 + (AFT * i + kmap(a_lo(kk))) * 1024); }
        if constexpr (a_hi(kk) >= 0) { if (i >= 2) af[a_hi(kk)][i - 2] = ld16_sc1_async(sws, lane * 16, a0 + (AFT * (i - 2) + kmap(a_hi(kk))) * 1024); }
      });
      if constexpr (kk == O_CK) {
        wait_vmf<g2_range(O_LD, O_CK + 1) - 11>(fl);
        if (!__all(fl[0] == taglo && fl[1] == tag32)) poll_flags(flag(FL_O0, ftile, 4 * m + p4), tag);
        res_loads();
#pragma unroll
        for (int i = 0; i < 4; ++i) {
          of[i][0] = ld16_sc1_async(sws, lane * 16, o0 + omap(i) * 1024);
          of[i][1] = ld16_sc1_async(sws, lane * 16, o0 + (AFT + omap(i)) * 1024);
        }
      }
    });
    static_for<0, 8>([&](auto kc) {
      constexpr int kk = kc.value;
      if (kk < 7) wrd(28 + kk + 1);
      if constexpr (kk == 1) {
        STAMP_AT(5);
        poll_flags(flag(FL_O1, ftile, 4 * m + p4), tag);
        STAMP_AT(6);
#pragma unroll
        for (int i = 4; i < 8; ++i) {
          of[i][0] = ld16_sc1_async(sws, lane * 16, o0 + omap(i) * 1024);
          of[i][1] = ld16_sc1_async(sws, lane * 16, o0 + (AFT + omap(i)) * 1024);
        }
      }
      wait_vm2<(kk == 0) ? 6 : 14 - 2 * kk>(of[kk][0], of[kk][1]);
      mm2(28 + kk, of[kk], [&](int) {});
    });
    PIN(xres); PIN(bres);
    asm volatile("" ::: "memory");
#pragma unroll
    for (int mt = 0; mt < 2; ++mt)
#pragma unroll
      for (int c = 0; c < 2; ++c)
#pragma unroll
        for (int qd = 0; qd < 4; ++qd) {
          const f32x4 v = {c2[mt][c][4 * qd], c2[mt][c][4 * qd + 1], c2[mt][c][4 * qd + 2], c2[mt][c][4 * qd + 3]};
          sts128(lds0 + L_WG + m * PART + ((mt * 8 + c * 4 + qd) * 66 + lane) * 16, __builtin_bit_cast(v4u, v));
        }
    STAMP_AT(7);
  } else {
    __builtin_amdgcn_s_setprio(1);
    lane_setup();
    v4u xr[2];
    xr[0] = ld16_async(sx, lane * 16, (128 * r + 8 * j + 2 * m) * 1024); xr[1] = ld16_async(sx, lane * 16, (128 * r + 8 * j + 2 * m + 1) * 1024);
    unsigned warm;
    {
      u64 pc; asm volatile("s_getpc_b64 %0" : "=s"(pc));
      warm = ld2_async(make_srd((const void*)(pc & ~127ull), CODEWARM), lane < 3 ? lane * 128 : 0x40000000u, (idx * 4 + m) * 384);
    }
    __builtin_amdgcn_s_barrier();
    v2u ep = ld8_sc1_async(sws, m == 0 ? WS_EPOCH : 0x40000000u);
    float sval;
    const int wi = blockIdx.x * 4 + m;
    const u64 wbase = wi < 448 ? (u64)w1 + wi * 4096 : wi < 896 ? (u64)w2 + (wi - 448) * 4096 : (u64)wo + (wi - 896) * 4096;
    auto make_stats = [&]() {
      wait_vm2<1>(xr[0], xr[1]);
      PIN(warm);
      float piv[2], a[4];
#pragma unroll
      for (int u = 0; u < 2; ++u) {
        piv[u] = rdl(bflo(xr[u][0]), 0);
        float s1 = 0.f, s2 = 0.f;
#pragma unroll
        for (int i = 0; i < 8; ++i) { const float d = ((i & 1) ? bfhi(xr[u][i >> 1]) : bflo(xr[u][i >> 1])) - piv[u]; s1 += d; s2 = __builtin_fmaf(d, d, s2); }
        a[2 * u] = s1; a[2 * u + 1] = s2;
      }
#pragma unroll
      for (int i = 0; i < 4; ++i) a[i] += dpp<0xB1>(a[i]);
#pragma unroll
      for (int i = 0; i < 4; ++i) a[i] += dpp<0x4E>(a[i]);
      float t = p4 == 0 ? a[0] : p4 == 1 ? a[1] : p4 == 2 ? a[2] : a[3];
      t += dpp<0x114>(t); t += dpp<0x118>(t);
      const auto s16 = __builtin_amdgcn_permlane16_swap(asu(t), asu(t), false, false);
      t = asf(s16[0]) + asf(s16[1]);
      const auto s32 = __builtin_amdgcn_permlane32_swap(asu(t), asu(t), false, false);
      t = (asf(s32[0]) + asf(s32[1])) * (1.f / DM);
      const float e1 = dpp<0xA0>(t);
      sval = (p4 & 1) ? __builtin_fmaxf(__builtin_fmaf(-e1, e1, t), 0.f) + 1e-5f : t + (p4 ? piv[1] : piv[0]);
      PIN(sval);
    };
    const unsigned acnt = lds0 + L_CNT;
    const unsigned vpos = 2048 + (g & 1) * 512 + h * 256 + (lane & 15) * 16;
    v4u qf[2], am0, am1;
    u64 um[16];
    auto masks = [&]() {
      const int dq = r32 - 4 * h;
#pragma unroll
      for (int d = 0; d < 4; ++d) {
        const int row = 2 * (d & 1) + 8 * (d >> 1);
        am0[d] = row > dq ? 0xffffffffu : row == dq ? 0xffff0000u : 0u;
        am1[d] = row + 16 > dq ? 0xffffffffu : row + 16 == dq ? 0xffff0000u : 0u;
      }
      PIN(am0); PIN(am1);
      static_for<0, 16>([&](auto u) { um[u.value] = upper_mask(u.value); pin_s(um[u.value]); });
    };
    {
      v4u xf[32];
      f32x16 acc[3] = {};
      f32x4 cs[3] = {};
      v4u wf[4][3];
      unsigned wb[4];
#pragma unroll
      for (int p = 0; p < 4; ++p) wb[p] = 16384 + ((p + m) & 3) * 1024 + (8 * h + q4) * 64 + (g & 1) * 32 + p4 * 8;
      auto rd = [&](int ks, int t) {
        const unsigned buf = lds0 + ((ks >> 2) % 3) * STG;
        if (t == 0) xf[ks] = lds128(buf + xo[ks & 3]);
        wf[ks & 3][t] = ldtr2(buf + wb[ks & 3] + t * 4096, 256);
      };
      auto mm = [&](int ks, int t) {
        acc[t] = t == 2 ? MFMA32(xf[ks], wf[ks & 3][t], acc[t]) : MFMA32(wf[ks & 3][t], xf[ks], acc[t]);
        if ((ks & 3) == 0) { cs[t] = MFMA16(sel, wf[ks & 3][t], cs[t]); PIN(cs[t]); }
        SBAR();
      };
      make_stats();
      bar();
      STAMP_AT(10);
#pragma unroll
      for (int t = 0; t < 6; ++t) rd(t / 3, t % 3);
      SBAR();
      static_for<0, 8>([&](auto sc) {
        constexpr int s = sc.value;
#pragma unroll
        for (int p = 0; p < 2; ++p) {
#pragma unroll
          for (int t = 0; t < 3; ++t) { mm(4 * s + p, t); rd(4 * s + p + 2, t); SBAR(); }
          if (s == 7 && p == 0) { cswr(4, cs[0]); cswr(5, cs[1]); cswr(6, cs[2]); SBAR(); }
        }
        if constexpr (s == 0) {
          wait_vmf<0>(ep);
          if (m == 0 && lane < 10)
            *(LAS unsigned*)(size_t)(lds0 + L_CNT + lane * 4) = lane == 8 ? ep[0] * 0x9E3779B1u + 0x7F4A7C15u : lane == 9 ? ep[1] * 0x85EBCA77u + 0x165667B1u : 0u;
        }
        if constexpr (s == 1) {
          get_tag();
          if ((lane >> 2) == 3) {
            const v2u gr = {asu(sval), tag32};
            __builtin_amdgcn_raw_buffer_store_b64(gr, rws, WS_STATS + (128 * r + 8 * j + 2 * m) * 16 + p4 * 8, 0, 16);
          }
        }
        if constexpr (s == MIDWARM) {
          warm = ld2_async(make_srd((const void*)wbase, 4096), lane * 128, 0);
        }
        if constexpr (s == 5) stat_load();
        if constexpr (s == 6) {
          wait_vm1<0>(st);
          PIN(warm);

          row_stats();
        }
        mm(4 * s + 2, 0); mm(4 * s + 2, 1);
        bar();
        if constexpr (s < 7) {
          STAMP_AT(11 + s);
          mm(4 * s + 2, 2);
          rd(4 * s + 4, 0); rd(4 * s + 4, 1); rd(4 * s + 4, 2); SBAR();
#pragma unroll
          for (int t = 0; t < 3; ++t) { mm(4 * s + 3, t); rd(4 * s + 5, t); SBAR(); }
        }
      });
      STAMP_AT(1);
      __builtin_amdgcn_s_setprio(3);
      const f32x4 c5 = csrd(5), c6 = csrd(6), c4 = csrd(4);
      SBAR();
      mm(30, 2); mm(31, 1);
      make_xaug();
      SBAR();
      mm(31, 2); mm(31, 0);
      if (h == 0) *(LAS float*)(size_t)(lds0 + L_RS + (m * 32 + r32) * 4) = rstd;
      acc[1] = MFMA32(waug(c5, 0u), xaug, acc[1]);
      acc[2] = MFMA32(xaug, waug(c6, 0u), acc[2]);
      acc[0] = MFMA32(waug(c4, 0u), xaug, acc[0]);
      SBAR();
      f32x4 rs[4];
#pragma unroll
      for (int qd = 0; qd < 4; ++qd) rs[qd] = __builtin_bit_cast(f32x4, lds128(lds0 + L_RS + (m * 32 + 8 * qd + 4 * h) * 4));
      const unsigned kvo = WS_KV + (j * 64 + T) * 4096;
#pragma unroll
      for (int e = 0; e < 16; ++e) acc[1][e] = MUL1(acc[1][e], rstd);
#pragma unroll
      for (int e = 0; e < 2; ++e) {
        const v4u f = pack8(acc[1], e);
        st16_sc1(f, sws, lane * 16, kvo + e * 1024);
        sts128(lds0 + L_KV + (8 + m) * KVT + e * 1024 + lane * 16, f);
      }
#pragma unroll
      for (int qd = 0; qd < 4; ++qd)
#pragma unroll
        for (int e = 0; e < 4; ++e) acc[2][4 * qd + e] = MUL1(acc[2][4 * qd + e], rs[qd][e]);
#pragma unroll
      for (int s = 0; s < 2; ++s) {
        const v4u f = pack8(acc[2], s);
        st16_sc1(f, sws, lane * 16, kvo + 2048 + s * 1024);
        sts128(lds0 + L_KV + (8 + m) * KVT + s * VB + vpos, f);
      }
      SBAR();
      const float qs = rstd * (0.25f * 1.4426950408889634f);
#pragma unroll
      for (int e = 0; e < 16; ++e) acc[0][e] = MUL1(acc[0][e], qs);
      qf[0] = pack8(acc[0], 0); qf[1] = pack8(acc[0], 1);
      PIN(qf[0]); PIN(qf[1]);
      STAMP_AT(2);
      const int T0 = 4 * r - 8 + 2 * m;
      const bool live = T0 >= 0;
      auto kv_load = [&](v4u (&kv)[8], int i0, int i1) {
        const unsigned src = WS_KV + (j * 64 + (live ? T0 : 0)) * 4096, vo = live ? lane * 16 : h * 512;
#pragma unroll
        for (int i = i0; i < i1; ++i) kv[i] = ld16_sc1_async(sws, vo, src + (live ? i : (i & 3)) * 1024);
      };
      auto kv_stage = [&](v4u (&kv)[8]) {
        const unsigned on = (lane < 2 || live) ? one2 : 0u;
        if (lane < 6) sts128(lds0 + L_KV + (lane < 2 ? 8 + m : 2 * m + ((lane - 2) >> 1)) * KVT + 2048 + (lane & 1) * VB + 1024, (v4u){on, on, on, on});
        const v4u zz = {};
#pragma unroll
        for (int i = 0; i < 2; ++i) {
          sts128(lds0 + L_KV + (2 * m + i) * KVT + lane * 16, kv[4 * i]);
          sts128(lds0 + L_KV + (2 * m + i) * KVT + 1024 + lane * 16, kv[4 * i + 1]);
          sts128(lds0 + L_KV + (2 * m + i) * KVT + vpos, live ? kv[4 * i + 2] : zz);
          sts128(lds0 + L_KV + (2 * m + i) * KVT + VB + vpos, live ? kv[4 * i + 3] : zz);
        }
        arrive_nowait(acnt);
      };
      auto kv_wait = [&](v4u (&kv)[8]) {
        wait_vm<0>();
#pragma unroll
        for (int i = 0; i < 8; ++i) PIN(kv[i]);
      };
      if (heavy) {
        hsetup(1);
        await(acnt + 16, 4);
        STAMP_AT(3);
        v2u fl;
        v4u kv[8];
        hmm(std::integral_constant<int, 1>{}, xf, [&](auto kc) {
          constexpr int ks = kc.value;
          if constexpr (ks == KVPUB) {
            wait_vm<0>();
            if (lane == 0) __hip_atomic_store(flag(FL_KV, T, j), tag, RLX_AGENT);
          }
          if constexpr (ks == KVPUB + 2) bias_load(1);
          if constexpr (ks == 31) { wait_vm<1>(); PIN(bias0); PIN(bias1); }
          if constexpr (ks == 24) fl = ld8_sc1_async(sws, flag_off(FL_KV, live ? T0 + (lane & 1) : 0, j));
        });
        STAMP_AT(18);
        wait_vmf<0>(fl);
        if (!__all(fl[0] == taglo && fl[1] == tag32)) poll_flags(flag(FL_KV, live ? T0 + (lane & 1) : 0, j), tag);
        kv_load(kv, 0, 8);
        hgelu(std::integral_constant<int, 1>{}, [&]() {});
        STAMP_AT(4);
        masks();
        wait_vm<4>();
#pragma unroll
        for (int i = 0; i < 8; ++i) PIN(kv[i]);
        kv_stage(kv);
      } else {
        wait_vm<0>();
        if (lane == 0) __hip_atomic_store(flag(FL_KV, T, j), tag, RLX_AGENT);
        v4u kv[8];
        poll_flags(flag(FL_KV, live ? T0 + (lane & 1) : 0, j), tag);
        kv_load(kv, 0, 8); masks(); kv_wait(kv); kv_stage(kv);
      }
    }
    STAMP_AT(19);
    const unsigned kb = lds0 + L_KV + m * KVT + lane * 16;
    unsigned vb[2];
#pragma unroll
    for (int e = 0; e < 2; ++e) { vb[e] = lds0 + L_KV + m * KVT + 2048 + ((g & 1) ? 1024 : e * 512 + h * 256 + (lane & 15) * 16); PIN(vb[e]); }
    await(acnt, heavy ? 8 : 4);
    __builtin_amdgcn_s_setprio(PRIO_ATT);
    STAMP_AT(5);
    const f32x16 zero = {};
    auto o_store = [&](f32x16& o, int e) {
      const float rl = __builtin_amdgcn_rcpf(o[8]);
#pragma unroll
      for (int u = 0; u < 8; ++u) o[u] = MUL1(o[u], rl);
      st16_sc1(pack8(o, 0), sws, lane * 16, WS_AF + (T * AFT + 112 + 2 * j + e) * 1024);
    };
    int redo = 0;
    {
      f32x16 o[2], sa, sb, su;
      bool pend = false;
      v4u kn, ku, va, vc, vn0, vn1, vu0, vu1, p0, p1, pu;
      auto kaddr = [&](int n) { return kb + ((n & 7) + 1) * KVT + (n >> 3) * 1024; };
      auto vaddr = [&](int n) { return vb[n >> 3] + ((n & 7) + 1) * KVT; };
      kn = lds128(kaddr(0)); vn0 = lds128(vaddr(0)); vn1 = lds128(vaddr(0) + VB);
      sa = MFMA32(kn, qf[0], zero);
      kn = lds128(kaddr(1));
      SBAR();
      auto step = [&](auto nc, f32x16& sc, f32x16& sn) {
        constexpr int n = nc.value, e = n >> 3, i = n & 7;
        if constexpr (n < 15) sn = MFMA32(kn, qf[(n + 1) >> 3], zero);
        if constexpr (n < 14) kn = lds128(kaddr(n + 2));
        if constexpr (i == 5) ku = lds128(kb + e * 1024);
        v4u v0 = vn0, v1 = vn1;
        if constexpr (n < 15) { vn0 = lds128(vaddr(n + 1)); vn1 = lds128(vaddr(n + 1) + VB); }
        if constexpr (i == 6) { vu0 = lds128(vb[e]); vu1 = lds128(vb[e] + VB); }
        SBAR();
        if constexpr (i == 7) {
#pragma unroll
          for (int u = 0; u < 16; ++u) sc[u] = __builtin_amdgcn_inverse_ballot_w64(um[u]) ? su[u] : sc[u];
          PIN(sc); SBAR();
        }
#pragma unroll
        for (int u = 0; u < 6; ++u) sc[u] = EXP2(sc[u]);
        PIN(sc); SBAR();
        if constexpr (n == 8) { o[0] = MFMA32(vc, p1, o[0]); SBAR(); }
        else if constexpr (n > 0) { o[e] = MFMA32(vc, p1, o[e]); SBAR(); }
        sc[6] = EXP2(sc[6]); sc[7] = EXP2(sc[7]);
        p0 = pack8(sc, 0);
        sc[8] = EXP2(sc[8]); sc[9] = EXP2(sc[9]);
        if constexpr (i == 7) { va = p0 & am0; p0 &= ~am0; PIN(va); }
        PIN(sc); PIN(p0); SBAR();
        if constexpr (n == 8) { o[0] = MFMA32(vu1, pu, o[0]); SBAR(); }
        o[e] = MFMA32(v0, p0, i == 0 ? zero : o[e]);
        SBAR();
        if constexpr (i == 6) { su = MFMA32(ku, qf[e], zero); SBAR(); }
#pragma unroll
        for (int u = 10; u < 13; ++u) sc[u] = EXP2(sc[u]);
        if constexpr (i == 7) { PIN(sc); SBAR(); o[e] = MFMA32(vu0, va, o[e]); SBAR(); }
#pragma unroll
        for (int u = 13; u < 16; ++u) sc[u] = EXP2(sc[u]);
        p1 = pack8(sc, 1); vc = v1;
        if constexpr (i == 7) { pu = p1 & am1; p1 &= ~am1; PIN(pu); }
        PIN(p1); SBAR();
        if constexpr (n == 8) {
          if (__all(o[0][8] >= 0x1p-30f && o[0][8] <= 0x1p30f)) o_store(o[0], 0); else redo = 1;
          SBAR();
        }
        if constexpr (i == 0) pend = !redo;
        if constexpr (n == 0 || n == 1 || n == 2 || i == 3 || i == 5 || i == 7) {
          if (pend && (i == 7 || (__builtin_amdgcn_s_getreg(7 | (3 << 11)) & 15) == 0)) {
            wait_vm<0>();
            if (lane == 0) __hip_atomic_store(flag(FL_H1 + e, T, j), tag, RLX_AGENT);
            pend = false;
          }
        }
      };
      static_for<0, 8>([&](auto hc) {
        step(std::integral_constant<int, 2 * hc.value>{}, sa, sb);
        step(std::integral_constant<int, 2 * hc.value + 1>{}, sb, sa);
      });
      o[1] = MFMA32(vc, p1, o[1]);
      o[1] = MFMA32(vu1, pu, o[1]);
      if (__all(o[1][8] >= 0x1p-30f && o[1][8] <= 0x1p30f)) o_store(o[1], 1); else redo |= 2;
    }
    if (redo) {
#pragma unroll 1
      for (int e = 0; e < 2; ++e) {
        if (!((redo >> e) & 1)) continue;
        const v4u q = e ? qf[1] : qf[0];
        const unsigned vbe = e ? vb[1] : vb[0];
        auto merged = [&](const f32x16& c) {
          f32x16 sl = MFMA32(lds128(kb + 8 * KVT + e * 1024), q, c);
          const f32x16 su = MFMA32(lds128(kb + e * 1024), q, c);
          static_for<0, 16>([&](auto u) { sl[u.value] = upper<u.value>() ? su[u.value] : sl[u.value]; });
          return sl;
        };
        float mx = -INFINITY;
#pragma unroll 1
        for (int i = 0; i < 8; ++i) {
          const f32x16 sc = i ? MFMA32(lds128(kb + i * KVT + e * 1024), q, zero) : merged(zero);
#pragma unroll
          for (int u = 0; u < 16; u += 2) mx = max3(mx, sc[u], sc[u + 1]);
        }
        const auto sw = __builtin_amdgcn_permlane32_swap(asu(mx), asu(mx), false, false);
        mx = __builtin_fmaxf(asf(sw[0]), asf(sw[1]));
        f32x16 negm, o;
#pragma unroll
        for (int u = 0; u < 16; ++u) negm[u] = -mx;
        {
          f32x16 sc = merged(negm);
#pragma unroll
          for (int u = 0; u < 16; ++u) sc[u] = EXP2(sc[u]);
          const v4u p0 = pack8(sc, 0), p1 = pack8(sc, 1);
          o = MFMA32(lds128(vbe), p0 & am0, zero);
          o = MFMA32(lds128(vbe + VB), p1 & am1, o);
          o = MFMA32(lds128(vbe + 8 * KVT), p0 & ~am0, o);
          o = MFMA32(lds128(vbe + 8 * KVT + VB), p1 & ~am1, o);
        }
#pragma unroll 1
        for (int i = 1; i < 8; ++i) {
          f32x16 sc = MFMA32(lds128(kb + i * KVT + e * 1024), q, negm);
#pragma unroll
          for (int u = 0; u < 16; ++u) sc[u] = EXP2(sc[u]);
          o = MFMA32(lds128(vbe + i * KVT), pack8(sc, 0), o);
          o = MFMA32(lds128(vbe + i * KVT + VB), pack8(sc, 1), o);
        }
        o_store(o, e);
      }
      wait_vm<0>();
      if (lane == 0) __hip_atomic_store(flag(FL_O0, T, j), tag, RLX_AGENT);
    }
    STAMP_AT(6);
    wait_vm<0>();
    if (lane == 0) __hip_atomic_store(flag(FL_O1, T, j), tag, RLX_AGENT);
    STAMP_AT(7);
    res_loads();
    wait_vm2<0>(xres, bres);
    if (blockIdx.x == 0 && wave == 7) {
#pragma unroll 1
      for (int i = 0; i < 4; ++i) poll_flags(flag(FL_KV, 4 * (4 * i + g), lane & 15), tag);
      if (lane == 0) __hip_atomic_store((GAS u64*)(ws + WS_EPOCH), tag, RLX_AGENT);
    }
  }
  bar();
  {
    int t2 = threadIdx.x; PIN(t2);
    const int row = t2 >> 3, ch = t2 & 7, orow = 64 * rt + row, ocol = 64 * ct + ch * 8;
    const unsigned a = lds0 + L_WG + (((row >> 5) * 8 + ch) * 66 + (row & 31)) * 16;
    f32x4 lo = {}, hi = {};
#pragma unroll
    for (int w = 0; w < 4; ++w) {
      lo += __builtin_bit_cast(f32x4, lds128(a + w * PART));
      hi += __builtin_bit_cast(f32x4, lds128(a + w * PART + 512));
    }
    v4u o;
    o[0] = pk2(lo[0] + bflo(xres[0]) + bflo(bres[0]), lo[1] + bfhi(xres[0]) + bfhi(bres[0]));
    o[1] = pk2(lo[2] + bflo(xres[1]) + bflo(bres[1]), lo[3] + bfhi(xres[1]) + bfhi(bres[1]));
    o[2] = pk2(hi[0] + bflo(xres[2]) + bflo(bres[2]), hi[1] + bfhi(xres[2]) + bfhi(bres[2]));
    o[3] = pk2(hi[2] + bflo(xres[3]) + bflo(bres[3]), hi[3] + bfhi(xres[3]) + bfhi(bres[3]));
    *(GAS v4u*)((char*)out + (size_t)orow * 1024 + ocol * 2) = o;
  }
  STAMP_AT(8);
}

extern "C" void kernel_launch(void* const* d_in, const int* in_sizes, int n_in, void* d_out, int out_size, void* d_ws, size_t ws_size,
                              hipStream_t stream) {
  (void)in_sizes; (void)n_in; (void)out_size; (void)ws_size;
  (void)hipFuncSetAttribute((const void*)fused_block, hipFuncAttributeMaxDynamicSharedMemorySize, LDS_BYTES);
  fused_block<<<256, 512, LDS_BYTES, stream>>>(d_in[0], d_in[1], d_in[2], d_in[4], d_in[5], d_in[6], d_in[7], d_out, (char*)d_ws);
}
```

```cpp
#include <hip/hip_runtime.h>
#include <hip/hip_bf16.h>
#include <stdint.h>
#include <type_traits>

typedef unsigned v4u __attribute__((ext_vector_type(4)));
typedef unsigned v2u __attribute__((ext_vector_type(2)));
typedef float f32x2 __attribute__((ext_vector_type(2)));
typedef float f32x4 __attribute__((ext_vector_type(4)));
typedef float f32x16 __attribute__((ext_vector_type(16)));
typedef short bf16x8 __attribute__((ext_vector_type(8)));
typedef short s16x4 __attribute__((ext_vector_type(4)));
typedef __bf16 bf16x2_t __attribute__((ext_vector_type(2)));
typedef unsigned long long u64;
#define GAS __attribute__((address_space(1)))
#define LAS __attribute__((address_space(3)))
#define DI __device__ __forceinline__
#define RLX_AGENT __ATOMIC_RELAXED, __HIP_MEMORY_SCOPE_AGENT

constexpr int SEQ = 2048, DM = 512, FF = 1792;
constexpr unsigned WS_EPOCH = 0;
constexpr unsigned WS_STATS = 32768;
constexpr unsigned WS_FLAG = 65536;
constexpr unsigned WS_KV = WS_FLAG + 5 * 1024 * 128;
constexpr int AFT = 145;
constexpr unsigned WS_AF = WS_KV + 16 * 64 * 4096;
constexpr unsigned WS_END = WS_AF + 64 * AFT * 1024;
constexpr unsigned WS_STAMP = WS_END;
enum { FL_KV = 0, FL_H0 = 1, FL_H1 = 2, FL_O0 = 3, FL_O1 = 4 };
constexpr int STG = 36864;
constexpr int L_WA = 0;
constexpr int L_WG = 86016;
constexpr int VB = 1040;
constexpr int KVT = 2048 + 2 * VB;
constexpr int L_KV = 65536 - 8 * KVT;
constexpr int RD = 8, NR = 12;
constexpr int PART = 16 * 66 * 16;
constexpr int L_CS = L_WG + 4 * PART;
constexpr int L_RS = L_CS + 3584;
constexpr int L_CNT = L_RS + 512;
constexpr int LDS_BYTES = L_CNT + 64;
static_assert(L_KV + 12 * KVT <= L_WG && L_WG >= 2 * STG && L_CS >= 3 * STG && PART >= RD * 2048 && LDS_BYTES <= 163840, "lds map");
#define AWIN 6
constexpr int AW = AWIN;
constexpr unsigned SPIN_CAP = 1u << 23;

#define MFMA32(a, b, c) __builtin_amdgcn_mfma_f32_32x32x16_bf16(__builtin_bit_cast(bf16x8, a), __builtin_bit_cast(bf16x8, b), c, 0, 0, 0)
#define MFMA16(a, b, c) __builtin_amdgcn_mfma_f32_16x16x32_bf16(__builtin_bit_cast(bf16x8, a), __builtin_bit_cast(bf16x8, b), c, 0, 0, 0)
#define SBAR() __builtin_amdgcn_sched_barrier(0)

#define STAMP_AT(i) do { } while (0)

DI float asf(unsigned u) { return __builtin_bit_cast(float, u); }
DI unsigned asu(float f) { return __builtin_bit_cast(unsigned, f); }
DI unsigned pk2(float lo, float hi) { return __builtin_bit_cast(unsigned, __builtin_convertvector((f32x2){lo, hi}, bf16x2_t)); }
DI float bflo(unsigned u) { return __builtin_bit_cast(float, u << 16); }
DI float bfhi(unsigned u) { return __builtin_bit_cast(float, u & 0xffff0000u); }
DI v4u make_srd(const void* p, unsigned bytes) { const u64 a = (u64)p; return (v4u){(unsigned)a, (unsigned)(a >> 32) & 0xffffu, bytes, 0x00020000u}; }
DI void dma16(v4u srd, unsigned voff, unsigned soff, unsigned lds) {
  asm volatile("s_mov_b32 m0, %3\n\ts_nop 0\n\tbuffer_load_dwordx4 %0, %1, %2 offen lds" :: "v"(voff), "s"(srd), "s"(soff), "s"(lds) : "memory");
}
DI void dma16_a(v4u srd, unsigned voff, unsigned soff, unsigned lds) { dma16(srd, voff, soff, lds); }
DI void dma16_b(v4u srd, unsigned voff1, unsigned soff) {
  asm volatile("buffer_load_dwordx4 %0, %1, %2 offen offset:1024 lds" :: "v"(voff1), "s"(srd), "s"(soff) : "memory");
}
DI v4u ld16_sc1_async(v4u srd, unsigned voff, unsigned soff) {
  v4u r; asm volatile("buffer_load_dwordx4 %0, %1, %2, %3 offen sc1" : "=v"(r) : "v"(voff), "s"(srd), "s"(soff) : "memory"); return r;
}
DI v4u ld16_async(v4u srd, unsigned voff, unsigned soff) {
  v4u r; asm volatile("buffer_load_dwordx4 %0, %1, %2, %3 offen" : "=v"(r) : "v"(voff), "s"(srd), "s"(soff) : "memory"); return r;
}
DI unsigned ld2_async(v4u srd, unsigned voff, unsigned soff) {
  unsigned r; asm volatile("buffer_load_ushort %0, %1, %2, %3 offen" : "=v"(r) : "v"(voff), "s"(srd), "s"(soff) : "memory"); return r;
}
DI void st16_sc1(v4u v, v4u srd, unsigned voff, unsigned soff) {
  asm volatile("buffer_store_dwordx4 %0, %1, %2, %3 offen sc1\n\ts_nop 1" :: "v"(v), "v"(voff), "s"(srd), "s"(soff) : "memory");
}
template <int N> DI void wait_vm1(v4u& a) { asm volatile("s_waitcnt vmcnt(%1)" : "+v"(a) : "i"(N) : "memory"); }
template <int N> DI void wait_vm2(v4u& a, v4u& b) { asm volatile("s_waitcnt vmcnt(%2)" : "+v"(a), "+v"(b) : "i"(N) : "memory"); }
template <int N> DI void wait_vm3(v4u& a, v4u& b, v4u& c) { asm volatile("s_waitcnt vmcnt(%3)" : "+v"(a), "+v"(b), "+v"(c) : "i"(N) : "memory"); }
DI void pin_s(u64& v) { asm volatile("" : "+s"(v)); }
template <class V> DI void pin(V& v) { asm volatile("" : "+v"(v)); }
template <int N> DI void wait_vm() { asm volatile("s_waitcnt vmcnt(%0)" :: "i"(N) : "memory"); }
DI void bar() { asm volatile("s_waitcnt lgkmcnt(0)\n\ts_barrier" ::: "memory"); }
DI v4u lds128(unsigned a) { return *(const LAS v4u*)(size_t)a; }
DI void sts128(unsigned a, v4u v) { *(LAS v4u*)(size_t)a = v; }
DI v2u ldtr(unsigned a) { return __builtin_bit_cast(v2u, __builtin_amdgcn_ds_read_tr16_b64_v4i16((LAS s16x4*)(size_t)a)); }
DI v4u ldtr2(unsigned a, int second) { const v2u lo = ldtr(a), hi = ldtr(a + second); return (v4u){lo.x, lo.y, hi.x, hi.y}; }
template <int CTRL> DI float dpp(float v) { return __builtin_bit_cast(float, __builtin_amdgcn_update_dpp(0, __builtin_bit_cast(int, v), CTRL, 0xf, 0xf, true)); }
DI float rdl(float v, int l) { return __builtin_bit_cast(float, __builtin_amdgcn_readlane(__builtin_bit_cast(int, v), l)); }
DI v4u pack8(const f32x16& a, int s) { return (v4u){pk2(a[8 * s], a[8 * s + 1]), pk2(a[8 * s + 2], a[8 * s + 3]), pk2(a[8 * s + 4], a[8 * s + 5]), pk2(a[8 * s + 6], a[8 * s + 7])}; }
DI void poll_flags(const GAS u64* p, u64 tag) {
#pragma unroll 1
  for (unsigned spin = 0; spin < SPIN_CAP; ++spin) {
    const u64 v = __hip_atomic_load(p, RLX_AGENT);
    if (__all(v == tag)) break;
    __builtin_amdgcn_s_sleep(2);
  }
  asm volatile("" ::: "memory");
}
constexpr u64 upper_mask(int reg) {
  u64 m = 0;
  for (int l = 0; l < 64; ++l) { const int row = (reg & 3) + 8 * (reg >> 2) + 4 * (l >> 5); if (row > (l & 31)) m |= 1ull << l; }
  return m;
}
template <int REG> DI bool upper() { return __builtin_amdgcn_inverse_ballot_w64(upper_mask(REG)); }
template <int I, int N, class Fn> DI void static_for(Fn&& f) { if constexpr (I < N) { f(std::integral_constant<int, I>{}); static_for<I + 1, N>(f); } }
DI float max3(float a, float b, float c) { return __builtin_fmaxf(__builtin_fmaxf(a, b), c); }

constexpr int H1_CK = 16 - AW, H1_LD = H1_CK - 3, O_CK = 28 - AW, O_LD = O_CK - 3;
constexpr int g2_issued(int t) { return ((t >= 1 && t - 1 + NR + RD < 36) ? 2 : 0) + ((t + AW < 28) ? 2 : 0) + (t == H1_LD) + (t == O_LD) + (t == O_CK ? 10 : 0); }
constexpr int g2_range(int a, int b) { int n = 0; for (int t = a; t < b; ++t) n += g2_issued(t); return n; }
#define GELU_PK(P) (P == 1)
#define PRIO_G2 2
#define PRIO_ATT 3
#define PRIO_G1 2
#define ROT r
#define CODEWARM 39000
#define MIDWARM 1
#define KVPUB 6
#define EXP2(v) __builtin_amdgcn_exp2f(v)
#define PIN(v) pin(v)
extern "C" __device__ float mul_unpacked(float, float) __asm("llvm.amdgcn.fmul.legacy");
#define MUL1(a, b) mul_unpacked(a, b)
#define FMA1(a, b, c) __builtin_amdgcn_div_fmasf(a, b, c, false)
DI v2u ld8_sc1_async(v4u srd, unsigned voff) {
  v2u r; asm volatile("buffer_load_dwordx2 %0, %1, %2, 0 offen sc1" : "=v"(r) : "v"(voff), "s"(srd) : "memory"); return r;
}
template <int N> DI void wait_vmf(v2u& a) { asm volatile("s_waitcnt vmcnt(%1)" : "+v"(a) : "i"(N) : "memory"); }
DI void arrive(unsigned cnt, int lane) {
  asm volatile("s_waitcnt lgkmcnt(0)" ::: "memory");
  if (lane == 0) __hip_atomic_fetch_add((LAS unsigned*)(size_t)cnt, 1u, __ATOMIC_RELAXED, __HIP_MEMORY_SCOPE_WORKGROUP);
}
DI void arrive_nowait(unsigned cnt) {
  asm volatile("s_mov_b64 exec, 1\n\tds_add_u32 %0, %1\n\ts_mov_b64 exec, -1" :: "v"(cnt), "v"(1u) : "memory");
}
DI unsigned peek(unsigned cnt) { return __hip_atomic_load((LAS unsigned*)(size_t)cnt, __ATOMIC_RELAXED, __HIP_MEMORY_SCOPE_WORKGROUP); }
DI void await(unsigned cnt, unsigned n) {
#pragma unroll 1
  for (unsigned spin = 0; spin < SPIN_CAP * 32; ++spin) {
    if (__builtin_amdgcn_readfirstlane(peek(cnt)) >= n) break;
    __builtin_amdgcn_s_sleep(1);
  }
  asm volatile("" ::: "memory");
}

__global__ __launch_bounds__(512, 2) void fused_block(const void* __restrict__ x, const void* __restrict__ wqkv, const void* __restrict__ wo,
                                                      const void* __restrict__ w1, const void* __restrict__ b1, const void* __restrict__ w2,
                                                      const void* __restrict__ b2, void* __restrict__ out, char* __restrict__ ws) {
  constexpr unsigned lds0 = 0;
  const int wave = __builtin_amdgcn_readfirstlane(threadIdx.x >> 6);
  const int m = wave & 3;
  const bool isG = wave < 4;
  const int xcd = blockIdx.x & 7, idx = blockIdx.x >> 3;
  const int r = (xcd >> 2) * 8 + (idx >> 2), j = (xcd & 3) * 4 + (idx & 3);
  const int rt = (xcd >> 1) * 8 + (idx >> 2), ct = (xcd & 1) * 4 + (idx & 3);
  int lane = threadIdx.x & 63, r32, h, g, q4, p4;
  STAMP_AT(0);
  asm volatile("" :: "s"(x), "s"(wqkv), "s"(wo), "s"(w1), "s"(b1), "s"(w2), "s"(b2), "s"(out), "s"(ws));

  const v4u sx = make_srd(x, SEQ * DM * 2), sws = make_srd(ws, WS_END);
  const __amdgpu_buffer_rsrc_t rws = __builtin_amdgcn_make_buffer_rsrc(ws, 0, WS_END, 0x00020000);
  const unsigned one2 = 0x3f803f80u;
  const int T = 4 * r + m;
  const int rot = ROT;
  auto flag_off = [&](int kind, int tile, int jj) { return WS_FLAG + ((kind * 64 + tile) * 16 + jj) * 128; };
  auto flag = [&](int kind, int tile, int jj) { return (GAS u64*)(ws + flag_off(kind, tile, jj)); };
  u64 tag;
  unsigned taglo, tag32;
  auto get_tag = [&]() {
    const v2u tg = *(const LAS v2u*)(size_t)(lds0 + L_CNT + 32);
    taglo = __builtin_amdgcn_readfirstlane(tg[0]); tag32 = __builtin_amdgcn_readfirstlane(tg[1]);
    tag = ((u64)tag32 << 32) | taglo;
  };
  v4u xres, bres, xaug, st;
  float mean, var, rstd, sd;
  v4u sel;
  unsigned xo[4], swz;
  auto lane_setup = [&]() {
    lane = threadIdx.x & 63; PIN(lane);
    r32 = lane & 31; h = lane >> 5; g = lane >> 4; q4 = (lane >> 2) & 3; p4 = lane & 3;
    const unsigned selv = ((lane & 15) == (g & 1)) ? one2 : 0u;
    sel = (v4u){selv, selv, selv, selv};
#pragma unroll
    for (int p = 0; p < 4; ++p) xo[p] = (32 * m + r32) * 128 + (((2 * ((p + m) & 3) + h) ^ ((r32 >> 1) & 7)) * 16);
    swz = ((((lane >> 2) & 1) ^ (g & 1)) * 64) + p4 * 16;
  };

  auto stat_load = [&]() { st = ld16_sc1_async(sws, WS_STATS + (128 * r + 32 * m + r32) * 16, 0); };
  auto res_loads = [&]() {
    int t2 = threadIdx.x; PIN(t2);
    const int row = 64 * rt + (t2 >> 3), col = 64 * ct + (t2 & 7) * 8;
    xres = ld16_async(sx, row * 1024 + col * 2, 0);
    bres = ld16_async(make_srd(b2, DM * 2), col * 2, 0);
  };
  auto row_stats = [&]() {
    if (!__all(st[1] == tag32 && st[3] == tag32)) {
#pragma unroll 1
      for (unsigned spin = 0; spin < SPIN_CAP; ++spin) {
        st = ld16_sc1_async(sws, WS_STATS + (128 * r + 32 * m + r32) * 16, 0);
        wait_vm1<0>(st);
        if (__all(st[1] == tag32 && st[3] == tag32)) break;
      }
    }
    mean = asf(st[0]); var = asf(st[2]);
    PIN(mean); PIN(var);
  };
  auto make_xaug = [&]() {
    PIN(mean); PIN(var);
    rstd = __builtin_amdgcn_rsqf(var); sd = var * rstd;
    const unsigned hi = pk2(-mean, sd);
    const unsigned lo = pk2(-mean - bflo(hi), FMA1(bfhi(hi), -1.f, sd));
    xaug = (v4u){(hi & 0xffffu) | (hi << 16), (lo & 0xffffu) | (hi & 0xffff0000u), lo >> 16, 0u};
    if (h) xaug = (v4u){0u, 0u, 0u, 0u};
    PIN(xaug);
  };
  auto cswr = [&](int tile, f32x4 c) {
    if (lane < 16) { LAS float* p = (LAS float*)(size_t)(lds0 + L_CS + (tile * 32 + lane) * 16 + m * 4); p[0] = c[0]; p[64] = c[1]; }
  };
  auto csrd = [&](int tile) { return __builtin_bit_cast(f32x4, lds128(lds0 + L_CS + (tile * 32 + r32) * 16)); };
  auto waug = [&](f32x4 c4, unsigned b16) {
    const float c = ((c4[0] + c4[1]) + c4[2]) + c4[3];
    const unsigned hi = pk2(c, 0.f);
    const unsigned lo = pk2(c - bflo(hi), 0.f);
    return (v4u){hi | (lo << 16), hi | (b16 << 16), b16, 0u};
  };

  const v4u sw1 = make_srd(w1, DM * FF * 2);
  const int hcol0 = 448 * (j >> 2) + 128 * (j & 3);
  const bool heavy = (j & 3) != 3;
  unsigned bias0, bias1;
  auto bias_load = [&](int P) {
    bias0 = ld2_async(make_srd(b1, FF * 2), r32 * 2, (hcol0 + 64 * P) * 2);
    bias1 = ld2_async(make_srd(b1, FF * 2), r32 * 2, (hcol0 + 64 * P + 32) * 2);
  };
  auto w1dma = [&](int P, int c, unsigned dst, int half) {
    const unsigned vw1 = (lane >> 3) * (FF * 2) + swz, vw1b = vw1 + 8 * (FF * 2) - 1024;
    const unsigned so = (64 * ((c + rot) & 7) + 16 * m) * (FF * 2) + (hcol0 + 64 * P) * 2;
    if (half == 0) dma16_a(sw1, vw1, so, dst + m * 2048); else dma16_b(sw1, vw1b, so);
  };
  f32x16 hac[2];
  unsigned rk[4][2];
  auto hsetup = [&](int P) {
#pragma unroll
    for (int p = 0; p < 4; ++p)
#pragma unroll
      for (int c = 0; c < 2; ++c) {
        rk[p][c] = lds0 + (P ? L_WA : 28672) + ((p + m) & 3) * 2048 + h * 1024 + q4 * 128 + ((c ^ (q4 >> 1)) * 64) + (g & 1) * 32 + p4 * 8;
        PIN(rk[p][c]);
      }
  };
  auto hmm = [&](auto pc, const v4u (&xf)[32], auto&& hook) {
    constexpr int P = pc.value;
    constexpr int D = 4;
    const unsigned cnt = lds0 + L_CNT;
    f32x4 cs[2] = {}, c0, c1;
    v4u wf[D][2], au0, au1;
    unsigned seen = 0;
    hac[0] = (f32x16){}; hac[1] = (f32x16){};
    auto rd = [&](int ks, int t) { wf[ks % D][t] = ldtr2(rk[ks & 3][t] + (ks >> 2) * 8192, 512); };
#pragma unroll
    for (int ks = 0; ks < D; ++ks) { rd(ks, 0); rd(ks, 1); }
    SBAR();
    static_for<0, 32>([&](auto kc) {
      constexpr int ks = kc.value;
      hook(kc);
      if constexpr (ks == 10) seen = peek(cnt + 20);
      if constexpr (ks == 29) seen = peek(cnt);
#pragma unroll
      for (int t = 0; t < 2; ++t) {
        hac[t] = MFMA32(wf[ks % D][t], xf[ks], hac[t]);
        if ((ks & 3) == 0 && ks < 28) { cs[t] = MFMA16(sel, wf[ks % D][t], cs[t]); PIN(cs[t]); }
        if (ks == 26) { cs[t] = MFMA16(sel, wf[28 % D][t], cs[t]); PIN(cs[t]); }
        SBAR();
        if constexpr (ks + D < 32) rd(ks + D, t);
        if constexpr (ks == 31) { if (t == 0) { au0 = waug(c0, bias0); PIN(au0); } else { au1 = waug(c1, bias1); PIN(au1); } }
        SBAR();
      }
      if constexpr (ks == 11 || ks == 30) {
        PIN(hac[0]); PIN(hac[1]);
        if (__builtin_amdgcn_readfirstlane(seen) < 4) await(ks == 11 ? cnt + 20 : cnt, 4);
        asm volatile("" ::: "memory");
        if constexpr (ks == 30) { c0 = csrd(2 * P); c1 = csrd(2 * P + 1); }
      }
      if constexpr (ks == 27) { cswr(2 * P, cs[0]); cswr(2 * P + 1, cs[1]); arrive_nowait(cnt); }
      SBAR();
    });
    hac[0] = MFMA32(au0, xaug, hac[0]);
    hac[1] = MFMA32(au1, xaug, hac[1]);
    SBAR();
  };
  auto hgelu = [&](auto pc, auto&& mid) {
    constexpr int P = pc.value;
    constexpr bool PK = GELU_PK(P);
    const float C0 = 2.3022081986f, C1 = C0 * 0.044715f;
    const float ga = -C1 * rstd * rstd * rstd, gb = -C0 * rstd;
    auto mul = [&](f32x2 a, f32x2 b) { return PK ? a * b : (f32x2){MUL1(a[0], b[0]), MUL1(a[1], b[1])}; };
    auto fma = [&](f32x2 a, float b, float c) {
      return PK ? __builtin_elementwise_fma(a, (f32x2){b, b}, (f32x2){c, c}) : (f32x2){FMA1(a[0], b, c), FMA1(a[1], b, c)}; };
#pragma unroll
    for (int t = 0; t < 2; ++t) {
      f32x2 v[8], z[8];
#pragma unroll
      for (int e = 0; e < 8; ++e) { v[e] = (f32x2){hac[t][2 * e], hac[t][2 * e + 1]}; z[e] = mul(v[e], v[e]); }
      SBAR();
#pragma unroll
      for (int e = 0; e < 8; ++e) z[e] = fma(z[e], ga, gb);
      SBAR();
#pragma unroll
      for (int e = 0; e < 8; ++e) z[e] = mul(z[e], v[e]);
      SBAR();
#pragma unroll
      for (int e = 0; e < 8; ++e) z[e] = (f32x2){EXP2(z[e][0]), EXP2(z[e][1])};
      SBAR();
#pragma unroll
      for (int e = 0; e < 8; ++e) z[e] = fma(z[e], sd, sd);
      SBAR();
#pragma unroll
      for (int e = 0; e < 8; ++e) z[e] = (f32x2){__builtin_amdgcn_rcpf(z[e][0]), __builtin_amdgcn_rcpf(z[e][1])};
      SBAR();
#pragma unroll
      for (int e = 0; e < 8; ++e) z[e] = mul(z[e], v[e]);
      SBAR();
#pragma unroll
      for (int s = 0; s < 2; ++s) {
        const v4u f = {pk2(z[4 * s][0], z[4 * s][1]), pk2(z[4 * s + 1][0], z[4 * s + 1][1]), pk2(z[4 * s + 2][0], z[4 * s + 2][1]), pk2(z[4 * s + 3][0], z[4 * s + 3][1])};
        st16_sc1(f, sws, lane * 16, WS_AF + (T * AFT + (hcol0 >> 4) + 4 * P + 2 * t + s) * 1024);
      }
      if (t == 0) mid();
      SBAR();
    }
  };

  if (isG) {
    lane_setup();
    const v4u sw2 = make_srd(w2, FF * DM * 2), swo = make_srd(wo, DM * DM * 2);
    const unsigned vw2 = (lane >> 3) * 1024 + swz, vw2b = vw2 + 7 * 1024;
    const int ro3 = rt % 3;
    auto kmap = [&](int kk) {
      if (kk < 16) return 8 * (((kk >> 2) + rt) & 3) + (kk & 3);
      const int p = ((kk - 16) >> 2) + ro3;
      return 8 * (p >= 3 ? p - 3 : p) + 4 + (kk & 3);
    };
    auto omap = [&](int i) { return 2 * ((i + rt) & 3) + (i >> 2); };
    auto wdmah = [&](int kk, int half) {
      const unsigned so = (kk < 28 ? 448 * m + 16 * kmap(kk) : 128 * m + 16 * omap(kk - 28)) * 1024 + 128 * ct;
      if (half == 0) dma16_a(kk < 28 ? sw2 : swo, vw2, so, lds0 + L_WG + m * PART + (kk % RD) * 2048); else dma16_b(kk < 28 ? sw2 : swo, vw2b, so);
    };
    auto wdma = [&](int kk) { wdmah(kk, 0); wdmah(kk, 1); };
    {
      const v4u swq = make_srd(wqkv, DM * 3 * DM * 2);
      const unsigned vx0 = (lane >> 3) * 1024 + (((lane & 7) ^ g) * 16), vx1 = (7 + (lane >> 3)) * 1024 + (((lane & 7) ^ (4 + g)) * 16);
      const unsigned vwq = (lane >> 2) * (3 * DM * 2) + p4 * 16;
      auto piece = [&](int s, int i) {
        const unsigned buf = lds0 + (s % 3) * STG;
        const int kc = (s + rot) & 7;
        if (i < 4) {
          const unsigned so = (128 * r + 32 * m + 16 * (i >> 1)) * 1024 + kc * 128;
          if (i & 1) dma16_b(sx, vx1, so); else dma16_a(sx, vx0, so, buf + m * 4096 + (i >> 1) * 2048);
        } else if (i < 7) dma16(swq, vwq, (64 * kc + 16 * m) * (3 * DM * 2) + (512 * (i - 4) + 32 * j) * 2, buf + 16384 + ((i - 4) * 4 + m) * 1024);
        else w1dma(0, s, buf + 28672, i - 7);
      };
#pragma unroll
      for (int i = 0; i < 18; ++i) { piece(i / 9, i % 9); if (i == 8) __builtin_amdgcn_s_barrier(); }
      __builtin_amdgcn_s_setprio(PRIO_G1);
      hsetup(0);
      f32x4 cs[2] = {};
      v4u xf[4], wf[4][2];
      hac[0] = (f32x16){}; hac[1] = (f32x16){};
      static_for<0, 8>([&](auto sc) {
        constexpr int s = sc.value;
        constexpr unsigned buf = (s % 3) * STG;
        auto fill = [&](int i) {
          if constexpr (s < 6) piece(s + 2, i);
          if constexpr (s == 6) { if (i == 0) { stat_load(); bias_load(0); } else wdmah((i - 1) >> 1, (i - 1) & 1); }
          if constexpr (s == 7) { if (heavy) { w1dma(1, i, lds0 + L_WA + i * 8192, 0); w1dma(1, i, 0, 1); } }
          SBAR();
        };
        auto rd = [&](int p) { xf[p] = lds128(buf + xo[p]); wf[p][0] = ldtr2(buf + rk[p][0], 512); wf[p][1] = ldtr2(buf + rk[p][1], 512); SBAR(); };
        if constexpr (s < 7) wait_vm<9>(); else { wait_vm1<8>(st); PIN(bias0); PIN(bias1); }
        bar();
        STAMP_AT(10 + s);
        rd(0);
        if constexpr (s < 7) fill(0);
        rd(1);
        if constexpr (s < 7) fill(1);
        if constexpr (s == 2) get_tag();
#pragma unroll
        for (int p = 0; p < 4; ++p) {
#pragma unroll
          for (int t = 0; t < 2; ++t) {
            hac[t] = MFMA32(wf[p][t], xf[p], hac[t]);
            if (p == 0) { cs[t] = MFMA16(sel, wf[p][t], cs[t]); PIN(cs[t]); }
            SBAR();
            if (p < 2 && t == 0) rd(p + 2);
            if (s == 7) fill(2 * p + t); else if (2 * p + t < 7) fill(2 + 2 * p + t);
          }
          if (s == 7 && p == 1) {
            cswr(0, cs[0]); cswr(1, cs[1]);
            bar();
            STAMP_AT(1);
          }
        }
        if constexpr (s == 7) {
          const f32x4 c0 = csrd(0), c1 = csrd(1);
          row_stats(); make_xaug();
          hac[0] = MFMA32(waug(c0, bias0), xaug, hac[0]);
          hac[1] = MFMA32(waug(c1, bias1), xaug, hac[1]);
          SBAR();
        }
      });
    }
    STAMP_AT(18);
    if (heavy) { wait_vm<8>(); arrive_nowait(lds0 + L_CNT + 16); }
    hgelu(std::integral_constant<int, 0>{}, [&]() {
      wait_vm<2>();
      if (heavy) arrive_nowait(lds0 + L_CNT + 20);
    });
    __builtin_amdgcn_s_setprio(0);
    STAMP_AT(19);
    unsigned rb2[2];
#pragma unroll
    for (int c = 0; c < 2; ++c) rb2[c] = lds0 + L_WG + m * PART + h * 512 + q4 * 128 + ((c ^ (q4 >> 1)) * 64) + (g & 1) * 32 + p4 * 8;
    v4u wq[36][2];
    auto wrd = [&](int kk) {
#pragma unroll
      for (int c = 0; c < 2; ++c) wq[kk][c] = ldtr2(rb2[c] + (kk % RD) * 2048, 1024);
    };
    auto wgot = [&](int kk) { PIN(wq[kk][0]); PIN(wq[kk][1]); };
    const int ftile = 2 * rt + ((lane >> 2) & 1);
    v4u af[28][2], of[8][2];
    v2u fl;
    const unsigned a0 = WS_AF + (2 * rt * AFT + 28 * m) * 1024, o0 = WS_AF + (2 * rt * AFT + 112 + 8 * m) * 1024;
    for (int kk = 0; kk < 4; ++kk) wrd(kk);
    for (int kk = 4; kk < 8; ++kk) wdma(kk);
    for (int kk = 0; kk < 4; ++kk) wgot(kk);
    wait_vm<8>();
    if (lane == 0) __hip_atomic_store(flag(FL_H0, T, j), tag, RLX_AGENT);
    STAMP_AT(2);
    for (int kk = 8; kk < 12; ++kk) wdma(kk);
    static_for<1, 3>([&](auto ic) {
      constexpr int i = ic.value;
      wait_vm<(i == 1) ? 9 : 8>();
      for (int kk = 4 * i; kk < 4 * i + 4; ++kk) wrd(kk);
      for (int kk = 4 * i; kk < 4 * i + 4; ++kk) wgot(kk);
      if constexpr (i == 2) fl = ld8_sc1_async(sws, flag_off(FL_H0, ftile, 4 * m + p4));
      for (int kk = 4 * i; kk < 4 * i + 4; ++kk) wdma(kk + RD);
    });
    STAMP_AT(3);
    wait_vmf<8>(fl);
    if (!__all(fl[0] == taglo && fl[1] == tag32)) poll_flags(flag(FL_H0, ftile, 4 * m + p4), tag);
    STAMP_AT(4);
#pragma unroll
    for (int kk = 0; kk < AW; ++kk) {
      af[kk][0] = ld16_sc1_async(sws, lane * 16, a0 + kmap(kk) * 1024);
      af[kk][1] = ld16_sc1_async(sws, lane * 16, a0 + (AFT + kmap(kk)) * 1024);
    }
    __builtin_amdgcn_s_setprio(PRIO_G2);
    f32x16 c2[2][2] = {};
    auto mm2 = [&](int kk, const v4u (&a)[2], auto&& fill) {
      SBAR();
#pragma unroll
      for (int i = 0; i < 4; ++i) { c2[i >> 1][i & 1] = MFMA32(wq[kk][i & 1], a[i >> 1], c2[i >> 1][i & 1]); SBAR(); fill(i); SBAR(); }
    };
    static_for<0, 28>([&](auto kc) {
      constexpr int kk = kc.value;
      wait_vm2<(kk < AW) ? 2 * (AW - 1 - kk) + g2_range(0, kk) : g2_range(kk - AW + 1, kk)>(af[kk][0], af[kk][1]);
      if constexpr (kk == H1_CK) {
        STAMP_AT(20);
        wait_vmf<g2_range(H1_LD, H1_CK) - 1>(fl);
        if (!__all(fl[0] == taglo && fl[1] == tag32)) poll_flags(flag(FL_H1, ftile, 4 * m + p4), tag);
        STAMP_AT(21);
      }
      if constexpr (kk == H1_LD) fl = ld8_sc1_async(sws, flag_off(FL_H1, ftile, 4 * m + p4));
      if constexpr (kk == O_LD) fl = ld8_sc1_async(sws, flag_off(FL_O0, ftile, 4 * m + p4));
      constexpr bool dw = kk >= 1 && kk - 1 + NR + RD < 36;
      if constexpr (dw) wgot(kk - 1 + NR);
      mm2(kk, af[kk], [&](int i) {
        if constexpr (dw) { if (i < 2) wdmah(kk - 1 + NR + RD, i); }
        if constexpr (kk + NR < 28) { if (i == 1) wrd(kk + NR); }
        if constexpr (kk == 27) { if (i == 0) wrd(28); }
        if constexpr (kk + AW < 28) { if (i >= 2) af[kk + AW][i - 2] = ld16_sc1_async(sws, lane * 16, a0 + (AFT * (i - 2) + kmap(kk + AW)) * 1024); }
      });
      if constexpr (kk == O_CK) {
        wait_vmf<g2_range(O_LD, O_CK) - 1>(fl);
        if (!__all(fl[0] == taglo && fl[1] == tag32)) poll_flags(flag(FL_O0, ftile, 4 * m + p4), tag);
        res_loads();
#pragma unroll
        for (int i = 0; i < 4; ++i) {
          of[i][0] = ld16_sc1_async(sws, lane * 16, o0 + omap(i) * 1024);
          of[i][1] = ld16_sc1_async(sws, lane * 16, o0 + (AFT + omap(i)) * 1024);
        }
      }
    });
    static_for<0, 8>([&](auto kc) {
      constexpr int kk = kc.value;
      if (kk < 7) wrd(28 + kk + 1);
      if constexpr (kk == 1) {
        STAMP_AT(5);
        poll_flags(flag(FL_O1, ftile, 4 * m + p4), tag);
        STAMP_AT(6);
#pragma unroll
        for (int i = 4; i < 8; ++i) {
          of[i][0] = ld16_sc1_async(sws, lane * 16, o0 + omap(i) * 1024);
          of[i][1] = ld16_sc1_async(sws, lane * 16, o0 + (AFT + omap(i)) * 1024);
        }
      }
      wait_vm2<(kk == 0) ? 6 : 14 - 2 * kk>(of[kk][0], of[kk][1]);
      mm2(28 + kk, of[kk], [&](int) {});
    });
    PIN(xres); PIN(bres);
    asm volatile("" ::: "memory");
#pragma unroll
    for (int mt = 0; mt < 2; ++mt)
#pragma unroll
      for (int c = 0; c < 2; ++c)
#pragma unroll
        for (int qd = 0; qd < 4; ++qd) {
          const f32x4 v = {c2[mt][c][4 * qd], c2[mt][c][4 * qd + 1], c2[mt][c][4 * qd + 2], c2[mt][c][4 * qd + 3]};
          sts128(lds0 + L_WG + m * PART + ((mt * 8 + c * 4 + qd) * 66 + lane) * 16, __builtin_bit_cast(v4u, v));
        }
    STAMP_AT(7);
  } else {
    __builtin_amdgcn_s_setprio(1);
    lane_setup();
    v4u xr[2];
    xr[0] = ld16_async(sx, lane * 16, (128 * r + 8 * j + 2 * m) * 1024); xr[1] = ld16_async(sx, lane * 16, (128 * r + 8 * j + 2 * m + 1) * 1024);
    unsigned warm;
    {
      u64 pc; asm volatile("s_getpc_b64 %0" : "=s"(pc));
      warm = ld2_async(make_srd((const void*)(pc & ~127ull), CODEWARM), lane < 3 ? lane * 128 : 0x40000000u, (idx * 4 + m) * 384);
    }
    __builtin_amdgcn_s_barrier();
    v2u ep = ld8_sc1_async(sws, m == 0 ? WS_EPOCH : 0x40000000u);
    float sval;
    const int wi = blockIdx.x * 4 + m;
    const u64 wbase = wi < 448 ? (u64)w1 + wi * 4096 : wi < 896 ? (u64)w2 + (wi - 448) * 4096 : (u64)wo + (wi - 896) * 4096;
    auto make_stats = [&]() {
      wait_vm2<1>(xr[0], xr[1]);
      PIN(warm);
      float piv[2], a[4];
#pragma unroll
      for (int u = 0; u < 2; ++u) {
        piv[u] = rdl(bflo(xr[u][0]), 0);
        float s1 = 0.f, s2 = 0.f;
#pragma unroll
        for (int i = 0; i < 8; ++i) { const float d = ((i & 1) ? bfhi(xr[u][i >> 1]) : bflo(xr[u][i >> 1])) - piv[u]; s1 += d; s2 = __builtin_fmaf(d, d, s2); }
        a[2 * u] = s1; a[2 * u + 1] = s2;
      }
#pragma unroll
      for (int i = 0; i < 4; ++i) a[i] += dpp<0xB1>(a[i]);
#pragma unroll
      for (int i = 0; i < 4; ++i) a[i] += dpp<0x4E>(a[i]);
      float t = p4 == 0 ? a[0] : p4 == 1 ? a[1] : p4 == 2 ? a[2] : a[3];
      t += dpp<0x114>(t); t += dpp<0x118>(t);
      const auto s16 = __builtin_amdgcn_permlane16_swap(asu(t), asu(t), false, false);
      t = asf(s16[0]) + asf(s16[1]);
      const auto s32 = __builtin_amdgcn_permlane32_swap(asu(t), asu(t), false, false);
      t = (asf(s32[0]) + asf(s32[1])) * (1.f / DM);
      const float e1 = dpp<0xA0>(t);
      sval = (p4 & 1) ? __builtin_fmaxf(__builtin_fmaf(-e1, e1, t), 0.f) + 1e-5f : t + (p4 ? piv[1] : piv[0]);
      PIN(sval);
    };
    const unsigned acnt = lds0 + L_CNT;
    const unsigned vpos = 2048 + (g & 1) * 512 + h * 256 + (lane & 15) * 16;
    v4u qf[2], am0, am1;
    u64 um[16];
    auto masks = [&]() {
      const int dq = r32 - 4 * h;
#pragma unroll
      for (int d = 0; d < 4; ++d) {
        const int row = 2 * (d & 1) + 8 * (d >> 1);
        am0[d] = row > dq ? 0xffffffffu : row == dq ? 0xffff0000u : 0u;
        am1[d] = row + 16 > dq ? 0xffffffffu : row + 16 == dq ? 0xffff0000u : 0u;
      }
      PIN(am0); PIN(am1);
      static_for<0, 16>([&](auto u) { um[u.value] = upper_mask(u.value); pin_s(um[u.value]); });
    };
    {
      v4u xf[32];
      f32x16 acc[3] = {};
      f32x4 cs[3] = {};
      v4u wf[4][3];
      unsigned wb[4];
#pragma unroll
      for (int p = 0; p < 4; ++p) wb[p] = 16384 + ((p + m) & 3) * 1024 + (8 * h + q4) * 64 + (g & 1) * 32 + p4 * 8;
      auto rd = [&](int ks, int t) {
        const unsigned buf = lds0 + ((ks >> 2) % 3) * STG;
        if (t == 0) xf[ks] = lds128(buf + xo[ks & 3]);
        wf[ks & 3][t] = ldtr2(buf + wb[ks & 3] + t * 4096, 256);
      };
      auto mm = [&](int ks, int t) {
        acc[t] = t == 2 ? MFMA32(xf[ks], wf[ks & 3][t], acc[t]) : MFMA32(wf[ks & 3][t], xf[ks], acc[t]);
        if ((ks & 3) == 0) { cs[t] = MFMA16(sel, wf[ks & 3][t], cs[t]); PIN(cs[t]); }
        SBAR();
      };
      make_stats();
      bar();
      STAMP_AT(10);
#pragma unroll
      for (int t = 0; t < 6; ++t) rd(t / 3, t % 3);
      SBAR();
      static_for<0, 8>([&](auto sc) {
        constexpr int s = sc.value;
#pragma unroll
        for (int p = 0; p < 2; ++p) {
#pragma unroll
          for (int t = 0; t < 3; ++t) { mm(4 * s + p, t); rd(4 * s + p + 2, t); SBAR(); }
          if (s == 7 && p == 0) { cswr(4, cs[0]); cswr(5, cs[1]); cswr(6, cs[2]); SBAR(); }
        }
        if constexpr (s == 0) {
          wait_vmf<0>(ep);
          if (m == 0 && lane < 10)
            *(LAS unsigned*)(size_t)(lds0 + L_CNT + lane * 4) = lane == 8 ? ep[0] * 0x9E3779B1u + 0x7F4A7C15u : lane == 9 ? ep[1] * 0x85EBCA77u + 0x165667B1u : 0u;
        }
        if constexpr (s == 1) {
          get_tag();
          if ((lane >> 2) == 3) {
            const v2u gr = {asu(sval), tag32};
            __builtin_amdgcn_raw_buffer_store_b64(gr, rws, WS_STATS + (128 * r + 8 * j + 2 * m) * 16 + p4 * 8, 0, 16);
          }
        }
        if constexpr (s == MIDWARM) {
          warm = ld2_async(make_srd((const void*)wbase, 4096), lane * 128, 0);
        }
        if constexpr (s == 5) stat_load();
        if constexpr (s == 6) {
          wait_vm1<0>(st);
          PIN(warm);

          row_stats();
        }
        mm(4 * s + 2, 0); mm(4 * s + 2, 1);
        bar();
        if constexpr (s < 7) {
          STAMP_AT(11 + s);
          mm(4 * s + 2, 2);
          rd(4 * s + 4, 0); rd(4 * s + 4, 1); rd(4 * s + 4, 2); SBAR();
#pragma unroll
          for (int t = 0; t < 3; ++t) { mm(4 * s + 3, t); rd(4 * s + 5, t); SBAR(); }
        }
      });
      STAMP_AT(1);
      __builtin_amdgcn_s_setprio(3);
      const f32x4 c5 = csrd(5), c6 = csrd(6), c4 = csrd(4);
      SBAR();
      mm(30, 2); mm(31, 1);
      make_xaug();
      SBAR();
      mm(31, 2); mm(31, 0);
      if (h == 0) *(LAS float*)(size_t)(lds0 + L_RS + (m * 32 + r32) * 4) = rstd;
      acc[1] = MFMA32(waug(c5, 0u), xaug, acc[1]);
      acc[2] = MFMA32(xaug, waug(c6, 0u), acc[2]);
      acc[0] = MFMA32(waug(c4, 0u), xaug, acc[0]);
      SBAR();
      f32x4 rs[4];
#pragma unroll
      for (int qd = 0; qd < 4; ++qd) rs[qd] = __builtin_bit_cast(f32x4, lds128(lds0 + L_RS + (m * 32 + 8 * qd + 4 * h) * 4));
      const unsigned kvo = WS_KV + (j * 64 + T) * 4096;
#pragma unroll
      for (int e = 0; e < 16; ++e) acc[1][e] = MUL1(acc[1][e], rstd);
#pragma unroll
      for (int e = 0; e < 2; ++e) {
        const v4u f = pack8(acc[1], e);
        st16_sc1(f, sws, lane * 16, kvo + e * 1024);
        sts128(lds0 + L_KV + (8 + m) * KVT + e * 1024 + lane * 16, f);
      }
#pragma unroll
      for (int qd = 0; qd < 4; ++qd)
#pragma unroll
        for (int e = 0; e < 4; ++e) acc[2][4 * qd + e] = MUL1(acc[2][4 * qd + e], rs[qd][e]);
#pragma unroll
      for (int s = 0; s < 2; ++s) {
        const v4u f = pack8(acc[2], s);
        st16_sc1(f, sws, lane * 16, kvo + 2048 + s * 1024);
        sts128(lds0 + L_KV + (8 + m) * KVT + s * VB + vpos, f);
      }
      SBAR();
      const float qs = rstd * (0.25f * 1.4426950408889634f);
#pragma unroll
      for (int e = 0; e < 16; ++e) acc[0][e] = MUL1(acc[0][e], qs);
      qf[0] = pack8(acc[0], 0); qf[1] = pack8(acc[0], 1);
      PIN(qf[0]); PIN(qf[1]);
      STAMP_AT(2);
      const int T0 = 4 * r - 8 + 2 * m;
      const bool live = T0 >= 0;
      auto kv_load = [&](v4u (&kv)[8], int i0, int i1) {
        const unsigned src = WS_KV + (j * 64 + (live ? T0 : 0)) * 4096, vo = live ? lane * 16 : h * 512;
#pragma unroll
        for (int i = i0; i < i1; ++i) kv[i] = ld16_sc1_async(sws, vo, src + (live ? i : (i & 3)) * 1024);
      };
      auto kv_stage = [&](v4u (&kv)[8]) {
        const unsigned on = (lane < 2 || live) ? one2 : 0u;
        if (lane < 6) sts128(lds0 + L_KV + (lane < 2 ? 8 + m : 2 * m + ((lane - 2) >> 1)) * KVT + 2048 + (lane & 1) * VB + 1024, (v4u){on, on, on, on});
        const v4u zz = {};
#pragma unroll
        for (int i = 0; i < 2; ++i) {
          sts128(lds0 + L_KV + (2 * m + i) * KVT + lane * 16, kv[4 * i]);
          sts128(lds0 + L_KV + (2 * m + i) * KVT + 1024 + lane * 16, kv[4 * i + 1]);
          sts128(lds0 + L_KV + (2 * m + i) * KVT + vpos, live ? kv[4 * i + 2] : zz);
          sts128(lds0 + L_KV + (2 * m + i) * KVT + VB + vpos, live ? kv[4 * i + 3] : zz);
        }
        arrive_nowait(acnt);
      };
      auto kv_wait = [&](v4u (&kv)[8]) {
        wait_vm<0>();
#pragma unroll
        for (int i = 0; i < 8; ++i) PIN(kv[i]);
      };
      if (heavy) {
        hsetup(1);
        await(acnt + 16, 4);
        STAMP_AT(3);
        v2u fl;
        v4u kv[8];
        hmm(std::integral_constant<int, 1>{}, xf, [&](auto kc) {
          constexpr int ks = kc.value;
          if constexpr (ks == KVPUB) {
            wait_vm<0>();
            if (lane == 0) __hip_atomic_store(flag(FL_KV, T, j), tag, RLX_AGENT);
          }
          if constexpr (ks == KVPUB + 2) bias_load(1);
          if constexpr (ks == 31) { wait_vm<1>(); PIN(bias0); PIN(bias1); }
          if constexpr (ks == 24) fl = ld8_sc1_async(sws, flag_off(FL_KV, live ? T0 + (lane & 1) : 0, j));
        });
        STAMP_AT(18);
        wait_vmf<0>(fl);
        if (!__all(fl[0] == taglo && fl[1] == tag32)) poll_flags(flag(FL_KV, live ? T0 + (lane & 1) : 0, j), tag);
        kv_load(kv, 0, 8);
        hgelu(std::integral_constant<int, 1>{}, [&]() {});
        STAMP_AT(4);
        masks();
        wait_vm<4>();
#pragma unroll
        for (int i = 0; i < 8; ++i) PIN(kv[i]);
        kv_stage(kv);
      } else {
        wait_vm<0>();
        if (lane == 0) __hip_atomic_store(flag(FL_KV, T, j), tag, RLX_AGENT);
        v4u kv[8];
        poll_flags(flag(FL_KV, live ? T0 + (lane & 1) : 0, j), tag);
        kv_load(kv, 0, 8); masks(); kv_wait(kv); kv_stage(kv);
      }
    }
    STAMP_AT(19);
    const unsigned kb = lds0 + L_KV + m * KVT + lane * 16;
    unsigned vb[2];
#pragma unroll
    for (int e = 0; e < 2; ++e) { vb[e] = lds0 + L_KV + m * KVT + 2048 + ((g & 1) ? 1024 : e * 512 + h * 256 + (lane & 15) * 16); PIN(vb[e]); }
    await(acnt, heavy ? 8 : 4);
    __builtin_amdgcn_s_setprio(PRIO_ATT);
    STAMP_AT(5);
    const f32x16 zero = {};
    auto o_store = [&](f32x16& o, int e) {
      const float rl = __builtin_amdgcn_rcpf(o[8]);
#pragma unroll
      for (int u = 0; u < 8; ++u) o[u] = MUL1(o[u], rl);
      st16_sc1(pack8(o, 0), sws, lane * 16, WS_AF + (T * AFT + 112 + 2 * j + e) * 1024);
    };
    int redo = 0;
    {
      f32x16 o[2], sa, sb, su;
      bool pend = false;
      v4u kn, ku, va, vc, vn0, vn1, vu0, vu1, p0, p1, pu;
      auto kaddr = [&](int n) { return kb + ((n & 7) + 1) * KVT + (n >> 3) * 1024; };
      auto vaddr = [&](int n) { return vb[n >> 3] + ((n & 7) + 1) * KVT; };
      kn = lds128(kaddr(0)); vn0 = lds128(vaddr(0)); vn1 = lds128(vaddr(0) + VB);
      sa = MFMA32(kn, qf[0], zero);
      kn = lds128(kaddr(1));
      SBAR();
      auto step = [&](auto nc, f32x16& sc, f32x16& sn) {
        constexpr int n = nc.value, e = n >> 3, i = n & 7;
        if constexpr (n < 15) sn = MFMA32(kn, qf[(n + 1) >> 3], zero);
        if constexpr (n < 14) kn = lds128(kaddr(n + 2));
        if constexpr (i == 5) ku = lds128(kb + e * 1024);
        v4u v0 = vn0, v1 = vn1;
        if constexpr (n < 15) { vn0 = lds128(vaddr(n + 1)); vn1 = lds128(vaddr(n + 1) + VB); }
        if constexpr (i == 6) { vu0 = lds128(vb[e]); vu1 = lds128(vb[e] + VB); }
        SBAR();
        if constexpr (i == 7) {
#pragma unroll
          for (int u = 0; u < 16; ++u) sc[u] = __builtin_amdgcn_inverse_ballot_w64(um[u]) ? su[u] : sc[u];
          PIN(sc); SBAR();
        }
#pragma unroll
        for (int u = 0; u < 6; ++u) sc[u] = EXP2(sc[u]);
        PIN(sc); SBAR();
        if constexpr (n == 8) { o[0] = MFMA32(vc, p1, o[0]); SBAR(); }
        else if constexpr (n > 0) { o[e] = MFMA32(vc, p1, o[e]); SBAR(); }
        sc[6] = EXP2(sc[6]); sc[7] = EXP2(sc[7]);
        p0 = pack8(sc, 0);
        sc[8] = EXP2(sc[8]); sc[9] = EXP2(sc[9]);
        if constexpr (i == 7) { va = p0 & am0; p0 &= ~am0; PIN(va); }
        PIN(sc); PIN(p0); SBAR();
        if constexpr (n == 8) { o[0] = MFMA32(vu1, pu, o[0]); SBAR(); }
        o[e] = MFMA32(v0, p0, i == 0 ? zero : o[e]);
        SBAR();
        if constexpr (i == 6) { su = MFMA32(ku, qf[e], zero); SBAR(); }
#pragma unroll
        for (int u = 10; u < 13; ++u) sc[u] = EXP2(sc[u]);
        if constexpr (i == 7) { PIN(sc); SBAR(); o[e] = MFMA32(vu0, va, o[e]); SBAR(); }
#pragma unroll
        for (int u = 13; u < 16; ++u) sc[u] = EXP2(sc[u]);
        p1 = pack8(sc, 1); vc = v1;
        if constexpr (i == 7) { pu = p1 & am1; p1 &= ~am1; PIN(pu); }
        PIN(p1); SBAR();
        if constexpr (n == 8) {
          if (__all(o[0][8] >= 0x1p-30f && o[0][8] <= 0x1p30f)) o_store(o[0], 0); else redo = 1;
          SBAR();
        }
        if constexpr (i == 2) pend = !redo;
        if constexpr (i == 3 || i == 5 || i == 7) {
          if (pend && (i == 7 || (__builtin_amdgcn_s_getreg(7 | (3 << 11)) & 15) == 0)) {
            wait_vm<0>();
            if (lane == 0) __hip_atomic_store(flag(FL_H1 + e, T, j), tag, RLX_AGENT);
            pend = false;
          }
        }
      };
      static_for<0, 8>([&](auto hc) {
        step(std::integral_constant<int, 2 * hc.value>{}, sa, sb);
        step(std::integral_constant<int, 2 * hc.value + 1>{}, sb, sa);
      });
      o[1] = MFMA32(vc, p1, o[1]);
      o[1] = MFMA32(vu1, pu, o[1]);
      if (__all(o[1][8] >= 0x1p-30f && o[1][8] <= 0x1p30f)) o_store(o[1], 1); else redo |= 2;
    }
    if (redo) {
#pragma unroll 1
      for (int e = 0; e < 2; ++e) {
        if (!((redo >> e) & 1)) continue;
        const v4u q = e ? qf[1] : qf[0];
        const unsigned vbe = e ? vb[1] : vb[0];
        auto merged = [&](const f32x16& c) {
          f32x16 sl = MFMA32(lds128(kb + 8 * KVT + e * 1024), q, c);
          const f32x16 su = MFMA32(lds128(kb + e * 1024), q, c);
          static_for<0, 16>([&](auto u) { sl[u.value] = upper<u.value>() ? su[u.value] : sl[u.value]; });
          return sl;
        };
        float mx = -INFINITY;
#pragma unroll 1
        for (int i = 0; i < 8; ++i) {
          const f32x16 sc = i ? MFMA32(lds128(kb + i * KVT + e * 1024), q, zero) : merged(zero);
#pragma unroll
          for (int u = 0; u < 16; u += 2) mx = max3(mx, sc[u], sc[u + 1]);
        }
        const auto sw = __builtin_amdgcn_permlane32_swap(asu(mx), asu(mx), false, false);
        mx = __builtin_fmaxf(asf(sw[0]), asf(sw[1]));
        f32x16 negm, o;
#pragma unroll
        for (int u = 0; u < 16; ++u) negm[u] = -mx;
        {
          f32x16 sc = merged(negm);
#pragma unroll
          for (int u = 0; u < 16; ++u) sc[u] = EXP2(sc[u]);
          const v4u p0 = pack8(sc, 0), p1 = pack8(sc, 1);
          o = MFMA32(lds128(vbe), p0 & am0, zero);
          o = MFMA32(lds128(vbe + VB), p1 & am1, o);
          o = MFMA32(lds128(vbe + 8 * KVT), p0 & ~am0, o);
          o = MFMA32(lds128(vbe + 8 * KVT + VB), p1 & ~am1, o);
        }
#pragma unroll 1
        for (int i = 1; i < 8; ++i) {
          f32x16 sc = MFMA32(lds128(kb + i * KVT + e * 1024), q, negm);
#pragma unroll
          for (int u = 0; u < 16; ++u) sc[u] = EXP2(sc[u]);
          o = MFMA32(lds128(vbe + i * KVT), pack8(sc, 0), o);
          o = MFMA32(lds128(vbe + i * KVT + VB), pack8(sc, 1), o);
        }
        o_store(o, e);
      }
      wait_vm<0>();
      if (lane == 0) __hip_atomic_store(flag(FL_O0, T, j), tag, RLX_AGENT);
    }
    STAMP_AT(6);
    wait_vm<0>();
    if (lane == 0) __hip_atomic_store(flag(FL_O1, T, j), tag, RLX_AGENT);
    STAMP_AT(7);
    res_loads();
    wait_vm2<0>(xres, bres);
    if (blockIdx.x == 0 && wave == 7) {
#pragma unroll 1
      for (int i = 0; i < 4; ++i) poll_flags(flag(FL_KV, 4 * (4 * i + g), lane & 15), tag);
      if (lane == 0) __hip_atomic_store((GAS u64*)(ws + WS_EPOCH), tag, RLX_AGENT);
    }
  }
  bar();
  {
    int t2 = threadIdx.x; PIN(t2);
    const int row = t2 >> 3, ch = t2 & 7, orow = 64 * rt + row, ocol = 64 * ct + ch * 8;
    const unsigned a = lds0 + L_WG + (((row >> 5) * 8 + ch) * 66 + (row & 31)) * 16;
    f32x4 lo = {}, hi = {};
#pragma unroll
    for (int w = 0; w < 4; ++w) {
      lo += __builtin_bit_cast(f32x4, lds128(a + w * PART));
      hi += __builtin_bit_cast(f32x4, lds128(a + w * PART + 512));
    }
    v4u o;
    o[0] = pk2(lo[0] + bflo(xres[0]) + bflo(bres[0]), lo[1] + bfhi(xres[0]) + bfhi(bres[0]));
    o[1] = pk2(lo[2] + bflo(xres[1]) + bflo(bres[1]), lo[3] + bfhi(xres[1]) + bfhi(bres[1]));
    o[2] = pk2(hi[0] + bflo(xres[2]) + bflo(bres[2]), hi[1] + bfhi(xres[2]) + bfhi(bres[2]));
    o[3] = pk2(hi[2] + bflo(xres[3]) + bflo(bres[3]), hi[3] + bfhi(xres[3]) + bfhi(bres[3]));
    __builtin_amdgcn_raw_buffer_store_b128(o, __builtin_amdgcn_make_buffer_rsrc(out, 0, SEQ * DM * 2, 0x00020000), orow * 1024 + ocol * 2, 0, 16);
  }
  STAMP_AT(8);
}

extern "C" void kernel_launch(void* const* d_in, const int* in_sizes, int n_in, void* d_out, int out_size, void* d_ws, size_t ws_size,
                              hipStream_t stream) {
  (void)in_sizes; (void)n_in; (void)out_size; (void)ws_size;
  (void)hipFuncSetAttribute((const void*)fused_block, hipFuncAttributeMaxDynamicSharedMemorySize, LDS_BYTES);
  fused_block<<<256, 512, LDS_BYTES, stream>>>(d_in[0], d_in[1], d_in[2], d_in[4], d_in[5], d_in[6], d_in[7], d_out, (char*)d_ws);
}
```

```cpp
#include <hip/hip_runtime.h>
#include <hip/hip_bf16.h>
#include <stdint.h>
#include <type_traits>

typedef unsigned v4u __attribute__((ext_vector_type(4)));
typedef unsigned v2u __attribute__((ext_vector_type(2)));
typedef float f32x2 __attribute__((ext_vector_type(2)));
typedef float f32x4 __attribute__((ext_vector_type(4)));
typedef float f32x16 __attribute__((ext_vector_type(16)));
typedef short bf16x8 __attribute__((ext_vector_type(8)));
typedef short s16x4 __attribute__((ext_vector_type(4)));
typedef __bf16 bf16x2_t __attribute__((ext_vector_type(2)));
typedef unsigned long long u64;
#define GAS __attribute__((address_space(1)))
#define LAS __attribute__((address_space(3)))
#define DI __device__ __forceinline__
#define RLX_AGENT __ATOMIC_RELAXED, __HIP_MEMORY_SCOPE_AGENT

constexpr int SEQ = 2048, DM = 512, FF = 1792;
constexpr unsigned WS_EPOCH = 0;
constexpr unsigned WS_STATS = 32768;
constexpr unsigned WS_FLAG = 65536;
constexpr unsigned WS_KV = WS_FLAG + 5 * 1024 * 128;
constexpr int AFT = 145;
constexpr unsigned WS_AF = WS_KV + 16 * 64 * 4096;
constexpr unsigned WS_END = WS_AF + 64 * AFT * 1024;
constexpr unsigned WS_STAMP = WS_END;
enum { FL_KV = 0, FL_H0 = 1, FL_H1 = 2, FL_O0 = 3, FL_O1 = 4 };
constexpr int L_WA = 0;
constexpr int L_WG = 86016;
constexpr int VB = 1040;
constexpr int KVT = 2048 + 2 * VB;
constexpr int L_KV = 65536 - 8 * KVT;
constexpr int RD = 8, NR = 12;
constexpr int PART = 16 * 66 * 16;
constexpr int L_CS = L_WG + 4 * PART;
constexpr int L_RS = L_CS + 3584;
constexpr int L_CNT = L_RS + 512;
constexpr int LDS_BYTES = L_CNT + 64;
constexpr int xbuf(int s) { return ((s + 1) % 3) * 16384; }
constexpr int qbuf(int s) { return 49152 + (s % 3) * 12288; }
constexpr int w1buf(int s) { return L_WG + (s % 3) * PART + 8192; }
static_assert(xbuf(7) == 32768 && qbuf(2) + 12288 <= L_WG && L_KV + 12 * KVT <= L_WG && PART >= RD * 2048 && LDS_BYTES <= 163840, "lds map");
#define AWIN 6
constexpr int AW = AWIN;
constexpr unsigned SPIN_CAP = 1u << 23;

#define MFMA32(a, b, c) __builtin_amdgcn_mfma_f32_32x32x16_bf16(__builtin_bit_cast(bf16x8, a), __builtin_bit_cast(bf16x8, b), c, 0, 0, 0)
#define MFMA16(a, b, c) __builtin_amdgcn_mfma_f32_16x16x32_bf16(__builtin_bit_cast(bf16x8, a), __builtin_bit_cast(bf16x8, b), c, 0, 0, 0)
#define SBAR() __builtin_amdgcn_sched_barrier(0)

#define STAMP_AT(i) do { } while (0)

DI float asf(unsigned u) { return __builtin_bit_cast(float, u); }
DI unsigned asu(float f) { return __builtin_bit_cast(unsigned, f); }
DI unsigned pk2(float lo, float hi) { return __builtin_bit_cast(unsigned, __builtin_convertvector((f32x2){lo, hi}, bf16x2_t)); }
DI float bflo(unsigned u) { return __builtin_bit_cast(float, u << 16); }
DI float bfhi(unsigned u) { return __builtin_bit_cast(float, u & 0xffff0000u); }
DI v4u make_srd(const void* p, unsigned bytes) { const u64 a = (u64)p; return (v4u){(unsigned)a, (unsigned)(a >> 32) & 0xffffu, bytes, 0x00020000u}; }
DI void dma16(v4u srd, unsigned voff, unsigned soff, unsigned lds) {
  asm volatile("s_mov_b32 m0, %3\n\ts_nop 0\n\tbuffer_load_dwordx4 %0, %1, %2 offen lds" :: "v"(voff), "s"(srd), "s"(soff), "s"(lds) : "memory");
}
DI void dma16_a(v4u srd, unsigned voff, unsigned soff, unsigned lds) { dma16(srd, voff, soff, lds); }
DI void dma16_b(v4u srd, unsigned voff1, unsigned soff) {
  asm volatile("buffer_load_dwordx4 %0, %1, %2 offen offset:1024 lds" :: "v"(voff1), "s"(srd), "s"(soff) : "memory");
}
DI v4u ld16_sc1_async(v4u srd, unsigned voff, unsigned soff) {
  v4u r; asm volatile("buffer_load_dwordx4 %0, %1, %2, %3 offen sc1" : "=v"(r) : "v"(voff), "s"(srd), "s"(soff) : "memory"); return r;
}
DI v4u ld16_async(v4u srd, unsigned voff, unsigned soff) {
  v4u r; asm volatile("buffer_load_dwordx4 %0, %1, %2, %3 offen" : "=v"(r) : "v"(voff), "s"(srd), "s"(soff) : "memory"); return r;
}
DI unsigned ld2_async(v4u srd, unsigned voff, unsigned soff) {
  unsigned r; asm volatile("buffer_load_ushort %0, %1, %2, %3 offen" : "=v"(r) : "v"(voff), "s"(srd), "s"(soff) : "memory"); return r;
}
DI void st16_sc1(v4u v, v4u srd, unsigned voff, unsigned soff) {
  asm volatile("buffer_store_dwordx4 %0, %1, %2, %3 offen sc1\n\ts_nop 1" :: "v"(v), "v"(voff), "s"(srd), "s"(soff) : "memory");
}
template <int N> DI void wait_vm1(v4u& a) { asm volatile("s_waitcnt vmcnt(%1)" : "+v"(a) : "i"(N) : "memory"); }
template <int N> DI void wait_vm2(v4u& a, v4u& b) { asm volatile("s_waitcnt vmcnt(%2)" : "+v"(a), "+v"(b) : "i"(N) : "memory"); }
template <int N> DI void wait_vm3(v4u& a, v4u& b, v4u& c) { asm volatile("s_waitcnt vmcnt(%3)" : "+v"(a), "+v"(b), "+v"(c) : "i"(N) : "memory"); }
DI void pin_s(u64& v) { asm volatile("" : "+s"(v)); }
template <class V> DI void pin(V& v) { asm volatile("" : "+v"(v)); }
template <int N> DI void wait_vm() { asm volatile("s_waitcnt vmcnt(%0)" :: "i"(N) : "memory"); }
DI void bar() { asm volatile("s_waitcnt lgkmcnt(0)\n\ts_barrier" ::: "memory"); }
DI v4u lds128(unsigned a) { return *(const LAS v4u*)(size_t)a; }
DI void sts128(unsigned a, v4u v) { *(LAS v4u*)(size_t)a = v; }
DI v2u ldtr(unsigned a) { return __builtin_bit_cast(v2u, __builtin_amdgcn_ds_read_tr16_b64_v4i16((LAS s16x4*)(size_t)a)); }
DI v4u ldtr2(unsigned a, int second) { const v2u lo = ldtr(a), hi = ldtr(a + second); return (v4u){lo.x, lo.y, hi.x, hi.y}; }
template <int CTRL> DI float dpp(float v) { return __builtin_bit_cast(float, __builtin_amdgcn_update_dpp(0, __builtin_bit_cast(int, v), CTRL, 0xf, 0xf, true)); }
DI float rdl(float v, int l) { return __builtin_bit_cast(float, __builtin_amdgcn_readlane(__builtin_bit_cast(int, v), l)); }
DI v4u pack8(const f32x16& a, int s) { return (v4u){pk2(a[8 * s], a[8 * s + 1]), pk2(a[8 * s + 2], a[8 * s + 3]), pk2(a[8 * s + 4], a[8 * s + 5]), pk2(a[8 * s + 6], a[8 * s + 7])}; }
DI void poll_flags(const GAS u64* p, u64 tag) {
#pragma unroll 1
  for (unsigned spin = 0; spin < SPIN_CAP; ++spin) {
    const u64 v = __hip_atomic_load(p, RLX_AGENT);
    if (__all(v == tag)) break;
    __builtin_amdgcn_s_sleep(2);
  }
  asm volatile("" ::: "memory");
}
constexpr u64 upper_mask(int reg) {
  u64 m = 0;
  for (int l = 0; l < 64; ++l) { const int row = (reg & 3) + 8 * (reg >> 2) + 4 * (l >> 5); if (row > (l & 31)) m |= 1ull << l; }
  return m;
}
template <int REG> DI bool upper() { return __builtin_amdgcn_inverse_ballot_w64(upper_mask(REG)); }
template <int I, int N, class Fn> DI void static_for(Fn&& f) { if constexpr (I < N) { f(std::integral_constant<int, I>{}); static_for<I + 1, N>(f); } }
DI float max3(float a, float b, float c) { return __builtin_fmaxf(__builtin_fmaxf(a, b), c); }

constexpr int H1_CK = 16 - AW, H1_LD = H1_CK - 3, O_CK = 28 - AW, O_LD = O_CK - 3;
constexpr int g2_issued(int t) { return ((t >= 1 && t - 1 + NR + RD < 36) ? 2 : 0) + ((t + AW < 28) ? 2 : 0) + (t == H1_LD) + (t == O_LD) + (t == O_CK ? 10 : 0); }
constexpr int g2_range(int a, int b) { int n = 0; for (int t = a; t < b; ++t) n += g2_issued(t); return n; }
#define GELU_PK(P) (P == 1)
#define PRIO_G2 2
#define PRIO_ATT 3
#define PRIO_G1 2
#define ROT r
#define CODEWARM 39000
#define KVPUB 6
#define EXP2(v) __builtin_amdgcn_exp2f(v)
#define PIN(v) pin(v)
extern "C" __device__ float mul_unpacked(float, float) __asm("llvm.amdgcn.fmul.legacy");
#define MUL1(a, b) mul_unpacked(a, b)
#define FMA1(a, b, c) __builtin_amdgcn_div_fmasf(a, b, c, false)
DI v2u ld8_sc1_async(v4u srd, unsigned voff) {
  v2u r; asm volatile("buffer_load_dwordx2 %0, %1, %2, 0 offen sc1" : "=v"(r) : "v"(voff), "s"(srd) : "memory"); return r;
}
template <int N> DI void wait_vmf(v2u& a) { asm volatile("s_waitcnt vmcnt(%1)" : "+v"(a) : "i"(N) : "memory"); }
DI void arrive(unsigned cnt, int lane) {
  asm volatile("s_waitcnt lgkmcnt(0)" ::: "memory");
  if (lane == 0) __hip_atomic_fetch_add((LAS unsigned*)(size_t)cnt, 1u, __ATOMIC_RELAXED, __HIP_MEMORY_SCOPE_WORKGROUP);
}
DI void arrive_nowait(unsigned cnt) {
  asm volatile("s_mov_b64 exec, 1\n\tds_add_u32 %0, %1\n\ts_mov_b64 exec, -1" :: "v"(cnt), "v"(1u) : "memory");
}
DI unsigned peek(unsigned cnt) { return __hip_atomic_load((LAS unsigned*)(size_t)cnt, __ATOMIC_RELAXED, __HIP_MEMORY_SCOPE_WORKGROUP); }
DI void await(unsigned cnt, unsigned n) {
#pragma unroll 1
  for (unsigned spin = 0; spin < SPIN_CAP * 32; ++spin) {
    if (__builtin_amdgcn_readfirstlane(peek(cnt)) >= n) break;
    __builtin_amdgcn_s_sleep(1);
  }
  asm volatile("" ::: "memory");
}

__global__ __launch_bounds__(512, 2) void fused_block(const void* __restrict__ x, const void* __restrict__ wqkv, const void* __restrict__ wo,
                                                      const void* __restrict__ w1, const void* __restrict__ b1, const void* __restrict__ w2,
                                                      const void* __restrict__ b2, void* __restrict__ out, char* __restrict__ ws) {
  constexpr unsigned lds0 = 0;
  const int wave = __builtin_amdgcn_readfirstlane(threadIdx.x >> 6);
  const int m = wave & 3;
  const bool isG = wave < 4;
  const int xcd = blockIdx.x & 7, idx = blockIdx.x >> 3;
  const int r = (xcd >> 2) * 8 + (idx >> 2), j = (xcd & 3) * 4 + (idx & 3);
  const int rt = (xcd >> 1) * 8 + (idx >> 2), ct = (xcd & 1) * 4 + (idx & 3);
  int lane = threadIdx.x & 63, r32, h, g, q4, p4;
  STAMP_AT(0);
  asm volatile("" :: "s"(x), "s"(wqkv), "s"(wo), "s"(w1), "s"(b1), "s"(w2), "s"(b2), "s"(out), "s"(ws));

  const v4u sx = make_srd(x, SEQ * DM * 2), sws = make_srd(ws, WS_END);
  const __amdgpu_buffer_rsrc_t rws = __builtin_amdgcn_make_buffer_rsrc(ws, 0, WS_END, 0x00020000);
  const unsigned one2 = 0x3f803f80u;
  const int T = 4 * r + m;
  const int rot = ROT;
  auto flag_off = [&](int kind, int tile, int jj) { return WS_FLAG + ((kind * 64 + tile) * 16 + jj) * 128; };
  auto flag = [&](int kind, int tile, int jj) { return (GAS u64*)(ws + flag_off(kind, tile, jj)); };
  u64 tag;
  unsigned taglo, tag32;
  v2u tg;
  auto ask_tag = [&]() { tg = *(const LAS v2u*)(size_t)(lds0 + L_CNT + 32); SBAR(); };
  auto get_tag = [&]() {
    taglo = __builtin_amdgcn_readfirstlane(tg[0]); tag32 = __builtin_amdgcn_readfirstlane(tg[1]);
    tag = ((u64)tag32 << 32) | taglo;
  };
  v4u xres, bres, xaug, st;
  float mean, var, rstd, sd;
  v4u sel;
  unsigned xo[4], swz;
  auto lane_setup = [&]() {
    lane = threadIdx.x & 63; PIN(lane);
    r32 = lane & 31; h = lane >> 5; g = lane >> 4; q4 = (lane >> 2) & 3; p4 = lane & 3;
    const unsigned selv = ((lane & 15) == (g & 1)) ? one2 : 0u;
    sel = (v4u){selv, selv, selv, selv};
#pragma unroll
    for (int p = 0; p < 4; ++p) xo[p] = (32 * m + r32) * 128 + (((2 * ((p + m) & 3) + h) ^ ((r32 >> 1) & 7)) * 16);
    swz = ((((lane >> 2) & 1) ^ (g & 1)) * 64) + p4 * 16;
  };

  auto stat_load = [&]() { st = ld16_sc1_async(sws, WS_STATS + (128 * r + 32 * m + r32) * 16, 0); };
  auto res_loads = [&]() {
    int t2 = threadIdx.x; PIN(t2);
    const int row = 64 * rt + (t2 >> 3), col = 64 * ct + (t2 & 7) * 8;
    xres = ld16_async(sx, row * 1024 + col * 2, 0);
    bres = ld16_async(make_srd(b2, DM * 2), col * 2, 0);
  };
  auto row_stats = [&]() {
    if (!__all(st[1] == tag32 && st[3] == tag32)) {
#pragma unroll 1
      for (unsigned spin = 0; spin < SPIN_CAP; ++spin) {
        st = ld16_sc1_async(sws, WS_STATS + (128 * r + 32 * m + r32) * 16, 0);
        wait_vm1<0>(st);
        if (__all(st[1] == tag32 && st[3] == tag32)) break;
      }
    }
    mean = asf(st[0]); var = asf(st[2]);
    PIN(mean); PIN(var);
  };
  auto make_xaug = [&]() {
    PIN(mean); PIN(var);
    rstd = __builtin_amdgcn_rsqf(var); sd = var * rstd;
    const unsigned hi = pk2(-mean, sd);
    const unsigned lo = pk2(-mean - bflo(hi), FMA1(bfhi(hi), -1.f, sd));
    xaug = (v4u){(hi & 0xffffu) | (hi << 16), (lo & 0xffffu) | (hi & 0xffff0000u), lo >> 16, 0u};
    if (h) xaug = (v4u){0u, 0u, 0u, 0u};
    PIN(xaug);
  };
  auto cswr = [&](int tile, f32x4 c) {
    if (lane < 16) { LAS float* p = (LAS float*)(size_t)(lds0 + L_CS + (tile * 32 + lane) * 16 + m * 4); p[0] = c[0]; p[64] = c[1]; }
  };
  auto csrd = [&](int tile) { return __builtin_bit_cast(f32x4, lds128(lds0 + L_CS + (tile * 32 + r32) * 16)); };
  auto waug = [&](f32x4 c4, unsigned b16) {
    const float c = ((c4[0] + c4[1]) + c4[2]) + c4[3];
    const unsigned hi = pk2(c, 0.f);
    const unsigned lo = pk2(c - bflo(hi), 0.f);
    return (v4u){hi | (lo << 16), hi | (b16 << 16), b16, 0u};
  };

  const v4u sw1 = make_srd(w1, DM * FF * 2);
  const int hcol0 = 448 * (j >> 2) + 128 * (j & 3);
  const bool heavy = (j & 3) != 3;
  unsigned bias0, bias1;
  auto bias_load = [&](int P) {
    bias0 = ld2_async(make_srd(b1, FF * 2), r32 * 2, (hcol0 + 64 * P) * 2);
    bias1 = ld2_async(make_srd(b1, FF * 2), r32 * 2, (hcol0 + 64 * P + 32) * 2);
  };
  auto w1dma = [&](int P, int c, unsigned dst, int half) {
    const unsigned vw1 = (lane >> 3) * (FF * 2) + swz, vw1b = vw1 + 8 * (FF * 2) - 1024;
    const unsigned so = (64 * ((c + rot) & 7) + 16 * m) * (FF * 2) + (hcol0 + 64 * P) * 2;
    if (half == 0) dma16_a(sw1, vw1, so, dst + m * 2048); else dma16_b(sw1, vw1b, so);
  };
  f32x16 hac[2];
  unsigned rk[4][2];
  auto hsetup = [&](int P) {
#pragma unroll
    for (int p = 0; p < 4; ++p)
#pragma unroll
      for (int c = 0; c < 2; ++c) {
        rk[p][c] = lds0 + (P ? L_WA : w1buf(0)) + ((p + m) & 3) * 2048 + h * 1024 + q4 * 128 + ((c ^ (q4 >> 1)) * 64) + (g & 1) * 32 + p4 * 8;
        PIN(rk[p][c]);
      }
  };
  auto hmm = [&](auto pc, const v4u (&xf)[32], auto&& hook) {
    constexpr int P = pc.value;
    constexpr int D = 4;
    const unsigned cnt = lds0 + L_CNT;
    f32x4 cs[2] = {}, c0, c1;
    v4u wf[D][2], au0, au1;
    unsigned seen = 0;
    hac[0] = (f32x16){}; hac[1] = (f32x16){};
    auto rd = [&](int ks, int t) { wf[ks % D][t] = ldtr2(rk[ks & 3][t] + (ks >> 2) * 8192, 512); };
#pragma unroll
    for (int ks = 0; ks < D; ++ks) { rd(ks, 0); rd(ks, 1); }
    SBAR();
    static_for<0, 32>([&](auto kc) {
      constexpr int ks = kc.value;
      hook(kc);
      if constexpr (ks == 10) seen = peek(cnt + 20);
      if constexpr (ks == 29) seen = peek(cnt);
#pragma unroll
      for (int t = 0; t < 2; ++t) {
        hac[t] = MFMA32(wf[ks % D][t], xf[ks], hac[t]);
        if ((ks & 3) == 0 && ks < 28) { cs[t] = MFMA16(sel, wf[ks % D][t], cs[t]); PIN(cs[t]); }
        if (ks == 26) { cs[t] = MFMA16(sel, wf[28 % D][t], cs[t]); PIN(cs[t]); }
        SBAR();
        if constexpr (ks + D < 32) rd(ks + D, t);
        if constexpr (ks == 31) { if (t == 0) { au0 = waug(c0, bias0); PIN(au0); } else { au1 = waug(c1, bias1); PIN(au1); } }
        SBAR();
      }
      if constexpr (ks == 11 || ks == 30) {
        PIN(hac[0]); PIN(hac[1]);
        if (__builtin_amdgcn_readfirstlane(seen) < 4) await(ks == 11 ? cnt + 20 : cnt, 4);
        asm volatile("" ::: "memory");
        if constexpr (ks == 30) { c0 = csrd(2 * P); c1 = csrd(2 * P + 1); }
      }
      if constexpr (ks == 27) { cswr(2 * P, cs[0]); cswr(2 * P + 1, cs[1]); arrive_nowait(cnt); }
      SBAR();
    });
    hac[0] = MFMA32(au0, xaug, hac[0]);
    hac[1] = MFMA32(au1, xaug, hac[1]);
    SBAR();
  };
  auto hgelu = [&](auto pc, auto&& mid) {
    constexpr int P = pc.value;
    constexpr bool PK = GELU_PK(P);
    const float C0 = 2.3022081986f, C1 = C0 * 0.044715f;
    const float ga = -C1 * rstd * rstd * rstd, gb = -C0 * rstd;
    auto mul = [&](f32x2 a, f32x2 b) { return PK ? a * b : (f32x2){MUL1(a[0], b[0]), MUL1(a[1], b[1])}; };
    auto fma = [&](f32x2 a, float b, float c) {
      return PK ? __builtin_elementwise_fma(a, (f32x2){b, b}, (f32x2){c, c}) : (f32x2){FMA1(a[0], b, c), FMA1(a[1], b, c)}; };
#pragma unroll
    for (int t = 0; t < 2; ++t) {
      f32x2 v[8], z[8];
#pragma unroll
      for (int e = 0; e < 8; ++e) { v[e] = (f32x2){hac[t][2 * e], hac[t][2 * e + 1]}; z[e] = mul(v[e], v[e]); }
      SBAR();
#pragma unroll
      for (int e = 0; e < 8; ++e) z[e] = fma(z[e], ga, gb);
      SBAR();
#pragma unroll
      for (int e = 0; e < 8; ++e) z[e] = mul(z[e], v[e]);
      SBAR();
#pragma unroll
      for (int e = 0; e < 8; ++e) z[e] = (f32x2){EXP2(z[e][0]), EXP2(z[e][1])};
      SBAR();
#pragma unroll
      for (int e = 0; e < 8; ++e) z[e] = fma(z[e], sd, sd);
      SBAR();
#pragma unroll
      for (int e = 0; e < 8; ++e) z[e] = (f32x2){__builtin_amdgcn_rcpf(z[e][0]), __builtin_amdgcn_rcpf(z[e][1])};
      SBAR();
#pragma unroll
      for (int e = 0; e < 8; ++e) z[e] = mul(z[e], v[e]);
      SBAR();
#pragma unroll
      for (int s = 0; s < 2; ++s) {
        const v4u f = {pk2(z[4 * s][0], z[4 * s][1]), pk2(z[4 * s + 1][0], z[4 * s + 1][1]), pk2(z[4 * s + 2][0], z[4 * s + 2][1]), pk2(z[4 * s + 3][0], z[4 * s + 3][1])};
        st16_sc1(f, sws, lane * 16, WS_AF + (T * AFT + (hcol0 >> 4) + 4 * P + 2 * t + s) * 1024);
      }
      if (t == 0) mid();
      SBAR();
    }
  };

  if (isG) {
    lane_setup();
    const v4u sw2 = make_srd(w2, FF * DM * 2), swo = make_srd(wo, DM * DM * 2);
    const unsigned vw2 = (lane >> 3) * 1024 + swz, vw2b = vw2 + 7 * 1024;
    const int ro3 = rt % 3;
    auto kmap = [&](int kk) {
      if (kk < 16) return 8 * (((kk >> 2) + rt) & 3) + (kk & 3);
      const int p = ((kk - 16) >> 2) + ro3;
      return 8 * (p >= 3 ? p - 3 : p) + 4 + (kk & 3);
    };
    auto omap = [&](int i) { return 2 * ((i + rt) & 3) + (i >> 2); };
    auto wdmah = [&](int kk, int half) {
      const unsigned so = (kk < 28 ? 448 * m + 16 * kmap(kk) : 128 * m + 16 * omap(kk - 28)) * 1024 + 128 * ct;
      if (half == 0) dma16_a(kk < 28 ? sw2 : swo, vw2, so, lds0 + L_WG + m * PART + (kk % RD) * 2048); else dma16_b(kk < 28 ? sw2 : swo, vw2b, so);
    };
    auto wdma = [&](int kk) { wdmah(kk, 0); wdmah(kk, 1); };
    {
      const v4u swq = make_srd(wqkv, DM * 3 * DM * 2);
      const unsigned vx0 = (lane >> 3) * 1024 + (((lane & 7) ^ g) * 16), vx1 = (7 + (lane >> 3)) * 1024 + (((lane & 7) ^ (4 + g)) * 16);
      const unsigned vwq = (lane >> 2) * (3 * DM * 2) + p4 * 16;
      auto piece = [&](int s, int i) {
        const int kc = (s + rot) & 7;
        if (i < 4) {
          const unsigned so = (128 * r + 32 * m + 16 * (i >> 1)) * 1024 + kc * 128;
          if (i & 1) dma16_b(sx, vx1, so); else dma16_a(sx, vx0, so, lds0 + xbuf(s) + m * 4096 + (i >> 1) * 2048);
        } else if (i < 7) dma16(swq, vwq, (64 * kc + 16 * m) * (3 * DM * 2) + (512 * (i - 4) + 32 * j) * 2, lds0 + qbuf(s) + ((i - 4) * 4 + m) * 1024);
        else w1dma(0, s, lds0 + w1buf(s), i - 7);
      };
      v2u ep;
      unsigned warm;
      const int wi = blockIdx.x * 4 + m;
      const u64 wbase = wi < 448 ? (u64)w1 + wi * 4096 : wi < 896 ? (u64)w2 + (wi - 448) * 4096 : (u64)wo + (wi - 896) * 4096;
#pragma unroll
      for (int i = 0; i < 18; ++i) {
        piece(i / 9, i % 9);
        if (i == 8) { __builtin_amdgcn_s_barrier(); ep = ld8_sc1_async(sws, m == 0 ? WS_EPOCH : 0x40000000u); }
      }
      __builtin_amdgcn_s_setprio(PRIO_G1);
      hsetup(0);
      f32x4 cs[2] = {};
      v4u xf[4], wf[4][2];
      hac[0] = (f32x16){}; hac[1] = (f32x16){};
      static_for<0, 8>([&](auto sc) {
        constexpr int s = sc.value;
        auto fill = [&](int i) {
          if constexpr (s < 6) piece(s + 2, i);
          if constexpr (s == 6) { if (i == 0) { stat_load(); bias_load(0); } else wdmah((i - 1) >> 1, (i - 1) & 1); }
          if constexpr (s == 7) { if (heavy) { w1dma(1, i, lds0 + L_WA + i * 8192, 0); w1dma(1, i, 0, 1); } }
          SBAR();
        };
        auto rd = [&](int p) {
          xf[p] = lds128(xbuf(s) + xo[p]);
          wf[p][0] = ldtr2(rk[p][0] + (s % 3) * PART, 512); wf[p][1] = ldtr2(rk[p][1] + (s % 3) * PART, 512);
          SBAR();
        };
        if constexpr (s == 0) wait_vmf<9>(ep);
        else if constexpr (s == 3) { wait_vm<9>(); PIN(warm); }
        else if constexpr (s < 7) wait_vm<(s == 2) ? 10 : 9>();
        else { wait_vm1<8>(st); PIN(bias0); PIN(bias1); }
        bar();
        STAMP_AT(10 + s);
        rd(0);
        if constexpr (s == 1) warm = ld2_async(make_srd((const void*)wbase, 4096), lane * 128, 0);
        if constexpr (s < 7) fill(0);
        rd(1);
        if constexpr (s < 7) fill(1);
        if constexpr (s == 2) ask_tag();
        if constexpr (s == 3) get_tag();
#pragma unroll
        for (int p = 0; p < 4; ++p) {
#pragma unroll
          for (int t = 0; t < 2; ++t) {
            hac[t] = MFMA32(wf[p][t], xf[p], hac[t]);
            if (p == 0) { cs[t] = MFMA16(sel, wf[p][t], cs[t]); PIN(cs[t]); }
            SBAR();
            if (p < 2 && t == 0) rd(p + 2);
            if (s == 7) fill(2 * p + t); else if (2 * p + t < 7) fill(2 + 2 * p + t);
          }
          if (s == 7 && p == 1) {
            cswr(0, cs[0]); cswr(1, cs[1]);
            bar();
            STAMP_AT(1);
          }
        }
        if constexpr (s == 0) {
          if (m == 0 && lane < 10)
            *(LAS unsigned*)(size_t)(lds0 + L_CNT + lane * 4) = lane == 8 ? ep[0] * 0x9E3779B1u + 0x7F4A7C15u : lane == 9 ? ep[1] * 0x85EBCA77u + 0x165667B1u : 0u;
        }
        if constexpr (s == 7) {
          const f32x4 c0 = csrd(0), c1 = csrd(1);
          row_stats(); make_xaug();
          hac[0] = MFMA32(waug(c0, bias0), xaug, hac[0]);
          hac[1] = MFMA32(waug(c1, bias1), xaug, hac[1]);
          SBAR();
        }
      });
    }
    STAMP_AT(18);
    if (heavy) { wait_vm<8>(); arrive_nowait(lds0 + L_CNT + 16); }
    hgelu(std::integral_constant<int, 0>{}, [&]() {
      wait_vm<2>();
      if (heavy) arrive_nowait(lds0 + L_CNT + 20);
    });
    __builtin_amdgcn_s_setprio(0);
    STAMP_AT(19);
    unsigned rb2[2];
#pragma unroll
    for (int c = 0; c < 2; ++c) rb2[c] = lds0 + L_WG + m * PART + h * 512 + q4 * 128 + ((c ^ (q4 >> 1)) * 64) + (g & 1) * 32 + p4 * 8;
    v4u wq[36][2];
    auto wrd = [&](int kk) {
#pragma unroll
      for (int c = 0; c < 2; ++c) wq[kk][c] = ldtr2(rb2[c] + (kk % RD) * 2048, 1024);
    };
    auto wgot = [&](int kk) { PIN(wq[kk][0]); PIN(wq[kk][1]); };
    const int ftile = 2 * rt + ((lane >> 2) & 1);
    v4u af[28][2], of[8][2];
    v2u fl;
    const unsigned a0 = WS_AF + (2 * rt * AFT + 28 * m) * 1024, o0 = WS_AF + (2 * rt * AFT + 112 + 8 * m) * 1024;
    for (int kk = 0; kk < 4; ++kk) wrd(kk);
    for (int kk = 4; kk < 8; ++kk) wdma(kk);
    for (int kk = 0; kk < 4; ++kk) wgot(kk);
    wait_vm<8>();
    if (lane == 0) __hip_atomic_store(flag(FL_H0, T, j), tag, RLX_AGENT);
    STAMP_AT(2);
    for (int kk = 8; kk < 12; ++kk) wdma(kk);
    static_for<1, 3>([&](auto ic) {
      constexpr int i = ic.value;
      wait_vm<(i == 1) ? 9 : 8>();
      for (int kk = 4 * i; kk < 4 * i + 4; ++kk) wrd(kk);
      for (int kk = 4 * i; kk < 4 * i + 4; ++kk) wgot(kk);
      if constexpr (i == 2) fl = ld8_sc1_async(sws, flag_off(FL_H0, ftile, 4 * m + p4));
      for (int kk = 4 * i; kk < 4 * i + 4; ++kk) wdma(kk + RD);
    });
    STAMP_AT(3);
    wait_vmf<8>(fl);
    if (!__all(fl[0] == taglo && fl[1] == tag32)) poll_flags(flag(FL_H0, ftile, 4 * m + p4), tag);
    STAMP_AT(4);
#pragma unroll
    for (int kk = 0; kk < AW; ++kk) {
      af[kk][0] = ld16_sc1_async(sws, lane * 16, a0 + kmap(kk) * 1024);
      af[kk][1] = ld16_sc1_async(sws, lane * 16, a0 + (AFT + kmap(kk)) * 1024);
    }
    __builtin_amdgcn_s_setprio(PRIO_G2);
    f32x16 c2[2][2] = {};
    auto mm2 = [&](int kk, const v4u (&a)[2], auto&& fill) {
      SBAR();
#pragma unroll
      for (int i = 0; i < 4; ++i) { c2[i >> 1][i & 1] = MFMA32(wq[kk][i & 1], a[i >> 1], c2[i >> 1][i & 1]); SBAR(); fill(i); SBAR(); }
    };
    static_for<0, 28>([&](auto kc) {
      constexpr int kk = kc.value;
      wait_vm2<(kk < AW) ? 2 * (AW - 1 - kk) + g2_range(0, kk) : g2_range(kk - AW + 1, kk)>(af[kk][0], af[kk][1]);
      if constexpr (kk == H1_CK) {
        STAMP_AT(20);
        wait_vmf<g2_range(H1_LD, H1_CK) - 1>(fl);
        if (!__all(fl[0] == taglo && fl[1] == tag32)) poll_flags(flag(FL_H1, ftile, 4 * m + p4), tag);
        STAMP_AT(21);
      }
      if constexpr (kk == H1_LD) fl = ld8_sc1_async(sws, flag_off(FL_H1, ftile, 4 * m + p4));
      if constexpr (kk == O_LD) fl = ld8_sc1_async(sws, flag_off(FL_O0, ftile, 4 * m + p4));
      constexpr bool dw = kk >= 1 && kk - 1 + NR + RD < 36;
      if constexpr (dw) wgot(kk - 1 + NR);
      mm2(kk, af[kk], [&](int i) {
        if constexpr (dw) { if (i < 2) wdmah(kk - 1 + NR + RD, i); }
        if constexpr (kk + NR < 28) { if (i == 1) wrd(kk + NR); }
        if constexpr (kk == 27) { if (i == 0) wrd(28); }
        if constexpr (kk + AW < 28) { if (i >= 2) af[kk + AW][i - 2] = ld16_sc1_async(sws, lane * 16, a0 + (AFT * (i - 2) + kmap(kk + AW)) * 1024); }
      });
      if constexpr (kk == O_CK) {
        wait_vmf<g2_range(O_LD, O_CK) - 1>(fl);
        if (!__all(fl[0] == taglo && fl[1] == tag32)) poll_flags(flag(FL_O0, ftile, 4 * m + p4), tag);
        res_loads();
#pragma unroll
        for (int i = 0; i < 4; ++i) {
          of[i][0] = ld16_sc1_async(sws, lane * 16, o0 + omap(i) * 1024);
          of[i][1] = ld16_sc1_async(sws, lane * 16, o0 + (AFT + omap(i)) * 1024);
        }
      }
    });
    static_for<0, 8>([&](auto kc) {
      constexpr int kk = kc.value;
      if (kk < 7) wrd(28 + kk + 1);
      if constexpr (kk == 1) {
        STAMP_AT(5);
        poll_flags(flag(FL_O1, ftile, 4 * m + p4), tag);
        STAMP_AT(6);
#pragma unroll
        for (int i = 4; i < 8; ++i) {
          of[i][0] = ld16_sc1_async(sws, lane * 16, o0 + omap(i) * 1024);
          of[i][1] = ld16_sc1_async(sws, lane * 16, o0 + (AFT + omap(i)) * 1024);
        }
      }
      wait_vm2<(kk == 0) ? 6 : 14 - 2 * kk>(of[kk][0], of[kk][1]);
      mm2(28 + kk, of[kk], [&](int) {});
    });
    PIN(xres); PIN(bres);
    asm volatile("" ::: "memory");
#pragma unroll
    for (int mt = 0; mt < 2; ++mt)
#pragma unroll
      for (int c = 0; c < 2; ++c)
#pragma unroll
        for (int qd = 0; qd < 4; ++qd) {
          const f32x4 v = {c2[mt][c][4 * qd], c2[mt][c][4 * qd + 1], c2[mt][c][4 * qd + 2], c2[mt][c][4 * qd + 3]};
          sts128(lds0 + L_WG + m * PART + ((mt * 8 + c * 4 + qd) * 66 + lane) * 16, __builtin_bit_cast(v4u, v));
        }
    STAMP_AT(7);
  } else {
    __builtin_amdgcn_s_setprio(1);
    lane_setup();
    v4u xr[2];
    xr[0] = ld16_async(sx, lane * 16, (128 * r + 8 * j + 2 * m) * 1024); xr[1] = ld16_async(sx, lane * 16, (128 * r + 8 * j + 2 * m + 1) * 1024);
    unsigned warm;
    {
      u64 pc; asm volatile("s_getpc_b64 %0" : "=s"(pc));
      warm = ld2_async(make_srd((const void*)(pc & ~127ull), CODEWARM), lane < 3 ? lane * 128 : 0x40000000u, (idx * 4 + m) * 384);
    }
    __builtin_amdgcn_s_barrier();
    float sval;
    auto make_stats = [&]() {
      wait_vm2<0>(xr[0], xr[1]);
      PIN(warm);
      float piv[2], a[4];
#pragma unroll
      for (int u = 0; u < 2; ++u) {
        piv[u] = rdl(bflo(xr[u][0]), 0);
        float s1 = 0.f, s2 = 0.f;
#pragma unroll
        for (int i = 0; i < 8; ++i) { const float d = ((i & 1) ? bfhi(xr[u][i >> 1]) : bflo(xr[u][i >> 1])) - piv[u]; s1 += d; s2 = __builtin_fmaf(d, d, s2); }
        a[2 * u] = s1; a[2 * u + 1] = s2;
      }
#pragma unroll
      for (int i = 0; i < 4; ++i) a[i] += dpp<0xB1>(a[i]);
#pragma unroll
      for (int i = 0; i < 4; ++i) a[i] += dpp<0x4E>(a[i]);
      float t = p4 == 0 ? a[0] : p4 == 1 ? a[1] : p4 == 2 ? a[2] : a[3];
      t += dpp<0x114>(t); t += dpp<0x118>(t);
      const auto s16 = __builtin_amdgcn_permlane16_swap(asu(t), asu(t), false, false);
      t = asf(s16[0]) + asf(s16[1]);
      const auto s32 = __builtin_amdgcn_permlane32_swap(asu(t), asu(t), false, false);
      t = (asf(s32[0]) + asf(s32[1])) * (1.f / DM);
      const float e1 = dpp<0xA0>(t);
      sval = (p4 & 1) ? __builtin_fmaxf(__builtin_fmaf(-e1, e1, t), 0.f) + 1e-5f : t + (p4 ? piv[1] : piv[0]);
      PIN(sval);
    };
    const unsigned acnt = lds0 + L_CNT;
    const unsigned vpos = 2048 + (g & 1) * 512 + h * 256 + (lane & 15) * 16;
    v4u qf[2], am0, am1;
    u64 um[16];
    auto masks = [&]() {
      const int dq = r32 - 4 * h;
#pragma unroll
      for (int d = 0; d < 4; ++d) {
        const int row = 2 * (d & 1) + 8 * (d >> 1);
        am0[d] = row > dq ? 0xffffffffu : row == dq ? 0xffff0000u : 0u;
        am1[d] = row + 16 > dq ? 0xffffffffu : row + 16 == dq ? 0xffff0000u : 0u;
      }
      PIN(am0); PIN(am1);
      static_for<0, 16>([&](auto u) { um[u.value] = upper_mask(u.value); pin_s(um[u.value]); });
    };
    {
      v4u xf[32];
      f32x16 acc[3] = {};
      f32x4 cs[3] = {};
      v4u wf[4][3];
      unsigned wb[4];
#pragma unroll
      for (int p = 0; p < 4; ++p) { wb[p] = qbuf(0) + ((p + m) & 3) * 1024 + (8 * h + q4) * 64 + (g & 1) * 32 + p4 * 8; PIN(wb[p]); }
      auto rd = [&](int ks, int t) {
        if (t == 0) xf[ks] = lds128(xbuf(ks >> 2) + xo[ks & 3]);
        wf[ks & 3][t] = ldtr2(wb[ks & 3] + ((ks >> 2) % 3) * 12288 + t * 4096, 256);
      };
      auto mm = [&](int ks, int t) {
        acc[t] = t == 2 ? MFMA32(xf[ks], wf[ks & 3][t], acc[t]) : MFMA32(wf[ks & 3][t], xf[ks], acc[t]);
        if ((ks & 3) == 0) { cs[t] = MFMA16(sel, wf[ks & 3][t], cs[t]); PIN(cs[t]); }
        SBAR();
      };
      make_stats();
      bar();
      STAMP_AT(10);
#pragma unroll
      for (int t = 0; t < 6; ++t) rd(t / 3, t % 3);
      SBAR();
      static_for<0, 8>([&](auto sc) {
        constexpr int s = sc.value;
#pragma unroll
        for (int p = 0; p < 2; ++p) {
#pragma unroll
          for (int t = 0; t < 3; ++t) { mm(4 * s + p, t); rd(4 * s + p + 2, t); SBAR(); }
          if (s == 7 && p == 0) { cswr(4, cs[0]); cswr(5, cs[1]); cswr(6, cs[2]); SBAR(); }
        }
        if constexpr (s == 1) ask_tag();
        if constexpr (s == 2) {
          get_tag();
          if ((lane >> 2) == 3) {
            const v2u gr = {asu(sval), tag32};
            __builtin_amdgcn_raw_buffer_store_b64(gr, rws, WS_STATS + (128 * r + 8 * j + 2 * m) * 16 + p4 * 8, 0, 16);
          }
        }
        if constexpr (s == 5) stat_load();
        if constexpr (s == 6) {
          wait_vm1<0>(st);

          row_stats();
        }
        mm(4 * s + 2, 0); mm(4 * s + 2, 1);
        bar();
        if constexpr (s < 7) {
          STAMP_AT(11 + s);
          mm(4 * s + 2, 2);
          rd(4 * s + 4, 0); rd(4 * s + 4, 1); rd(4 * s + 4, 2); SBAR();
#pragma unroll
          for (int t = 0; t < 3; ++t) { mm(4 * s + 3, t); rd(4 * s + 5, t); SBAR(); }
        }
      });
      STAMP_AT(1);
      __builtin_amdgcn_s_setprio(3);
      const f32x4 c5 = csrd(5), c6 = csrd(6), c4 = csrd(4);
      SBAR();
      mm(30, 2); mm(31, 1);
      make_xaug();
      SBAR();
      mm(31, 2); mm(31, 0);
      if (h == 0) *(LAS float*)(size_t)(lds0 + L_RS + (m * 32 + r32) * 4) = rstd;
      acc[1] = MFMA32(waug(c5, 0u), xaug, acc[1]);
      acc[2] = MFMA32(xaug, waug(c6, 0u), acc[2]);
      acc[0] = MFMA32(waug(c4, 0u), xaug, acc[0]);
      SBAR();
      f32x4 rs[4];
#pragma unroll
      for (int qd = 0; qd < 4; ++qd) rs[qd] = __builtin_bit_cast(f32x4, lds128(lds0 + L_RS + (m * 32 + 8 * qd + 4 * h) * 4));
      const unsigned kvo = WS_KV + (j * 64 + T) * 4096;
#pragma unroll
      for (int e = 0; e < 16; ++e) acc[1][e] = MUL1(acc[1][e], rstd);
#pragma unroll
      for (int e = 0; e < 2; ++e) {
        const v4u f = pack8(acc[1], e);
        st16_sc1(f, sws, lane * 16, kvo + e * 1024);
        sts128(lds0 + L_KV + (8 + m) * KVT + e * 1024 + lane * 16, f);
      }
#pragma unroll
      for (int qd = 0; qd < 4; ++qd)
#pragma unroll
        for (int e = 0; e < 4; ++e) acc[2][4 * qd + e] = MUL1(acc[2][4 * qd + e], rs[qd][e]);
#pragma unroll
      for (int s = 0; s < 2; ++s) {
        const v4u f = pack8(acc[2], s);
        st16_sc1(f, sws, lane * 16, kvo + 2048 + s * 1024);
        sts128(lds0 + L_KV + (8 + m) * KVT + s * VB + vpos, f);
      }
      SBAR();
      const float qs = rstd * (0.25f * 1.4426950408889634f);
#pragma unroll
      for (int e = 0; e < 16; ++e) acc[0][e] = MUL1(acc[0][e], qs);
      qf[0] = pack8(acc[0], 0); qf[1] = pack8(acc[0], 1);
      PIN(qf[0]); PIN(qf[1]);
      STAMP_AT(2);
      const int T0 = 4 * r - 8 + 2 * m;
      const bool live = T0 >= 0;
      auto kv_load = [&](v4u (&kv)[8], int i0, int i1) {
        const unsigned src = WS_KV + (j * 64 + (live ? T0 : 0)) * 4096, vo = live ? lane * 16 : h * 512;
#pragma unroll
        for (int i = i0; i < i1; ++i) kv[i] = ld16_sc1_async(sws, vo, src + (live ? i : (i & 3)) * 1024);
      };
      auto kv_stage = [&](v4u (&kv)[8]) {
        const unsigned on = (lane < 2 || live) ? one2 : 0u;
        if (lane < 6) sts128(lds0 + L_KV + (lane < 2 ? 8 + m : 2 * m + ((lane - 2) >> 1)) * KVT + 2048 + (lane & 1) * VB + 1024, (v4u){on, on, on, on});
        const v4u zz = {};
#pragma unroll
        for (int i = 0; i < 2; ++i) {
          sts128(lds0 + L_KV + (2 * m + i) * KVT + lane * 16, kv[4 * i]);
          sts128(lds0 + L_KV + (2 * m + i) * KVT + 1024 + lane * 16, kv[4 * i + 1]);
          sts128(lds0 + L_KV + (2 * m + i) * KVT + vpos, live ? kv[4 * i + 2] : zz);
          sts128(lds0 + L_KV + (2 * m + i) * KVT + VB + vpos, live ? kv[4 * i + 3] : zz);
        }
        arrive_nowait(acnt);
      };
      auto kv_wait = [&](v4u (&kv)[8]) {
        wait_vm<0>();
#pragma unroll
        for (int i = 0; i < 8; ++i) PIN(kv[i]);
      };
      if (heavy) {
        hsetup(1);
        await(acnt + 16, 4);
        STAMP_AT(3);
        v2u fl;
        v4u kv[8];
        hmm(std::integral_constant<int, 1>{}, xf, [&](auto kc) {
          constexpr int ks = kc.value;
          if constexpr (ks == KVPUB) {
            wait_vm<0>();
            if (lane == 0) __hip_atomic_store(flag(FL_KV, T, j), tag, RLX_AGENT);
          }
          if constexpr (ks == KVPUB + 2) bias_load(1);
          if constexpr (ks == 31) { wait_vm<1>(); PIN(bias0); PIN(bias1); }
          if constexpr (ks == 24) fl = ld8_sc1_async(sws, flag_off(FL_KV, live ? T0 + (lane & 1) : 0, j));
        });
        STAMP_AT(18);
        wait_vmf<0>(fl);
        if (!__all(fl[0] == taglo && fl[1] == tag32)) poll_flags(flag(FL_KV, live ? T0 + (lane & 1) : 0, j), tag);
        kv_load(kv, 0, 8);
        hgelu(std::integral_constant<int, 1>{}, [&]() {});
        STAMP_AT(4);
        masks();
        wait_vm<4>();
#pragma unroll
        for (int i = 0; i < 8; ++i) PIN(kv[i]);
        kv_stage(kv);
      } else {
        wait_vm<0>();
        if (lane == 0) __hip_atomic_store(flag(FL_KV, T, j), tag, RLX_AGENT);
        v4u kv[8];
        poll_flags(flag(FL_KV, live ? T0 + (lane & 1) : 0, j), tag);
        kv_load(kv, 0, 8); masks(); kv_wait(kv); kv_stage(kv);
      }
    }
    STAMP_AT(19);
    const unsigned kb = lds0 + L_KV + m * KVT + lane * 16;
    unsigned vb[2];
#pragma unroll
    for (int e = 0; e < 2; ++e) { vb[e] = lds0 + L_KV + m * KVT + 2048 + ((g & 1) ? 1024 : e * 512 + h * 256 + (lane & 15) * 16); PIN(vb[e]); }
    await(acnt, heavy ? 8 : 4);
    __builtin_amdgcn_s_setprio(PRIO_ATT);
    STAMP_AT(5);
    const f32x16 zero = {};
    auto o_store = [&](f32x16& o, int e) {
      const float rl = __builtin_amdgcn_rcpf(o[8]);
#pragma unroll
      for (int u = 0; u < 8; ++u) o[u] = MUL1(o[u], rl);
      st16_sc1(pack8(o, 0), sws, lane * 16, WS_AF + (T * AFT + 112 + 2 * j + e) * 1024);
    };
    int redo = 0;
    {
      f32x16 o[2], sa, sb, su;
      bool pend = false;
      v4u kn, ku, va, vc, vn0, vn1, vu0, vu1, p0, p1, pu;
      auto kaddr = [&](int n) { return kb + ((n & 7) + 1) * KVT + (n >> 3) * 1024; };
      auto vaddr = [&](int n) { return vb[n >> 3] + ((n & 7) + 1) * KVT; };
      kn = lds128(kaddr(0)); vn0 = lds128(vaddr(0)); vn1 = lds128(vaddr(0) + VB);
      sa = MFMA32(kn, qf[0], zero);
      kn = lds128(kaddr(1));
      SBAR();
      auto step = [&](auto nc, f32x16& sc, f32x16& sn) {
        constexpr int n = nc.value, e = n >> 3, i = n & 7;
        if constexpr (n < 15) sn = MFMA32(kn, qf[(n + 1) >> 3], zero);
        if constexpr (n < 14) kn = lds128(kaddr(n + 2));
        if constexpr (i == 5) ku = lds128(kb + e * 1024);
        v4u v0 = vn0, v1 = vn1;
        if constexpr (n < 15) { vn0 = lds128(vaddr(n + 1)); vn1 = lds128(vaddr(n + 1) + VB); }
        if constexpr (i == 6) { vu0 = lds128(vb[e]); vu1 = lds128(vb[e] + VB); }
        SBAR();
        if constexpr (i == 7) {
#pragma unroll
          for (int u = 0; u < 16; ++u) sc[u] = __builtin_amdgcn_inverse_ballot_w64(um[u]) ? su[u] : sc[u];
          PIN(sc); SBAR();
        }
#pragma unroll
        for (int u = 0; u < 6; ++u) sc[u] = EXP2(sc[u]);
        PIN(sc); SBAR();
        if constexpr (n == 8) { o[0] = MFMA32(vc, p1, o[0]); SBAR(); }
        else if constexpr (n > 0) { o[e] = MFMA32(vc, p1, o[e]); SBAR(); }
        sc[6] = EXP2(sc[6]); sc[7] = EXP2(sc[7]);
        p0 = pack8(sc, 0);
        sc[8] = EXP2(sc[8]); sc[9] = EXP2(sc[9]);
        if constexpr (i == 7) { va = p0 & am0; p0 &= ~am0; PIN(va); }
        PIN(sc); PIN(p0); SBAR();
        if constexpr (n == 8) { o[0] = MFMA32(vu1, pu, o[0]); SBAR(); }
        o[e] = MFMA32(v0, p0, i == 0 ? zero : o[e]);
        SBAR();
        if constexpr (i == 6) { su = MFMA32(ku, qf[e], zero); SBAR(); }
#pragma unroll
        for (int u = 10; u < 13; ++u) sc[u] = EXP2(sc[u]);
        if constexpr (i == 7) { PIN(sc); SBAR(); o[e] = MFMA32(vu0, va, o[e]); SBAR(); }
#pragma unroll
        for (int u = 13; u < 16; ++u) sc[u] = EXP2(sc[u]);
        p1 = pack8(sc, 1); vc = v1;
        if constexpr (i == 7) { pu = p1 & am1; p1 &= ~am1; PIN(pu); }
        PIN(p1); SBAR();
        if constexpr (n == 8) {
          if (__all(o[0][8] >= 0x1p-30f && o[0][8] <= 0x1p30f)) o_store(o[0], 0); else redo = 1;
          SBAR();
        }
        if constexpr (i == 2) pend = !redo;
        if constexpr (i == 3 || i == 5 || i == 7) {
          if (pend && (i == 7 || (__builtin_amdgcn_s_getreg(7 | (3 << 11)) & 15) == 0)) {
            wait_vm<0>();
            if (lane == 0) __hip_atomic_store(flag(FL_H1 + e, T, j), tag, RLX_AGENT);
            pend = false;
          }
        }
      };
      static_for<0, 8>([&](auto hc) {
        step(std::integral_constant<int, 2 * hc.value>{}, sa, sb);
        step(std::integral_constant<int, 2 * hc.value + 1>{}, sb, sa);
      });
      o[1] = MFMA32(vc, p1, o[1]);
      o[1] = MFMA32(vu1, pu, o[1]);
      if (__all(o[1][8] >= 0x1p-30f && o[1][8] <= 0x1p30f)) o_store(o[1], 1); else redo |= 2;
    }
    if (redo) {
#pragma unroll 1
      for (int e = 0; e < 2; ++e) {
        if (!((redo >> e) & 1)) continue;
        const v4u q = e ? qf[1] : qf[0];
        const unsigned vbe = e ? vb[1] : vb[0];
        auto merged = [&](const f32x16& c) {
          f32x16 sl = MFMA32(lds128(kb + 8 * KVT + e * 1024), q, c);
          const f32x16 su = MFMA32(lds128(kb + e * 1024), q, c);
          static_for<0, 16>([&](auto u) { sl[u.value] = upper<u.value>() ? su[u.value] : sl[u.value]; });
          return sl;
        };
        float mx = -INFINITY;
#pragma unroll 1
        for (int i = 0; i < 8; ++i) {
          const f32x16 sc = i ? MFMA32(lds128(kb + i * KVT + e * 1024), q, zero) : merged(zero);
#pragma unroll
          for (int u = 0; u < 16; u += 2) mx = max3(mx, sc[u], sc[u + 1]);
        }
        const auto sw = __builtin_amdgcn_permlane32_swap(asu(mx), asu(mx), false, false);
        mx = __builtin_fmaxf(asf(sw[0]), asf(sw[1]));
        f32x16 negm, o;
#pragma unroll
        for (int u = 0; u < 16; ++u) negm[u] = -mx;
        {
          f32x16 sc = merged(negm);
#pragma unroll
          for (int u = 0; u < 16; ++u) sc[u] = EXP2(sc[u]);
          const v4u p0 = pack8(sc, 0), p1 = pack8(sc, 1);
          o = MFMA32(lds128(vbe), p0 & am0, zero);
          o = MFMA32(lds128(vbe + VB), p1 & am1, o);
          o = MFMA32(lds128(vbe + 8 * KVT), p0 & ~am0, o);
          o = MFMA32(lds128(vbe + 8 * KVT + VB), p1 & ~am1, o);
        }
#pragma unroll 1
        for (int i = 1; i < 8; ++i) {
          f32x16 sc = MFMA32(lds128(kb + i * KVT + e * 1024), q, negm);
#pragma unroll
          for (int u = 0; u < 16; ++u) sc[u] = EXP2(sc[u]);
          o = MFMA32(lds128(vbe + i * KVT), pack8(sc, 0), o);
          o = MFMA32(lds128(vbe + i * KVT + VB), pack8(sc, 1), o);
        }
        o_store(o, e);
      }
      wait_vm<0>();
      if (lane == 0) __hip_atomic_store(flag(FL_O0, T, j), tag, RLX_AGENT);
    }
    STAMP_AT(6);
    wait_vm<0>();
    if (lane == 0) __hip_atomic_store(flag(FL_O1, T, j), tag, RLX_AGENT);
    STAMP_AT(7);
    res_loads();
    wait_vm2<0>(xres, bres);
    if (blockIdx.x == 0 && wave == 7) {
#pragma unroll 1
      for (int i = 0; i < 4; ++i) poll_flags(flag(FL_KV, 4 * (4 * i + g), lane & 15), tag);
      if (lane == 0) __hip_atomic_store((GAS u64*)(ws + WS_EPOCH), tag, RLX_AGENT);
    }
  }
  bar();
  {
    int t2 = threadIdx.x; PIN(t2);
    const int row = t2 >> 3, ch = t2 & 7, orow = 64 * rt + row, ocol = 64 * ct + ch * 8;
    const unsigned a = lds0 + L_WG + (((row >> 5) * 8 + ch) * 66 + (row & 31)) * 16;
    f32x4 lo = {}, hi = {};
#pragma unroll
    for (int w = 0; w < 4; ++w) {
      lo += __builtin_bit_cast(f32x4, lds128(a + w * PART));
      hi += __builtin_bit_cast(f32x4, lds128(a + w * PART + 512));
    }
    v4u o;
    o[0] = pk2(lo[0] + bflo(xres[0]) + bflo(bres[0]), lo[1] + bfhi(xres[0]) + bfhi(bres[0]));
    o[1] = pk2(lo[2] + bflo(xres[1]) + bflo(bres[1]), lo[3] + bfhi(xres[1]) + bfhi(bres[1]));
    o[2] = pk2(hi[0] + bflo(xres[2]) + bflo(bres[2]), hi[1] + bfhi(xres[2]) + bfhi(bres[2]));
    o[3] = pk2(hi[2] + bflo(xres[3]) + bflo(bres[3]), hi[3] + bfhi(xres[3]) + bfhi(bres[3]));
    __builtin_amdgcn_raw_buffer_store_b128(o, __builtin_amdgcn_make_buffer_rsrc(out, 0, SEQ * DM * 2, 0x00020000), orow * 1024 + ocol * 2, 0, 16);
  }
  STAMP_AT(8);
}

extern "C" void kernel_launch(void* const* d_in, const int* in_sizes, int n_in, void* d_out, int out_size, void* d_ws, size_t ws_size,
                              hipStream_t stream) {
  (void)in_sizes; (void)n_in; (void)out_size; (void)ws_size;
  (void)hipFuncSetAttribute((const void*)fused_block, hipFuncAttributeMaxDynamicSharedMemorySize, LDS_BYTES);
  fused_block<<<256, 512, LDS_BYTES, stream>>>(d_in[0], d_in[1], d_in[2], d_in[4], d_in[5], d_in[6], d_in[7], d_out, (char*)d_ws);
}
```

```cpp
#include <hip/hip_runtime.h>
#include <hip/hip_bf16.h>
#include <stdint.h>
#include <type_traits>

typedef unsigned v4u __attribute__((ext_vector_type(4)));
typedef unsigned v2u __attribute__((ext_vector_type(2)));
typedef float f32x2 __attribute__((ext_vector_type(2)));
typedef float f32x4 __attribute__((ext_vector_type(4)));
typedef float f32x16 __attribute__((ext_vector_type(16)));
typedef short bf16x8 __attribute__((ext_vector_type(8)));
typedef short s16x4 __attribute__((ext_vector_type(4)));
typedef __bf16 bf16x2_t __attribute__((ext_vector_type(2)));
typedef unsigned long long u64;
#define GAS __attribute__((address_space(1)))
#define LAS __attribute__((address_space(3)))
#define DI __device__ __forceinline__
#define RLX_AGENT __ATOMIC_RELAXED, __HIP_MEMORY_SCOPE_AGENT

constexpr int SEQ = 2048, DM = 512, FF = 1792;
constexpr unsigned WS_EPOCH = 0;
constexpr unsigned WS_STATS = 32768;
constexpr unsigned WS_FLAG = 65536;
constexpr unsigned WS_KV = WS_FLAG + 5 * 1024 * 128;
constexpr int AFT = 145;
constexpr unsigned WS_AF = WS_KV + 16 * 64 * 4096;
constexpr unsigned WS_END = WS_AF + 64 * AFT * 1024;
constexpr unsigned WS_STAMP = WS_END;
enum { FL_KV = 0, FL_H0 = 1, FL_H1 = 2, FL_O0 = 3, FL_O1 = 4 };
constexpr int L_WA = 0;
constexpr int L_WG = 86016;
constexpr int VB = 1040;
constexpr int KVT = 2048 + 2 * VB;
constexpr int L_KV = 65536 - 8 * KVT;
constexpr int RD = 8, NR = 12;
constexpr int PART = 16 * 66 * 16;
constexpr int L_CS = L_WG + 4 * PART;
constexpr int L_RS = L_CS + 3584;
constexpr int L_CNT = L_RS + 512;
constexpr int LDS_BYTES = L_CNT + 64;
constexpr int xbuf(int s) { return ((s + 1) % 3) * 16384; }
constexpr int qbuf(int s) { return 49152 + (s % 3) * 12288; }
constexpr int w1buf(int s) { return L_WG + (s % 3) * PART + 8192; }
static_assert(xbuf(7) == 32768 && qbuf(2) + 12288 <= L_WG && L_KV + 12 * KVT <= L_WG && PART >= RD * 2048 && LDS_BYTES <= 163840, "lds map");
#define AWIN 6
constexpr int AW = AWIN;
constexpr unsigned SPIN_CAP = 1u << 23;

#define MFMA32(a, b, c) __builtin_amdgcn_mfma_f32_32x32x16_bf16(__builtin_bit_cast(bf16x8, a), __builtin_bit_cast(bf16x8, b), c, 0, 0, 0)
#define MFMA16(a, b, c) __builtin_amdgcn_mfma_f32_16x16x32_bf16(__builtin_bit_cast(bf16x8, a), __builtin_bit_cast(bf16x8, b), c, 0, 0, 0)
#define SBAR() __builtin_amdgcn_sched_barrier(0)

#define STAMP_AT(i) do { } while (0)

DI float asf(unsigned u) { return __builtin_bit_cast(float, u); }
DI unsigned asu(float f) { return __builtin_bit_cast(unsigned, f); }
DI unsigned pk2(float lo, float hi) { return __builtin_bit_cast(unsigned, __builtin_convertvector((f32x2){lo, hi}, bf16x2_t)); }
DI float bflo(unsigned u) { return __builtin_bit_cast(float, u << 16); }
DI float bfhi(unsigned u) { return __builtin_bit_cast(float, u & 0xffff0000u); }
DI v4u make_srd(const void* p, unsigned bytes) { const u64 a = (u64)p; return (v4u){(unsigned)a, (unsigned)(a >> 32) & 0xffffu, bytes, 0x00020000u}; }
DI void dma16(v4u srd, unsigned voff, unsigned soff, unsigned lds) {
  asm volatile("s_mov_b32 m0, %3\n\ts_nop 0\n\tbuffer_load_dwordx4 %0, %1, %2 offen lds" :: "v"(voff), "s"(srd), "s"(soff), "s"(lds) : "memory");
}
DI void dma16_a(v4u srd, unsigned voff, unsigned soff, unsigned lds) { dma16(srd, voff, soff, lds); }
DI void dma16_b(v4u srd, unsigned voff1, unsigned soff) {
  asm volatile("buffer_load_dwordx4 %0, %1, %2 offen offset:1024 lds" :: "v"(voff1), "s"(srd), "s"(soff) : "memory");
}
DI v4u ld16_sc1_async(v4u srd, unsigned voff, unsigned soff) {
  v4u r; asm volatile("buffer_load_dwordx4 %0, %1, %2, %3 offen sc1" : "=v"(r) : "v"(voff), "s"(srd), "s"(soff) : "memory"); return r;
}
DI v4u ld16_async(v4u srd, unsigned voff, unsigned soff) {
  v4u r; asm volatile("buffer_load_dwordx4 %0, %1, %2, %3 offen" : "=v"(r) : "v"(voff), "s"(srd), "s"(soff) : "memory"); return r;
}
DI unsigned ld2_async(v4u srd, unsigned voff, unsigned soff) {
  unsigned r; asm volatile("buffer_load_ushort %0, %1, %2, %3 offen" : "=v"(r) : "v"(voff), "s"(srd), "s"(soff) : "memory"); return r;
}
DI void st16_sc1(v4u v, v4u srd, unsigned voff, unsigned soff) {
  asm volatile("buffer_store_dwordx4 %0, %1, %2, %3 offen sc1\n\ts_nop 1" :: "v"(v), "v"(voff), "s"(srd), "s"(soff) : "memory");
}
template <int N> DI void wait_vm1(v4u& a) { asm volatile("s_waitcnt vmcnt(%1)" : "+v"(a) : "i"(N) : "memory"); }
template <int N> DI void wait_vm2(v4u& a, v4u& b) { asm volatile("s_waitcnt vmcnt(%2)" : "+v"(a), "+v"(b) : "i"(N) : "memory"); }
template <int N> DI void wait_vm3(v4u& a, v4u& b, v4u& c) { asm volatile("s_waitcnt vmcnt(%3)" : "+v"(a), "+v"(b), "+v"(c) : "i"(N) : "memory"); }
DI void pin_s(u64& v) { asm volatile("" : "+s"(v)); }
template <class V> DI void pin(V& v) { asm volatile("" : "+v"(v)); }
template <int N> DI void wait_vm() { asm volatile("s_waitcnt vmcnt(%0)" :: "i"(N) : "memory"); }
DI void bar() { asm volatile("s_waitcnt lgkmcnt(0)\n\ts_barrier" ::: "memory"); }
DI v4u lds128(unsigned a) { return *(const LAS v4u*)(size_t)a; }
DI void sts128(unsigned a, v4u v) { *(LAS v4u*)(size_t)a = v; }
DI v2u ldtr(unsigned a) { return __builtin_bit_cast(v2u, __builtin_amdgcn_ds_read_tr16_b64_v4i16((LAS s16x4*)(size_t)a)); }
DI v4u ldtr2(unsigned a, int second) { const v2u lo = ldtr(a), hi = ldtr(a + second); return (v4u){lo.x, lo.y, hi.x, hi.y}; }
template <int CTRL> DI float dpp(float v) { return __builtin_bit_cast(float, __builtin_amdgcn_update_dpp(0, __builtin_bit_cast(int, v), CTRL, 0xf, 0xf, true)); }
DI float rdl(float v, int l) { return __builtin_bit_cast(float, __builtin_amdgcn_readlane(__builtin_bit_cast(int, v), l)); }
DI v4u pack8(const f32x16& a, int s) { return (v4u){pk2(a[8 * s], a[8 * s + 1]), pk2(a[8 * s + 2], a[8 * s + 3]), pk2(a[8 * s + 4], a[8 * s + 5]), pk2(a[8 * s + 6], a[8 * s + 7])}; }
DI void poll_flags(const GAS u64* p, u64 tag) {
#pragma unroll 1
  for (unsigned spin = 0; spin < SPIN_CAP; ++spin) {
    const u64 v = __hip_atomic_load(p, RLX_AGENT);
    if (__all(v == tag)) break;
    __builtin_amdgcn_s_sleep(2);
  }
  asm volatile("" ::: "memory");
}
constexpr u64 upper_mask(int reg) {
  u64 m = 0;
  for (int l = 0; l < 64; ++l) { const int row = (reg & 3) + 8 * (reg >> 2) + 4 * (l >> 5); if (row > (l & 31)) m |= 1ull << l; }
  return m;
}
template <int REG> DI bool upper() { return __builtin_amdgcn_inverse_ballot_w64(upper_mask(REG)); }
template <int I, int N, class Fn> DI void static_for(Fn&& f) { if constexpr (I < N) { f(std::integral_constant<int, I>{}); static_for<I + 1, N>(f); } }
DI float max3(float a, float b, float c) { return __builtin_fmaxf(__builtin_fmaxf(a, b), c); }

constexpr int H1_CK = 16 - AW, H1_LD = H1_CK - 3, O_CK = 28 - AW, O_LD = O_CK - 3;
constexpr int g2_issued(int t) { return ((t >= 1 && t - 1 + NR + RD < 36) ? 2 : 0) + ((t + AW < 28) ? 2 : 0) + (t == H1_LD) + (t == O_LD) + (t == O_CK ? 10 : 0); }
constexpr int g2_range(int a, int b) { int n = 0; for (int t = a; t < b; ++t) n += g2_issued(t); return n; }
#define GELU_PK(P) (P == 1)
#define PRIO_G2 2
#define PRIO_ATT 3
#define PRIO_G1 2
#define ROT r
#define CODEWARM 38000
#define MIDWARM 1
#define KVPUB 6
#define EXP2(v) __builtin_amdgcn_exp2f(v)
#define PIN(v) pin(v)
extern "C" __device__ float mul_unpacked(float, float) __asm("llvm.amdgcn.fmul.legacy");
#define MUL1(a, b) mul_unpacked(a, b)
#define FMA1(a, b, c) __builtin_amdgcn_div_fmasf(a, b, c, false)
DI v2u ld8_sc1_async(v4u srd, unsigned voff) {
  v2u r; asm volatile("buffer_load_dwordx2 %0, %1, %2, 0 offen sc1" : "=v"(r) : "v"(voff), "s"(srd) : "memory"); return r;
}
template <int N> DI void wait_vmf(v2u& a) { asm volatile("s_waitcnt vmcnt(%1)" : "+v"(a) : "i"(N) : "memory"); }
DI void arrive(unsigned cnt, int lane) {
  asm volatile("s_waitcnt lgkmcnt(0)" ::: "memory");
  if (lane == 0) __hip_atomic_fetch_add((LAS unsigned*)(size_t)cnt, 1u, __ATOMIC_RELAXED, __HIP_MEMORY_SCOPE_WORKGROUP);
}
DI void arrive_nowait(unsigned cnt) {
  asm volatile("s_mov_b64 exec, 1\n\tds_add_u32 %0, %1\n\ts_mov_b64 exec, -1" :: "v"(cnt), "v"(1u) : "memory");
}
DI unsigned peek(unsigned cnt) { return __hip_atomic_load((LAS unsigned*)(size_t)cnt, __ATOMIC_RELAXED, __HIP_MEMORY_SCOPE_WORKGROUP); }
DI void await(unsigned cnt, unsigned n) {
#pragma unroll 1
  for (unsigned spin = 0; spin < SPIN_CAP * 32; ++spin) {
    if (__builtin_amdgcn_readfirstlane(peek(cnt)) >= n) break;
    __builtin_amdgcn_s_sleep(1);
  }
  asm volatile("" ::: "memory");
}

__global__ __launch_bounds__(512, 2) void fused_block(const void* __restrict__ x, const void* __restrict__ wqkv, const void* __restrict__ wo,
                                                      const void* __restrict__ w1, const void* __restrict__ b1, const void* __restrict__ w2,
                                                      const void* __restrict__ b2, void* __restrict__ out, char* __restrict__ ws) {
  constexpr unsigned lds0 = 0;
  const int wave = __builtin_amdgcn_readfirstlane(threadIdx.x >> 6);
  const int m = wave & 3;
  const bool isG = wave < 4;
  const int xcd = blockIdx.x & 7, idx = blockIdx.x >> 3;
  const int r = (xcd >> 2) * 8 + (idx >> 2), j = (xcd & 3) * 4 + (idx & 3);
  const int rt = (xcd >> 1) * 8 + (idx >> 2), ct = (xcd & 1) * 4 + (idx & 3);
  int lane = threadIdx.x & 63, r32, h, g, q4, p4;
  STAMP_AT(0);
  asm volatile("" :: "s"(x), "s"(wqkv), "s"(wo), "s"(w1), "s"(b1), "s"(w2), "s"(b2), "s"(out), "s"(ws));

  const v4u sx = make_srd(x, SEQ * DM * 2), sws = make_srd(ws, WS_END);
  const __amdgpu_buffer_rsrc_t rws = __builtin_amdgcn_make_buffer_rsrc(ws, 0, WS_END, 0x00020000);
  const unsigned one2 = 0x3f803f80u;
  const int T = 4 * r + m;
  const int rot = ROT;
  auto flag_off = [&](int kind, int tile, int jj) { return WS_FLAG + ((kind * 64 + tile) * 16 + jj) * 128; };
  auto flag = [&](int kind, int tile, int jj) { return (GAS u64*)(ws + flag_off(kind, tile, jj)); };
  u64 tag;
  unsigned taglo, tag32;
  auto get_tag = [&]() {
    const v2u tg = *(const LAS v2u*)(size_t)(lds0 + L_CNT + 32);
    taglo = __builtin_amdgcn_readfirstlane(tg[0]); tag32 = __builtin_amdgcn_readfirstlane(tg[1]);
    tag = ((u64)tag32 << 32) | taglo;
  };
  v4u xres, bres, xaug, st;
  float mean, var, rstd, sd;
  v4u sel;
  unsigned xo[4], swz;
  auto lane_setup = [&]() {
    lane = threadIdx.x & 63; PIN(lane);
    r32 = lane & 31; h = lane >> 5; g = lane >> 4; q4 = (lane >> 2) & 3; p4 = lane & 3;
    const unsigned selv = ((lane & 15) == (g & 1)) ? one2 : 0u;
    sel = (v4u){selv, selv, selv, selv};
#pragma unroll
    for (int p = 0; p < 4; ++p) xo[p] = (32 * m + r32) * 128 + (((2 * ((p + m) & 3) + h) ^ ((r32 >> 1) & 7)) * 16);
    swz = ((((lane >> 2) & 1) ^ (g & 1)) * 64) + p4 * 16;
  };

  auto stat_load = [&]() { st = ld16_sc1_async(sws, WS_STATS + (128 * r + 32 * m + r32) * 16, 0); };
  auto res_loads = [&]() {
    int t2 = threadIdx.x; PIN(t2);
    const int row = 64 * rt + (t2 >> 3), col = 64 * ct + (t2 & 7) * 8;
    xres = ld16_async(sx, row * 1024 + col * 2, 0);
    bres = ld16_async(make_srd(b2, DM * 2), col * 2, 0);
  };
  auto row_stats = [&]() {
    if (!__all(st[1] == tag32 && st[3] == tag32)) {
#pragma unroll 1
      for (unsigned spin = 0; spin < SPIN_CAP; ++spin) {
        st = ld16_sc1_async(sws, WS_STATS + (128 * r + 32 * m + r32) * 16, 0);
        wait_vm1<0>(st);
        if (__all(st[1] == tag32 && st[3] == tag32)) break;
      }
    }
    mean = asf(st[0]); var = asf(st[2]);
    PIN(mean); PIN(var);
  };
  auto make_xaug = [&]() {
    PIN(mean); PIN(var);
    rstd = __builtin_amdgcn_rsqf(var); sd = var * rstd;
    const unsigned hi = pk2(-mean, sd);
    const unsigned lo = pk2(-mean - bflo(hi), FMA1(bfhi(hi), -1.f, sd));
    xaug = (v4u){(hi & 0xffffu) | (hi << 16), (lo & 0xffffu) | (hi & 0xffff0000u), lo >> 16, 0u};
    if (h) xaug = (v4u){0u, 0u, 0u, 0u};
    PIN(xaug);
  };
  auto cswr = [&](int tile, f32x4 c) {
    if (lane < 16) { LAS float* p = (LAS float*)(size_t)(lds0 + L_CS + (tile * 32 + lane) * 16 + m * 4); p[0] = c[0]; p[64] = c[1]; }
  };
  auto csrd = [&](int tile) { return __builtin_bit_cast(f32x4, lds128(lds0 + L_CS + (tile * 32 + r32) * 16)); };
  auto waug = [&](f32x4 c4, unsigned b16) {
    const float c = ((c4[0] + c4[1]) + c4[2]) + c4[3];
    const unsigned hi = pk2(c, 0.f);
    const unsigned lo = pk2(c - bflo(hi), 0.f);
    return (v4u){hi | (lo << 16), hi | (b16 << 16), b16, 0u};
  };

  const v4u sw1 = make_srd(w1, DM * FF * 2);
  const int hcol0 = 448 * (j >> 2) + 128 * (j & 3);
  const bool heavy = (j & 3) != 3;
  unsigned bias0, bias1;
  auto bias_load = [&](int P) {
    bias0 = ld2_async(make_srd(b1, FF * 2), r32 * 2, (hcol0 + 64 * P) * 2);
    bias1 = ld2_async(make_srd(b1, FF * 2), r32 * 2, (hcol0 + 64 * P + 32) * 2);
  };
  auto w1dma = [&](int P, int c, unsigned dst, int half) {
    const unsigned vw1 = (lane >> 3) * (FF * 2) + swz, vw1b = vw1 + 8 * (FF * 2) - 1024;
    const unsigned so = (64 * ((c + rot) & 7) + 16 * m) * (FF * 2) + (hcol0 + 64 * P) * 2;
    if (half == 0) dma16_a(sw1, vw1, so, dst + m * 2048); else dma16_b(sw1, vw1b, so);
  };
  f32x16 hac[2];
  unsigned rk[4][2];
  auto hsetup = [&](int P) {
#pragma unroll
    for (int p = 0; p < 4; ++p)
#pragma unroll
      for (int c = 0; c < 2; ++c) {
        rk[p][c] = lds0 + (P ? L_WA : w1buf(0)) + ((p + m) & 3) * 2048 + h * 1024 + q4 * 128 + ((c ^ (q4 >> 1)) * 64) + (g & 1) * 32 + p4 * 8;
        PIN(rk[p][c]);
      }
  };
  auto hmm = [&](auto pc, const v4u (&xf)[32], auto&& hook) {
    constexpr int P = pc.value;
    constexpr int D = 4;
    const unsigned cnt = lds0 + L_CNT;
    f32x4 cs[2] = {}, c0, c1;
    v4u wf[D][2], au0, au1;
    unsigned seen = 0;
    hac[0] = (f32x16){}; hac[1] = (f32x16){};
    auto rd = [&](int ks, int t) { wf[ks % D][t] = ldtr2(rk[ks & 3][t] + (ks >> 2) * 8192, 512); };
#pragma unroll
    for (int ks = 0; ks < D; ++ks) { rd(ks, 0); rd(ks, 1); }
    SBAR();
    static_for<0, 32>([&](auto kc) {
      constexpr int ks = kc.value;
      hook(kc);
      if constexpr (ks == 10) seen = peek(cnt + 20);
      if constexpr (ks == 29) seen = peek(cnt);
#pragma unroll
      for (int t = 0; t < 2; ++t) {
        hac[t] = MFMA32(wf[ks % D][t], xf[ks], hac[t]);
        if ((ks & 3) == 0 && ks < 28) { cs[t] = MFMA16(sel, wf[ks % D][t], cs[t]); PIN(cs[t]); }
        if (ks == 26) { cs[t] = MFMA16(sel, wf[28 % D][t], cs[t]); PIN(cs[t]); }
        SBAR();
        if constexpr (ks + D < 32) rd(ks + D, t);
        if constexpr (ks == 31) { if (t == 0) { au0 = waug(c0, bias0); PIN(au0); } else { au1 = waug(c1, bias1); PIN(au1); } }
        SBAR();
      }
      if constexpr (ks == 11 || ks == 30) {
        PIN(hac[0]); PIN(hac[1]);
        if (__builtin_amdgcn_readfirstlane(seen) < 4) await(ks == 11 ? cnt + 20 : cnt, 4);
        asm volatile("" ::: "memory");
        if constexpr (ks == 30) { c0 = csrd(2 * P); c1 = csrd(2 * P + 1); }
      }
      if constexpr (ks == 27) { cswr(2 * P, cs[0]); cswr(2 * P + 1, cs[1]); arrive_nowait(cnt); }
      SBAR();
    });
    hac[0] = MFMA32(au0, xaug, hac[0]);
    hac[1] = MFMA32(au1, xaug, hac[1]);
    SBAR();
  };
  auto hgelu = [&](auto pc, auto&& mid) {
    constexpr int P = pc.value;
    constexpr bool PK = GELU_PK(P);
    const float C0 = 2.3022081986f, C1 = C0 * 0.044715f;
    const float ga = -C1 * rstd * rstd * rstd, gb = -C0 * rstd;
    auto mul = [&](f32x2 a, f32x2 b) { return PK ? a * b : (f32x2){MUL1(a[0], b[0]), MUL1(a[1], b[1])}; };
    auto fma = [&](f32x2 a, float b, float c) {
      return PK ? __builtin_elementwise_fma(a, (f32x2){b, b}, (f32x2){c, c}) : (f32x2){FMA1(a[0], b, c), FMA1(a[1], b, c)}; };
#pragma unroll
    for (int t = 0; t < 2; ++t) {
      f32x2 v[8], z[8];
#pragma unroll
      for (int e = 0; e < 8; ++e) { v[e] = (f32x2){hac[t][2 * e], hac[t][2 * e + 1]}; z[e] = mul(v[e], v[e]); }
      SBAR();
#pragma unroll
      for (int e = 0; e < 8; ++e) z[e] = fma(z[e], ga, gb);
      SBAR();
#pragma unroll
      for (int e = 0; e < 8; ++e) z[e] = mul(z[e], v[e]);
      SBAR();
#pragma unroll
      for (int e = 0; e < 8; ++e) z[e] = (f32x2){EXP2(z[e][0]), EXP2(z[e][1])};
      SBAR();
#pragma unroll
      for (int e = 0; e < 8; ++e) z[e] = fma(z[e], sd, sd);
      SBAR();
#pragma unroll
      for (int e = 0; e < 8; ++e) z[e] = (f32x2){__builtin_amdgcn_rcpf(z[e][0]), __builtin_amdgcn_rcpf(z[e][1])};
      SBAR();
#pragma unroll
      for (int e = 0; e < 8; ++e) z[e] = mul(z[e], v[e]);
      SBAR();
#pragma unroll
      for (int s = 0; s < 2; ++s) {
        const v4u f = {pk2(z[4 * s][0], z[4 * s][1]), pk2(z[4 * s + 1][0], z[4 * s + 1][1]), pk2(z[4 * s + 2][0], z[4 * s + 2][1]), pk2(z[4 * s + 3][0], z[4 * s + 3][1])};
        st16_sc1(f, sws, lane * 16, WS_AF + (T * AFT + (hcol0 >> 4) + 4 * P + 2 * t + s) * 1024);
      }
      if (t == 0) mid();
      SBAR();
    }
  };

  if (isG) {
    lane_setup();
    const v4u sw2 = make_srd(w2, FF * DM * 2), swo = make_srd(wo, DM * DM * 2);
    const unsigned vw2 = (lane >> 3) * 1024 + swz, vw2b = vw2 + 7 * 1024;
    const int ro3 = rt % 3;
    auto kmap = [&](int kk) {
      if (kk < 16) return 8 * (((kk >> 2) + rt) & 3) + (kk & 3);
      const int p = ((kk - 16) >> 2) + ro3;
      return 8 * (p >= 3 ? p - 3 : p) + 4 + (kk & 3);
    };
    auto omap = [&](int i) { return 2 * ((i + rt) & 3) + (i >> 2); };
    auto wdmah = [&](int kk, int half) {
      const unsigned so = (kk < 28 ? 448 * m + 16 * kmap(kk) : 128 * m + 16 * omap(kk - 28)) * 1024 + 128 * ct;
      if (half == 0) dma16_a(kk < 28 ? sw2 : swo, vw2, so, lds0 + L_WG + m * PART + (kk % RD) * 2048); else dma16_b(kk < 28 ? sw2 : swo, vw2b, so);
    };
    auto wdma = [&](int kk) { wdmah(kk, 0); wdmah(kk, 1); };
    {
      const v4u swq = make_srd(wqkv, DM * 3 * DM * 2);
      const unsigned vx0 = (lane >> 3) * 1024 + (((lane & 7) ^ g) * 16), vx1 = (7 + (lane >> 3)) * 1024 + (((lane & 7) ^ (4 + g)) * 16);
      const unsigned vwq = (lane >> 2) * (3 * DM * 2) + p4 * 16;
      auto piece = [&](int s, int i) {
        const int kc = (s + rot) & 7;
        if (i < 4) {
          const unsigned so = (128 * r + 32 * m + 16 * (i >> 1)) * 1024 + kc * 128;
          if (i & 1) dma16_b(sx, vx1, so); else dma16_a(sx, vx0, so, lds0 + xbuf(s) + m * 4096 + (i >> 1) * 2048);
        } else if (i < 7) dma16(swq, vwq, (64 * kc + 16 * m) * (3 * DM * 2) + (512 * (i - 4) + 32 * j) * 2, lds0 + qbuf(s) + ((i - 4) * 4 + m) * 1024);
        else w1dma(0, s, lds0 + w1buf(s), i - 7);
      };
#pragma unroll
      for (int i = 0; i < 18; ++i) { piece(i / 9, i % 9); if (i == 8) __builtin_amdgcn_s_barrier(); }
      __builtin_amdgcn_s_setprio(PRIO_G1);
      hsetup(0);
      f32x4 cs[2] = {};
      v4u xf[4], wf[4][2];
      hac[0] = (f32x16){}; hac[1] = (f32x16){};
      static_for<0, 8>([&](auto sc) {
        constexpr int s = sc.value;
        auto fill = [&](int i) {
          if constexpr (s < 6) piece(s + 2, i);
          if constexpr (s == 6) {
            if (i == 0) { stat_load(); bias_load(0); }
            else if (i < 5) { if (heavy) w1dma(1, (i - 1) >> 1, lds0 + L_WA + ((i - 1) >> 1) * 8192, (i - 1) & 1); }
            else wdmah((i - 5) >> 1, (i - 5) & 1);
          }
          if constexpr (s == 7) {
            if (heavy && i < 4) w1dma(1, 2 + (i >> 1), lds0 + L_WA + (2 + (i >> 1)) * 8192, i & 1);
            if (heavy && i >= 4) { w1dma(1, i, lds0 + L_WA + i * 8192, 0); w1dma(1, i, 0, 1); }
          }
          SBAR();
        };
        auto rd = [&](int p) {
          xf[p] = lds128(xbuf(s) + xo[p]);
          wf[p][0] = ldtr2(rk[p][0] + (s % 3) * PART, 512); wf[p][1] = ldtr2(rk[p][1] + (s % 3) * PART, 512);
          SBAR();
        };
        if constexpr (s < 7) wait_vm<9>(); else { wait_vm1<4>(st); PIN(bias0); PIN(bias1); }
        bar();
        STAMP_AT(10 + s);
        rd(0);
        if constexpr (s < 7) fill(0);
        rd(1);
        if constexpr (s < 7) fill(1);
        if constexpr (s == 2) get_tag();
#pragma unroll
        for (int p = 0; p < 4; ++p) {
#pragma unroll
          for (int t = 0; t < 2; ++t) {
            hac[t] = MFMA32(wf[p][t], xf[p], hac[t]);
            if (p == 0) { cs[t] = MFMA16(sel, wf[p][t], cs[t]); PIN(cs[t]); }
            SBAR();
            if (p < 2 && t == 0) rd(p + 2);
            if (s == 7) fill(2 * p + t); else if (2 * p + t < 7) fill(2 + 2 * p + t);
          }
          if (s == 7 && p == 1) {
            cswr(0, cs[0]); cswr(1, cs[1]);
            bar();
            STAMP_AT(1);
          }
        }
        if constexpr (s == 7) {
          const f32x4 c0 = csrd(0), c1 = csrd(1);
          row_stats(); make_xaug();
          hac[0] = MFMA32(waug(c0, bias0), xaug, hac[0]);
          hac[1] = MFMA32(waug(c1, bias1), xaug, hac[1]);
          SBAR();
        }
      });
    }
    STAMP_AT(18);
    if (heavy) { wait_vm<8>(); arrive_nowait(lds0 + L_CNT + 16); }
    hgelu(std::integral_constant<int, 0>{}, [&]() {
      wait_vm<2>();
      if (heavy) arrive_nowait(lds0 + L_CNT + 20);
    });
    __builtin_amdgcn_s_setprio(0);
    STAMP_AT(19);
    unsigned rb2[2];
#pragma unroll
    for (int c = 0; c < 2; ++c) rb2[c] = lds0 + L_WG + m * PART + h * 512 + q4 * 128 + ((c ^ (q4 >> 1)) * 64) + (g & 1) * 32 + p4 * 8;
    v4u wq[36][2];
    auto wrd = [&](int kk) {
#pragma unroll
      for (int c = 0; c < 2; ++c) wq[kk][c] = ldtr2(rb2[c] + (kk % RD) * 2048, 1024);
    };
    auto wgot = [&](int kk) { PIN(wq[kk][0]); PIN(wq[kk][1]); };
    const int ftile = 2 * rt + ((lane >> 2) & 1);
    v4u af[28][2], of[8][2];
    v2u fl;
    const unsigned a0 = WS_AF + (2 * rt * AFT + 28 * m) * 1024, o0 = WS_AF + (2 * rt * AFT + 112 + 8 * m) * 1024;
    for (int kk = 2; kk < 6; ++kk) wdma(kk);
    wait_vm<8>();
    if (lane == 0) __hip_atomic_store(flag(FL_H0, T, j), tag, RLX_AGENT);
    STAMP_AT(2);
    wdma(6); wdma(7);
    static_for<0, 3>([&](auto ic) {
      constexpr int i = ic.value;
      wait_vm<(i == 0) ? 9 : 8>();
      for (int kk = 4 * i; kk < 4 * i + 4; ++kk) wrd(kk);
      for (int kk = 4 * i; kk < 4 * i + 4; ++kk) wgot(kk);
      if constexpr (i == 2) fl = ld8_sc1_async(sws, flag_off(FL_H0, ftile, 4 * m + p4));
      for (int kk = 4 * i; kk < 4 * i + 4; ++kk) wdma(kk + RD);
    });
    STAMP_AT(3);
    wait_vmf<8>(fl);
    if (!__all(fl[0] == taglo && fl[1] == tag32)) poll_flags(flag(FL_H0, ftile, 4 * m + p4), tag);
    STAMP_AT(4);
#pragma unroll
    for (int kk = 0; kk < AW; ++kk) {
      af[kk][0] = ld16_sc1_async(sws, lane * 16, a0 + kmap(kk) * 1024);
      af[kk][1] = ld16_sc1_async(sws, lane * 16, a0 + (AFT + kmap(kk)) * 1024);
    }
    __builtin_amdgcn_s_setprio(PRIO_G2);
    f32x16 c2[2][2] = {};
    auto mm2 = [&](int kk, const v4u (&a)[2], auto&& fill) {
      SBAR();
#pragma unroll
      for (int i = 0; i < 4; ++i) { c2[i >> 1][i & 1] = MFMA32(wq[kk][i & 1], a[i >> 1], c2[i >> 1][i & 1]); SBAR(); fill(i); SBAR(); }
    };
    static_for<0, 28>([&](auto kc) {
      constexpr int kk = kc.value;
      wait_vm2<(kk < AW) ? 2 * (AW - 1 - kk) + g2_range(0, kk) : g2_range(kk - AW + 1, kk)>(af[kk][0], af[kk][1]);
      if constexpr (kk == H1_CK) {
        STAMP_AT(20);
        wait_vmf<g2_range(H1_LD, H1_CK) - 1>(fl);
        if (!__all(fl[0] == taglo && fl[1] == tag32)) poll_flags(flag(FL_H1, ftile, 4 * m + p4), tag);
        STAMP_AT(21);
      }
      if constexpr (kk == H1_LD) fl = ld8_sc1_async(sws, flag_off(FL_H1, ftile, 4 * m + p4));
      if constexpr (kk == O_LD) fl = ld8_sc1_async(sws, flag_off(FL_O0, ftile, 4 * m + p4));
      constexpr bool dw = kk >= 1 && kk - 1 + NR + RD < 36;
      if constexpr (dw) wgot(kk - 1 + NR);
      mm2(kk, af[kk], [&](int i) {
        if constexpr (dw) { if (i < 2) wdmah(kk - 1 + NR + RD, i); }
        if constexpr (kk + NR < 28) { if (i == 1) wrd(kk + NR); }
        if constexpr (kk == 27) { if (i == 0) wrd(28); }
        if constexpr (kk + AW < 28) { if (i >= 2) af[kk + AW][i - 2] = ld16_sc1_async(sws, lane * 16, a0 + (AFT * (i - 2) + kmap(kk + AW)) * 1024); }
      });
      if constexpr (kk == O_CK) {
        wait_vmf<g2_range(O_LD, O_CK) - 1>(fl);
        if (!__all(fl[0] == taglo && fl[1] == tag32)) poll_flags(flag(FL_O0, ftile, 4 * m + p4), tag);
        res_loads();
#pragma unroll
        for (int i = 0; i < 4; ++i) {
          of[i][0] = ld16_sc1_async(sws, lane * 16, o0 + omap(i) * 1024);
          of[i][1] = ld16_sc1_async(sws, lane * 16, o0 + (AFT + omap(i)) * 1024);
        }
      }
    });
    static_for<0, 8>([&](auto kc) {
      constexpr int kk = kc.value;
      if (kk < 7) wrd(28 + kk + 1);
      if constexpr (kk == 1) {
        STAMP_AT(5);
        poll_flags(flag(FL_O1, ftile, 4 * m + p4), tag);
        STAMP_AT(6);
#pragma unroll
        for (int i = 4; i < 8; ++i) {
          of[i][0] = ld16_sc1_async(sws, lane * 16, o0 + omap(i) * 1024);
          of[i][1] = ld16_sc1_async(sws, lane * 16, o0 + (AFT + omap(i)) * 1024);
        }
      }
      wait_vm2<(kk == 0) ? 6 : 14 - 2 * kk>(of[kk][0], of[kk][1]);
      mm2(28 + kk, of[kk], [&](int) {});
    });
    PIN(xres); PIN(bres);
    asm volatile("" ::: "memory");
#pragma unroll
    for (int mt = 0; mt < 2; ++mt)
#pragma unroll
      for (int c = 0; c < 2; ++c)
#pragma unroll
        for (int qd = 0; qd < 4; ++qd) {
          const f32x4 v = {c2[mt][c][4 * qd], c2[mt][c][4 * qd + 1], c2[mt][c][4 * qd + 2], c2[mt][c][4 * qd + 3]};
          sts128(lds0 + L_WG + m * PART + ((mt * 8 + c * 4 + qd) * 66 + lane) * 16, __builtin_bit_cast(v4u, v));
        }
    STAMP_AT(7);
  } else {
    __builtin_amdgcn_s_setprio(1);
    lane_setup();
    v4u xr[2];
    xr[0] = ld16_async(sx, lane * 16, (128 * r + 8 * j + 2 * m) * 1024); xr[1] = ld16_async(sx, lane * 16, (128 * r + 8 * j + 2 * m + 1) * 1024);
    unsigned warm;
    {
      u64 pc; asm volatile("s_getpc_b64 %0" : "=s"(pc));
      warm = ld2_async(make_srd((const void*)(pc & ~127ull), CODEWARM), lane < 3 ? lane * 128 : 0x40000000u, (idx * 4 + m) * 384);
    }
    __builtin_amdgcn_s_barrier();
    v2u ep = ld8_sc1_async(sws, m == 0 ? WS_EPOCH : 0x40000000u);
    float sval;
    const int wi = blockIdx.x * 4 + m;
    const u64 wbase = wi < 448 ? (u64)w1 + wi * 4096 : wi < 896 ? (u64)w2 + (wi - 448) * 4096 : (u64)wo + (wi - 896) * 4096;
    auto make_stats = [&]() {
      wait_vm2<1>(xr[0], xr[1]);
      PIN(warm);
      float piv[2], a[4];
#pragma unroll
      for (int u = 0; u < 2; ++u) {
        piv[u] = rdl(bflo(xr[u][0]), 0);
        float s1 = 0.f, s2 = 0.f;
#pragma unroll
        for (int i = 0; i < 8; ++i) { const float d = ((i & 1) ? bfhi(xr[u][i >> 1]) : bflo(xr[u][i >> 1])) - piv[u]; s1 += d; s2 = __builtin_fmaf(d, d, s2); }
        a[2 * u] = s1; a[2 * u + 1] = s2;
      }
#pragma unroll
      for (int i = 0; i < 4; ++i) a[i] += dpp<0xB1>(a[i]);
#pragma unroll
      for (int i = 0; i < 4; ++i) a[i] += dpp<0x4E>(a[i]);
      float t = p4 == 0 ? a[0] : p4 == 1 ? a[1] : p4 == 2 ? a[2] : a[3];
      t += dpp<0x114>(t); t += dpp<0x118>(t);
      const auto s16 = __builtin_amdgcn_permlane16_swap(asu(t), asu(t), false, false);
      t = asf(s16[0]) + asf(s16[1]);
      const auto s32 = __builtin_amdgcn_permlane32_swap(asu(t), asu(t), false, false);
      t = (asf(s32[0]) + asf(s32[1])) * (1.f / DM);
      const float e1 = dpp<0xA0>(t);
      sval = (p4 & 1) ? __builtin_fmaxf(__builtin_fmaf(-e1, e1, t), 0.f) + 1e-5f : t + (p4 ? piv[1] : piv[0]);
      PIN(sval);
    };
    const unsigned acnt = lds0 + L_CNT;
    const unsigned vpos = 2048 + (g & 1) * 512 + h * 256 + (lane & 15) * 16;
    v4u qf[2], am0, am1;
    u64 um[16];
    auto masks = [&]() {
      const int dq = r32 - 4 * h;
#pragma unroll
      for (int d = 0; d < 4; ++d) {
        const int row = 2 * (d & 1) + 8 * (d >> 1);
        am0[d] = row > dq ? 0xffffffffu : row == dq ? 0xffff0000u : 0u;
        am1[d] = row + 16 > dq ? 0xffffffffu : row + 16 == dq ? 0xffff0000u : 0u;
      }
      PIN(am0); PIN(am1);
      static_for<0, 16>([&](auto u) { um[u.value] = upper_mask(u.value); pin_s(um[u.value]); });
    };
    {
      v4u xf[32];
      f32x16 acc[3] = {};
      f32x4 cs[3] = {};
      v4u wf[4][3];
      unsigned wb[4];
#pragma unroll
      for (int p = 0; p < 4; ++p) { wb[p] = qbuf(0) + ((p + m) & 3) * 1024 + (8 * h + q4) * 64 + (g & 1) * 32 + p4 * 8; PIN(wb[p]); }
      auto rd = [&](int ks, int t) {
        if (t == 0) xf[ks] = lds128(xbuf(ks >> 2) + xo[ks & 3]);
        wf[ks & 3][t] = ldtr2(wb[ks & 3] + ((ks >> 2) % 3) * 12288 + t * 4096, 256);
      };
      auto mm = [&](int ks, int t) {
        acc[t] = t == 2 ? MFMA32(xf[ks], wf[ks & 3][t], acc[t]) : MFMA32(wf[ks & 3][t], xf[ks], acc[t]);
        if ((ks & 3) == 0) { cs[t] = MFMA16(sel, wf[ks & 3][t], cs[t]); PIN(cs[t]); }
        SBAR();
      };
      make_stats();
      bar();
      STAMP_AT(10);
#pragma unroll
      for (int t = 0; t < 6; ++t) rd(t / 3, t % 3);
      SBAR();
      static_for<0, 8>([&](auto sc) {
        constexpr int s = sc.value;
#pragma unroll
        for (int p = 0; p < 2; ++p) {
#pragma unroll
          for (int t = 0; t < 3; ++t) { mm(4 * s + p, t); rd(4 * s + p + 2, t); SBAR(); }
          if (s == 7 && p == 0) { cswr(4, cs[0]); cswr(5, cs[1]); cswr(6, cs[2]); SBAR(); }
        }
        if constexpr (s == 0) {
          wait_vmf<0>(ep);
          if (m == 0 && lane < 10)
            *(LAS unsigned*)(size_t)(lds0 + L_CNT + lane * 4) = lane == 8 ? ep[0] * 0x9E3779B1u + 0x7F4A7C15u : lane == 9 ? ep[1] * 0x85EBCA77u + 0x165667B1u : 0u;
        }
        if constexpr (s == 1) {
          get_tag();
          if ((lane >> 2) == 3) {
            const v2u gr = {asu(sval), tag32};
            __builtin_amdgcn_raw_buffer_store_b64(gr, rws, WS_STATS + (128 * r + 8 * j + 2 * m) * 16 + p4 * 8, 0, 16);
          }
        }
        if constexpr (s == MIDWARM) {
          warm = ld2_async(make_srd((const void*)wbase, 4096), lane * 128, 0);
        }
        if constexpr (s == 5) stat_load();
        if constexpr (s == 6) {
          wait_vm1<0>(st);
          PIN(warm);

          row_stats();
        }
        mm(4 * s + 2, 0); mm(4 * s + 2, 1);
        bar();
        if constexpr (s < 7) {
          STAMP_AT(11 + s);
          mm(4 * s + 2, 2);
          rd(4 * s + 4, 0); rd(4 * s + 4, 1); rd(4 * s + 4, 2); SBAR();
#pragma unroll
          for (int t = 0; t < 3; ++t) { mm(4 * s + 3, t); rd(4 * s + 5, t); SBAR(); }
        }
      });
      STAMP_AT(1);
      __builtin_amdgcn_s_setprio(3);
      const f32x4 c5 = csrd(5), c6 = csrd(6), c4 = csrd(4);
      SBAR();
      mm(30, 2); mm(31, 1);
      make_xaug();
      SBAR();
      mm(31, 2); mm(31, 0);
      if (h == 0) *(LAS float*)(size_t)(lds0 + L_RS + (m * 32 + r32) * 4) = rstd;
      acc[1] = MFMA32(waug(c5, 0u), xaug, acc[1]);
      acc[2] = MFMA32(xaug, waug(c6, 0u), acc[2]);
      acc[0] = MFMA32(waug(c4, 0u), xaug, acc[0]);
      SBAR();
      f32x4 rs[4];
#pragma unroll
      for (int qd = 0; qd < 4; ++qd) rs[qd] = __builtin_bit_cast(f32x4, lds128(lds0 + L_RS + (m * 32 + 8 * qd + 4 * h) * 4));
      const unsigned kvo = WS_KV + (j * 64 + T) * 4096;
#pragma unroll
      for (int e = 0; e < 16; ++e) acc[1][e] = MUL1(acc[1][e], rstd);
#pragma unroll
      for (int e = 0; e < 2; ++e) {
        const v4u f = pack8(acc[1], e);
        st16_sc1(f, sws, lane * 16, kvo + e * 1024);
        sts128(lds0 + L_KV + (8 + m) * KVT + e * 1024 + lane * 16, f);
      }
#pragma unroll
      for (int qd = 0; qd < 4; ++qd)
#pragma unroll
        for (int e = 0; e < 4; ++e) acc[2][4 * qd + e] = MUL1(acc[2][4 * qd + e], rs[qd][e]);
#pragma unroll
      for (int s = 0; s < 2; ++s) {
        const v4u f = pack8(acc[2], s);
        st16_sc1(f, sws, lane * 16, kvo + 2048 + s * 1024);
        sts128(lds0 + L_KV + (8 + m) * KVT + s * VB + vpos, f);
      }
      SBAR();
      const float qs = rstd * (0.25f * 1.4426950408889634f);
#pragma unroll
      for (int e = 0; e < 16; ++e) acc[0][e] = MUL1(acc[0][e], qs);
      qf[0] = pack8(acc[0], 0); qf[1] = pack8(acc[0], 1);
      PIN(qf[0]); PIN(qf[1]);
      STAMP_AT(2);
      const int T0 = 4 * r - 8 + 2 * m;
      const bool live = T0 >= 0;
      auto kv_load = [&](v4u (&kv)[8], int i0, int i1) {
        const unsigned src = WS_KV + (j * 64 + (live ? T0 : 0)) * 4096, vo = live ? lane * 16 : h * 512;
#pragma unroll
        for (int i = i0; i < i1; ++i) kv[i] = ld16_sc1_async(sws, vo, src + (live ? i : (i & 3)) * 1024);
      };
      auto kv_stage = [&](v4u (&kv)[8]) {
        const unsigned on = (lane < 2 || live) ? one2 : 0u;
        if (lane < 6) sts128(lds0 + L_KV + (lane < 2 ? 8 + m : 2 * m + ((lane - 2) >> 1)) * KVT + 2048 + (lane & 1) * VB + 1024, (v4u){on, on, on, on});
        const v4u zz = {};
#pragma unroll
        for (int i = 0; i < 2; ++i) {
          sts128(lds0 + L_KV + (2 * m + i) * KVT + lane * 16, kv[4 * i]);
          sts128(lds0 + L_KV + (2 * m + i) * KVT + 1024 + lane * 16, kv[4 * i + 1]);
          sts128(lds0 + L_KV + (2 * m + i) * KVT + vpos, live ? kv[4 * i + 2] : zz);
          sts128(lds0 + L_KV + (2 * m + i) * KVT + VB + vpos, live ? kv[4 * i + 3] : zz);
        }
        arrive_nowait(acnt);
      };
      auto kv_wait = [&](v4u (&kv)[8]) {
        wait_vm<0>();
#pragma unroll
        for (int i = 0; i < 8; ++i) PIN(kv[i]);
      };
      if (heavy) {
        hsetup(1);
        await(acnt + 16, 4);
        STAMP_AT(3);
        v2u fl;
        v4u kv[8];
        hmm(std::integral_constant<int, 1>{}, xf, [&](auto kc) {
          constexpr int ks = kc.value;
          if constexpr (ks == KVPUB) {
            wait_vm<0>();
            if (lane == 0) __hip_atomic_store(flag(FL_KV, T, j), tag, RLX_AGENT);
          }
          if constexpr (ks == KVPUB + 2) bias_load(1);
          if constexpr (ks == 31) { wait_vm<1>(); PIN(bias0); PIN(bias1); }
          if constexpr (ks == 24) fl = ld8_sc1_async(sws, flag_off(FL_KV, live ? T0 + (lane & 1) : 0, j));
        });
        STAMP_AT(18);
        wait_vmf<0>(fl);
        if (!__all(fl[0] == taglo && fl[1] == tag32)) poll_flags(flag(FL_KV, live ? T0 + (lane & 1) : 0, j), tag);
        kv_load(kv, 0, 8);
        hgelu(std::integral_constant<int, 1>{}, [&]() {});
        STAMP_AT(4);
        masks();
        wait_vm<4>();
#pragma unroll
        for (int i = 0; i < 8; ++i) PIN(kv[i]);
        kv_stage(kv);
      } else {
        wait_vm<0>();
        if (lane == 0) __hip_atomic_store(flag(FL_KV, T, j), tag, RLX_AGENT);
        v4u kv[8];
        poll_flags(flag(FL_KV, live ? T0 + (lane & 1) : 0, j), tag);
        kv_load(kv, 0, 8); masks(); kv_wait(kv); kv_stage(kv);
      }
    }
    STAMP_AT(19);
    const unsigned kb = lds0 + L_KV + m * KVT + lane * 16;
    unsigned vb[2];
#pragma unroll
    for (int e = 0; e < 2; ++e) { vb[e] = lds0 + L_KV + m * KVT + 2048 + ((g & 1) ? 1024 : e * 512 + h * 256 + (lane & 15) * 16); PIN(vb[e]); }
    await(acnt, heavy ? 8 : 4);
    __builtin_amdgcn_s_setprio(PRIO_ATT);
    STAMP_AT(5);
    const f32x16 zero = {};
    auto o_store = [&](f32x16& o, int e) {
      const float rl = __builtin_amdgcn_rcpf(o[8]);
#pragma unroll
      for (int u = 0; u < 8; ++u) o[u] = MUL1(o[u], rl);
      st16_sc1(pack8(o, 0), sws, lane * 16, WS_AF + (T * AFT + 112 + 2 * j + e) * 1024);
    };
    int redo = 0;
    {
      f32x16 o[2], sa, sb, su;
      bool pend = false;
      v4u kn, ku, va, vc, vn0, vn1, vu0, vu1, p0, p1, pu;
      auto kaddr = [&](int n) { return kb + ((n & 7) + 1) * KVT + (n >> 3) * 1024; };
      auto vaddr = [&](int n) { return vb[n >> 3] + ((n & 7) + 1) * KVT; };
      kn = lds128(kaddr(0)); vn0 = lds128(vaddr(0)); vn1 = lds128(vaddr(0) + VB);
      sa = MFMA32(kn, qf[0], zero);
      kn = lds128(kaddr(1));
      SBAR();
      auto step = [&](auto nc, f32x16& sc, f32x16& sn) {
        constexpr int n = nc.value, e = n >> 3, i = n & 7;
        if constexpr (n < 15) sn = MFMA32(kn, qf[(n + 1) >> 3], zero);
        if constexpr (n < 14) kn = lds128(kaddr(n + 2));
        if constexpr (i == 5) ku = lds128(kb + e * 1024);
        v4u v0 = vn0, v1 = vn1;
        if constexpr (n < 15) { vn0 = lds128(vaddr(n + 1)); vn1 = lds128(vaddr(n + 1) + VB); }
        if constexpr (i == 6) { vu0 = lds128(vb[e]); vu1 = lds128(vb[e] + VB); }
        SBAR();
        if constexpr (i == 7) {
#pragma unroll
          for (int u = 0; u < 16; ++u) sc[u] = __builtin_amdgcn_inverse_ballot_w64(um[u]) ? su[u] : sc[u];
          PIN(sc); SBAR();
        }
#pragma unroll
        for (int u = 0; u < 6; ++u) sc[u] = EXP2(sc[u]);
        PIN(sc); SBAR();
        if constexpr (n == 8) { o[0] = MFMA32(vc, p1, o[0]); SBAR(); }
        else if constexpr (n > 0) { o[e] = MFMA32(vc, p1, o[e]); SBAR(); }
        sc[6] = EXP2(sc[6]); sc[7] = EXP2(sc[7]);
        p0 = pack8(sc, 0);
        sc[8] = EXP2(sc[8]); sc[9] = EXP2(sc[9]);
        if constexpr (i == 7) { va = p0 & am0; p0 &= ~am0; PIN(va); }
        PIN(sc); PIN(p0); SBAR();
        if constexpr (n == 8) { o[0] = MFMA32(vu1, pu, o[0]); SBAR(); }
        o[e] = MFMA32(v0, p0, i == 0 ? zero : o[e]);
        SBAR();
        if constexpr (i == 6) { su = MFMA32(ku, qf[e], zero); SBAR(); }
#pragma unroll
        for (int u = 10; u < 13; ++u) sc[u] = EXP2(sc[u]);
        if constexpr (i == 7) { PIN(sc); SBAR(); o[e] = MFMA32(vu0, va, o[e]); SBAR(); }
#pragma unroll
        for (int u = 13; u < 16; ++u) sc[u] = EXP2(sc[u]);
        p1 = pack8(sc, 1); vc = v1;
        if constexpr (i == 7) { pu = p1 & am1; p1 &= ~am1; PIN(pu); }
        PIN(p1); SBAR();
        if constexpr (n == 8) {
          if (__all(o[0][8] >= 0x1p-30f && o[0][8] <= 0x1p30f)) o_store(o[0], 0); else redo = 1;
          SBAR();
        }
        if constexpr (i == 2) pend = !redo;
        if constexpr (i == 3 || i == 5 || i == 7) {
          if (pend && (i == 7 || (__builtin_amdgcn_s_getreg(7 | (3 << 11)) & 15) == 0)) {
            wait_vm<0>();
            if (lane == 0) __hip_atomic_store(flag(FL_H1 + e, T, j), tag, RLX_AGENT);
            pend = false;
          }
        }
      };
      static_for<0, 8>([&](auto hc) {
        step(std::integral_constant<int, 2 * hc.value>{}, sa, sb);
        step(std::integral_constant<int, 2 * hc.value + 1>{}, sb, sa);
      });
      o[1] = MFMA32(vc, p1, o[1]);
      o[1] = MFMA32(vu1, pu, o[1]);
      if (__all(o[1][8] >= 0x1p-30f && o[1][8] <= 0x1p30f)) o_store(o[1], 1); else redo |= 2;
    }
    if (redo) {
#pragma unroll 1
      for (int e = 0; e < 2; ++e) {
        if (!((redo >> e) & 1)) continue;
        const v4u q = e ? qf[1] : qf[0];
        const unsigned vbe = e ? vb[1] : vb[0];
        auto merged = [&](const f32x16& c) {
          f32x16 sl = MFMA32(lds128(kb + 8 * KVT + e * 1024), q, c);
          const f32x16 su = MFMA32(lds128(kb + e * 1024), q, c);
          static_for<0, 16>([&](auto u) { sl[u.value] = upper<u.value>() ? su[u.value] : sl[u.value]; });
          return sl;
        };
        float mx = -INFINITY;
#pragma unroll 1
        for (int i = 0; i < 8; ++i) {
          const f32x16 sc = i ? MFMA32(lds128(kb + i * KVT + e * 1024), q, zero) : merged(zero);
#pragma unroll
          for (int u = 0; u < 16; u += 2) mx = max3(mx, sc[u], sc[u + 1]);
        }
        const auto sw = __builtin_amdgcn_permlane32_swap(asu(mx), asu(mx), false, false);
        mx = __builtin_fmaxf(asf(sw[0]), asf(sw[1]));
        f32x16 negm, o;
#pragma unroll
        for (int u = 0; u < 16; ++u) negm[u] = -mx;
        {
          f32x16 sc = merged(negm);
#pragma unroll
          for (int u = 0; u < 16; ++u) sc[u] = EXP2(sc[u]);
          const v4u p0 = pack8(sc, 0), p1 = pack8(sc, 1);
          o = MFMA32(lds128(vbe), p0 & am0, zero);
          o = MFMA32(lds128(vbe + VB), p1 & am1, o);
          o = MFMA32(lds128(vbe + 8 * KVT), p0 & ~am0, o);
          o = MFMA32(lds128(vbe + 8 * KVT + VB), p1 & ~am1, o);
        }
#pragma unroll 1
        for (int i = 1; i < 8; ++i) {
          f32x16 sc = MFMA32(lds128(kb + i * KVT + e * 1024), q, negm);
#pragma unroll
          for (int u = 0; u < 16; ++u) sc[u] = EXP2(sc[u]);
          o = MFMA32(lds128(vbe + i * KVT), pack8(sc, 0), o);
          o = MFMA32(lds128(vbe + i * KVT + VB), pack8(sc, 1), o);
        }
        o_store(o, e);
      }
      wait_vm<0>();
      if (lane == 0) __hip_atomic_store(flag(FL_O0, T, j), tag, RLX_AGENT);
    }
    STAMP_AT(6);
    wait_vm<0>();
    if (lane == 0) __hip_atomic_store(flag(FL_O1, T, j), tag, RLX_AGENT);
    STAMP_AT(7);
    res_loads();
    wait_vm2<0>(xres, bres);
    if (blockIdx.x == 0 && wave == 7) {
#pragma unroll 1
      for (int i = 0; i < 4; ++i) poll_flags(flag(FL_KV, 4 * (4 * i + g), lane & 15), tag);
      if (lane == 0) __hip_atomic_store((GAS u64*)(ws + WS_EPOCH), tag, RLX_AGENT);
    }
  }
  bar();
  {
    int t2 = threadIdx.x; PIN(t2);
    const int row = t2 >> 3, ch = t2 & 7, orow = 64 * rt + row, ocol = 64 * ct + ch * 8;
    const unsigned a = lds0 + L_WG + (((row >> 5) * 8 + ch) * 66 + (row & 31)) * 16;
    f32x4 lo = {}, hi = {};
#pragma unroll
    for (int w = 0; w < 4; ++w) {
      lo += __builtin_bit_cast(f32x4, lds128(a + w * PART));
      hi += __builtin_bit_cast(f32x4, lds128(a + w * PART + 512));
    }
    v4u o;
    o[0] = pk2(lo[0] + bflo(xres[0]) + bflo(bres[0]), lo[1] + bfhi(xres[0]) + bfhi(bres[0]));
    o[1] = pk2(lo[2] + bflo(xres[1]) + bflo(bres[1]), lo[3] + bfhi(xres[1]) + bfhi(bres[1]));
    o[2] = pk2(hi[0] + bflo(xres[2]) + bflo(bres[2]), hi[1] + bfhi(xres[2]) + bfhi(bres[2]));
    o[3] = pk2(hi[2] + bflo(xres[3]) + bflo(bres[3]), hi[3] + bfhi(xres[3]) + bfhi(bres[3]));
    __builtin_amdgcn_raw_buffer_store_b128(o, __builtin_amdgcn_make_buffer_rsrc(out, 0, SEQ * DM * 2, 0x00020000), orow * 1024 + ocol * 2, 0, 16);
  }
  STAMP_AT(8);
}

extern "C" void kernel_launch(void* const* d_in, const int* in_sizes, int n_in, void* d_out, int out_size, void* d_ws, size_t ws_size,
                              hipStream_t stream) {
  (void)in_sizes; (void)n_in; (void)out_size; (void)ws_size;
  (void)hipFuncSetAttribute((const void*)fused_block, hipFuncAttributeMaxDynamicSharedMemorySize, LDS_BYTES);
  fused_block<<<256, 512, LDS_BYTES, stream>>>(d_in[0], d_in[1], d_in[2], d_in[4], d_in[5], d_in[6], d_in[7], d_out, (char*)d_ws);
}
```

```cpp
#include <hip/hip_runtime.h>
#include <hip/hip_bf16.h>
#include <stdint.h>
#include <type_traits>

typedef unsigned v4u __attribute__((ext_vector_type(4)));
typedef unsigned v2u __attribute__((ext_vector_type(2)));
typedef float f32x2 __attribute__((ext_vector_type(2)));
typedef float f32x4 __attribute__((ext_vector_type(4)));
typedef float f32x16 __attribute__((ext_vector_type(16)));
typedef short bf16x8 __attribute__((ext_vector_type(8)));
typedef short s16x4 __attribute__((ext_vector_type(4)));
typedef __bf16 bf16x2_t __attribute__((ext_vector_type(2)));
typedef unsigned long long u64;
#define GAS __attribute__((address_space(1)))
#define LAS __attribute__((address_space(3)))
#define DI __device__ __forceinline__
#define RLX_AGENT __ATOMIC_RELAXED, __HIP_MEMORY_SCOPE_AGENT

constexpr int SEQ = 2048, DM = 512, FF = 1792;
constexpr unsigned WS_EPOCH = 0;
constexpr unsigned WS_STATS = 32768;
constexpr unsigned WS_FLAG = 65536;
constexpr unsigned WS_KV = WS_FLAG + 5 * 1024 * 128;
constexpr int AFT = 145;
constexpr unsigned WS_AF = WS_KV + 16 * 64 * 4096;
constexpr unsigned WS_END = WS_AF + 64 * AFT * 1024;
constexpr unsigned WS_STAMP = WS_END;
enum { FL_KV = 0, FL_H0 = 1, FL_H1 = 2, FL_O0 = 3, FL_O1 = 4 };
constexpr int L_WA = 0;
constexpr int L_WG = 86016;
constexpr int VB = 1040;
constexpr int KVT = 2048 + 2 * VB;
constexpr int L_KV = 65536 - 8 * KVT;
constexpr int RD = 8, NR = 12;
constexpr int PART = 16 * 66 * 16;
constexpr int L_CS = L_WG + 4 * PART;
constexpr int L_RS = L_CS + 3584;
constexpr int L_CNT = L_RS + 512;
constexpr int LDS_BYTES = L_CNT + 64;
constexpr int xbuf(int s) { return ((s + 1) % 3) * 16384; }
constexpr int qbuf(int s) { return 49152 + (s % 3) * 12288; }
constexpr int w1buf(int s) { return L_WG + (s % 3) * PART + 8192; }
static_assert(xbuf(7) == 32768 && qbuf(2) + 12288 <= L_WG && L_KV + 12 * KVT <= L_WG && PART >= RD * 2048 && LDS_BYTES <= 163840, "lds map");
#define AWIN 6
constexpr int AW = AWIN;
constexpr unsigned SPIN_CAP = 1u << 23;

#define MFMA32(a, b, c) __builtin_amdgcn_mfma_f32_32x32x16_bf16(__builtin_bit_cast(bf16x8, a), __builtin_bit_cast(bf16x8, b), c, 0, 0, 0)
#define MFMA16(a, b, c) __builtin_amdgcn_mfma_f32_16x16x32_bf16(__builtin_bit_cast(bf16x8, a), __builtin_bit_cast(bf16x8, b), c, 0, 0, 0)
#define SBAR() __builtin_amdgcn_sched_barrier(0)

#define STAMP_AT(i) do { } while (0)

DI float asf(unsigned u) { return __builtin_bit_cast(float, u); }
DI unsigned asu(float f) { return __builtin_bit_cast(unsigned, f); }
DI unsigned pk2(float lo, float hi) { return __builtin_bit_cast(unsigned, __builtin_convertvector((f32x2){lo, hi}, bf16x2_t)); }
DI float bflo(unsigned u) { return __builtin_bit_cast(float, u << 16); }
DI float bfhi(unsigned u) { return __builtin_bit_cast(float, u & 0xffff0000u); }
DI v4u make_srd(const void* p, unsigned bytes) { const u64 a = (u64)p; return (v4u){(unsigned)a, (unsigned)(a >> 32) & 0xffffu, bytes, 0x00020000u}; }
DI void dma16(v4u srd, unsigned voff, unsigned soff, unsigned lds) {
  asm volatile("s_mov_b32 m0, %3\n\ts_nop 0\n\tbuffer_load_dwordx4 %0, %1, %2 offen lds" :: "v"(voff), "s"(srd), "s"(soff), "s"(lds) : "memory");
}
DI void dma16_a(v4u srd, unsigned voff, unsigned soff, unsigned lds) { dma16(srd, voff, soff, lds); }
DI void dma16_b(v4u srd, unsigned voff1, unsigned soff) {
  asm volatile("buffer_load_dwordx4 %0, %1, %2 offen offset:1024 lds" :: "v"(voff1), "s"(srd), "s"(soff) : "memory");
}
DI v4u ld16_sc1_async(v4u srd, unsigned voff, unsigned soff) {
  v4u r; asm volatile("buffer_load_dwordx4 %0, %1, %2, %3 offen sc1" : "=v"(r) : "v"(voff), "s"(srd), "s"(soff) : "memory"); return r;
}
DI v4u ld16_async(v4u srd, unsigned voff, unsigned soff) {
  v4u r; asm volatile("buffer_load_dwordx4 %0, %1, %2, %3 offen" : "=v"(r) : "v"(voff), "s"(srd), "s"(soff) : "memory"); return r;
}
DI unsigned ld2_async(v4u srd, unsigned voff, unsigned soff) {
  unsigned r; asm volatile("buffer_load_ushort %0, %1, %2, %3 offen" : "=v"(r) : "v"(voff), "s"(srd), "s"(soff) : "memory"); return r;
}
DI void st16_sc1(v4u v, v4u srd, unsigned voff, unsigned soff) {
  asm volatile("buffer_store_dwordx4 %0, %1, %2, %3 offen sc1\n\ts_nop 1" :: "v"(v), "v"(voff), "s"(srd), "s"(soff) : "memory");
}
template <int N> DI void wait_vm1(v4u& a) { asm volatile("s_waitcnt vmcnt(%1)" : "+v"(a) : "i"(N) : "memory"); }
template <int N> DI void wait_vm2(v4u& a, v4u& b) { asm volatile("s_waitcnt vmcnt(%2)" : "+v"(a), "+v"(b) : "i"(N) : "memory"); }
template <int N> DI void wait_vm3(v4u& a, v4u& b, v4u& c) { asm volatile("s_waitcnt vmcnt(%3)" : "+v"(a), "+v"(b), "+v"(c) : "i"(N) : "memory"); }
DI void pin_s(u64& v) { asm volatile("" : "+s"(v)); }
template <class V> DI void pin(V& v) { asm volatile("" : "+v"(v)); }
template <int N> DI void wait_vm() { asm volatile("s_waitcnt vmcnt(%0)" :: "i"(N) : "memory"); }
DI void bar() { asm volatile("s_waitcnt lgkmcnt(0)\n\ts_barrier" ::: "memory"); }
DI v4u lds128(unsigned a) { return *(const LAS v4u*)(size_t)a; }
DI void sts128(unsigned a, v4u v) { *(LAS v4u*)(size_t)a = v; }
DI v2u ldtr(unsigned a) { return __builtin_bit_cast(v2u, __builtin_amdgcn_ds_read_tr16_b64_v4i16((LAS s16x4*)(size_t)a)); }
DI v4u ldtr2(unsigned a, int second) { const v2u lo = ldtr(a), hi = ldtr(a + second); return (v4u){lo.x, lo.y, hi.x, hi.y}; }
template <int CTRL> DI float dpp(float v) { return __builtin_bit_cast(float, __builtin_amdgcn_update_dpp(0, __builtin_bit_cast(int, v), CTRL, 0xf, 0xf, true)); }
DI float rdl(float v, int l) { return __builtin_bit_cast(float, __builtin_amdgcn_readlane(__builtin_bit_cast(int, v), l)); }
DI v4u pack8(const f32x16& a, int s) { return (v4u){pk2(a[8 * s], a[8 * s + 1]), pk2(a[8 * s + 2], a[8 * s + 3]), pk2(a[8 * s + 4], a[8 * s + 5]), pk2(a[8 * s + 6], a[8 * s + 7])}; }
DI void poll_flags(const GAS u64* p, u64 tag) {
#pragma unroll 1
  for (unsigned spin = 0; spin < SPIN_CAP; ++spin) {
    const u64 v = __hip_atomic_load(p, RLX_AGENT);
    if (__all(v == tag)) break;
    __builtin_amdgcn_s_sleep(2);
  }
  asm volatile("" ::: "memory");
}
constexpr u64 upper_mask(int reg) {
  u64 m = 0;
  for (int l = 0; l < 64; ++l) { const int row = (reg & 3) + 8 * (reg >> 2) + 4 * (l >> 5); if (row > (l & 31)) m |= 1ull << l; }
  return m;
}
template <int REG> DI bool upper() { return __builtin_amdgcn_inverse_ballot_w64(upper_mask(REG)); }
template <int I, int N, class Fn> DI void static_for(Fn&& f) { if constexpr (I < N) { f(std::integral_constant<int, I>{}); static_for<I + 1, N>(f); } }
DI float max3(float a, float b, float c) { return __builtin_fmaxf(__builtin_fmaxf(a, b), c); }

constexpr int H1_CK = 16 - AW, H1_LD = H1_CK - 3, O_CK = 28 - AW, O_LD = O_CK - 3;
constexpr int g2_issued(int t) { return ((t >= 1 && t - 1 + NR + RD < 36) ? 2 : 0) + ((t + AW < 28) ? 2 : 0) + (t == H1_LD) + (t == O_LD) + (t == O_CK ? 10 : 0); }
constexpr int g2_range(int a, int b) { int n = 0; for (int t = a; t < b; ++t) n += g2_issued(t); return n; }
#define GELU_PK(P) (P == 1)
#define PRIO_G2 2
#define PRIO_ATT 3
#define PRIO_G1 2
#define ROT r
#define CODEWARM 38000
#define MIDWARM 1
#define KVPUB 6
#define EXP2(v) __builtin_amdgcn_exp2f(v)
#define PIN(v) pin(v)
extern "C" __device__ float mul_unpacked(float, float) __asm("llvm.amdgcn.fmul.legacy");
#define MUL1(a, b) mul_unpacked(a, b)
#define FMA1(a, b, c) __builtin_amdgcn_div_fmasf(a, b, c, false)
DI v2u ld8_sc1_async(v4u srd, unsigned voff) {
  v2u r; asm volatile("buffer_load_dwordx2 %0, %1, %2, 0 offen sc1" : "=v"(r) : "v"(voff), "s"(srd) : "memory"); return r;
}
template <int N> DI void wait_vmf(v2u& a) { asm volatile("s_waitcnt vmcnt(%1)" : "+v"(a) : "i"(N) : "memory"); }
DI void arrive(unsigned cnt, int lane) {
  asm volatile("s_waitcnt lgkmcnt(0)" ::: "memory");
  if (lane == 0) __hip_atomic_fetch_add((LAS unsigned*)(size_t)cnt, 1u, __ATOMIC_RELAXED, __HIP_MEMORY_SCOPE_WORKGROUP);
}
DI void arrive_nowait(unsigned cnt) {
  asm volatile("s_mov_b64 exec, 1\n\tds_add_u32 %0, %1\n\ts_mov_b64 exec, -1" :: "v"(cnt), "v"(1u) : "memory");
}
DI unsigned peek(unsigned cnt) { return __hip_atomic_load((LAS unsigned*)(size_t)cnt, __ATOMIC_RELAXED, __HIP_MEMORY_SCOPE_WORKGROUP); }
DI void await(unsigned cnt, unsigned n) {
#pragma unroll 1
  for (unsigned spin = 0; spin < SPIN_CAP * 32; ++spin) {
    if (__builtin_amdgcn_readfirstlane(peek(cnt)) >= n) break;
    __builtin_amdgcn_s_sleep(1);
  }
  asm volatile("" ::: "memory");
}

__global__ __launch_bounds__(512, 2) void fused_block(const void* __restrict__ x, const void* __restrict__ wqkv, const void* __restrict__ wo,
                                                      const void* __restrict__ w1, const void* __restrict__ b1, const void* __restrict__ w2,
                                                      const void* __restrict__ b2, void* __restrict__ out, char* __restrict__ ws) {
  constexpr unsigned lds0 = 0;
  const int wave = __builtin_amdgcn_readfirstlane(threadIdx.x >> 6);
  const int m = wave & 3;
  const bool isG = wave < 4;
  const int xcd = blockIdx.x & 7, idx = blockIdx.x >> 3;
  const int r = (xcd >> 2) * 8 + (idx >> 2), j = (xcd & 3) * 4 + (idx & 3);
  const int rt = (xcd >> 1) * 8 + (idx >> 2), ct = (xcd & 1) * 4 + (idx & 3);
  int lane = threadIdx.x & 63, r32, h, g, q4, p4;
  STAMP_AT(0);
  unsigned vx0 = (lane >> 3) * 1024 + (((lane & 7) ^ (lane >> 4)) * 16), vx1 = (7 + (lane >> 3)) * 1024 + (((lane & 7) ^ (4 + (lane >> 4))) * 16);
  unsigned vwq = (lane >> 2) * (3 * DM * 2) + (lane & 3) * 16;
  unsigned vw1 = (lane >> 3) * (FF * 2) + ((((lane >> 2) ^ (lane >> 4)) & 1) * 64) + (lane & 3) * 16, vw1b = vw1 + 8 * (FF * 2) - 1024;
  PIN(vx0); PIN(vx1); PIN(vwq); PIN(vw1); PIN(vw1b);
  asm volatile("" :: "s"(x), "s"(wqkv), "s"(wo), "s"(w1), "s"(b1), "s"(w2), "s"(b2), "s"(out), "s"(ws));

  const v4u sx = make_srd(x, SEQ * DM * 2), sws = make_srd(ws, WS_END);
  const __amdgpu_buffer_rsrc_t rws = __builtin_amdgcn_make_buffer_rsrc(ws, 0, WS_END, 0x00020000);
  const unsigned one2 = 0x3f803f80u;
  const int T = 4 * r + m;
  const int rot = ROT;
  auto flag_off = [&](int kind, int tile, int jj) { return WS_FLAG + ((kind * 64 + tile) * 16 + jj) * 128; };
  auto flag = [&](int kind, int tile, int jj) { return (GAS u64*)(ws + flag_off(kind, tile, jj)); };
  u64 tag;
  unsigned taglo, tag32;
  auto get_tag = [&]() {
    const v2u tg = *(const LAS v2u*)(size_t)(lds0 + L_CNT + 32);
    taglo = __builtin_amdgcn_readfirstlane(tg[0]); tag32 = __builtin_amdgcn_readfirstlane(tg[1]);
    tag = ((u64)tag32 << 32) | taglo;
  };
  v4u xres, bres, xaug, st;
  float mean, var, rstd, sd;
  v4u sel;
  unsigned xo[4], swz;
  auto lane_setup = [&]() {
    lane = threadIdx.x & 63; PIN(lane);
    r32 = lane & 31; h = lane >> 5; g = lane >> 4; q4 = (lane >> 2) & 3; p4 = lane & 3;
    const unsigned selv = ((lane & 15) == (g & 1)) ? one2 : 0u;
    sel = (v4u){selv, selv, selv, selv};
#pragma unroll
    for (int p = 0; p < 4; ++p) xo[p] = (32 * m + r32) * 128 + (((2 * ((p + m) & 3) + h) ^ ((r32 >> 1) & 7)) * 16);
    swz = ((((lane >> 2) & 1) ^ (g & 1)) * 64) + p4 * 16;
  };

  auto stat_load = [&]() { st = ld16_sc1_async(sws, WS_STATS + (128 * r + 32 * m + r32) * 16, 0); };
  auto res_loads = [&]() {
    int t2 = threadIdx.x; PIN(t2);
    const int row = 64 * rt + (t2 >> 3), col = 64 * ct + (t2 & 7) * 8;
    xres = ld16_async(sx, row * 1024 + col * 2, 0);
    bres = ld16_async(make_srd(b2, DM * 2), col * 2, 0);
  };
  auto row_stats = [&]() {
    if (!__all(st[1] == tag32 && st[3] == tag32)) {
#pragma unroll 1
      for (unsigned spin = 0; spin < SPIN_CAP; ++spin) {
        st = ld16_sc1_async(sws, WS_STATS + (128 * r + 32 * m + r32) * 16, 0);
        wait_vm1<0>(st);
        if (__all(st[1] == tag32 && st[3] == tag32)) break;
      }
    }
    mean = asf(st[0]); var = asf(st[2]);
    PIN(mean); PIN(var);
  };
  auto make_xaug = [&]() {
    PIN(mean); PIN(var);
    rstd = __builtin_amdgcn_rsqf(var); sd = var * rstd;
    const unsigned hi = pk2(-mean, sd);
    const unsigned lo = pk2(-mean - bflo(hi), FMA1(bfhi(hi), -1.f, sd));
    xaug = (v4u){(hi & 0xffffu) | (hi << 16), (lo & 0xffffu) | (hi & 0xffff0000u), lo >> 16, 0u};
    if (h) xaug = (v4u){0u, 0u, 0u, 0u};
    PIN(xaug);
  };
  auto cswr = [&](int tile, f32x4 c) {
    if (lane < 16) { LAS float* p = (LAS float*)(size_t)(lds0 + L_CS + (tile * 32 + lane) * 16 + m * 4); p[0] = c[0]; p[64] = c[1]; }
  };
  auto csrd = [&](int tile) { return __builtin_bit_cast(f32x4, lds128(lds0 + L_CS + (tile * 32 + r32) * 16)); };
  auto waug = [&](f32x4 c4, unsigned b16) {
    const float c = ((c4[0] + c4[1]) + c4[2]) + c4[3];
    const unsigned hi = pk2(c, 0.f);
    const unsigned lo = pk2(c - bflo(hi), 0.f);
    return (v4u){hi | (lo << 16), hi | (b16 << 16), b16, 0u};
  };

  const v4u sw1 = make_srd(w1, DM * FF * 2);
  const int hcol0 = 448 * (j >> 2) + 128 * (j & 3);
  const bool heavy = (j & 3) != 3;
  unsigned bias0, bias1;
  auto bias_load = [&](int P) {
    bias0 = ld2_async(make_srd(b1, FF * 2), r32 * 2, (hcol0 + 64 * P) * 2);
    bias1 = ld2_async(make_srd(b1, FF * 2), r32 * 2, (hcol0 + 64 * P + 32) * 2);
  };
  auto w1dma = [&](int P, int c, unsigned dst, int half) {
    const unsigned so = (64 * ((c + rot) & 7) + 16 * m) * (FF * 2) + (hcol0 + 64 * P) * 2;
    if (half == 0) dma16_a(sw1, vw1, so, dst + m * 2048); else dma16_b(sw1, vw1b, so);
  };
  f32x16 hac[2];
  unsigned rk[4][2];
  auto hsetup = [&](int P) {
#pragma unroll
    for (int p = 0; p < 4; ++p)
#pragma unroll
      for (int c = 0; c < 2; ++c) {
        rk[p][c] = lds0 + (P ? L_WA : w1buf(0)) + ((p + m) & 3) * 2048 + h * 1024 + q4 * 128 + ((c ^ (q4 >> 1)) * 64) + (g & 1) * 32 + p4 * 8;
        PIN(rk[p][c]);
      }
  };
  auto hmm = [&](auto pc, const v4u (&xf)[32], auto&& hook) {
    constexpr int P = pc.value;
    constexpr int D = 4;
    const unsigned cnt = lds0 + L_CNT;
    f32x4 cs[2] = {}, c0, c1;
    v4u wf[D][2], au0, au1;
    unsigned seen = 0;
    hac[0] = (f32x16){}; hac[1] = (f32x16){};
    auto rd = [&](int ks, int t) { wf[ks % D][t] = ldtr2(rk[ks & 3][t] + (ks >> 2) * 8192, 512); };
#pragma unroll
    for (int ks = 0; ks < D; ++ks) { rd(ks, 0); rd(ks, 1); }
    SBAR();
    static_for<0, 32>([&](auto kc) {
      constexpr int ks = kc.value;
      hook(kc);
      if constexpr (ks == 10) seen = peek(cnt + 20);
      if constexpr (ks == 29) seen = peek(cnt);
#pragma unroll
      for (int t = 0; t < 2; ++t) {
        hac[t] = MFMA32(wf[ks % D][t], xf[ks], hac[t]);
        if ((ks & 3) == 0 && ks < 28) { cs[t] = MFMA16(sel, wf[ks % D][t], cs[t]); PIN(cs[t]); }
        if (ks == 26) { cs[t] = MFMA16(sel, wf[28 % D][t], cs[t]); PIN(cs[t]); }
        SBAR();
        if constexpr (ks + D < 32) rd(ks + D, t);
        if constexpr (ks == 31) { if (t == 0) { au0 = waug(c0, bias0); PIN(au0); } else { au1 = waug(c1, bias1); PIN(au1); } }
        SBAR();
      }
      if constexpr (ks == 11 || ks == 30) {
        PIN(hac[0]); PIN(hac[1]);
        if (__builtin_amdgcn_readfirstlane(seen) < 4) await(ks == 11 ? cnt + 20 : cnt, 4);
        asm volatile("" ::: "memory");
        if constexpr (ks == 30) { c0 = csrd(2 * P); c1 = csrd(2 * P + 1); }
      }
      if constexpr (ks == 27) { cswr(2 * P, cs[0]); cswr(2 * P + 1, cs[1]); arrive_nowait(cnt); }
      SBAR();
    });
    hac[0] = MFMA32(au0, xaug, hac[0]);
    hac[1] = MFMA32(au1, xaug, hac[1]);
    SBAR();
  };
  auto hgelu = [&](auto pc, auto&& mid) {
    constexpr int P = pc.value;
    constexpr bool PK = GELU_PK(P);
    const float C0 = 2.3022081986f, C1 = C0 * 0.044715f;
    const float ga = -C1 * rstd * rstd * rstd, gb = -C0 * rstd;
    auto mul = [&](f32x2 a, f32x2 b) { return PK ? a * b : (f32x2){MUL1(a[0], b[0]), MUL1(a[1], b[1])}; };
    auto fma = [&](f32x2 a, float b, float c) {
      return PK ? __builtin_elementwise_fma(a, (f32x2){b, b}, (f32x2){c, c}) : (f32x2){FMA1(a[0], b, c), FMA1(a[1], b, c)}; };
#pragma unroll
    for (int t = 0; t < 2; ++t) {
      f32x2 v[8], z[8];
#pragma unroll
      for (int e = 0; e < 8; ++e) { v[e] = (f32x2){hac[t][2 * e], hac[t][2 * e + 1]}; z[e] = mul(v[e], v[e]); }
      SBAR();
#pragma unroll
      for (int e = 0; e < 8; ++e) z[e] = fma(z[e], ga, gb);
      SBAR();
#pragma unroll
      for (int e = 0; e < 8; ++e) z[e] = mul(z[e], v[e]);
      SBAR();
#pragma unroll
      for (int e = 0; e < 8; ++e) z[e] = (f32x2){EXP2(z[e][0]), EXP2(z[e][1])};
      SBAR();
#pragma unroll
      for (int e = 0; e < 8; ++e) z[e] = fma(z[e], sd, sd);
      SBAR();
#pragma unroll
      for (int e = 0; e < 8; ++e) z[e] = (f32x2){__builtin_amdgcn_rcpf(z[e][0]), __builtin_amdgcn_rcpf(z[e][1])};
      SBAR();
#pragma unroll
      for (int e = 0; e < 8; ++e) z[e] = mul(z[e], v[e]);
      SBAR();
#pragma unroll
      for (int s = 0; s < 2; ++s) {
        const v4u f = {pk2(z[4 * s][0], z[4 * s][1]), pk2(z[4 * s + 1][0], z[4 * s + 1][1]), pk2(z[4 * s + 2][0], z[4 * s + 2][1]), pk2(z[4 * s + 3][0], z[4 * s + 3][1])};
        st16_sc1(f, sws, lane * 16, WS_AF + (T * AFT + (hcol0 >> 4) + 4 * P + 2 * t + s) * 1024);
      }
      if (t == 0) mid();
      SBAR();
    }
  };

  const v4u swq = make_srd(wqkv, DM * 3 * DM * 2);
  auto piece = [&](int s, int i) {
    const int kc = (s + rot) & 7;
    if (i < 4) {
      const unsigned so = (128 * r + 32 * m + 16 * (i >> 1)) * 1024 + kc * 128;
      if (i & 1) dma16_b(sx, vx1, so); else dma16_a(sx, vx0, so, lds0 + xbuf(s) + m * 4096 + (i >> 1) * 2048);
    } else if (i < 7) dma16(swq, vwq, (64 * kc + 16 * m) * (3 * DM * 2) + (512 * (i - 4) + 32 * j) * 2, lds0 + qbuf(s) + ((i - 4) * 4 + m) * 1024);
    else w1dma(0, s, lds0 + w1buf(s), i - 7);
  };
  if (isG) {
#pragma unroll
    for (int i = 0; i < 9; ++i) piece(0, i);
  }
  asm volatile("" ::: "memory");

  if (isG) {
    __builtin_amdgcn_s_barrier();
    lane_setup();
    const v4u sw2 = make_srd(w2, FF * DM * 2), swo = make_srd(wo, DM * DM * 2);
    const unsigned vw2 = (lane >> 3) * 1024 + swz, vw2b = vw2 + 7 * 1024;
    const int ro3 = rt % 3;
    auto kmap = [&](int kk) {
      if (kk < 16) return 8 * (((kk >> 2) + rt) & 3) + (kk & 3);
      const int p = ((kk - 16) >> 2) + ro3;
      return 8 * (p >= 3 ? p - 3 : p) + 4 + (kk & 3);
    };
    auto omap = [&](int i) { return 2 * ((i + rt) & 3) + (i >> 2); };
    auto wdmah = [&](int kk, int half) {
      const unsigned so = (kk < 28 ? 448 * m + 16 * kmap(kk) : 128 * m + 16 * omap(kk - 28)) * 1024 + 128 * ct;
      if (half == 0) dma16_a(kk < 28 ? sw2 : swo, vw2, so, lds0 + L_WG + m * PART + (kk % RD) * 2048); else dma16_b(kk < 28 ? sw2 : swo, vw2b, so);
    };
    auto wdma = [&](int kk) { wdmah(kk, 0); wdmah(kk, 1); };
    {
#pragma unroll
      for (int i = 0; i < 9; ++i) piece(1, i);
      __builtin_amdgcn_s_setprio(PRIO_G1);
      hsetup(0);
      f32x4 cs[2] = {};
      v4u xf[4], wf[4][2];
      hac[0] = (f32x16){}; hac[1] = (f32x16){};
      static_for<0, 8>([&](auto sc) {
        constexpr int s = sc.value;
        auto fill = [&](int i) {
          if constexpr (s < 6) piece(s + 2, i);
          if constexpr (s == 6) {
            if (i == 0) { stat_load(); bias_load(0); }
            else if (i < 5) { if (heavy) w1dma(1, (i - 1) >> 1, lds0 + L_WA + ((i - 1) >> 1) * 8192, (i - 1) & 1); }
            else wdmah((i - 5) >> 1, (i - 5) & 1);
          }
          if constexpr (s == 7) {
            if (heavy && i < 4) w1dma(1, 2 + (i >> 1), lds0 + L_WA + (2 + (i >> 1)) * 8192, i & 1);
            if (heavy && i >= 4) { w1dma(1, i, lds0 + L_WA + i * 8192, 0); w1dma(1, i, 0, 1); }
          }
          SBAR();
        };
        auto rd = [&](int p) {
          xf[p] = lds128(xbuf(s) + xo[p]);
          wf[p][0] = ldtr2(rk[p][0] + (s % 3) * PART, 512); wf[p][1] = ldtr2(rk[p][1] + (s % 3) * PART, 512);
          SBAR();
        };
        if constexpr (s < 7) wait_vm<9>(); else { wait_vm1<4>(st); PIN(bias0); PIN(bias1); }
        bar();
        STAMP_AT(10 + s);
        rd(0);
        if constexpr (s < 7) fill(0);
        rd(1);
        if constexpr (s < 7) fill(1);
        if constexpr (s == 2) get_tag();
#pragma unroll
        for (int p = 0; p < 4; ++p) {
#pragma unroll
          for (int t = 0; t < 2; ++t) {
            hac[t] = MFMA32(wf[p][t], xf[p], hac[t]);
            if (p == 0) { cs[t] = MFMA16(sel, wf[p][t], cs[t]); PIN(cs[t]); }
            SBAR();
            if (p < 2 && t == 0) rd(p + 2);
            if (s == 7) fill(2 * p + t); else if (2 * p + t < 7) fill(2 + 2 * p + t);
          }
          if (s == 7 && p == 1) {
            cswr(0, cs[0]); cswr(1, cs[1]);
            bar();
            STAMP_AT(1);
          }
        }
        if constexpr (s == 7) {
          const f32x4 c0 = csrd(0), c1 = csrd(1);
          row_stats(); make_xaug();
          hac[0] = MFMA32(waug(c0, bias0), xaug, hac[0]);
          hac[1] = MFMA32(waug(c1, bias1), xaug, hac[1]);
          SBAR();
        }
      });
    }
    STAMP_AT(18);
    if (heavy) { wait_vm<8>(); arrive_nowait(lds0 + L_CNT + 16); }
    hgelu(std::integral_constant<int, 0>{}, [&]() {
      wait_vm<2>();
      if (heavy) arrive_nowait(lds0 + L_CNT + 20);
    });
    __builtin_amdgcn_s_setprio(0);
    STAMP_AT(19);
    unsigned rb2[2];
#pragma unroll
    for (int c = 0; c < 2; ++c) rb2[c] = lds0 + L_WG + m * PART + h * 512 + q4 * 128 + ((c ^ (q4 >> 1)) * 64) + (g & 1) * 32 + p4 * 8;
    v4u wq[36][2];
    auto wrd = [&](int kk) {
#pragma unroll
      for (int c = 0; c < 2; ++c) wq[kk][c] = ldtr2(rb2[c] + (kk % RD) * 2048, 1024);
    };
    auto wgot = [&](int kk) { PIN(wq[kk][0]); PIN(wq[kk][1]); };
    const int ftile = 2 * rt + ((lane >> 2) & 1);
    v4u af[28][2], of[8][2];
    v2u fl;
    const unsigned a0 = WS_AF + (2 * rt * AFT + 28 * m) * 1024, o0 = WS_AF + (2 * rt * AFT + 112 + 8 * m) * 1024;
    for (int kk = 2; kk < 6; ++kk) wdma(kk);
    wait_vm<8>();
    if (lane == 0) __hip_atomic_store(flag(FL_H0, T, j), tag, RLX_AGENT);
    STAMP_AT(2);
    wdma(6); wdma(7);
    static_for<0, 3>([&](auto ic) {
      constexpr int i = ic.value;
      wait_vm<(i == 0) ? 9 : 8>();
      for (int kk = 4 * i; kk < 4 * i + 4; ++kk) wrd(kk);
      for (int kk = 4 * i; kk < 4 * i + 4; ++kk) wgot(kk);
      if constexpr (i == 2) fl = ld8_sc1_async(sws, flag_off(FL_H0, ftile, 4 * m + p4));
      for (int kk = 4 * i; kk < 4 * i + 4; ++kk) wdma(kk + RD);
    });
    STAMP_AT(3);
    wait_vmf<8>(fl);
    if (!__all(fl[0] == taglo && fl[1] == tag32)) poll_flags(flag(FL_H0, ftile, 4 * m + p4), tag);
    STAMP_AT(4);
#pragma unroll
    for (int kk = 0; kk < AW; ++kk) {
      af[kk][0] = ld16_sc1_async(sws, lane * 16, a0 + kmap(kk) * 1024);
      af[kk][1] = ld16_sc1_async(sws, lane * 16, a0 + (AFT + kmap(kk)) * 1024);
    }
    __builtin_amdgcn_s_setprio(PRIO_G2);
    f32x16 c2[2][2] = {};
    auto mm2 = [&](int kk, const v4u (&a)[2], auto&& fill) {
      SBAR();
#pragma unroll
      for (int i = 0; i < 4; ++i) { c2[i >> 1][i & 1] = MFMA32(wq[kk][i & 1], a[i >> 1], c2[i >> 1][i & 1]); SBAR(); fill(i); SBAR(); }
    };
    static_for<0, 28>([&](auto kc) {
      constexpr int kk = kc.value;
      wait_vm2<(kk < AW) ? 2 * (AW - 1 - kk) + g2_range(0, kk) : g2_range(kk - AW + 1, kk)>(af[kk][0], af[kk][1]);
      if constexpr (kk == H1_CK) {
        STAMP_AT(20);
        wait_vmf<g2_range(H1_LD, H1_CK) - 1>(fl);
        if (!__all(fl[0] == taglo && fl[1] == tag32)) poll_flags(flag(FL_H1, ftile, 4 * m + p4), tag);
        STAMP_AT(21);
      }
      if constexpr (kk == H1_LD) fl = ld8_sc1_async(sws, flag_off(FL_H1, ftile, 4 * m + p4));
      if constexpr (kk == O_LD) fl = ld8_sc1_async(sws, flag_off(FL_O0, ftile, 4 * m + p4));
      constexpr bool dw = kk >= 1 && kk - 1 + NR + RD < 36;
      if constexpr (dw) wgot(kk - 1 + NR);
      mm2(kk, af[kk], [&](int i) {
        if constexpr (dw) { if (i < 2) wdmah(kk - 1 + NR + RD, i); }
        if constexpr (kk + NR < 28) { if (i == 1) wrd(kk + NR); }
        if constexpr (kk == 27) { if (i == 0) wrd(28); }
        if constexpr (kk + AW < 28) { if (i >= 2) af[kk + AW][i - 2] = ld16_sc1_async(sws, lane * 16, a0 + (AFT * (i - 2) + kmap(kk + AW)) * 1024); }
      });
      if constexpr (kk == O_CK) {
        wait_vmf<g2_range(O_LD, O_CK) - 1>(fl);
        if (!__all(fl[0] == taglo && fl[1] == tag32)) poll_flags(flag(FL_O0, ftile, 4 * m + p4), tag);
        res_loads();
#pragma unroll
        for (int i = 0; i < 4; ++i) {
          of[i][0] = ld16_sc1_async(sws, lane * 16, o0 + omap(i) * 1024);
          of[i][1] = ld16_sc1_async(sws, lane * 16, o0 + (AFT + omap(i)) * 1024);
        }
      }
    });
    static_for<0, 8>([&](auto kc) {
      constexpr int kk = kc.value;
      if (kk < 7) wrd(28 + kk + 1);
      if constexpr (kk == 1) {
        STAMP_AT(5);
        poll_flags(flag(FL_O1, ftile, 4 * m + p4), tag);
        STAMP_AT(6);
#pragma unroll
        for (int i = 4; i < 8; ++i) {
          of[i][0] = ld16_sc1_async(sws, lane * 16, o0 + omap(i) * 1024);
          of[i][1] = ld16_sc1_async(sws, lane * 16, o0 + (AFT + omap(i)) * 1024);
        }
      }
      wait_vm2<(kk == 0) ? 6 : 14 - 2 * kk>(of[kk][0], of[kk][1]);
      mm2(28 + kk, of[kk], [&](int) {});
    });
    PIN(xres); PIN(bres);
    asm volatile("" ::: "memory");
#pragma unroll
    for (int mt = 0; mt < 2; ++mt)
#pragma unroll
      for (int c = 0; c < 2; ++c)
#pragma unroll
        for (int qd = 0; qd < 4; ++qd) {
          const f32x4 v = {c2[mt][c][4 * qd], c2[mt][c][4 * qd + 1], c2[mt][c][4 * qd + 2], c2[mt][c][4 * qd + 3]};
          sts128(lds0 + L_WG + m * PART + ((mt * 8 + c * 4 + qd) * 66 + lane) * 16, __builtin_bit_cast(v4u, v));
        }
    STAMP_AT(7);
  } else {
    __builtin_amdgcn_s_setprio(1);
    lane_setup();
    v4u xr[2];
    xr[0] = ld16_async(sx, lane * 16, (128 * r + 8 * j + 2 * m) * 1024); xr[1] = ld16_async(sx, lane * 16, (128 * r + 8 * j + 2 * m + 1) * 1024);
    unsigned warm;
    {
      u64 pc; asm volatile("s_getpc_b64 %0" : "=s"(pc));
      warm = ld2_async(make_srd((const void*)(pc & ~127ull), CODEWARM), lane < 3 ? lane * 128 : 0x40000000u, (idx * 4 + m) * 384);
    }
    __builtin_amdgcn_s_barrier();
    v2u ep = ld8_sc1_async(sws, m == 0 ? WS_EPOCH : 0x40000000u);
    float sval;
    const int wi = blockIdx.x * 4 + m;
    const u64 wbase = wi < 448 ? (u64)w1 + wi * 4096 : wi < 896 ? (u64)w2 + (wi - 448) * 4096 : (u64)wo + (wi - 896) * 4096;
    auto make_stats = [&]() {
      wait_vm2<1>(xr[0], xr[1]);
      PIN(warm);
      float piv[2], a[4];
#pragma unroll
      for (int u = 0; u < 2; ++u) {
        piv[u] = rdl(bflo(xr[u][0]), 0);
        float s1 = 0.f, s2 = 0.f;
#pragma unroll
        for (int i = 0; i < 8; ++i) { const float d = ((i & 1) ? bfhi(xr[u][i >> 1]) : bflo(xr[u][i >> 1])) - piv[u]; s1 += d; s2 = __builtin_fmaf(d, d, s2); }
        a[2 * u] = s1; a[2 * u + 1] = s2;
      }
#pragma unroll
      for (int i = 0; i < 4; ++i) a[i] += dpp<0xB1>(a[i]);
#pragma unroll
      for (int i = 0; i < 4; ++i) a[i] += dpp<0x4E>(a[i]);
      float t = p4 == 0 ? a[0] : p4 == 1 ? a[1] : p4 == 2 ? a[2] : a[3];
      t += dpp<0x114>(t); t += dpp<0x118>(t);
      const auto s16 = __builtin_amdgcn_permlane16_swap(asu(t), asu(t), false, false);
      t = asf(s16[0]) + asf(s16[1]);
      const auto s32 = __builtin_amdgcn_permlane32_swap(asu(t), asu(t), false, false);
      t = (asf(s32[0]) + asf(s32[1])) * (1.f / DM);
      const float e1 = dpp<0xA0>(t);
      sval = (p4 & 1) ? __builtin_fmaxf(__builtin_fmaf(-e1, e1, t), 0.f) + 1e-5f : t + (p4 ? piv[1] : piv[0]);
      PIN(sval);
    };
    const unsigned acnt = lds0 + L_CNT;
    const unsigned vpos = 2048 + (g & 1) * 512 + h * 256 + (lane & 15) * 16;
    v4u qf[2], am0, am1;
    u64 um[16];
    auto masks = [&]() {
      const int dq = r32 - 4 * h;
#pragma unroll
      for (int d = 0; d < 4; ++d) {
        const int row = 2 * (d & 1) + 8 * (d >> 1);
        am0[d] = row > dq ? 0xffffffffu : row == dq ? 0xffff0000u : 0u;
        am1[d] = row + 16 > dq ? 0xffffffffu : row + 16 == dq ? 0xffff0000u : 0u;
      }
      PIN(am0); PIN(am1);
      static_for<0, 16>([&](auto u) { um[u.value] = upper_mask(u.value); pin_s(um[u.value]); });
    };
    {
      v4u xf[32];
      f32x16 acc[3] = {};
      f32x4 cs[3] = {};
      v4u wf[4][3];
      unsigned wb[4];
#pragma unroll
      for (int p = 0; p < 4; ++p) { wb[p] = qbuf(0) + ((p + m) & 3) * 1024 + (8 * h + q4) * 64 + (g & 1) * 32 + p4 * 8; PIN(wb[p]); }
      auto rd = [&](int ks, int t) {
        if (t == 0) xf[ks] = lds128(xbuf(ks >> 2) + xo[ks & 3]);
        wf[ks & 3][t] = ldtr2(wb[ks & 3] + ((ks >> 2) % 3) * 12288 + t * 4096, 256);
      };
      auto mm = [&](int ks, int t) {
        acc[t] = t == 2 ? MFMA32(xf[ks], wf[ks & 3][t], acc[t]) : MFMA32(wf[ks & 3][t], xf[ks], acc[t]);
        if ((ks & 3) == 0) { cs[t] = MFMA16(sel, wf[ks & 3][t], cs[t]); PIN(cs[t]); }
        SBAR();
      };
      make_stats();
      bar();
      STAMP_AT(10);
#pragma unroll
      for (int t = 0; t < 6; ++t) rd(t / 3, t % 3);
      SBAR();
      static_for<0, 8>([&](auto sc) {
        constexpr int s = sc.value;
#pragma unroll
        for (int p = 0; p < 2; ++p) {
#pragma unroll
          for (int t = 0; t < 3; ++t) { mm(4 * s + p, t); rd(4 * s + p + 2, t); SBAR(); }
          if (s == 7 && p == 0) { cswr(4, cs[0]); cswr(5, cs[1]); cswr(6, cs[2]); SBAR(); }
        }
        if constexpr (s == 0) {
          wait_vmf<0>(ep);
          if (m == 0 && lane < 10)
            *(LAS unsigned*)(size_t)(lds0 + L_CNT + lane * 4) = lane == 8 ? ep[0] * 0x9E3779B1u + 0x7F4A7C15u : lane == 9 ? ep[1] * 0x85EBCA77u + 0x165667B1u : 0u;
        }
        if constexpr (s == 1) {
          get_tag();
          if ((lane >> 2) == 3) {
            const v2u gr = {asu(sval), tag32};
            __builtin_amdgcn_raw_buffer_store_b64(gr, rws, WS_STATS + (128 * r + 8 * j + 2 * m) * 16 + p4 * 8, 0, 16);
          }
        }
        if constexpr (s == MIDWARM) {
          warm = ld2_async(make_srd((const void*)wbase, 4096), lane * 128, 0);
        }
        if constexpr (s == 5) stat_load();
        if constexpr (s == 6) {
          wait_vm1<0>(st);
          PIN(warm);

          row_stats();
        }
        mm(4 * s + 2, 0); mm(4 * s + 2, 1);
        bar();
        if constexpr (s < 7) {
          STAMP_AT(11 + s);
          mm(4 * s + 2, 2);
          rd(4 * s + 4, 0); rd(4 * s + 4, 1); rd(4 * s + 4, 2); SBAR();
#pragma unroll
          for (int t = 0; t < 3; ++t) { mm(4 * s + 3, t); rd(4 * s + 5, t); SBAR(); }
        }
      });
      STAMP_AT(1);
      __builtin_amdgcn_s_setprio(3);
      const f32x4 c5 = csrd(5), c6 = csrd(6), c4 = csrd(4);
      SBAR();
      mm(30, 2); mm(31, 1);
      make_xaug();
      SBAR();
      mm(31, 2); mm(31, 0);
      if (h == 0) *(LAS float*)(size_t)(lds0 + L_RS + (m * 32 + r32) * 4) = rstd;
      acc[1] = MFMA32(waug(c5, 0u), xaug, acc[1]);
      acc[2] = MFMA32(xaug, waug(c6, 0u), acc[2]);
      acc[0] = MFMA32(waug(c4, 0u), xaug, acc[0]);
      SBAR();
      f32x4 rs[4];
#pragma unroll
      for (int qd = 0; qd < 4; ++qd) rs[qd] = __builtin_bit_cast(f32x4, lds128(lds0 + L_RS + (m * 32 + 8 * qd + 4 * h) * 4));
      const unsigned kvo = WS_KV + (j * 64 + T) * 4096;
#pragma unroll
      for (int e = 0; e < 16; ++e) acc[1][e] = MUL1(acc[1][e], rstd);
#pragma unroll
      for (int e = 0; e < 2; ++e) {
        const v4u f = pack8(acc[1], e);
        st16_sc1(f, sws, lane * 16, kvo + e * 1024);
        sts128(lds0 + L_KV + (8 + m) * KVT + e * 1024 + lane * 16, f);
      }
#pragma unroll
      for (int qd = 0; qd < 4; ++qd)
#pragma unroll
        for (int e = 0; e < 4; ++e) acc[2][4 * qd + e] = MUL1(acc[2][4 * qd + e], rs[qd][e]);
#pragma unroll
      for (int s = 0; s < 2; ++s) {
        const v4u f = pack8(acc[2], s);
        st16_sc1(f, sws, lane * 16, kvo + 2048 + s * 1024);
        sts128(lds0 + L_KV + (8 + m) * KVT + s * VB + vpos, f);
      }
      SBAR();
      const float qs = rstd * (0.25f * 1.4426950408889634f);
#pragma unroll
      for (int e = 0; e < 16; ++e) acc[0][e] = MUL1(acc[0][e], qs);
      qf[0] = pack8(acc[0], 0); qf[1] = pack8(acc[0], 1);
      PIN(qf[0]); PIN(qf[1]);
      STAMP_AT(2);
      const int T0 = 4 * r - 8 + 2 * m;
      const bool live = T0 >= 0;
      auto kv_load = [&](v4u (&kv)[8], int i0, int i1) {
        const unsigned src = WS_KV + (j * 64 + (live ? T0 : 0)) * 4096, vo = live ? lane * 16 : h * 512;
#pragma unroll
        for (int i = i0; i < i1; ++i) kv[i] = ld16_sc1_async(sws, vo, src + (live ? i : (i & 3)) * 1024);
      };
      auto kv_stage = [&](v4u (&kv)[8]) {
        const unsigned on = (lane < 2 || live) ? one2 : 0u;
        if (lane < 6) sts128(lds0 + L_KV + (lane < 2 ? 8 + m : 2 * m + ((lane - 2) >> 1)) * KVT + 2048 + (lane & 1) * VB + 1024, (v4u){on, on, on, on});
        const v4u zz = {};
#pragma unroll
        for (int i = 0; i < 2; ++i) {
          sts128(lds0 + L_KV + (2 * m + i) * KVT + lane * 16, kv[4 * i]);
          sts128(lds0 + L_KV + (2 * m + i) * KVT + 1024 + lane * 16, kv[4 * i + 1]);
          sts128(lds0 + L_KV + (2 * m + i) * KVT + vpos, live ? kv[4 * i + 2] : zz);
          sts128(lds0 + L_KV + (2 * m + i) * KVT + VB + vpos, live ? kv[4 * i + 3] : zz);
        }
        arrive_nowait(acnt);
      };
      auto kv_wait = [&](v4u (&kv)[8]) {
        wait_vm<0>();
#pragma unroll
        for (int i = 0; i < 8; ++i) PIN(kv[i]);
      };
      if (heavy) {
        hsetup(1);
        await(acnt + 16, 4);
        STAMP_AT(3);
        v2u fl;
        v4u kv[8];
        hmm(std::integral_constant<int, 1>{}, xf, [&](auto kc) {
          constexpr int ks = kc.value;
          if constexpr (ks == KVPUB) {
            wait_vm<0>();
            if (lane == 0) __hip_atomic_store(flag(FL_KV, T, j), tag, RLX_AGENT);
          }
          if constexpr (ks == KVPUB + 2) bias_load(1);
          if constexpr (ks == 31) { wait_vm<1>(); PIN(bias0); PIN(bias1); }
          if constexpr (ks == 24) fl = ld8_sc1_async(sws, flag_off(FL_KV, live ? T0 + (lane & 1) : 0, j));
        });
        STAMP_AT(18);
        wait_vmf<0>(fl);
        if (!__all(fl[0] == taglo && fl[1] == tag32)) poll_flags(flag(FL_KV, live ? T0 + (lane & 1) : 0, j), tag);
        kv_load(kv, 0, 8);
        hgelu(std::integral_constant<int, 1>{}, [&]() {});
        STAMP_AT(4);
        masks();
        wait_vm<4>();
#pragma unroll
        for (int i = 0; i < 8; ++i) PIN(kv[i]);
        kv_stage(kv);
      } else {
        wait_vm<0>();
        if (lane == 0) __hip_atomic_store(flag(FL_KV, T, j), tag, RLX_AGENT);
        v4u kv[8];
        poll_flags(flag(FL_KV, live ? T0 + (lane & 1) : 0, j), tag);
        kv_load(kv, 0, 8); masks(); kv_wait(kv); kv_stage(kv);
      }
    }
    STAMP_AT(19);
    const unsigned kb = lds0 + L_KV + m * KVT + lane * 16;
    unsigned vb[2];
#pragma unroll
    for (int e = 0; e < 2; ++e) { vb[e] = lds0 + L_KV + m * KVT + 2048 + ((g & 1) ? 1024 : e * 512 + h * 256 + (lane & 15) * 16); PIN(vb[e]); }
    await(acnt, heavy ? 8 : 4);
    __builtin_amdgcn_s_setprio(PRIO_ATT);
    STAMP_AT(5);
    const f32x16 zero = {};
    auto o_store = [&](f32x16& o, int e) {
      const float rl = __builtin_amdgcn_rcpf(o[8]);
#pragma unroll
      for (int u = 0; u < 8; ++u) o[u] = MUL1(o[u], rl);
      st16_sc1(pack8(o, 0), sws, lane * 16, WS_AF + (T * AFT + 112 + 2 * j + e) * 1024);
    };
    int redo = 0;
    {
      f32x16 o[2], sa, sb, su;
      bool pend = false;
      v4u kn, ku, va, vc, vn0, vn1, vu0, vu1, p0, p1, pu;
      auto kaddr = [&](int n) { return kb + ((n & 7) + 1) * KVT + (n >> 3) * 1024; };
      auto vaddr = [&](int n) { return vb[n >> 3] + ((n & 7) + 1) * KVT; };
      kn = lds128(kaddr(0)); vn0 = lds128(vaddr(0)); vn1 = lds128(vaddr(0) + VB);
      sa = MFMA32(kn, qf[0], zero);
      kn = lds128(kaddr(1));
      SBAR();
      auto step = [&](auto nc, f32x16& sc, f32x16& sn) {
        constexpr int n = nc.value, e = n >> 3, i = n & 7;
        if constexpr (n < 15) sn = MFMA32(kn, qf[(n + 1) >> 3], zero);
        if constexpr (n < 14) kn = lds128(kaddr(n + 2));
        if constexpr (i == 5) ku = lds128(kb + e * 1024);
        v4u v0 = vn0, v1 = vn1;
        if constexpr (n < 15) { vn0 = lds128(vaddr(n + 1)); vn1 = lds128(vaddr(n + 1) + VB); }
        if constexpr (i == 6) { vu0 = lds128(vb[e]); vu1 = lds128(vb[e] + VB); }
        SBAR();
        if constexpr (i == 7) {
#pragma unroll
          for (int u = 0; u < 16; ++u) sc[u] = __builtin_amdgcn_inverse_ballot_w64(um[u]) ? su[u] : sc[u];
          PIN(sc); SBAR();
        }
#pragma unroll
        for (int u = 0; u < 6; ++u) sc[u] = EXP2(sc[u]);
        PIN(sc); SBAR();
        if constexpr (n == 8) { o[0] = MFMA32(vc, p1, o[0]); SBAR(); }
        else if constexpr (n > 0) { o[e] = MFMA32(vc, p1, o[e]); SBAR(); }
        sc[6] = EXP2(sc[6]); sc[7] = EXP2(sc[7]);
        p0 = pack8(sc, 0);
        sc[8] = EXP2(sc[8]); sc[9] = EXP2(sc[9]);
        if constexpr (i == 7) { va = p0 & am0; p0 &= ~am0; PIN(va); }
        PIN(sc); PIN(p0); SBAR();
        if constexpr (n == 8) { o[0] = MFMA32(vu1, pu, o[0]); SBAR(); }
        o[e] = MFMA32(v0, p0, i == 0 ? zero : o[e]);
        SBAR();
        if constexpr (i == 6) { su = MFMA32(ku, qf[e], zero); SBAR(); }
#pragma unroll
        for (int u = 10; u < 13; ++u) sc[u] = EXP2(sc[u]);
        if constexpr (i == 7) { PIN(sc); SBAR(); o[e] = MFMA32(vu0, va, o[e]); SBAR(); }
#pragma unroll
        for (int u = 13; u < 16; ++u) sc[u] = EXP2(sc[u]);
        p1 = pack8(sc, 1); vc = v1;
        if constexpr (i == 7) { pu = p1 & am1; p1 &= ~am1; PIN(pu); }
        PIN(p1); SBAR();
        if constexpr (n == 8) {
          if (__all(o[0][8] >= 0x1p-30f && o[0][8] <= 0x1p30f)) o_store(o[0], 0); else redo = 1;
          SBAR();
        }
        if constexpr (i == 2) pend = !redo;
        if constexpr (i == 3 || i == 5 || i == 7) {
          if (pend && (i == 7 || (__builtin_amdgcn_s_getreg(7 | (3 << 11)) & 15) == 0)) {
            wait_vm<0>();
            if (lane == 0) __hip_atomic_store(flag(FL_H1 + e, T, j), tag, RLX_AGENT);
            pend = false;
          }
        }
      };
      static_for<0, 8>([&](auto hc) {
        step(std::integral_constant<int, 2 * hc.value>{}, sa, sb);
        step(std::integral_constant<int, 2 * hc.value + 1>{}, sb, sa);
      });
      o[1] = MFMA32(vc, p1, o[1]);
      o[1] = MFMA32(vu1, pu, o[1]);
      if (__all(o[1][8] >= 0x1p-30f && o[1][8] <= 0x1p30f)) o_store(o[1], 1); else redo |= 2;
    }
    if (redo) {
#pragma unroll 1
      for (int e = 0; e < 2; ++e) {
        if (!((redo >> e) & 1)) continue;
        const v4u q = e ? qf[1] : qf[0];
        const unsigned vbe = e ? vb[1] : vb[0];
        auto merged = [&](const f32x16& c) {
          f32x16 sl = MFMA32(lds128(kb + 8 * KVT + e * 1024), q, c);
          const f32x16 su = MFMA32(lds128(kb + e * 1024), q, c);
          static_for<0, 16>([&](auto u) { sl[u.value] = upper<u.value>() ? su[u.value] : sl[u.value]; });
          return sl;
        };
        float mx = -INFINITY;
#pragma unroll 1
        for (int i = 0; i < 8; ++i) {
          const f32x16 sc = i ? MFMA32(lds128(kb + i * KVT + e * 1024), q, zero) : merged(zero);
#pragma unroll
          for (int u = 0; u < 16; u += 2) mx = max3(mx, sc[u], sc[u + 1]);
        }
        const auto sw = __builtin_amdgcn_permlane32_swap(asu(mx), asu(mx), false, false);
        mx = __builtin_fmaxf(asf(sw[0]), asf(sw[1]));
        f32x16 negm, o;
#pragma unroll
        for (int u = 0; u < 16; ++u) negm[u] = -mx;
        {
          f32x16 sc = merged(negm);
#pragma unroll
          for (int u = 0; u < 16; ++u) sc[u] = EXP2(sc[u]);
          const v4u p0 = pack8(sc, 0), p1 = pack8(sc, 1);
          o = MFMA32(lds128(vbe), p0 & am0, zero);
          o = MFMA32(lds128(vbe + VB), p1 & am1, o);
          o = MFMA32(lds128(vbe + 8 * KVT), p0 & ~am0, o);
          o = MFMA32(lds128(vbe + 8 * KVT + VB), p1 & ~am1, o);
        }
#pragma unroll 1
        for (int i = 1; i < 8; ++i) {
          f32x16 sc = MFMA32(lds128(kb + i * KVT + e * 1024), q, negm);
#pragma unroll
          for (int u = 0; u < 16; ++u) sc[u] = EXP2(sc[u]);
          o = MFMA32(lds128(vbe + i * KVT), pack8(sc, 0), o);
          o = MFMA32(lds128(vbe + i * KVT + VB), pack8(sc, 1), o);
        }
        o_store(o, e);
      }
      wait_vm<0>();
      if (lane == 0) __hip_atomic_store(flag(FL_O0, T, j), tag, RLX_AGENT);
    }
    STAMP_AT(6);
    wait_vm<0>();
    if (lane == 0) __hip_atomic_store(flag(FL_O1, T, j), tag, RLX_AGENT);
    STAMP_AT(7);
    res_loads();
    wait_vm2<0>(xres, bres);
    if (blockIdx.x == 0 && wave == 7) {
#pragma unroll 1
      for (int i = 0; i < 4; ++i) poll_flags(flag(FL_KV, 4 * (4 * i + g), lane & 15), tag);
      if (lane == 0) __hip_atomic_store((GAS u64*)(ws + WS_EPOCH), tag, RLX_AGENT);
    }
  }
  bar();
  {
    int t2 = threadIdx.x; PIN(t2);
    const int row = t2 >> 3, ch = t2 & 7, orow = 64 * rt + row, ocol = 64 * ct + ch * 8;
    const unsigned a = lds0 + L_WG + (((row >> 5) * 8 + ch) * 66 + (row & 31)) * 16;
    f32x4 lo = {}, hi = {};
#pragma unroll
    for (int w = 0; w < 4; ++w) {
      lo += __builtin_bit_cast(f32x4, lds128(a + w * PART));
      hi += __builtin_bit_cast(f32x4, lds128(a + w * PART + 512));
    }
    v4u o;
    o[0] = pk2(lo[0] + bflo(xres[0]) + bflo(bres[0]), lo[1] + bfhi(xres[0]) + bfhi(bres[0]));
    o[1] = pk2(lo[2] + bflo(xres[1]) + bflo(bres[1]), lo[3] + bfhi(xres[1]) + bfhi(bres[1]));
    o[2] = pk2(hi[0] + bflo(xres[2]) + bflo(bres[2]), hi[1] + bfhi(xres[2]) + bfhi(bres[2]));
    o[3] = pk2(hi[2] + bflo(xres[3]) + bflo(bres[3]), hi[3] + bfhi(xres[3]) + bfhi(bres[3]));
    __builtin_amdgcn_raw_buffer_store_b128(o, __builtin_amdgcn_make_buffer_rsrc(out, 0, SEQ * DM * 2, 0x00020000), orow * 1024 + ocol * 2, 0, 16);
  }
  STAMP_AT(8);
}

extern "C" void kernel_launch(void* const* d_in, const int* in_sizes, int n_in, void* d_out, int out_size, void* d_ws, size_t ws_size,
                              hipStream_t stream) {
  (void)in_sizes; (void)n_in; (void)out_size; (void)ws_size;
  (void)hipFuncSetAttribute((const void*)fused_block, hipFuncAttributeMaxDynamicSharedMemorySize, LDS_BYTES);
  fused_block<<<256, 512, LDS_BYTES, stream>>>(d_in[0], d_in[1], d_in[2], d_in[4], d_in[5], d_in[6], d_in[7], d_out, (char*)d_ws);
}
```

```cpp
#include <hip/hip_runtime.h>
#include <hip/hip_bf16.h>
#include <stdint.h>
#include <type_traits>

typedef unsigned v4u __attribute__((ext_vector_type(4)));
typedef unsigned v2u __attribute__((ext_vector_type(2)));
typedef float f32x2 __attribute__((ext_vector_type(2)));
typedef float f32x4 __attribute__((ext_vector_type(4)));
typedef float f32x16 __attribute__((ext_vector_type(16)));
typedef short bf16x8 __attribute__((ext_vector_type(8)));
typedef short s16x4 __attribute__((ext_vector_type(4)));
typedef __bf16 bf16x2_t __attribute__((ext_vector_type(2)));
typedef unsigned long long u64;
#define GAS __attribute__((address_space(1)))
#define LAS __attribute__((address_space(3)))
#define DI __device__ __forceinline__
#define RLX_AGENT __ATOMIC_RELAXED, __HIP_MEMORY_SCOPE_AGENT

constexpr int SEQ = 2048, DM = 512, FF = 1792;
constexpr unsigned WS_EPOCH = 0;
constexpr unsigned WS_STATS = 32768;
constexpr unsigned WS_FLAG = 65536;
constexpr unsigned WS_KV = WS_FLAG + 5 * 1024 * 128;
constexpr int AFT = 145;
constexpr unsigned WS_AF = WS_KV + 16 * 64 * 4096;
constexpr unsigned WS_END = WS_AF + 64 * AFT * 1024;
constexpr unsigned WS_STAMP = WS_END;
enum { FL_KV = 0, FL_H0 = 1, FL_H1 = 2, FL_O0 = 3, FL_O1 = 4 };
constexpr int L_WA = 0;
constexpr int L_WG = 86016;
constexpr int VB = 1040;
constexpr int KVT = 2048 + 2 * VB;
constexpr int L_KV = 65536 - 8 * KVT;
constexpr int RD = 8, NR = 12;
constexpr int PART = 16 * 66 * 16;
constexpr int L_CS = L_WG + 4 * PART;
constexpr int L_RS = L_CS + 3584;
constexpr int L_CNT = L_RS + 512;
constexpr int LDS_BYTES = L_CNT + 64;
constexpr int xbuf(int s) { return ((s + 1) % 3) * 16384; }
constexpr int qbuf(int s) { return 49152 + (s % 3) * 12288; }
constexpr int w1buf(int s) { return L_WG + (s % 3) * PART + 8192; }
static_assert(xbuf(7) == 32768 && qbuf(2) + 12288 <= L_WG && L_KV + 12 * KVT <= L_WG && PART >= RD * 2048 && LDS_BYTES <= 163840, "lds map");
#define AWIN 6
constexpr int AW = AWIN;
constexpr unsigned SPIN_CAP = 1u << 23;

#define MFMA32(a, b, c) __builtin_amdgcn_mfma_f32_32x32x16_bf16(__builtin_bit_cast(bf16x8, a), __builtin_bit_cast(bf16x8, b), c, 0, 0, 0)
#define MFMA16(a, b, c) __builtin_amdgcn_mfma_f32_16x16x32_bf16(__builtin_bit_cast(bf16x8, a), __builtin_bit_cast(bf16x8, b), c, 0, 0, 0)
#define SBAR() __builtin_amdgcn_sched_barrier(0)

#define STAMP_AT(i) do { } while (0)

DI float asf(unsigned u) { return __builtin_bit_cast(float, u); }
DI unsigned asu(float f) { return __builtin_bit_cast(unsigned, f); }
DI unsigned pk2(float lo, float hi) { return __builtin_bit_cast(unsigned, __builtin_convertvector((f32x2){lo, hi}, bf16x2_t)); }
DI float bflo(unsigned u) { return __builtin_bit_cast(float, u << 16); }
DI float bfhi(unsigned u) { return __builtin_bit_cast(float, u & 0xffff0000u); }
DI v4u make_srd(const void* p, unsigned bytes) { const u64 a = (u64)p; return (v4u){(unsigned)a, (unsigned)(a >> 32) & 0xffffu, bytes, 0x00020000u}; }
DI void dma16(v4u srd, unsigned voff, unsigned soff, unsigned lds) {
  asm volatile("s_mov_b32 m0, %3\n\ts_nop 0\n\tbuffer_load_dwordx4 %0, %1, %2 offen lds" :: "v"(voff), "s"(srd), "s"(soff), "s"(lds) : "memory");
}
DI void dma16_a(v4u srd, unsigned voff, unsigned soff, unsigned lds) { dma16(srd, voff, soff, lds); }
DI void dma16_b(v4u srd, unsigned voff1, unsigned soff) {
  asm volatile("buffer_load_dwordx4 %0, %1, %2 offen offset:1024 lds" :: "v"(voff1), "s"(srd), "s"(soff) : "memory");
}
DI v4u ld16_sc1_async(v4u srd, unsigned voff, unsigned soff) {
  v4u r; asm volatile("buffer_load_dwordx4 %0, %1, %2, %3 offen sc1" : "=v"(r) : "v"(voff), "s"(srd), "s"(soff) : "memory"); return r;
}
DI v4u ld16_async(v4u srd, unsigned voff, unsigned soff) {
  v4u r; asm volatile("buffer_load_dwordx4 %0, %1, %2, %3 offen" : "=v"(r) : "v"(voff), "s"(srd), "s"(soff) : "memory"); return r;
}
DI unsigned ld2_async(v4u srd, unsigned voff, unsigned soff) {
  unsigned r; asm volatile("buffer_load_ushort %0, %1, %2, %3 offen" : "=v"(r) : "v"(voff), "s"(srd), "s"(soff) : "memory"); return r;
}
DI void st16_sc1(v4u v, v4u srd, unsigned voff, unsigned soff) {
  asm volatile("buffer_store_dwordx4 %0, %1, %2, %3 offen sc1\n\ts_nop 1" :: "v"(v), "v"(voff), "s"(srd), "s"(soff) : "memory");
}
template <int N> DI void wait_vm1(v4u& a) { asm volatile("s_waitcnt vmcnt(%1)" : "+v"(a) : "i"(N) : "memory"); }
template <int N> DI void wait_vm2(v4u& a, v4u& b) { asm volatile("s_waitcnt vmcnt(%2)" : "+v"(a), "+v"(b) : "i"(N) : "memory"); }
template <int N> DI void wait_vm3(v4u& a, v4u& b, v4u& c) { asm volatile("s_waitcnt vmcnt(%3)" : "+v"(a), "+v"(b), "+v"(c) : "i"(N) : "memory"); }
DI void pin_s(u64& v) { asm volatile("" : "+s"(v)); }
template <class V> DI void pin(V& v) { asm volatile("" : "+v"(v)); }
template <int N> DI void wait_vm() { asm volatile("s_waitcnt vmcnt(%0)" :: "i"(N) : "memory"); }
DI void bar() { asm volatile("s_waitcnt lgkmcnt(0)\n\ts_barrier" ::: "memory"); }
DI v4u lds128(unsigned a) { return *(const LAS v4u*)(size_t)a; }
DI void sts128(unsigned a, v4u v) { *(LAS v4u*)(size_t)a = v; }
DI v2u ldtr(unsigned a) { return __builtin_bit_cast(v2u, __builtin_amdgcn_ds_read_tr16_b64_v4i16((LAS s16x4*)(size_t)a)); }
DI v4u ldtr2(unsigned a, int second) { const v2u lo = ldtr(a), hi = ldtr(a + second); return (v4u){lo.x, lo.y, hi.x, hi.y}; }
template <int CTRL> DI float dpp(float v) { return __builtin_bit_cast(float, __builtin_amdgcn_update_dpp(0, __builtin_bit_cast(int, v), CTRL, 0xf, 0xf, true)); }
DI float rdl(float v, int l) { return __builtin_bit_cast(float, __builtin_amdgcn_readlane(__builtin_bit_cast(int, v), l)); }
DI v4u pack8(const f32x16& a, int s) { return (v4u){pk2(a[8 * s], a[8 * s + 1]), pk2(a[8 * s + 2], a[8 * s + 3]), pk2(a[8 * s + 4], a[8 * s + 5]), pk2(a[8 * s + 6], a[8 * s + 7])}; }
DI void poll_flags(const GAS u64* p, u64 tag) {
#pragma unroll 1
  for (unsigned spin = 0; spin < SPIN_CAP; ++spin) {
    const u64 v = __hip_atomic_load(p, RLX_AGENT);
    if (__all(v == tag)) break;
    __builtin_amdgcn_s_sleep(2);
  }
  asm volatile("" ::: "memory");
}
constexpr u64 upper_mask(int reg) {
  u64 m = 0;
  for (int l = 0; l < 64; ++l) { const int row = (reg & 3) + 8 * (reg >> 2) + 4 * (l >> 5); if (row > (l & 31)) m |= 1ull << l; }
  return m;
}
template <int REG> DI bool upper() { return __builtin_amdgcn_inverse_ballot_w64(upper_mask(REG)); }
template <int I, int N, class Fn> DI void static_for(Fn&& f) { if constexpr (I < N) { f(std::integral_constant<int, I>{}); static_for<I + 1, N>(f); } }
DI float max3(float a, float b, float c) { return __builtin_fmaxf(__builtin_fmaxf(a, b), c); }

constexpr int H1_CK = 16 - AW, H1_LD = H1_CK - 3, O_CK = 28 - AW, O_LD = O_CK - 3;
constexpr int g2_issued(int t) { return ((t >= 1 && t - 1 + NR + RD < 36) ? 2 : 0) + ((t + AW < 28) ? 2 : 0) + (t == H1_LD) + (t == O_LD) + (t == O_CK ? 10 : 0); }
constexpr int g2_range(int a, int b) { int n = 0; for (int t = a; t < b; ++t) n += g2_issued(t); return n; }
#define GELU_PK(P) (P == 1)
#define PRIO_G2 2
#define PRIO_ATT 3
#define PRIO_G1 2
#define ROT r
#define CODEWARM 38000
#define MIDWARM 1
#define KVPUB 6
#define EXP2(v) __builtin_amdgcn_exp2f(v)
#define PIN(v) pin(v)
extern "C" __device__ float mul_unpacked(float, float) __asm("llvm.amdgcn.fmul.legacy");
#define MUL1(a, b) mul_unpacked(a, b)
#define FMA1(a, b, c) __builtin_amdgcn_div_fmasf(a, b, c, false)
DI v2u ld8_sc1_async(v4u srd, unsigned voff) {
  v2u r; asm volatile("buffer_load_dwordx2 %0, %1, %2, 0 offen sc1" : "=v"(r) : "v"(voff), "s"(srd) : "memory"); return r;
}
template <int N> DI void wait_vmf(v2u& a) { asm volatile("s_waitcnt vmcnt(%1)" : "+v"(a) : "i"(N) : "memory"); }
DI void arrive(unsigned cnt, int lane) {
  asm volatile("s_waitcnt lgkmcnt(0)" ::: "memory");
  if (lane == 0) __hip_atomic_fetch_add((LAS unsigned*)(size_t)cnt, 1u, __ATOMIC_RELAXED, __HIP_MEMORY_SCOPE_WORKGROUP);
}
DI void arrive_nowait(unsigned cnt) {
  asm volatile("s_mov_b64 exec, 1\n\tds_add_u32 %0, %1\n\ts_mov_b64 exec, -1" :: "v"(cnt), "v"(1u) : "memory");
}
DI unsigned peek(unsigned cnt) { return __hip_atomic_load((LAS unsigned*)(size_t)cnt, __ATOMIC_RELAXED, __HIP_MEMORY_SCOPE_WORKGROUP); }
DI void await(unsigned cnt, unsigned n) {
#pragma unroll 1
  for (unsigned spin = 0; spin < SPIN_CAP * 32; ++spin) {
    if (__builtin_amdgcn_readfirstlane(peek(cnt)) >= n) break;
    __builtin_amdgcn_s_sleep(1);
  }
  asm volatile("" ::: "memory");
}

__global__ __launch_bounds__(512, 2) void fused_block(const void* __restrict__ x, const void* __restrict__ wqkv, const void* __restrict__ wo,
                                                      const void* __restrict__ w1, const void* __restrict__ b1, const void* __restrict__ w2,
                                                      const void* __restrict__ b2, void* __restrict__ out, char* __restrict__ ws) {
  constexpr unsigned lds0 = 0;
  const int wave = __builtin_amdgcn_readfirstlane(threadIdx.x >> 6);
  const int m = wave & 3;
  const bool isG = wave < 4;
  const int xcd = blockIdx.x & 7, idx = blockIdx.x >> 3;
  const int r = (xcd >> 2) * 8 + (idx >> 2), j = (xcd & 3) * 4 + (idx & 3);
  const int rt = (xcd >> 1) * 8 + (idx >> 2), ct = (xcd & 1) * 4 + (idx & 3);
  int lane = threadIdx.x & 63, r32, h, g, q4, p4;
  STAMP_AT(0);
  asm volatile("" :: "s"(x), "s"(wqkv), "s"(wo), "s"(w1), "s"(b1), "s"(w2), "s"(b2), "s"(out), "s"(ws));

  const v4u sx = make_srd(x, SEQ * DM * 2), sws = make_srd(ws, WS_END);
  const __amdgpu_buffer_rsrc_t rws = __builtin_amdgcn_make_buffer_rsrc(ws, 0, WS_END, 0x00020000);
  const unsigned one2 = 0x3f803f80u;
  const int T = 4 * r + m;
  const int rot = ROT;
  auto flag_off = [&](int kind, int tile, int jj) { return WS_FLAG + ((kind * 64 + tile) * 16 + jj) * 128; };
  auto flag = [&](int kind, int tile, int jj) { return (GAS u64*)(ws + flag_off(kind, tile, jj)); };
  u64 tag;
  unsigned taglo, tag32;
  auto get_tag = [&]() {
    const v2u tg = *(const LAS v2u*)(size_t)(lds0 + L_CNT + 32);
    taglo = __builtin_amdgcn_readfirstlane(tg[0]); tag32 = __builtin_amdgcn_readfirstlane(tg[1]);
    tag = ((u64)tag32 << 32) | taglo;
  };
  v4u xres, bres, xaug, st;
  float mean, var, rstd, sd;
  v4u sel;
  unsigned xo[4], swz;
  auto lane_setup = [&]() {
    lane = threadIdx.x & 63; PIN(lane);
    r32 = lane & 31; h = lane >> 5; g = lane >> 4; q4 = (lane >> 2) & 3; p4 = lane & 3;
    const unsigned selv = ((lane & 15) == (g & 1)) ? one2 : 0u;
    sel = (v4u){selv, selv, selv, selv};
#pragma unroll
    for (int p = 0; p < 4; ++p) xo[p] = (32 * m + r32) * 128 + (((2 * ((p + m) & 3) + h) ^ ((r32 >> 1) & 7)) * 16);
    swz = ((((lane >> 2) & 1) ^ (g & 1)) * 64) + p4 * 16;
  };

  auto stat_load = [&]() { st = ld16_sc1_async(sws, WS_STATS + (128 * r + 32 * m + r32) * 16, 0); };
  auto res_loads = [&]() {
    int t2 = threadIdx.x; PIN(t2);
    const int row = 64 * rt + (t2 >> 3), col = 64 * ct + (t2 & 7) * 8;
    xres = ld16_async(sx, row * 1024 + col * 2, 0);
    bres = ld16_async(make_srd(b2, DM * 2), col * 2, 0);
  };
  auto row_stats = [&]() {
    if (!__all(st[1] == tag32 && st[3] == tag32)) {
#pragma unroll 1
      for (unsigned spin = 0; spin < SPIN_CAP; ++spin) {
        st = ld16_sc1_async(sws, WS_STATS + (128 * r + 32 * m + r32) * 16, 0);
        wait_vm1<0>(st);
        if (__all(st[1] == tag32 && st[3] == tag32)) break;
      }
    }
    mean = asf(st[0]); var = asf(st[2]);
    PIN(mean); PIN(var);
  };
  auto make_xaug = [&]() {
    PIN(mean); PIN(var);
    rstd = __builtin_amdgcn_rsqf(var); sd = var * rstd;
    const unsigned hi = pk2(-mean, sd);
    const unsigned lo = pk2(-mean - bflo(hi), FMA1(bfhi(hi), -1.f, sd));
    xaug = (v4u){(hi & 0xffffu) | (hi << 16), (lo & 0xffffu) | (hi & 0xffff0000u), lo >> 16, 0u};
    if (h) xaug = (v4u){0u, 0u, 0u, 0u};
    PIN(xaug);
  };
  auto cswr = [&](int tile, f32x4 c) {
    if (lane < 16) { LAS float* p = (LAS float*)(size_t)(lds0 + L_CS + (tile * 32 + lane) * 16 + m * 4); p[0] = c[0]; p[64] = c[1]; }
  };
  auto csrd = [&](int tile) { return __builtin_bit_cast(f32x4, lds128(lds0 + L_CS + (tile * 32 + r32) * 16)); };
  auto waug = [&](f32x4 c4, unsigned b16) {
    const float c = ((c4[0] + c4[1]) + c4[2]) + c4[3];
    const unsigned hi = pk2(c, 0.f);
    const unsigned lo = pk2(c - bflo(hi), 0.f);
    return (v4u){hi | (lo << 16), hi | (b16 << 16), b16, 0u};
  };

  const v4u sw1 = make_srd(w1, DM * FF * 2);
  const int hcol0 = 448 * (j >> 2) + 128 * (j & 3);
  const bool heavy = (j & 3) != 3;
  unsigned bias0, bias1;
  auto bias_load = [&](int P) {
    bias0 = ld2_async(make_srd(b1, FF * 2), r32 * 2, (hcol0 + 64 * P) * 2);
    bias1 = ld2_async(make_srd(b1, FF * 2), r32 * 2, (hcol0 + 64 * P + 32) * 2);
  };
  auto w1dma = [&](int P, int c, unsigned dst, int half) {
    const unsigned vw1 = (lane >> 3) * (FF * 2) + swz, vw1b = vw1 + 8 * (FF * 2) - 1024;
    const unsigned so = (64 * ((c + rot) & 7) + 16 * m) * (FF * 2) + (hcol0 + 64 * P) * 2;
    if (half == 0) dma16_a(sw1, vw1, so, dst + m * 2048); else dma16_b(sw1, vw1b, so);
  };
  f32x16 hac[2];
  unsigned rk[4][2];
  auto hsetup = [&](int P) {
#pragma unroll
    for (int p = 0; p < 4; ++p)
#pragma unroll
      for (int c = 0; c < 2; ++c) {
        rk[p][c] = lds0 + (P ? L_WA : w1buf(0)) + ((p + m) & 3) * 2048 + h * 1024 + q4 * 128 + ((c ^ (q4 >> 1)) * 64) + (g & 1) * 32 + p4 * 8;
        PIN(rk[p][c]);
      }
  };
  auto hmm = [&](auto pc, const v4u (&xf)[32], auto&& hook) {
    constexpr int P = pc.value;
    constexpr int D = 4;
    const unsigned cnt = lds0 + L_CNT;
    f32x4 cs[2] = {}, c0, c1;
    v4u wf[D][2], au0, au1;
    unsigned seen = 0;
    hac[0] = (f32x16){}; hac[1] = (f32x16){};
    auto rd = [&](int ks, int t) { wf[ks % D][t] = ldtr2(rk[ks & 3][t] + (ks >> 2) * 8192, 512); };
#pragma unroll
    for (int ks = 0; ks < D; ++ks) { rd(ks, 0); rd(ks, 1); }
    SBAR();
    static_for<0, 32>([&](auto kc) {
      constexpr int ks = kc.value;
      hook(kc);
      if constexpr (ks == 10) seen = peek(cnt + 20);
      if constexpr (ks == 29) seen = peek(cnt);
#pragma unroll
      for (int t = 0; t < 2; ++t) {
        hac[t] = MFMA32(wf[ks % D][t], xf[ks], hac[t]);
        if ((ks & 3) == 0 && ks < 28) { cs[t] = MFMA16(sel, wf[ks % D][t], cs[t]); PIN(cs[t]); }
        if (ks == 26) { cs[t] = MFMA16(sel, wf[28 % D][t], cs[t]); PIN(cs[t]); }
        SBAR();
        if constexpr (ks + D < 32) rd(ks + D, t);
        if constexpr (ks == 31) { if (t == 0) { au0 = waug(c0, bias0); PIN(au0); } else { au1 = waug(c1, bias1); PIN(au1); } }
        SBAR();
      }
      if constexpr (ks == 11 || ks == 30) {
        PIN(hac[0]); PIN(hac[1]);
        if (__builtin_amdgcn_readfirstlane(seen) < 4) await(ks == 11 ? cnt + 20 : cnt, 4);
        asm volatile("" ::: "memory");
        if constexpr (ks == 30) { c0 = csrd(2 * P); c1 = csrd(2 * P + 1); }
      }
      if constexpr (ks == 27) { cswr(2 * P, cs[0]); cswr(2 * P + 1, cs[1]); arrive_nowait(cnt); }
      SBAR();
    });
    hac[0] = MFMA32(au0, xaug, hac[0]);
    hac[1] = MFMA32(au1, xaug, hac[1]);
    SBAR();
  };
  auto hgelu = [&](auto pc, auto&& mid) {
    constexpr int P = pc.value;
    constexpr bool PK = GELU_PK(P);
    const float C0 = 2.3022081986f, C1 = C0 * 0.044715f;
    const float ga = -C1 * rstd * rstd * rstd, gb = -C0 * rstd;
    auto mul = [&](f32x2 a, f32x2 b) { return PK ? a * b : (f32x2){MUL1(a[0], b[0]), MUL1(a[1], b[1])}; };
    auto fma = [&](f32x2 a, float b, float c) {
      return PK ? __builtin_elementwise_fma(a, (f32x2){b, b}, (f32x2){c, c}) : (f32x2){FMA1(a[0], b, c), FMA1(a[1], b, c)}; };
#pragma unroll
    for (int t = 0; t < 2; ++t) {
      f32x2 v[8], z[8];
#pragma unroll
      for (int e = 0; e < 8; ++e) { v[e] = (f32x2){hac[t][2 * e], hac[t][2 * e + 1]}; z[e] = mul(v[e], v[e]); }
      SBAR();
#pragma unroll
      for (int e = 0; e < 8; ++e) z[e] = fma(z[e], ga, gb);
      SBAR();
#pragma unroll
      for (int e = 0; e < 8; ++e) z[e] = mul(z[e], v[e]);
      SBAR();
#pragma unroll
      for (int e = 0; e < 8; ++e) z[e] = (f32x2){EXP2(z[e][0]), EXP2(z[e][1])};
      SBAR();
#pragma unroll
      for (int e = 0; e < 8; ++e) z[e] = fma(z[e], sd, sd);
      SBAR();
#pragma unroll
      for (int e = 0; e < 8; ++e) z[e] = (f32x2){__builtin_amdgcn_rcpf(z[e][0]), __builtin_amdgcn_rcpf(z[e][1])};
      SBAR();
#pragma unroll
      for (int e = 0; e < 8; ++e) z[e] = mul(z[e], v[e]);
      SBAR();
#pragma unroll
      for (int s = 0; s < 2; ++s) {
        const v4u f = {pk2(z[4 * s][0], z[4 * s][1]), pk2(z[4 * s + 1][0], z[4 * s + 1][1]), pk2(z[4 * s + 2][0], z[4 * s + 2][1]), pk2(z[4 * s + 3][0], z[4 * s + 3][1])};
        st16_sc1(f, sws, lane * 16, WS_AF + (T * AFT + (hcol0 >> 4) + 4 * P + 2 * t + s) * 1024);
      }
      if (t == 0) mid();
      SBAR();
    }
  };

  if (isG) {
    lane_setup();
    const v4u sw2 = make_srd(w2, FF * DM * 2), swo = make_srd(wo, DM * DM * 2);
    const unsigned vw2 = (lane >> 3) * 1024 + swz, vw2b = vw2 + 7 * 1024;
    const int ro3 = rt % 3;
    auto kmap = [&](int kk) {
      if (kk < 16) return 8 * (((kk >> 2) + rt) & 3) + (kk & 3);
      const int p = ((kk - 16) >> 2) + ro3;
      return 8 * (p >= 3 ? p - 3 : p) + 4 + (kk & 3);
    };
    auto omap = [&](int i) { return 2 * ((i + rt) & 3) + (i >> 2); };
    auto wdmah = [&](int kk, int half) {
      const unsigned so = (kk < 28 ? 448 * m + 16 * kmap(kk) : 128 * m + 16 * omap(kk - 28)) * 1024 + 128 * ct;
      if (half == 0) dma16_a(kk < 28 ? sw2 : swo, vw2, so, lds0 + L_WG + m * PART + (kk % RD) * 2048); else dma16_b(kk < 28 ? sw2 : swo, vw2b, so);
    };
    auto wdma = [&](int kk) { wdmah(kk, 0); wdmah(kk, 1); };
    {
      const v4u swq = make_srd(wqkv, DM * 3 * DM * 2);
      const unsigned vx0 = (lane >> 3) * 1024 + (((lane & 7) ^ g) * 16), vx1 = (7 + (lane >> 3)) * 1024 + (((lane & 7) ^ (4 + g)) * 16);
      const unsigned vwq = (lane >> 2) * (3 * DM * 2) + p4 * 16;
      auto piece = [&](int s, int i) {
        const int kc = (s + rot) & 7;
        if (i < 4) {
          const unsigned so = (128 * r + 32 * m + 16 * (i >> 1)) * 1024 + kc * 128;
          if (i & 1) dma16_b(sx, vx1, so); else dma16_a(sx, vx0, so, lds0 + xbuf(s) + m * 4096 + (i >> 1) * 2048);
        } else if (i < 7) dma16(swq, vwq, (64 * kc + 16 * m) * (3 * DM * 2) + (512 * (i - 4) + 32 * j) * 2, lds0 + qbuf(s) + ((i - 4) * 4 + m) * 1024);
        else w1dma(0, s, lds0 + w1buf(s), i - 7);
      };
#pragma unroll
      for (int i = 0; i < 18; ++i) { piece(i / 9, i % 9); if (i == 8) __builtin_amdgcn_s_barrier(); }
      __builtin_amdgcn_s_setprio(PRIO_G1);
      hsetup(0);
      f32x4 cs[2] = {};
      v4u xf[4], wf[4][2];
      hac[0] = (f32x16){}; hac[1] = (f32x16){};
      static_for<0, 8>([&](auto sc) {
        constexpr int s = sc.value;
        auto fill = [&](int i) {
          if constexpr (s < 6) piece(s + 2, i);
          if constexpr (s == 6) {
            if (i == 0) { stat_load(); bias_load(0); }
            else if (i < 5) { if (heavy) w1dma(1, (i - 1) >> 1, lds0 + L_WA + ((i - 1) >> 1) * 8192, (i - 1) & 1); }
            else wdmah((i - 5) >> 1, (i - 5) & 1);
          }
          if constexpr (s == 7) {
            if (heavy && i < 4) w1dma(1, 2 + (i >> 1), lds0 + L_WA + (2 + (i >> 1)) * 8192, i & 1);
            if (heavy && i >= 4) { w1dma(1, i, lds0 + L_WA + i * 8192, 0); w1dma(1, i, 0, 1); }
          }
          SBAR();
        };
        auto rd = [&](int p) {
          xf[p] = lds128(xbuf(s) + xo[p]);
          wf[p][0] = ldtr2(rk[p][0] + (s % 3) * PART, 512); wf[p][1] = ldtr2(rk[p][1] + (s % 3) * PART, 512);
          SBAR();
        };
        if constexpr (s < 7) wait_vm<9>(); else { if (heavy) wait_vm<11>(); else wait_vm<7>(); }
        bar();
        STAMP_AT(10 + s);
        rd(0);
        if constexpr (s < 7) fill(0);
        rd(1);
        if constexpr (s < 7) fill(1);
        if constexpr (s == 2) get_tag();
#pragma unroll
        for (int p = 0; p < 4; ++p) {
#pragma unroll
          for (int t = 0; t < 2; ++t) {
            hac[t] = MFMA32(wf[p][t], xf[p], hac[t]);
            if (p == 0) { cs[t] = MFMA16(sel, wf[p][t], cs[t]); PIN(cs[t]); }
            SBAR();
            if (p < 2 && t == 0) rd(p + 2);
            if (s == 7) fill(2 * p + t); else if (2 * p + t < 7) fill(2 + 2 * p + t);
          }
          if (s == 7 && p == 1) {
            cswr(0, cs[0]); cswr(1, cs[1]);
            bar();
            STAMP_AT(1);
          }
        }
        if constexpr (s == 7) {
          const f32x4 c0 = csrd(0), c1 = csrd(1);
          if (heavy) wait_vm<20>(); else wait_vm<4>();
          PIN(st); PIN(bias0); PIN(bias1);
          row_stats(); make_xaug();
          hac[0] = MFMA32(waug(c0, bias0), xaug, hac[0]);
          hac[1] = MFMA32(waug(c1, bias1), xaug, hac[1]);
          SBAR();
        }
      });
    }
    STAMP_AT(18);
    if (heavy) { wait_vm<8>(); arrive_nowait(lds0 + L_CNT + 16); }
    hgelu(std::integral_constant<int, 0>{}, [&]() {
      wait_vm<2>();
      if (heavy) arrive_nowait(lds0 + L_CNT + 20);
    });
    __builtin_amdgcn_s_setprio(0);
    STAMP_AT(19);
    unsigned rb2[2];
#pragma unroll
    for (int c = 0; c < 2; ++c) rb2[c] = lds0 + L_WG + m * PART + h * 512 + q4 * 128 + ((c ^ (q4 >> 1)) * 64) + (g & 1) * 32 + p4 * 8;
    v4u wq[36][2];
    auto wrd = [&](int kk) {
#pragma unroll
      for (int c = 0; c < 2; ++c) wq[kk][c] = ldtr2(rb2[c] + (kk % RD) * 2048, 1024);
    };
    auto wgot = [&](int kk) { PIN(wq[kk][0]); PIN(wq[kk][1]); };
    const int ftile = 2 * rt + ((lane >> 2) & 1);
    v4u af[28][2], of[8][2];
    v2u fl;
    const unsigned a0 = WS_AF + (2 * rt * AFT + 28 * m) * 1024, o0 = WS_AF + (2 * rt * AFT + 112 + 8 * m) * 1024;
    for (int kk = 2; kk < 6; ++kk) wdma(kk);
    wait_vm<8>();
    if (lane == 0) __hip_atomic_store(flag(FL_H0, T, j), tag, RLX_AGENT);
    STAMP_AT(2);
    wdma(6); wdma(7);
    static_for<0, 3>([&](auto ic) {
      constexpr int i = ic.value;
      wait_vm<(i == 0) ? 9 : 8>();
      for (int kk = 4 * i; kk < 4 * i + 4; ++kk) wrd(kk);
      for (int kk = 4 * i; kk < 4 * i + 4; ++kk) wgot(kk);
      if constexpr (i == 2) fl = ld8_sc1_async(sws, flag_off(FL_H0, ftile, 4 * m + p4));
      for (int kk = 4 * i; kk < 4 * i + 4; ++kk) wdma(kk + RD);
    });
    STAMP_AT(3);
    wait_vmf<8>(fl);
    if (!__all(fl[0] == taglo && fl[1] == tag32)) poll_flags(flag(FL_H0, ftile, 4 * m + p4), tag);
    STAMP_AT(4);
#pragma unroll
    for (int kk = 0; kk < AW; ++kk) {
      af[kk][0] = ld16_sc1_async(sws, lane * 16, a0 + kmap(kk) * 1024);
      af[kk][1] = ld16_sc1_async(sws, lane * 16, a0 + (AFT + kmap(kk)) * 1024);
    }
    __builtin_amdgcn_s_setprio(PRIO_G2);
    f32x16 c2[2][2] = {};
    auto mm2 = [&](int kk, const v4u (&a)[2], auto&& fill) {
      SBAR();
#pragma unroll
      for (int i = 0; i < 4; ++i) { c2[i >> 1][i & 1] = MFMA32(wq[kk][i & 1], a[i >> 1], c2[i >> 1][i & 1]); SBAR(); fill(i); SBAR(); }
    };
    static_for<0, 28>([&](auto kc) {
      constexpr int kk = kc.value;
      wait_vm2<(kk < AW) ? 2 * (AW - 1 - kk) + g2_range(0, kk) : g2_range(kk - AW + 1, kk)>(af[kk][0], af[kk][1]);
      if constexpr (kk == H1_CK) {
        STAMP_AT(20);
        wait_vmf<g2_range(H1_LD, H1_CK) - 1>(fl);
        if (!__all(fl[0] == taglo && fl[1] == tag32)) poll_flags(flag(FL_H1, ftile, 4 * m + p4), tag);
        STAMP_AT(21);
      }
      if constexpr (kk == H1_LD) fl = ld8_sc1_async(sws, flag_off(FL_H1, ftile, 4 * m + p4));
      if constexpr (kk == O_LD) fl = ld8_sc1_async(sws, flag_off(FL_O0, ftile, 4 * m + p4));
      constexpr bool dw = kk >= 1 && kk - 1 + NR + RD < 36;
      if constexpr (dw) wgot(kk - 1 + NR);
      mm2(kk, af[kk], [&](int i) {
        if constexpr (dw) { if (i < 2) wdmah(kk - 1 + NR + RD, i); }
        if constexpr (kk + NR < 28) { if (i == 1) wrd(kk + NR); }
        if constexpr (kk == 27) { if (i == 0) wrd(28); }
        if constexpr (kk + AW < 28) { if (i >= 2) af[kk + AW][i - 2] = ld16_sc1_async(sws, lane * 16, a0 + (AFT * (i - 2) + kmap(kk + AW)) * 1024); }
      });
      if constexpr (kk == O_CK) {
        wait_vmf<g2_range(O_LD, O_CK) - 1>(fl);
        if (!__all(fl[0] == taglo && fl[1] == tag32)) poll_flags(flag(FL_O0, ftile, 4 * m + p4), tag);
        res_loads();
#pragma unroll
        for (int i = 0; i < 4; ++i) {
          of[i][0] = ld16_sc1_async(sws, lane * 16, o0 + omap(i) * 1024);
          of[i][1] = ld16_sc1_async(sws, lane * 16, o0 + (AFT + omap(i)) * 1024);
        }
      }
    });
    static_for<0, 8>([&](auto kc) {
      constexpr int kk = kc.value;
      if (kk < 7) wrd(28 + kk + 1);
      if constexpr (kk == 1) {
        STAMP_AT(5);
        poll_flags(flag(FL_O1, ftile, 4 * m + p4), tag);
        STAMP_AT(6);
#pragma unroll
        for (int i = 4; i < 8; ++i) {
          of[i][0] = ld16_sc1_async(sws, lane * 16, o0 + omap(i) * 1024);
          of[i][1] = ld16_sc1_async(sws, lane * 16, o0 + (AFT + omap(i)) * 1024);
        }
      }
      wait_vm2<(kk == 0) ? 6 : 14 - 2 * kk>(of[kk][0], of[kk][1]);
      mm2(28 + kk, of[kk], [&](int) {});
    });
    PIN(xres); PIN(bres);
    asm volatile("" ::: "memory");
#pragma unroll
    for (int mt = 0; mt < 2; ++mt)
#pragma unroll
      for (int c = 0; c < 2; ++c)
#pragma unroll
        for (int qd = 0; qd < 4; ++qd) {
          const f32x4 v = {c2[mt][c][4 * qd], c2[mt][c][4 * qd + 1], c2[mt][c][4 * qd + 2], c2[mt][c][4 * qd + 3]};
          sts128(lds0 + L_WG + m * PART + ((mt * 8 + c * 4 + qd) * 66 + lane) * 16, __builtin_bit_cast(v4u, v));
        }
    STAMP_AT(7);
  } else {
    __builtin_amdgcn_s_setprio(1);
    lane_setup();
    v4u xr[2];
    xr[0] = ld16_async(sx, lane * 16, (128 * r + 8 * j + 2 * m) * 1024); xr[1] = ld16_async(sx, lane * 16, (128 * r + 8 * j + 2 * m + 1) * 1024);
    unsigned warm;
    {
      u64 pc; asm volatile("s_getpc_b64 %0" : "=s"(pc));
      warm = ld2_async(make_srd((const void*)(pc & ~127ull), CODEWARM), lane < 3 ? lane * 128 : 0x40000000u, (idx * 4 + m) * 384);
    }
    __builtin_amdgcn_s_barrier();
    v2u ep = ld8_sc1_async(sws, m == 0 ? WS_EPOCH : 0x40000000u);
    float sval;
    const int wi = blockIdx.x * 4 + m;
    const u64 wbase = wi < 448 ? (u64)w1 + wi * 4096 : wi < 896 ? (u64)w2 + (wi - 448) * 4096 : (u64)wo + (wi - 896) * 4096;
    auto make_stats = [&]() {
      wait_vm2<1>(xr[0], xr[1]);
      PIN(warm);
      float piv[2], a[4];
#pragma unroll
      for (int u = 0; u < 2; ++u) {
        piv[u] = rdl(bflo(xr[u][0]), 0);
        float s1 = 0.f, s2 = 0.f;
#pragma unroll
        for (int i = 0; i < 8; ++i) { const float d = ((i & 1) ? bfhi(xr[u][i >> 1]) : bflo(xr[u][i >> 1])) - piv[u]; s1 += d; s2 = __builtin_fmaf(d, d, s2); }
        a[2 * u] = s1; a[2 * u + 1] = s2;
      }
#pragma unroll
      for (int i = 0; i < 4; ++i) a[i] += dpp<0xB1>(a[i]);
#pragma unroll
      for (int i = 0; i < 4; ++i) a[i] += dpp<0x4E>(a[i]);
      float t = p4 == 0 ? a[0] : p4 == 1 ? a[1] : p4 == 2 ? a[2] : a[3];
      t += dpp<0x114>(t); t += dpp<0x118>(t);
      const auto s16 = __builtin_amdgcn_permlane16_swap(asu(t), asu(t), false, false);
      t = asf(s16[0]) + asf(s16[1]);
      const auto s32 = __builtin_amdgcn_permlane32_swap(asu(t), asu(t), false, false);
      t = (asf(s32[0]) + asf(s32[1])) * (1.f / DM);
      const float e1 = dpp<0xA0>(t);
      sval = (p4 & 1) ? __builtin_fmaxf(__builtin_fmaf(-e1, e1, t), 0.f) + 1e-5f : t + (p4 ? piv[1] : piv[0]);
      PIN(sval);
    };
    const unsigned acnt = lds0 + L_CNT;
    const unsigned vpos = 2048 + (g & 1) * 512 + h * 256 + (lane & 15) * 16;
    v4u qf[2], am0, am1;
    u64 um[16];
    auto masks = [&]() {
      const int dq = r32 - 4 * h;
#pragma unroll
      for (int d = 0; d < 4; ++d) {
        const int row = 2 * (d & 1) + 8 * (d >> 1);
        am0[d] = row > dq ? 0xffffffffu : row == dq ? 0xffff0000u : 0u;
        am1[d] = row + 16 > dq ? 0xffffffffu : row + 16 == dq ? 0xffff0000u : 0u;
      }
      PIN(am0); PIN(am1);
      static_for<0, 16>([&](auto u) { um[u.value] = upper_mask(u.value); pin_s(um[u.value]); });
    };
    {
      v4u xf[32];
      f32x16 acc[3] = {};
      f32x4 cs[3] = {};
      v4u wf[4][3];
      unsigned wb[4];
#pragma unroll
      for (int p = 0; p < 4; ++p) { wb[p] = qbuf(0) + ((p + m) & 3) * 1024 + (8 * h + q4) * 64 + (g & 1) * 32 + p4 * 8; PIN(wb[p]); }
      auto rd = [&](int ks, int t) {
        if (t == 0) xf[ks] = lds128(xbuf(ks >> 2) + xo[ks & 3]);
        wf[ks & 3][t] = ldtr2(wb[ks & 3] + ((ks >> 2) % 3) * 12288 + t * 4096, 256);
      };
      auto mm = [&](int ks, int t) {
        acc[t] = t == 2 ? MFMA32(xf[ks], wf[ks & 3][t], acc[t]) : MFMA32(wf[ks & 3][t], xf[ks], acc[t]);
        if ((ks & 3) == 0) { cs[t] = MFMA16(sel, wf[ks & 3][t], cs[t]); PIN(cs[t]); }
        SBAR();
      };
      make_stats();
      bar();
      STAMP_AT(10);
#pragma unroll
      for (int t = 0; t < 6; ++t) rd(t / 3, t % 3);
      SBAR();
      static_for<0, 8>([&](auto sc) {
        constexpr int s = sc.value;
#pragma unroll
        for (int p = 0; p < 2; ++p) {
#pragma unroll
          for (int t = 0; t < 3; ++t) { mm(4 * s + p, t); rd(4 * s + p + 2, t); SBAR(); }
          if (s == 7 && p == 0) { cswr(4, cs[0]); cswr(5, cs[1]); cswr(6, cs[2]); SBAR(); }
        }
        if constexpr (s == 0) {
          wait_vmf<0>(ep);
          if (m == 0 && lane < 10)
            *(LAS unsigned*)(size_t)(lds0 + L_CNT + lane * 4) = lane == 8 ? ep[0] * 0x9E3779B1u + 0x7F4A7C15u : lane == 9 ? ep[1] * 0x85EBCA77u + 0x165667B1u : 0u;
        }
        if constexpr (s == 1) {
          get_tag();
          if ((lane >> 2) == 3) {
            const v2u gr = {asu(sval), tag32};
            __builtin_amdgcn_raw_buffer_store_b64(gr, rws, WS_STATS + (128 * r + 8 * j + 2 * m) * 16 + p4 * 8, 0, 16);
          }
        }
        if constexpr (s == MIDWARM) {
          warm = ld2_async(make_srd((const void*)wbase, 4096), lane * 128, 0);
        }
        if constexpr (s == 5) stat_load();
        if constexpr (s == 6) {
          wait_vm1<0>(st);
          PIN(warm);

          row_stats();
        }
        mm(4 * s + 2, 0); mm(4 * s + 2, 1);
        bar();
        if constexpr (s < 7) {
          STAMP_AT(11 + s);
          mm(4 * s + 2, 2);
          rd(4 * s + 4, 0); rd(4 * s + 4, 1); rd(4 * s + 4, 2); SBAR();
#pragma unroll
          for (int t = 0; t < 3; ++t) { mm(4 * s + 3, t); rd(4 * s + 5, t); SBAR(); }
        }
      });
      STAMP_AT(1);
      __builtin_amdgcn_s_setprio(3);
      const f32x4 c5 = csrd(5), c6 = csrd(6), c4 = csrd(4);
      SBAR();
      mm(30, 2); mm(31, 1);
      make_xaug();
      SBAR();
      mm(31, 2); mm(31, 0);
      if (h == 0) *(LAS float*)(size_t)(lds0 + L_RS + (m * 32 + r32) * 4) = rstd;
      acc[1] = MFMA32(waug(c5, 0u), xaug, acc[1]);
      acc[2] = MFMA32(xaug, waug(c6, 0u), acc[2]);
      acc[0] = MFMA32(waug(c4, 0u), xaug, acc[0]);
      SBAR();
      f32x4 rs[4];
#pragma unroll
      for (int qd = 0; qd < 4; ++qd) rs[qd] = __builtin_bit_cast(f32x4, lds128(lds0 + L_RS + (m * 32 + 8 * qd + 4 * h) * 4));
      const unsigned kvo = WS_KV + (j * 64 + T) * 4096;
#pragma unroll
      for (int e = 0; e < 16; ++e) acc[1][e] = MUL1(acc[1][e], rstd);
#pragma unroll
      for (int e = 0; e < 2; ++e) {
        const v4u f = pack8(acc[1], e);
        st16_sc1(f, sws, lane * 16, kvo + e * 1024);
        sts128(lds0 + L_KV + (8 + m) * KVT + e * 1024 + lane * 16, f);
      }
#pragma unroll
      for (int qd = 0; qd < 4; ++qd)
#pragma unroll
        for (int e = 0; e < 4; ++e) acc[2][4 * qd + e] = MUL1(acc[2][4 * qd + e], rs[qd][e]);
#pragma unroll
      for (int s = 0; s < 2; ++s) {
        const v4u f = pack8(acc[2], s);
        st16_sc1(f, sws, lane * 16, kvo + 2048 + s * 1024);
        sts128(lds0 + L_KV + (8 + m) * KVT + s * VB + vpos, f);
      }
      SBAR();
      const float qs = rstd * (0.25f * 1.4426950408889634f);
#pragma unroll
      for (int e = 0; e < 16; ++e) acc[0][e] = MUL1(acc[0][e], qs);
      qf[0] = pack8(acc[0], 0); qf[1] = pack8(acc[0], 1);
      PIN(qf[0]); PIN(qf[1]);
      STAMP_AT(2);
      const int T0 = 4 * r - 8 + 2 * m;
      const bool live = T0 >= 0;
      auto kv_load = [&](v4u (&kv)[8], int i0, int i1) {
        const unsigned src = WS_KV + (j * 64 + (live ? T0 : 0)) * 4096, vo = live ? lane * 16 : h * 512;
#pragma unroll
        for (int i = i0; i < i1; ++i) kv[i] = ld16_sc1_async(sws, vo, src + (live ? i : (i & 3)) * 1024);
      };
      auto kv_stage = [&](v4u (&kv)[8]) {
        const unsigned on = (lane < 2 || live) ? one2 : 0u;
        if (lane < 6) sts128(lds0 + L_KV + (lane < 2 ? 8 + m : 2 * m + ((lane - 2) >> 1)) * KVT + 2048 + (lane & 1) * VB + 1024, (v4u){on, on, on, on});
        const v4u zz = {};
#pragma unroll
        for (int i = 0; i < 2; ++i) {
          sts128(lds0 + L_KV + (2 * m + i) * KVT + lane * 16, kv[4 * i]);
          sts128(lds0 + L_KV + (2 * m + i) * KVT + 1024 + lane * 16, kv[4 * i + 1]);
          sts128(lds0 + L_KV + (2 * m + i) * KVT + vpos, live ? kv[4 * i + 2] : zz);
          sts128(lds0 + L_KV + (2 * m + i) * KVT + VB + vpos, live ? kv[4 * i + 3] : zz);
        }
        arrive_nowait(acnt);
      };
      auto kv_wait = [&](v4u (&kv)[8]) {
        wait_vm<0>();
#pragma unroll
        for (int i = 0; i < 8; ++i) PIN(kv[i]);
      };
      if (heavy) {
        hsetup(1);
        await(acnt + 16, 4);
        STAMP_AT(3);
        v2u fl;
        v4u kv[8];
        hmm(std::integral_constant<int, 1>{}, xf, [&](auto kc) {
          constexpr int ks = kc.value;
          if constexpr (ks == KVPUB) {
            wait_vm<0>();
            if (lane == 0) __hip_atomic_store(flag(FL_KV, T, j), tag, RLX_AGENT);
          }
          if constexpr (ks == KVPUB + 2) bias_load(1);
          if constexpr (ks == 31) { wait_vm<1>(); PIN(bias0); PIN(bias1); }
          if constexpr (ks == 24) fl = ld8_sc1_async(sws, flag_off(FL_KV, live ? T0 + (lane & 1) : 0, j));
        });
        STAMP_AT(18);
        wait_vmf<0>(fl);
        if (!__all(fl[0] == taglo && fl[1] == tag32)) poll_flags(flag(FL_KV, live ? T0 + (lane & 1) : 0, j), tag);
        kv_load(kv, 0, 8);
        hgelu(std::integral_constant<int, 1>{}, [&]() {});
        STAMP_AT(4);
        masks();
        wait_vm<4>();
#pragma unroll
        for (int i = 0; i < 8; ++i) PIN(kv[i]);
        kv_stage(kv);
      } else {
        wait_vm<0>();
        if (lane == 0) __hip_atomic_store(flag(FL_KV, T, j), tag, RLX_AGENT);
        v4u kv[8];
        poll_flags(flag(FL_KV, live ? T0 + (lane & 1) : 0, j), tag);
        kv_load(kv, 0, 8); masks(); kv_wait(kv); kv_stage(kv);
      }
    }
    STAMP_AT(19);
    const unsigned kb = lds0 + L_KV + m * KVT + lane * 16;
    unsigned vb[2];
#pragma unroll
    for (int e = 0; e < 2; ++e) { vb[e] = lds0 + L_KV + m * KVT + 2048 + ((g & 1) ? 1024 : e * 512 + h * 256 + (lane & 15) * 16); PIN(vb[e]); }
    await(acnt, heavy ? 8 : 4);
    __builtin_amdgcn_s_setprio(PRIO_ATT);
    STAMP_AT(5);
    const f32x16 zero = {};
    auto o_store = [&](f32x16& o, int e) {
      const float rl = __builtin_amdgcn_rcpf(o[8]);
#pragma unroll
      for (int u = 0; u < 8; ++u) o[u] = MUL1(o[u], rl);
      st16_sc1(pack8(o, 0), sws, lane * 16, WS_AF + (T * AFT + 112 + 2 * j + e) * 1024);
    };
    int redo = 0;
    {
      f32x16 o[2], sa, sb, su;
      bool pend = false;
      v4u kn, ku, va, vc, vn0, vn1, vu0, vu1, p0, p1, pu;
      auto kaddr = [&](int n) { return kb + ((n & 7) + 1) * KVT + (n >> 3) * 1024; };
      auto vaddr = [&](int n) { return vb[n >> 3] + ((n & 7) + 1) * KVT; };
      kn = lds128(kaddr(0)); vn0 = lds128(vaddr(0)); vn1 = lds128(vaddr(0) + VB);
      sa = MFMA32(kn, qf[0], zero);
      kn = lds128(kaddr(1));
      SBAR();
      auto step = [&](auto nc, f32x16& sc, f32x16& sn) {
        constexpr int n = nc.value, e = n >> 3, i = n & 7;
        if constexpr (n < 15) sn = MFMA32(kn, qf[(n + 1) >> 3], zero);
        if constexpr (n < 14) kn = lds128(kaddr(n + 2));
        if constexpr (i == 5) ku = lds128(kb + e * 1024);
        v4u v0 = vn0, v1 = vn1;
        if constexpr (n < 15) { vn0 = lds128(vaddr(n + 1)); vn1 = lds128(vaddr(n + 1) + VB); }
        if constexpr (i == 6) { vu0 = lds128(vb[e]); vu1 = lds128(vb[e] + VB); }
        SBAR();
        if constexpr (i == 7) {
#pragma unroll
          for (int u = 0; u < 16; ++u) sc[u] = __builtin_amdgcn_inverse_ballot_w64(um[u]) ? su[u] : sc[u];
          PIN(sc); SBAR();
        }
#pragma unroll
        for (int u = 0; u < 6; ++u) sc[u] = EXP2(sc[u]);
        PIN(sc); SBAR();
        if constexpr (n == 8) { o[0] = MFMA32(vc, p1, o[0]); SBAR(); }
        else if constexpr (n > 0) { o[e] = MFMA32(vc, p1, o[e]); SBAR(); }
        sc[6] = EXP2(sc[6]); sc[7] = EXP2(sc[7]);
        p0 = pack8(sc, 0);
        sc[8] = EXP2(sc[8]); sc[9] = EXP2(sc[9]);
        if constexpr (i == 7) { va = p0 & am0; p0 &= ~am0; PIN(va); }
        PIN(sc); PIN(p0); SBAR();
        if constexpr (n == 8) { o[0] = MFMA32(vu1, pu, o[0]); SBAR(); }
        o[e] = MFMA32(v0, p0, i == 0 ? zero : o[e]);
        SBAR();
        if constexpr (i == 6) { su = MFMA32(ku, qf[e], zero); SBAR(); }
#pragma unroll
        for (int u = 10; u < 13; ++u) sc[u] = EXP2(sc[u]);
        if constexpr (i == 7) { PIN(sc); SBAR(); o[e] = MFMA32(vu0, va, o[e]); SBAR(); }
#pragma unroll
        for (int u = 13; u < 16; ++u) sc[u] = EXP2(sc[u]);
        p1 = pack8(sc, 1); vc = v1;
        if constexpr (i == 7) { pu = p1 & am1; p1 &= ~am1; PIN(pu); }
        PIN(p1); SBAR();
        if constexpr (n == 8) {
          if (__all(o[0][8] >= 0x1p-30f && o[0][8] <= 0x1p30f)) o_store(o[0], 0); else redo = 1;
          SBAR();
        }
        if constexpr (i == 2) pend = !redo;
        if constexpr (i == 3 || i == 5 || i == 7) {
          if (pend && (i == 7 || (__builtin_amdgcn_s_getreg(7 | (3 << 11)) & 15) == 0)) {
            wait_vm<0>();
            if (lane == 0) __hip_atomic_store(flag(FL_H1 + e, T, j), tag, RLX_AGENT);
            pend = false;
          }
        }
      };
      static_for<0, 8>([&](auto hc) {
        step(std::integral_constant<int, 2 * hc.value>{}, sa, sb);
        step(std::integral_constant<int, 2 * hc.value + 1>{}, sb, sa);
      });
      o[1] = MFMA32(vc, p1, o[1]);
      o[1] = MFMA32(vu1, pu, o[1]);
      if (__all(o[1][8] >= 0x1p-30f && o[1][8] <= 0x1p30f)) o_store(o[1], 1); else redo |= 2;
    }
    if (redo) {
#pragma unroll 1
      for (int e = 0; e < 2; ++e) {
        if (!((redo >> e) & 1)) continue;
        const v4u q = e ? qf[1] : qf[0];
        const unsigned vbe = e ? vb[1] : vb[0];
        auto merged = [&](const f32x16& c) {
          f32x16 sl = MFMA32(lds128(kb + 8 * KVT + e * 1024), q, c);
          const f32x16 su = MFMA32(lds128(kb + e * 1024), q, c);
          static_for<0, 16>([&](auto u) { sl[u.value] = upper<u.value>() ? su[u.value] : sl[u.value]; });
          return sl;
        };
        float mx = -INFINITY;
#pragma unroll 1
        for (int i = 0; i < 8; ++i) {
          const f32x16 sc = i ? MFMA32(lds128(kb + i * KVT + e * 1024), q, zero) : merged(zero);
#pragma unroll
          for (int u = 0; u < 16; u += 2) mx = max3(mx, sc[u], sc[u + 1]);
        }
        const auto sw = __builtin_amdgcn_permlane32_swap(asu(mx), asu(mx), false, false);
        mx = __builtin_fmaxf(asf(sw[0]), asf(sw[1]));
        f32x16 negm, o;
#pragma unroll
        for (int u = 0; u < 16; ++u) negm[u] = -mx;
        {
          f32x16 sc = merged(negm);
#pragma unroll
          for (int u = 0; u < 16; ++u) sc[u] = EXP2(sc[u]);
          const v4u p0 = pack8(sc, 0), p1 = pack8(sc, 1);
          o = MFMA32(lds128(vbe), p0 & am0, zero);
          o = MFMA32(lds128(vbe + VB), p1 & am1, o);
          o = MFMA32(lds128(vbe + 8 * KVT), p0 & ~am0, o);
          o = MFMA32(lds128(vbe + 8 * KVT + VB), p1 & ~am1, o);
        }
#pragma unroll 1
        for (int i = 1; i < 8; ++i) {
          f32x16 sc = MFMA32(lds128(kb + i * KVT + e * 1024), q, negm);
#pragma unroll
          for (int u = 0; u < 16; ++u) sc[u] = EXP2(sc[u]);
          o = MFMA32(lds128(vbe + i * KVT), pack8(sc, 0), o);
          o = MFMA32(lds128(vbe + i * KVT + VB), pack8(sc, 1), o);
        }
        o_store(o, e);
      }
      wait_vm<0>();
      if (lane == 0) __hip_atomic_store(flag(FL_O0, T, j), tag, RLX_AGENT);
    }
    STAMP_AT(6);
    wait_vm<0>();
    if (lane == 0) __hip_atomic_store(flag(FL_O1, T, j), tag, RLX_AGENT);
    STAMP_AT(7);
    res_loads();
    wait_vm2<0>(xres, bres);
    if (blockIdx.x == 0 && wave == 7) {
#pragma unroll 1
      for (int i = 0; i < 4; ++i) poll_flags(flag(FL_KV, 4 * (4 * i + g), lane & 15), tag);
      if (lane == 0) __hip_atomic_store((GAS u64*)(ws + WS_EPOCH), tag, RLX_AGENT);
    }
  }
  bar();
  {
    int t2 = threadIdx.x; PIN(t2);
    const int row = t2 >> 3, ch = t2 & 7, orow = 64 * rt + row, ocol = 64 * ct + ch * 8;
    const unsigned a = lds0 + L_WG + (((row >> 5) * 8 + ch) * 66 + (row & 31)) * 16;
    f32x4 lo = {}, hi = {};
#pragma unroll
    for (int w = 0; w < 4; ++w) {
      lo += __builtin_bit_cast(f32x4, lds128(a + w * PART));
      hi += __builtin_bit_cast(f32x4, lds128(a + w * PART + 512));
    }
    v4u o;
    o[0] = pk2(lo[0] + bflo(xres[0]) + bflo(bres[0]), lo[1] + bfhi(xres[0]) + bfhi(bres[0]));
    o[1] = pk2(lo[2] + bflo(xres[1]) + bflo(bres[1]), lo[3] + bfhi(xres[1]) + bfhi(bres[1]));
    o[2] = pk2(hi[0] + bflo(xres[2]) + bflo(bres[2]), hi[1] + bfhi(xres[2]) + bfhi(bres[2]));
    o[3] = pk2(hi[2] + bflo(xres[3]) + bflo(bres[3]), hi[3] + bfhi(xres[3]) + bfhi(bres[3]));
    __builtin_amdgcn_raw_buffer_store_b128(o, __builtin_amdgcn_make_buffer_rsrc(out, 0, SEQ * DM * 2, 0x00020000), orow * 1024 + ocol * 2, 0, 16);
  }
  STAMP_AT(8);
}

extern "C" void kernel_launch(void* const* d_in, const int* in_sizes, int n_in, void* d_out, int out_size, void* d_ws, size_t ws_size,
                              hipStream_t stream) {
  (void)in_sizes; (void)n_in; (void)out_size; (void)ws_size;
  (void)hipFuncSetAttribute((const void*)fused_block, hipFuncAttributeMaxDynamicSharedMemorySize, LDS_BYTES);
  fused_block<<<256, 512, LDS_BYTES, stream>>>(d_in[0], d_in[1], d_in[2], d_in[4], d_in[5], d_in[6], d_in[7], d_out, (char*)d_ws);
}
```

```cpp
#include <hip/hip_runtime.h>
#include <hip/hip_bf16.h>
#include <stdint.h>
#include <type_traits>

typedef unsigned v4u __attribute__((ext_vector_type(4)));
typedef unsigned v2u __attribute__((ext_vector_type(2)));
typedef float f32x2 __attribute__((ext_vector_type(2)));
typedef float f32x4 __attribute__((ext_vector_type(4)));
typedef float f32x16 __attribute__((ext_vector_type(16)));
typedef short bf16x8 __attribute__((ext_vector_type(8)));
typedef short s16x4 __attribute__((ext_vector_type(4)));
typedef __bf16 bf16x2_t __attribute__((ext_vector_type(2)));
typedef unsigned long long u64;
#define GAS __attribute__((address_space(1)))
#define LAS __attribute__((address_space(3)))
#define DI __device__ __forceinline__
#define RLX_AGENT __ATOMIC_RELAXED, __HIP_MEMORY_SCOPE_AGENT

constexpr int SEQ = 2048, DM = 512, FF = 1792;
constexpr unsigned WS_EPOCH = 0;
constexpr unsigned WS_STATS = 32768;
constexpr unsigned WS_FLAG = 65536;
constexpr unsigned WS_KV = WS_FLAG + 5 * 1024 * 128;
constexpr int AFT = 145;
constexpr unsigned WS_AF = WS_KV + 16 * 64 * 4096;
constexpr unsigned WS_END = WS_AF + 64 * AFT * 1024;
constexpr unsigned WS_STAMP = WS_END;
enum { FL_KV = 0, FL_H0 = 1, FL_H1 = 2, FL_O0 = 3, FL_O1 = 4 };
constexpr int L_WA = 0;
constexpr int L_WG = 86016;
constexpr int VB = 1040;
constexpr int KVT = 2048 + 2 * VB;
constexpr int L_KV = 65536 - 8 * KVT;
constexpr int RD = 8, NR = 12;
constexpr int PART = 16 * 66 * 16;
constexpr int L_CS = L_WG + 4 * PART;
constexpr int L_RS = L_CS + 3584;
constexpr int L_CNT = L_RS + 512;
constexpr int LDS_BYTES = L_CNT + 64;
constexpr int xbuf(int s) { return ((s + 1) % 3) * 16384; }
constexpr int qbuf(int s) { return 49152 + (s % 3) * 12288; }
constexpr int w1buf(int s) { return L_WG + (s % 3) * PART + 8192; }
static_assert(xbuf(7) == 32768 && qbuf(2) + 12288 <= L_WG && L_KV + 12 * KVT <= L_WG && PART >= RD * 2048 && LDS_BYTES <= 163840, "lds map");
#define AWIN 6
constexpr int AW = AWIN;
constexpr unsigned SPIN_CAP = 1u << 23;

#define MFMA32(a, b, c) __builtin_amdgcn_mfma_f32_32x32x16_bf16(__builtin_bit_cast(bf16x8, a), __builtin_bit_cast(bf16x8, b), c, 0, 0, 0)
#define MFMA16(a, b, c) __builtin_amdgcn_mfma_f32_16x16x32_bf16(__builtin_bit_cast(bf16x8, a), __builtin_bit_cast(bf16x8, b), c, 0, 0, 0)
#define SBAR() __builtin_amdgcn_sched_barrier(0)

#define STAMP_AT(i) do { } while (0)

DI float asf(unsigned u) { return __builtin_bit_cast(float, u); }
DI unsigned asu(float f) { return __builtin_bit_cast(unsigned, f); }
DI unsigned pk2(float lo, float hi) { return __builtin_bit_cast(unsigned, __builtin_convertvector((f32x2){lo, hi}, bf16x2_t)); }
DI float bflo(unsigned u) { return __builtin_bit_cast(float, u << 16); }
DI float bfhi(unsigned u) { return __builtin_bit_cast(float, u & 0xffff0000u); }
DI v4u make_srd(const void* p, unsigned bytes) { const u64 a = (u64)p; return (v4u){(unsigned)a, (unsigned)(a >> 32) & 0xffffu, bytes, 0x00020000u}; }
DI void dma16(v4u srd, unsigned voff, unsigned soff, unsigned lds) {
  asm volatile("s_mov_b32 m0, %3\n\ts_nop 0\n\tbuffer_load_dwordx4 %0, %1, %2 offen lds" :: "v"(voff), "s"(srd), "s"(soff), "s"(lds) : "memory");
}
DI void dma16_a(v4u srd, unsigned voff, unsigned soff, unsigned lds) { dma16(srd, voff, soff, lds); }
DI void dma16_b(v4u srd, unsigned voff1, unsigned soff) {
  asm volatile("buffer_load_dwordx4 %0, %1, %2 offen offset:1024 lds" :: "v"(voff1), "s"(srd), "s"(soff) : "memory");
}
DI v4u ld16_sc1_async(v4u srd, unsigned voff, unsigned soff) {
  v4u r; asm volatile("buffer_load_dwordx4 %0, %1, %2, %3 offen sc1" : "=v"(r) : "v"(voff), "s"(srd), "s"(soff) : "memory"); return r;
}
DI v4u ld16_async(v4u srd, unsigned voff, unsigned soff) {
  v4u r; asm volatile("buffer_load_dwordx4 %0, %1, %2, %3 offen" : "=v"(r) : "v"(voff), "s"(srd), "s"(soff) : "memory"); return r;
}
DI unsigned ld2_async(v4u srd, unsigned voff, unsigned soff) {
  unsigned r; asm volatile("buffer_load_ushort %0, %1, %2, %3 offen" : "=v"(r) : "v"(voff), "s"(srd), "s"(soff) : "memory"); return r;
}
DI void st16_sc1(v4u v, v4u srd, unsigned voff, unsigned soff) {
  asm volatile("buffer_store_dwordx4 %0, %1, %2, %3 offen sc1\n\ts_nop 1" :: "v"(v), "v"(voff), "s"(srd), "s"(soff) : "memory");
}
template <int N> DI void wait_vm1(v4u& a) { asm volatile("s_waitcnt vmcnt(%1)" : "+v"(a) : "i"(N) : "memory"); }
template <int N> DI void wait_vm2(v4u& a, v4u& b) { asm volatile("s_waitcnt vmcnt(%2)" : "+v"(a), "+v"(b) : "i"(N) : "memory"); }
template <int N> DI void wait_vm3(v4u& a, v4u& b, v4u& c) { asm volatile("s_waitcnt vmcnt(%3)" : "+v"(a), "+v"(b), "+v"(c) : "i"(N) : "memory"); }
DI void pin_s(u64& v) { asm volatile("" : "+s"(v)); }
template <class V> DI void pin(V& v) { asm volatile("" : "+v"(v)); }
template <int N> DI void wait_vm() { asm volatile("s_waitcnt vmcnt(%0)" :: "i"(N) : "memory"); }
DI void bar() { asm volatile("s_waitcnt lgkmcnt(0)\n\ts_barrier" ::: "memory"); }
DI v4u lds128(unsigned a) { return *(const LAS v4u*)(size_t)a; }
DI void sts128(unsigned a, v4u v) { *(LAS v4u*)(size_t)a = v; }
DI v2u ldtr(unsigned a) { return __builtin_bit_cast(v2u, __builtin_amdgcn_ds_read_tr16_b64_v4i16((LAS s16x4*)(size_t)a)); }
DI v4u ldtr2(unsigned a, int second) { const v2u lo = ldtr(a), hi = ldtr(a + second); return (v4u){lo.x, lo.y, hi.x, hi.y}; }
template <int CTRL> DI float dpp(float v) { return __builtin_bit_cast(float, __builtin_amdgcn_update_dpp(0, __builtin_bit_cast(int, v), CTRL, 0xf, 0xf, true)); }
DI float rdl(float v, int l) { return __builtin_bit_cast(float, __builtin_amdgcn_readlane(__builtin_bit_cast(int, v), l)); }
DI v4u pack8(const f32x16& a, int s) { return (v4u){pk2(a[8 * s], a[8 * s + 1]), pk2(a[8 * s + 2], a[8 * s + 3]), pk2(a[8 * s + 4], a[8 * s + 5]), pk2(a[8 * s + 6], a[8 * s + 7])}; }
DI void poll_flags(const GAS u64* p, u64 tag) {
#pragma unroll 1
  for (unsigned spin = 0; spin < SPIN_CAP; ++spin) {
    const u64 v = __hip_atomic_load(p, RLX_AGENT);
    if (__all(v == tag)) break;
    __builtin_amdgcn_s_sleep(2);
  }
  asm volatile("" ::: "memory");
}
constexpr u64 upper_mask(int reg) {
  u64 m = 0;
  for (int l = 0; l < 64; ++l) { const int row = (reg & 3) + 8 * (reg >> 2) + 4 * (l >> 5); if (row > (l & 31)) m |= 1ull << l; }
  return m;
}
template <int REG> DI bool upper() { return __builtin_amdgcn_inverse_ballot_w64(upper_mask(REG)); }
template <int I, int N, class Fn> DI void static_for(Fn&& f) { if constexpr (I < N) { f(std::integral_constant<int, I>{}); static_for<I + 1, N>(f); } }
DI float max3(float a, float b, float c) { return __builtin_fmaxf(__builtin_fmaxf(a, b), c); }

constexpr int H1_CK = 16 - AW, H1_LD = H1_CK - 3, O_CK = 28 - AW, O_LD = O_CK - 3;
constexpr int g2_issued(int t) { return ((t >= 1 && t - 1 + NR + RD < 36) ? 2 : 0) + ((t + AW < 28) ? 2 : 0) + (t == H1_LD) + (t == O_LD) + (t == O_CK ? 10 : 0); }
constexpr int g2_range(int a, int b) { int n = 0; for (int t = a; t < b; ++t) n += g2_issued(t); return n; }
#define GELU_PK(P) (P == 1)
#define PRIO_G2 2
#define PRIO_ATT 3
#define PRIO_G1 2
#define ROT r
#define CODEWARM 38000
#define MIDWARM 1
#define KVPUB 6
#define EXP2(v) __builtin_amdgcn_exp2f(v)
#define PIN(v) pin(v)
extern "C" __device__ float mul_unpacked(float, float) __asm("llvm.amdgcn.fmul.legacy");
#define MUL1(a, b) mul_unpacked(a, b)
#define FMA1(a, b, c) __builtin_amdgcn_div_fmasf(a, b, c, false)
DI v2u ld8_sc1_async(v4u srd, unsigned voff) {
  v2u r; asm volatile("buffer_load_dwordx2 %0, %1, %2, 0 offen sc1" : "=v"(r) : "v"(voff), "s"(srd) : "memory"); return r;
}
template <int N> DI void wait_vmf(v2u& a) { asm volatile("s_waitcnt vmcnt(%1)" : "+v"(a) : "i"(N) : "memory"); }
DI void arrive(unsigned cnt, int lane) {
  asm volatile("s_waitcnt lgkmcnt(0)" ::: "memory");
  if (lane == 0) __hip_atomic_fetch_add((LAS unsigned*)(size_t)cnt, 1u, __ATOMIC_RELAXED, __HIP_MEMORY_SCOPE_WORKGROUP);
}
DI void arrive_nowait(unsigned cnt) {
  asm volatile("s_mov_b64 exec, 1\n\tds_add_u32 %0, %1\n\ts_mov_b64 exec, -1" :: "v"(cnt), "v"(1u) : "memory");
}
DI unsigned peek(unsigned cnt) { return __hip_atomic_load((LAS unsigned*)(size_t)cnt, __ATOMIC_RELAXED, __HIP_MEMORY_SCOPE_WORKGROUP); }
DI void await(unsigned cnt, unsigned n) {
#pragma unroll 1
  for (unsigned spin = 0; spin < SPIN_CAP * 32; ++spin) {
    if (__builtin_amdgcn_readfirstlane(peek(cnt)) >= n) break;
    __builtin_amdgcn_s_sleep(1);
  }
  asm volatile("" ::: "memory");
}

__global__ __launch_bounds__(512, 2) void fused_block(const void* __restrict__ x, const void* __restrict__ wqkv, const void* __restrict__ wo,
                                                      const void* __restrict__ w1, const void* __restrict__ b1, const void* __restrict__ w2,
                                                      const void* __restrict__ b2, void* __restrict__ out, char* __restrict__ ws) {
  constexpr unsigned lds0 = 0;
  const int wave = __builtin_amdgcn_readfirstlane(threadIdx.x >> 6);
  const int m = wave & 3;
  const bool isG = wave < 4;
  const int xcd = blockIdx.x & 7, idx = blockIdx.x >> 3;
  const int r = (xcd >> 2) * 8 + (idx >> 2), j = (xcd & 3) * 4 + (idx & 3);
  const int rt = (xcd >> 1) * 8 + (idx >> 2), ct = (xcd & 1) * 4 + (idx & 3);
  int lane = threadIdx.x & 63, r32, h, g, q4, p4;
  STAMP_AT(0);
  asm volatile("" :: "s"(x), "s"(wqkv), "s"(wo), "s"(w1), "s"(b1), "s"(w2), "s"(b2), "s"(out), "s"(ws));

  const v4u sx = make_srd(x, SEQ * DM * 2), sws = make_srd(ws, WS_END);
  const __amdgpu_buffer_rsrc_t rws = __builtin_amdgcn_make_buffer_rsrc(ws, 0, WS_END, 0x00020000);
  const unsigned one2 = 0x3f803f80u;
  const int T = 4 * r + m;
  const int rot = ROT;
  auto flag_off = [&](int kind, int tile, int jj) { return WS_FLAG + ((kind * 64 + tile) * 16 + jj) * 128; };
  auto flag = [&](int kind, int tile, int jj) { return (GAS u64*)(ws + flag_off(kind, tile, jj)); };
  u64 tag;
  unsigned taglo, tag32;
  auto get_tag = [&]() {
    const v2u tg = *(const LAS v2u*)(size_t)(lds0 + L_CNT + 32);
    taglo = __builtin_amdgcn_readfirstlane(tg[0]); tag32 = __builtin_amdgcn_readfirstlane(tg[1]);
    tag = ((u64)tag32 << 32) | taglo;
  };
  v4u xres, bres, xaug, st;
  float mean, var, rstd, sd;
  v4u sel;
  unsigned xo[4], swz;
  auto lane_setup = [&]() {
    lane = threadIdx.x & 63; PIN(lane);
    r32 = lane & 31; h = lane >> 5; g = lane >> 4; q4 = (lane >> 2) & 3; p4 = lane & 3;
    const unsigned selv = ((lane & 15) == (g & 1)) ? one2 : 0u;
    sel = (v4u){selv, selv, selv, selv};
#pragma unroll
    for (int p = 0; p < 4; ++p) xo[p] = (32 * m + r32) * 128 + (((2 * ((p + m) & 3) + h) ^ ((r32 >> 1) & 7)) * 16);
    swz = ((((lane >> 2) & 1) ^ (g & 1)) * 64) + p4 * 16;
  };

  auto stat_load = [&]() { st = ld16_sc1_async(sws, WS_STATS + (128 * r + 32 * m + r32) * 16, 0); };
  auto res_loads = [&]() {
    int t2 = threadIdx.x; PIN(t2);
    const int row = 64 * rt + (t2 >> 3), col = 64 * ct + (t2 & 7) * 8;
    xres = ld16_async(sx, row * 1024 + col * 2, 0);
    bres = ld16_async(make_srd(b2, DM * 2), col * 2, 0);
  };
  f32x4 lo, hi;
  auto res_sum = [&]() {
    PIN(xres); PIN(bres);
    lo = (f32x4){bflo(xres[0]) + bflo(bres[0]), bfhi(xres[0]) + bfhi(bres[0]), bflo(xres[1]) + bflo(bres[1]), bfhi(xres[1]) + bfhi(bres[1])};
    hi = (f32x4){bflo(xres[2]) + bflo(bres[2]), bfhi(xres[2]) + bfhi(bres[2]), bflo(xres[3]) + bflo(bres[3]), bfhi(xres[3]) + bfhi(bres[3])};
    PIN(lo); PIN(hi);
  };
  auto row_stats = [&]() {
    if (!__all(st[1] == tag32 && st[3] == tag32)) {
#pragma unroll 1
      for (unsigned spin = 0; spin < SPIN_CAP; ++spin) {
        st = ld16_sc1_async(sws, WS_STATS + (128 * r + 32 * m + r32) * 16, 0);
        wait_vm1<0>(st);
        if (__all(st[1] == tag32 && st[3] == tag32)) break;
      }
    }
    mean = asf(st[0]); var = asf(st[2]);
    PIN(mean); PIN(var);
  };
  auto make_xaug = [&]() {
    PIN(mean); PIN(var);
    rstd = __builtin_amdgcn_rsqf(var); sd = var * rstd;
    const unsigned uh = pk2(-mean, sd);
    const unsigned ul = pk2(-mean - bflo(uh), FMA1(bfhi(uh), -1.f, sd));
    xaug = (v4u){(uh & 0xffffu) | (uh << 16), (ul & 0xffffu) | (uh & 0xffff0000u), ul >> 16, 0u};
    if (h) xaug = (v4u){0u, 0u, 0u, 0u};
    PIN(xaug);
  };
  auto cswr = [&](int tile, f32x4 c) {
    if (lane < 16) { LAS float* p = (LAS float*)(size_t)(lds0 + L_CS + (tile * 32 + lane) * 16 + m * 4); p[0] = c[0]; p[64] = c[1]; }
  };
  auto csrd = [&](int tile) { return __builtin_bit_cast(f32x4, lds128(lds0 + L_CS + (tile * 32 + r32) * 16)); };
  auto waug = [&](f32x4 c4, unsigned b16) {
    const float c = ((c4[0] + c4[1]) + c4[2]) + c4[3];
    const unsigned uh = pk2(c, 0.f);
    const unsigned ul = pk2(c - bflo(uh), 0.f);
    return (v4u){uh | (ul << 16), uh | (b16 << 16), b16, 0u};
  };

  const v4u sw1 = make_srd(w1, DM * FF * 2);
  const int hcol0 = 448 * (j >> 2) + 128 * (j & 3);
  const bool heavy = (j & 3) != 3;
  unsigned bias0, bias1;
  auto bias_load = [&](int P) {
    bias0 = ld2_async(make_srd(b1, FF * 2), r32 * 2, (hcol0 + 64 * P) * 2);
    bias1 = ld2_async(make_srd(b1, FF * 2), r32 * 2, (hcol0 + 64 * P + 32) * 2);
  };
  auto w1dma = [&](int P, int c, unsigned dst, int half) {
    const unsigned vw1 = (lane >> 3) * (FF * 2) + swz, vw1b = vw1 + 8 * (FF * 2) - 1024;
    const unsigned so = (64 * ((c + rot) & 7) + 16 * m) * (FF * 2) + (hcol0 + 64 * P) * 2;
    if (half == 0) dma16_a(sw1, vw1, so, dst + m * 2048); else dma16_b(sw1, vw1b, so);
  };
  f32x16 hac[2];
  unsigned rk[4][2];
  auto hsetup = [&](int P) {
#pragma unroll
    for (int p = 0; p < 4; ++p)
#pragma unroll
      for (int c = 0; c < 2; ++c) {
        rk[p][c] = lds0 + (P ? L_WA : w1buf(0)) + ((p + m) & 3) * 2048 + h * 1024 + q4 * 128 + ((c ^ (q4 >> 1)) * 64) + (g & 1) * 32 + p4 * 8;
        PIN(rk[p][c]);
      }
  };
  auto hmm = [&](auto pc, const v4u (&xf)[32], auto&& hook) {
    constexpr int P = pc.value;
    constexpr int D = 4;
    const unsigned cnt = lds0 + L_CNT;
    f32x4 cs[2] = {}, c0, c1;
    v4u wf[D][2], au0, au1;
    unsigned seen = 0;
    hac[0] = (f32x16){}; hac[1] = (f32x16){};
    auto rd = [&](int ks, int t) { wf[ks % D][t] = ldtr2(rk[ks & 3][t] + (ks >> 2) * 8192, 512); };
#pragma unroll
    for (int ks = 0; ks < D; ++ks) { rd(ks, 0); rd(ks, 1); }
    SBAR();
    static_for<0, 32>([&](auto kc) {
      constexpr int ks = kc.value;
      hook(kc);
      if constexpr (ks == 10) seen = peek(cnt + 20);
      if constexpr (ks == 29) seen = peek(cnt);
#pragma unroll
      for (int t = 0; t < 2; ++t) {
        hac[t] = MFMA32(wf[ks % D][t], xf[ks], hac[t]);
        if ((ks & 3) == 0 && ks < 28) { cs[t] = MFMA16(sel, wf[ks % D][t], cs[t]); PIN(cs[t]); }
        if (ks == 26) { cs[t] = MFMA16(sel, wf[28 % D][t], cs[t]); PIN(cs[t]); }
        SBAR();
        if constexpr (ks + D < 32) rd(ks + D, t);
        if constexpr (ks == 31) { if (t == 0) { au0 = waug(c0, bias0); PIN(au0); } else { au1 = waug(c1, bias1); PIN(au1); } }
        SBAR();
      }
      if constexpr (ks == 11 || ks == 30) {
        PIN(hac[0]); PIN(hac[1]);
        if (__builtin_amdgcn_readfirstlane(seen) < 4) await(ks == 11 ? cnt + 20 : cnt, 4);
        asm volatile("" ::: "memory");
        if constexpr (ks == 30) { c0 = csrd(2 * P); c1 = csrd(2 * P + 1); }
      }
      if constexpr (ks == 27) { cswr(2 * P, cs[0]); cswr(2 * P + 1, cs[1]); arrive_nowait(cnt); }
      SBAR();
    });
    hac[0] = MFMA32(au0, xaug, hac[0]);
    hac[1] = MFMA32(au1, xaug, hac[1]);
    SBAR();
  };
  auto hgelu = [&](auto pc, auto&& mid) {
    constexpr int P = pc.value;
    constexpr bool PK = GELU_PK(P);
    const float C0 = 2.3022081986f, C1 = C0 * 0.044715f;
    const float ga = -C1 * rstd * rstd * rstd, gb = -C0 * rstd;
    auto mul = [&](f32x2 a, f32x2 b) { return PK ? a * b : (f32x2){MUL1(a[0], b[0]), MUL1(a[1], b[1])}; };
    auto fma = [&](f32x2 a, float b, float c) {
      return PK ? __builtin_elementwise_fma(a, (f32x2){b, b}, (f32x2){c, c}) : (f32x2){FMA1(a[0], b, c), FMA1(a[1], b, c)}; };
#pragma unroll
    for (int t = 0; t < 2; ++t) {
      f32x2 v[8], z[8];
#pragma unroll
      for (int e = 0; e < 8; ++e) { v[e] = (f32x2){hac[t][2 * e], hac[t][2 * e + 1]}; z[e] = mul(v[e], v[e]); }
      SBAR();
#pragma unroll
      for (int e = 0; e < 8; ++e) z[e] = fma(z[e], ga, gb);
      SBAR();
#pragma unroll
      for (int e = 0; e < 8; ++e) z[e] = mul(z[e], v[e]);
      SBAR();
#pragma unroll
      for (int e = 0; e < 8; ++e) z[e] = (f32x2){EXP2(z[e][0]), EXP2(z[e][1])};
      SBAR();
#pragma unroll
      for (int e = 0; e < 8; ++e) z[e] = fma(z[e], sd, sd);
      SBAR();
#pragma unroll
      for (int e = 0; e < 8; ++e) z[e] = (f32x2){__builtin_amdgcn_rcpf(z[e][0]), __builtin_amdgcn_rcpf(z[e][1])};
      SBAR();
#pragma unroll
      for (int e = 0; e < 8; ++e) z[e] = mul(z[e], v[e]);
      SBAR();
#pragma unroll
      for (int s = 0; s < 2; ++s) {
        const v4u f = {pk2(z[4 * s][0], z[4 * s][1]), pk2(z[4 * s + 1][0], z[4 * s + 1][1]), pk2(z[4 * s + 2][0], z[4 * s + 2][1]), pk2(z[4 * s + 3][0], z[4 * s + 3][1])};
        st16_sc1(f, sws, lane * 16, WS_AF + (T * AFT + (hcol0 >> 4) + 4 * P + 2 * t + s) * 1024);
      }
      if (t == 0) mid();
      SBAR();
    }
  };

  if (isG) {
    lane_setup();
    const v4u sw2 = make_srd(w2, FF * DM * 2), swo = make_srd(wo, DM * DM * 2);
    const unsigned vw2 = (lane >> 3) * 1024 + swz, vw2b = vw2 + 7 * 1024;
    const int ro3 = rt % 3;
    auto kmap = [&](int kk) {
      if (kk < 16) return 8 * (((kk >> 2) + rt) & 3) + (kk & 3);
      const int p = ((kk - 16) >> 2) + ro3;
      return 8 * (p >= 3 ? p - 3 : p) + 4 + (kk & 3);
    };
    auto omap = [&](int i) { return 2 * ((i + rt) & 3) + (i >> 2); };
    auto wdmah = [&](int kk, int half) {
      const unsigned so = (kk < 28 ? 448 * m + 16 * kmap(kk) : 128 * m + 16 * omap(kk - 28)) * 1024 + 128 * ct;
      if (half == 0) dma16_a(kk < 28 ? sw2 : swo, vw2, so, lds0 + L_WG + m * PART + (kk % RD) * 2048); else dma16_b(kk < 28 ? sw2 : swo, vw2b, so);
    };
    auto wdma = [&](int kk) { wdmah(kk, 0); wdmah(kk, 1); };
    {
      const v4u swq = make_srd(wqkv, DM * 3 * DM * 2);
      const unsigned vx0 = (lane >> 3) * 1024 + (((lane & 7) ^ g) * 16), vx1 = (7 + (lane >> 3)) * 1024 + (((lane & 7) ^ (4 + g)) * 16);
      const unsigned vwq = (lane >> 2) * (3 * DM * 2) + p4 * 16;
      auto piece = [&](int s, int i) {
        const int kc = (s + rot) & 7;
        if (i < 4) {
          const unsigned so = (128 * r + 32 * m + 16 * (i >> 1)) * 1024 + kc * 128;
          if (i & 1) dma16_b(sx, vx1, so); else dma16_a(sx, vx0, so, lds0 + xbuf(s) + m * 4096 + (i >> 1) * 2048);
        } else if (i < 7) dma16(swq, vwq, (64 * kc + 16 * m) * (3 * DM * 2) + (512 * (i - 4) + 32 * j) * 2, lds0 + qbuf(s) + ((i - 4) * 4 + m) * 1024);
        else w1dma(0, s, lds0 + w1buf(s), i - 7);
      };
#pragma unroll
      for (int i = 0; i < 18; ++i) { piece(i / 9, i % 9); if (i == 8) __builtin_amdgcn_s_barrier(); }
      __builtin_amdgcn_s_setprio(PRIO_G1);
      hsetup(0);
      f32x4 cs[2] = {};
      v4u xf[4], wf[4][2];
      hac[0] = (f32x16){}; hac[1] = (f32x16){};
      static_for<0, 8>([&](auto sc) {
        constexpr int s = sc.value;
        auto fill = [&](int i) {
          if constexpr (s < 6) piece(s + 2, i);
          if constexpr (s == 6) {
            if (i == 0) { stat_load(); bias_load(0); }
            else if (i < 5) { if (heavy) w1dma(1, (i - 1) >> 1, lds0 + L_WA + ((i - 1) >> 1) * 8192, (i - 1) & 1); }
            else wdmah((i - 5) >> 1, (i - 5) & 1);
          }
          if constexpr (s == 7) {
            if (heavy && i < 4) w1dma(1, 2 + (i >> 1), lds0 + L_WA + (2 + (i >> 1)) * 8192, i & 1);
            if (heavy && i >= 4) { w1dma(1, i, lds0 + L_WA + i * 8192, 0); w1dma(1, i, 0, 1); }
          }
          SBAR();
        };
        auto rd = [&](int p) {
          xf[p] = lds128(xbuf(s) + xo[p]);
          wf[p][0] = ldtr2(rk[p][0] + (s % 3) * PART, 512); wf[p][1] = ldtr2(rk[p][1] + (s % 3) * PART, 512);
          SBAR();
        };
        if constexpr (s < 7) wait_vm<9>(); else { if (heavy) wait_vm<11>(); else wait_vm<7>(); }
        bar();
        STAMP_AT(10 + s);
        rd(0);
        if constexpr (s < 7) fill(0);
        rd(1);
        if constexpr (s < 7) fill(1);
        if constexpr (s == 2) get_tag();
#pragma unroll
        for (int p = 0; p < 4; ++p) {
#pragma unroll
          for (int t = 0; t < 2; ++t) {
            hac[t] = MFMA32(wf[p][t], xf[p], hac[t]);
            if (p == 0) { cs[t] = MFMA16(sel, wf[p][t], cs[t]); PIN(cs[t]); }
            SBAR();
            if (p < 2 && t == 0) rd(p + 2);
            if (s == 7) fill(2 * p + t); else if (2 * p + t < 7) fill(2 + 2 * p + t);
          }
          if (s == 7 && p == 1) {
            cswr(0, cs[0]); cswr(1, cs[1]);
            bar();
            STAMP_AT(1);
          }
        }
        if constexpr (s == 7) {
          const f32x4 c0 = csrd(0), c1 = csrd(1);
          if (heavy) wait_vm<20>(); else wait_vm<4>();
          PIN(st); PIN(bias0); PIN(bias1);
          row_stats(); make_xaug();
          hac[0] = MFMA32(waug(c0, bias0), xaug, hac[0]);
          hac[1] = MFMA32(waug(c1, bias1), xaug, hac[1]);
          SBAR();
        }
      });
    }
    STAMP_AT(18);
    if (heavy) { wait_vm<8>(); arrive_nowait(lds0 + L_CNT + 16); }
    hgelu(std::integral_constant<int, 0>{}, [&]() {
      wait_vm<2>();
      if (heavy) arrive_nowait(lds0 + L_CNT + 20);
    });
    __builtin_amdgcn_s_setprio(0);
    STAMP_AT(19);
    unsigned rb2[2];
#pragma unroll
    for (int c = 0; c < 2; ++c) rb2[c] = lds0 + L_WG + m * PART + h * 512 + q4 * 128 + ((c ^ (q4 >> 1)) * 64) + (g & 1) * 32 + p4 * 8;
    v4u wq[36][2];
    auto wrd = [&](int kk) {
#pragma unroll
      for (int c = 0; c < 2; ++c) wq[kk][c] = ldtr2(rb2[c] + (kk % RD) * 2048, 1024);
    };
    auto wgot = [&](int kk) { PIN(wq[kk][0]); PIN(wq[kk][1]); };
    const int ftile = 2 * rt + ((lane >> 2) & 1);
    v4u af[28][2], of[8][2];
    v2u fl;
    const unsigned a0 = WS_AF + (2 * rt * AFT + 28 * m) * 1024, o0 = WS_AF + (2 * rt * AFT + 112 + 8 * m) * 1024;
    for (int kk = 2; kk < 6; ++kk) wdma(kk);
    wait_vm<8>();
    if (lane == 0) __hip_atomic_store(flag(FL_H0, T, j), tag, RLX_AGENT);
    STAMP_AT(2);
    wdma(6); wdma(7);
    static_for<0, 3>([&](auto ic) {
      constexpr int i = ic.value;
      wait_vm<(i == 0) ? 9 : 8>();
      for (int kk = 4 * i; kk < 4 * i + 4; ++kk) wrd(kk);
      for (int kk = 4 * i; kk < 4 * i + 4; ++kk) wgot(kk);
      if constexpr (i == 2) fl = ld8_sc1_async(sws, flag_off(FL_H0, ftile, 4 * m + p4));
      for (int kk = 4 * i; kk < 4 * i + 4; ++kk) wdma(kk + RD);
    });
    STAMP_AT(3);
    wait_vmf<8>(fl);
    if (!__all(fl[0] == taglo && fl[1] == tag32)) poll_flags(flag(FL_H0, ftile, 4 * m + p4), tag);
    STAMP_AT(4);
#pragma unroll
    for (int kk = 0; kk < AW; ++kk) {
      af[kk][0] = ld16_sc1_async(sws, lane * 16, a0 + kmap(kk) * 1024);
      af[kk][1] = ld16_sc1_async(sws, lane * 16, a0 + (AFT + kmap(kk)) * 1024);
    }
    __builtin_amdgcn_s_setprio(PRIO_G2);
    f32x16 c2[2][2] = {};
    auto mm2 = [&](int kk, const v4u (&a)[2], auto&& fill) {
      SBAR();
#pragma unroll
      for (int i = 0; i < 4; ++i) { c2[i >> 1][i & 1] = MFMA32(wq[kk][i & 1], a[i >> 1], c2[i >> 1][i & 1]); SBAR(); fill(i); SBAR(); }
    };
    static_for<0, 28>([&](auto kc) {
      constexpr int kk = kc.value;
      wait_vm2<(kk < AW) ? 2 * (AW - 1 - kk) + g2_range(0, kk) : g2_range(kk - AW + 1, kk)>(af[kk][0], af[kk][1]);
      if constexpr (kk == H1_CK) {
        STAMP_AT(20);
        wait_vmf<g2_range(H1_LD, H1_CK) - 1>(fl);
        if (!__all(fl[0] == taglo && fl[1] == tag32)) poll_flags(flag(FL_H1, ftile, 4 * m + p4), tag);
        STAMP_AT(21);
      }
      if constexpr (kk == H1_LD) fl = ld8_sc1_async(sws, flag_off(FL_H1, ftile, 4 * m + p4));
      if constexpr (kk == O_LD) fl = ld8_sc1_async(sws, flag_off(FL_O0, ftile, 4 * m + p4));
      constexpr bool dw = kk >= 1 && kk - 1 + NR + RD < 36;
      if constexpr (dw) wgot(kk - 1 + NR);
      mm2(kk, af[kk], [&](int i) {
        if constexpr (dw) { if (i < 2) wdmah(kk - 1 + NR + RD, i); }
        if constexpr (kk + NR < 28) { if (i == 1) wrd(kk + NR); }
        if constexpr (kk == 27) { if (i == 0) wrd(28); }
        if constexpr (kk + AW < 28) { if (i >= 2) af[kk + AW][i - 2] = ld16_sc1_async(sws, lane * 16, a0 + (AFT * (i - 2) + kmap(kk + AW)) * 1024); }
      });
      if constexpr (kk == O_CK) {
        wait_vmf<g2_range(O_LD, O_CK) - 1>(fl);
        if (!__all(fl[0] == taglo && fl[1] == tag32)) poll_flags(flag(FL_O0, ftile, 4 * m + p4), tag);
        res_loads();
#pragma unroll
        for (int i = 0; i < 4; ++i) {
          of[i][0] = ld16_sc1_async(sws, lane * 16, o0 + omap(i) * 1024);
          of[i][1] = ld16_sc1_async(sws, lane * 16, o0 + (AFT + omap(i)) * 1024);
        }
      }
    });
    static_for<0, 4>([&](auto kc) {
      constexpr int kk = kc.value;
      if (kk < 3) wrd(28 + kk + 1);
      if constexpr (kk == 1) {
        res_sum();
        STAMP_AT(5);
        poll_flags(flag(FL_O1, ftile, 4 * m + p4), tag);
        STAMP_AT(6);
#pragma unroll
        for (int i = 0; i < 8; ++i) of[4 + (i & 3)][i >> 2] = ld16_sc1_async(sws, lane * 16, o0 + (AFT * (i >> 2) + omap(4 + (i & 3))) * 1024);
      }
      wait_vm2<(kk == 0) ? 6 : 14 - 2 * kk>(of[kk][0], of[kk][1]);
      mm2(28 + kk, of[kk], [&](int i) { PIN(c2[i >> 1][i & 1]); if (kk == 3) wrd(32 + i); });
    });
    for (int kk = 32; kk < 36; ++kk) wgot(kk);
    unsigned pa = lds0 + L_WG + m * PART + lane * 16;
    PIN(pa);
    auto put = [&](int qn, int qd) {
      const f32x16& c = c2[qn >> 1][qn & 1];
      sts128(pa + (qn * 4 + qd) * 66 * 16, __builtin_bit_cast(v4u, (f32x4){c[4 * qd], c[4 * qd + 1], c[4 * qd + 2], c[4 * qd + 3]}));
    };
    static_for<0, 4>([&](auto qc) {
      constexpr int qn = qc.value;
      if constexpr ((qn & 1) == 0) { wait_vm2<qn ? 0 : 4>(of[4][qn >> 1], of[5][qn >> 1]); PIN(of[6][qn >> 1]); PIN(of[7][qn >> 1]); }
      SBAR();
#pragma unroll
      for (int i = 0; i < 4; ++i) {
        c2[qn >> 1][qn & 1] = MFMA32(wq[32 + i][qn & 1], of[4 + i][qn >> 1], c2[qn >> 1][qn & 1]);
        SBAR();
        if (qn > 0 && i >= 2) { put(qn - 1, 2 * i - 4); put(qn - 1, 2 * i - 3); }
        SBAR();
      }
    });
#pragma unroll
    for (int qd = 0; qd < 4; ++qd) put(3, qd);
    STAMP_AT(7);
  } else {
    __builtin_amdgcn_s_setprio(1);
    lane_setup();
    v4u xr[2];
    xr[0] = ld16_async(sx, lane * 16, (128 * r + 8 * j + 2 * m) * 1024); xr[1] = ld16_async(sx, lane * 16, (128 * r + 8 * j + 2 * m + 1) * 1024);
    unsigned warm;
    {
      u64 pc; asm volatile("s_getpc_b64 %0" : "=s"(pc));
      warm = ld2_async(make_srd((const void*)(pc & ~127ull), CODEWARM), lane < 3 ? lane * 128 : 0x40000000u, (idx * 4 + m) * 384);
    }
    __builtin_amdgcn_s_barrier();
    v2u ep = ld8_sc1_async(sws, m == 0 ? WS_EPOCH : 0x40000000u);
    float sval;
    const int wi = blockIdx.x * 4 + m;
    const u64 wbase = wi < 448 ? (u64)w1 + wi * 4096 : wi < 896 ? (u64)w2 + (wi - 448) * 4096 : (u64)wo + (wi - 896) * 4096;
    auto make_stats = [&]() {
      wait_vm2<1>(xr[0], xr[1]);
      PIN(warm);
      float piv[2], a[4];
#pragma unroll
      for (int u = 0; u < 2; ++u) {
        piv[u] = rdl(bflo(xr[u][0]), 0);
        float s1 = 0.f, s2 = 0.f;
#pragma unroll
        for (int i = 0; i < 8; ++i) { const float d = ((i & 1) ? bfhi(xr[u][i >> 1]) : bflo(xr[u][i >> 1])) - piv[u]; s1 += d; s2 = __builtin_fmaf(d, d, s2); }
        a[2 * u] = s1; a[2 * u + 1] = s2;
      }
#pragma unroll
      for (int i = 0; i < 4; ++i) a[i] += dpp<0xB1>(a[i]);
#pragma unroll
      for (int i = 0; i < 4; ++i) a[i] += dpp<0x4E>(a[i]);
      float t = p4 == 0 ? a[0] : p4 == 1 ? a[1] : p4 == 2 ? a[2] : a[3];
      t += dpp<0x114>(t); t += dpp<0x118>(t);
      const auto s16 = __builtin_amdgcn_permlane16_swap(asu(t), asu(t), false, false);
      t = asf(s16[0]) + asf(s16[1]);
      const auto s32 = __builtin_amdgcn_permlane32_swap(asu(t), asu(t), false, false);
      t = (asf(s32[0]) + asf(s32[1])) * (1.f / DM);
      const float e1 = dpp<0xA0>(t);
      sval = (p4 & 1) ? __builtin_fmaxf(__builtin_fmaf(-e1, e1, t), 0.f) + 1e-5f : t + (p4 ? piv[1] : piv[0]);
      PIN(sval);
    };
    const unsigned acnt = lds0 + L_CNT;
    const unsigned vpos = 2048 + (g & 1) * 512 + h * 256 + (lane & 15) * 16;
    v4u qf[2], am0, am1;
    u64 um[16];
    auto masks = [&]() {
      const int dq = r32 - 4 * h;
#pragma unroll
      for (int d = 0; d < 4; ++d) {
        const int row = 2 * (d & 1) + 8 * (d >> 1);
        am0[d] = row > dq ? 0xffffffffu : row == dq ? 0xffff0000u : 0u;
        am1[d] = row + 16 > dq ? 0xffffffffu : row + 16 == dq ? 0xffff0000u : 0u;
      }
      PIN(am0); PIN(am1);
      static_for<0, 16>([&](auto u) { um[u.value] = upper_mask(u.value); pin_s(um[u.value]); });
    };
    {
      v4u xf[32];
      f32x16 acc[3] = {};
      f32x4 cs[3] = {};
      v4u wf[4][3];
      unsigned wb[4];
#pragma unroll
      for (int p = 0; p < 4; ++p) { wb[p] = qbuf(0) + ((p + m) & 3) * 1024 + (8 * h + q4) * 64 + (g & 1) * 32 + p4 * 8; PIN(wb[p]); }
      auto rd = [&](int ks, int t) {
        if (t == 0) xf[ks] = lds128(xbuf(ks >> 2) + xo[ks & 3]);
        wf[ks & 3][t] = ldtr2(wb[ks & 3] + ((ks >> 2) % 3) * 12288 + t * 4096, 256);
      };
      auto mm = [&](int ks, int t) {
        acc[t] = t == 2 ? MFMA32(xf[ks], wf[ks & 3][t], acc[t]) : MFMA32(wf[ks & 3][t], xf[ks], acc[t]);
        if ((ks & 3) == 0) { cs[t] = MFMA16(sel, wf[ks & 3][t], cs[t]); PIN(cs[t]); }
        SBAR();
      };
      make_stats();
      bar();
      STAMP_AT(10);
#pragma unroll
      for (int t = 0; t < 6; ++t) rd(t / 3, t % 3);
      SBAR();
      static_for<0, 8>([&](auto sc) {
        constexpr int s = sc.value;
#pragma unroll
        for (int p = 0; p < 2; ++p) {
#pragma unroll
          for (int t = 0; t < 3; ++t) { mm(4 * s + p, t); rd(4 * s + p + 2, t); SBAR(); }
          if (s == 7 && p == 0) { cswr(4, cs[0]); cswr(5, cs[1]); cswr(6, cs[2]); SBAR(); }
        }
        if constexpr (s == 0) {
          wait_vmf<0>(ep);
          if (m == 0 && lane < 10)
            *(LAS unsigned*)(size_t)(lds0 + L_CNT + lane * 4) = lane == 8 ? ep[0] * 0x9E3779B1u + 0x7F4A7C15u : lane == 9 ? ep[1] * 0x85EBCA77u + 0x165667B1u : 0u;
        }
        if constexpr (s == 1) {
          get_tag();
          if ((lane >> 2) == 3) {
            const v2u gr = {asu(sval), tag32};
            __builtin_amdgcn_raw_buffer_store_b64(gr, rws, WS_STATS + (128 * r + 8 * j + 2 * m) * 16 + p4 * 8, 0, 16);
          }
        }
        if constexpr (s == MIDWARM) {
          warm = ld2_async(make_srd((const void*)wbase, 4096), lane * 128, 0);
        }
        if constexpr (s == 5) stat_load();
        if constexpr (s == 6) {
          wait_vm1<0>(st);
          PIN(warm);

          row_stats();
        }
        mm(4 * s + 2, 0); mm(4 * s + 2, 1);
        bar();
        if constexpr (s < 7) {
          STAMP_AT(11 + s);
          mm(4 * s + 2, 2);
          rd(4 * s + 4, 0); rd(4 * s + 4, 1); rd(4 * s + 4, 2); SBAR();
#pragma unroll
          for (int t = 0; t < 3; ++t) { mm(4 * s + 3, t); rd(4 * s + 5, t); SBAR(); }
        }
      });
      STAMP_AT(1);
      __builtin_amdgcn_s_setprio(3);
      const f32x4 c5 = csrd(5), c6 = csrd(6), c4 = csrd(4);
      SBAR();
      mm(30, 2); mm(31, 1);
      make_xaug();
      SBAR();
      mm(31, 2); mm(31, 0);
      if (h == 0) *(LAS float*)(size_t)(lds0 + L_RS + (m * 32 + r32) * 4) = rstd;
      acc[1] = MFMA32(waug(c5, 0u), xaug, acc[1]);
      acc[2] = MFMA32(xaug, waug(c6, 0u), acc[2]);
      acc[0] = MFMA32(waug(c4, 0u), xaug, acc[0]);
      SBAR();
      f32x4 rs[4];
#pragma unroll
      for (int qd = 0; qd < 4; ++qd) rs[qd] = __builtin_bit_cast(f32x4, lds128(lds0 + L_RS + (m * 32 + 8 * qd + 4 * h) * 4));
      const unsigned kvo = WS_KV + (j * 64 + T) * 4096;
#pragma unroll
      for (int e = 0; e < 16; ++e) acc[1][e] = MUL1(acc[1][e], rstd);
#pragma unroll
      for (int e = 0; e < 2; ++e) {
        const v4u f = pack8(acc[1], e);
        st16_sc1(f, sws, lane * 16, kvo + e * 1024);
        sts128(lds0 + L_KV + (8 + m) * KVT + e * 1024 + lane * 16, f);
      }
#pragma unroll
      for (int qd = 0; qd < 4; ++qd)
#pragma unroll
        for (int e = 0; e < 4; ++e) acc[2][4 * qd + e] = MUL1(acc[2][4 * qd + e], rs[qd][e]);
#pragma unroll
      for (int s = 0; s < 2; ++s) {
        const v4u f = pack8(acc[2], s);
        st16_sc1(f, sws, lane * 16, kvo + 2048 + s * 1024);
        sts128(lds0 + L_KV + (8 + m) * KVT + s * VB + vpos, f);
      }
      SBAR();
      const float qs = rstd * (0.25f * 1.4426950408889634f);
#pragma unroll
      for (int e = 0; e < 16; ++e) acc[0][e] = MUL1(acc[0][e], qs);
      qf[0] = pack8(acc[0], 0); qf[1] = pack8(acc[0], 1);
      PIN(qf[0]); PIN(qf[1]);
      STAMP_AT(2);
      const int T0 = 4 * r - 8 + 2 * m;
      const bool live = T0 >= 0;
      auto kv_load = [&](v4u (&kv)[8], int i0, int i1) {
        const unsigned src = WS_KV + (j * 64 + (live ? T0 : 0)) * 4096, vo = live ? lane * 16 : h * 512;
#pragma unroll
        for (int i = i0; i < i1; ++i) kv[i] = ld16_sc1_async(sws, vo, src + (live ? i : (i & 3)) * 1024);
      };
      auto kv_stage = [&](v4u (&kv)[8]) {
        const unsigned on = (lane < 2 || live) ? one2 : 0u;
        if (lane < 6) sts128(lds0 + L_KV + (lane < 2 ? 8 + m : 2 * m + ((lane - 2) >> 1)) * KVT + 2048 + (lane & 1) * VB + 1024, (v4u){on, on, on, on});
        const v4u zz = {};
#pragma unroll
        for (int i = 0; i < 2; ++i) {
          sts128(lds0 + L_KV + (2 * m + i) * KVT + lane * 16, kv[4 * i]);
          sts128(lds0 + L_KV + (2 * m + i) * KVT + 1024 + lane * 16, kv[4 * i + 1]);
          sts128(lds0 + L_KV + (2 * m + i) * KVT + vpos, live ? kv[4 * i + 2] : zz);
          sts128(lds0 + L_KV + (2 * m + i) * KVT + VB + vpos, live ? kv[4 * i + 3] : zz);
        }
        arrive_nowait(acnt);
      };
      auto kv_wait = [&](v4u (&kv)[8]) {
        wait_vm<0>();
#pragma unroll
        for (int i = 0; i < 8; ++i) PIN(kv[i]);
      };
      if (heavy) {
        hsetup(1);
        await(acnt + 16, 4);
        STAMP_AT(3);
        v2u fl;
        v4u kv[8];
        hmm(std::integral_constant<int, 1>{}, xf, [&](auto kc) {
          constexpr int ks = kc.value;
          if constexpr (ks == KVPUB) {
            wait_vm<0>();
            if (lane == 0) __hip_atomic_store(flag(FL_KV, T, j), tag, RLX_AGENT);
          }
          if constexpr (ks == KVPUB + 2) bias_load(1);
          if constexpr (ks == 31) { wait_vm<1>(); PIN(bias0); PIN(bias1); }
          if constexpr (ks == 24) fl = ld8_sc1_async(sws, flag_off(FL_KV, live ? T0 + (lane & 1) : 0, j));
        });
        STAMP_AT(18);
        wait_vmf<0>(fl);
        if (!__all(fl[0] == taglo && fl[1] == tag32)) poll_flags(flag(FL_KV, live ? T0 + (lane & 1) : 0, j), tag);
        kv_load(kv, 0, 8);
        hgelu(std::integral_constant<int, 1>{}, [&]() {});
        STAMP_AT(4);
        masks();
        wait_vm<4>();
#pragma unroll
        for (int i = 0; i < 8; ++i) PIN(kv[i]);
        kv_stage(kv);
      } else {
        wait_vm<0>();
        if (lane == 0) __hip_atomic_store(flag(FL_KV, T, j), tag, RLX_AGENT);
        v4u kv[8];
        poll_flags(flag(FL_KV, live ? T0 + (lane & 1) : 0, j), tag);
        kv_load(kv, 0, 8); masks(); kv_wait(kv); kv_stage(kv);
      }
    }
    STAMP_AT(19);
    const unsigned kb = lds0 + L_KV + m * KVT + lane * 16;
    unsigned vb[2];
#pragma unroll
    for (int e = 0; e < 2; ++e) { vb[e] = lds0 + L_KV + m * KVT + 2048 + ((g & 1) ? 1024 : e * 512 + h * 256 + (lane & 15) * 16); PIN(vb[e]); }
    await(acnt, heavy ? 8 : 4);
    __builtin_amdgcn_s_setprio(PRIO_ATT);
    STAMP_AT(5);
    const f32x16 zero = {};
    auto o_store = [&](f32x16& o, int e) {
      const float rl = __builtin_amdgcn_rcpf(o[8]);
#pragma unroll
      for (int u = 0; u < 8; ++u) o[u] = MUL1(o[u], rl);
      st16_sc1(pack8(o, 0), sws, lane * 16, WS_AF + (T * AFT + 112 + 2 * j + e) * 1024);
    };
    int redo = 0;
    {
      f32x16 o[2], sa, sb, su;
      bool pend = false;
      v4u kn, ku, va, vc, vn0, vn1, vu0, vu1, p0, p1, pu;
      auto kaddr = [&](int n) { return kb + ((n & 7) + 1) * KVT + (n >> 3) * 1024; };
      auto vaddr = [&](int n) { return vb[n >> 3] + ((n & 7) + 1) * KVT; };
      kn = lds128(kaddr(0)); vn0 = lds128(vaddr(0)); vn1 = lds128(vaddr(0) + VB);
      sa = MFMA32(kn, qf[0], zero);
      kn = lds128(kaddr(1));
      SBAR();
      auto step = [&](auto nc, f32x16& sc, f32x16& sn) {
        constexpr int n = nc.value, e = n >> 3, i = n & 7;
        if constexpr (n < 15) sn = MFMA32(kn, qf[(n + 1) >> 3], zero);
        if constexpr (n < 14) kn = lds128(kaddr(n + 2));
        if constexpr (i == 5) ku = lds128(kb + e * 1024);
        v4u v0 = vn0, v1 = vn1;
        if constexpr (n < 15) { vn0 = lds128(vaddr(n + 1)); vn1 = lds128(vaddr(n + 1) + VB); }
        if constexpr (i == 6) { vu0 = lds128(vb[e]); vu1 = lds128(vb[e] + VB); }
        SBAR();
        if constexpr (i == 7) {
#pragma unroll
          for (int u = 0; u < 16; ++u) sc[u] = __builtin_amdgcn_inverse_ballot_w64(um[u]) ? su[u] : sc[u];
          PIN(sc); SBAR();
        }
#pragma unroll
        for (int u = 0; u < 6; ++u) sc[u] = EXP2(sc[u]);
        PIN(sc); SBAR();
        if constexpr (n == 8) { o[0] = MFMA32(vc, p1, o[0]); SBAR(); }
        else if constexpr (n > 0) { o[e] = MFMA32(vc, p1, o[e]); SBAR(); }
        sc[6] = EXP2(sc[6]); sc[7] = EXP2(sc[7]);
        p0 = pack8(sc, 0);
        sc[8] = EXP2(sc[8]); sc[9] = EXP2(sc[9]);
        if constexpr (i == 7) { va = p0 & am0; p0 &= ~am0; PIN(va); }
        PIN(sc); PIN(p0); SBAR();
        if constexpr (n == 8) { o[0] = MFMA32(vu1, pu, o[0]); SBAR(); }
        o[e] = MFMA32(v0, p0, i == 0 ? zero : o[e]);
        SBAR();
        if constexpr (i == 6) { su = MFMA32(ku, qf[e], zero); SBAR(); }
#pragma unroll
        for (int u = 10; u < 13; ++u) sc[u] = EXP2(sc[u]);
        if constexpr (i == 7) { PIN(sc); SBAR(); o[e] = MFMA32(vu0, va, o[e]); SBAR(); }
#pragma unroll
        for (int u = 13; u < 16; ++u) sc[u] = EXP2(sc[u]);
        p1 = pack8(sc, 1); vc = v1;
        if constexpr (i == 7) { pu = p1 & am1; p1 &= ~am1; PIN(pu); }
        PIN(p1); SBAR();
        if constexpr (n == 8) {
          if (__all(o[0][8] >= 0x1p-30f && o[0][8] <= 0x1p30f)) o_store(o[0], 0); else redo = 1;
          SBAR();
        }
        if constexpr (i == 2) pend = !redo;
        if constexpr (i == 3 || i == 5 || i == 7) {
          if (pend && (i == 7 || (__builtin_amdgcn_s_getreg(7 | (3 << 11)) & 15) == 0)) {
            wait_vm<0>();
            if (lane == 0) __hip_atomic_store(flag(FL_H1 + e, T, j), tag, RLX_AGENT);
            pend = false;
          }
        }
      };
      static_for<0, 8>([&](auto hc) {
        step(std::integral_constant<int, 2 * hc.value>{}, sa, sb);
        step(std::integral_constant<int, 2 * hc.value + 1>{}, sb, sa);
      });
      o[1] = MFMA32(vc, p1, o[1]);
      o[1] = MFMA32(vu1, pu, o[1]);
      if (__all(o[1][8] >= 0x1p-30f && o[1][8] <= 0x1p30f)) o_store(o[1], 1); else redo |= 2;
    }
    if (redo) {
#pragma unroll 1
      for (int e = 0; e < 2; ++e) {
        if (!((redo >> e) & 1)) continue;
        const v4u q = e ? qf[1] : qf[0];
        const unsigned vbe = e ? vb[1] : vb[0];
        auto merged = [&](const f32x16& c) {
          f32x16 sl = MFMA32(lds128(kb + 8 * KVT + e * 1024), q, c);
          const f32x16 su = MFMA32(lds128(kb + e * 1024), q, c);
          static_for<0, 16>([&](auto u) { sl[u.value] = upper<u.value>() ? su[u.value] : sl[u.value]; });
          return sl;
        };
        float mx = -INFINITY;
#pragma unroll 1
        for (int i = 0; i < 8; ++i) {
          const f32x16 sc = i ? MFMA32(lds128(kb + i * KVT + e * 1024), q, zero) : merged(zero);
#pragma unroll
          for (int u = 0; u < 16; u += 2) mx = max3(mx, sc[u], sc[u + 1]);
        }
        const auto sw = __builtin_amdgcn_permlane32_swap(asu(mx), asu(mx), false, false);
        mx = __builtin_fmaxf(asf(sw[0]), asf(sw[1]));
        f32x16 negm, o;
#pragma unroll
        for (int u = 0; u < 16; ++u) negm[u] = -mx;
        {
          f32x16 sc = merged(negm);
#pragma unroll
          for (int u = 0; u < 16; ++u) sc[u] = EXP2(sc[u]);
          const v4u p0 = pack8(sc, 0), p1 = pack8(sc, 1);
          o = MFMA32(lds128(vbe), p0 & am0, zero);
          o = MFMA32(lds128(vbe + VB), p1 & am1, o);
          o = MFMA32(lds128(vbe + 8 * KVT), p0 & ~am0, o);
          o = MFMA32(lds128(vbe + 8 * KVT + VB), p1 & ~am1, o);
        }
#pragma unroll 1
        for (int i = 1; i < 8; ++i) {
          f32x16 sc = MFMA32(lds128(kb + i * KVT + e * 1024), q, negm);
#pragma unroll
          for (int u = 0; u < 16; ++u) sc[u] = EXP2(sc[u]);
          o = MFMA32(lds128(vbe + i * KVT), pack8(sc, 0), o);
          o = MFMA32(lds128(vbe + i * KVT + VB), pack8(sc, 1), o);
        }
        o_store(o, e);
      }
      wait_vm<0>();
      if (lane == 0) __hip_atomic_store(flag(FL_O0, T, j), tag, RLX_AGENT);
    }
    STAMP_AT(6);
    wait_vm<0>();
    if (lane == 0) __hip_atomic_store(flag(FL_O1, T, j), tag, RLX_AGENT);
    STAMP_AT(7);
    res_loads();
    wait_vm<0>();
    res_sum();
    if (blockIdx.x == 0 && wave == 7) {
#pragma unroll 1
      for (int i = 0; i < 4; ++i) poll_flags(flag(FL_KV, 4 * (4 * i + g), lane & 15), tag);
      if (lane == 0) __hip_atomic_store((GAS u64*)(ws + WS_EPOCH), tag, RLX_AGENT);
    }
  }
  {
    int t2 = threadIdx.x; PIN(t2);
    const int row = t2 >> 3, ch = t2 & 7;
    unsigned a = lds0 + L_WG + (((row >> 5) * 8 + ch) * 66 + (row & 31)) * 16, oo = (64 * rt + row) * 1024 + (64 * ct + ch * 8) * 2;
    PIN(a); PIN(oo);
    bar();
    f32x4 pl[4], ph[4];
#pragma unroll
    for (int w = 0; w < 4; ++w) { pl[w] = __builtin_bit_cast(f32x4, lds128(a + w * PART)); ph[w] = __builtin_bit_cast(f32x4, lds128(a + w * PART + 512)); }
    SBAR();
#pragma unroll
    for (int w = 0; w < 4; ++w) { lo += pl[w]; hi += ph[w]; }
    const v4u o = {pk2(lo[0], lo[1]), pk2(lo[2], lo[3]), pk2(hi[0], hi[1]), pk2(hi[2], hi[3])};
    __builtin_amdgcn_raw_buffer_store_b128(o, __builtin_amdgcn_make_buffer_rsrc(out, 0, SEQ * DM * 2, 0x00020000), oo, 0, 16);
  }
  STAMP_AT(8);
}

extern "C" void kernel_launch(void* const* d_in, const int* in_sizes, int n_in, void* d_out, int out_size, void* d_ws, size_t ws_size,
                              hipStream_t stream) {
  (void)in_sizes; (void)n_in; (void)out_size; (void)ws_size;
  (void)hipFuncSetAttribute((const void*)fused_block, hipFuncAttributeMaxDynamicSharedMemorySize, LDS_BYTES);
  fused_block<<<256, 512, LDS_BYTES, stream>>>(d_in[0], d_in[1], d_in[2], d_in[4], d_in[5], d_in[6], d_in[7], d_out, (char*)d_ws);
}
```

```cpp
#include <hip/hip_runtime.h>
#include <hip/hip_bf16.h>
#include <stdint.h>
#include <type_traits>

typedef unsigned v4u __attribute__((ext_vector_type(4)));
typedef unsigned v2u __attribute__((ext_vector_type(2)));
typedef float f32x2 __attribute__((ext_vector_type(2)));
typedef float f32x4 __attribute__((ext_vector_type(4)));
typedef float f32x16 __attribute__((ext_vector_type(16)));
typedef short bf16x8 __attribute__((ext_vector_type(8)));
typedef short s16x4 __attribute__((ext_vector_type(4)));
typedef __bf16 bf16x2_t __attribute__((ext_vector_type(2)));
typedef unsigned long long u64;
#define GAS __attribute__((address_space(1)))
#define LAS __attribute__((address_space(3)))
#define DI __device__ __forceinline__
#define RLX_AGENT __ATOMIC_RELAXED, __HIP_MEMORY_SCOPE_AGENT

constexpr int SEQ = 2048, DM = 512, FF = 1792;
constexpr unsigned WS_EPOCH = 0;
constexpr unsigned WS_STATS = 32768;
constexpr unsigned WS_FLAG = 65536;
constexpr unsigned WS_KV = WS_FLAG + 5 * 1024 * 128;
constexpr int AFT = 145;
constexpr unsigned WS_AF = WS_KV + 16 * 64 * 4096;
constexpr unsigned WS_END = WS_AF + 64 * AFT * 1024;
enum { FL_KV = 0, FL_H0 = 1, FL_H1 = 2, FL_O0 = 3, FL_O1 = 4 };
constexpr int L_WA = 0;
constexpr int L_WG = 86016;
constexpr int VB = 1040;
constexpr int KVT = 2048 + 2 * VB;
constexpr int L_KV = 65536 - 8 * KVT;
constexpr int RD = 8, NR = 12;
constexpr int PART = 16 * 66 * 16;
constexpr int L_CS = L_WG + 4 * PART;
constexpr int L_RS = L_CS + 3584;
constexpr int L_CNT = L_RS + 512;
constexpr int LDS_BYTES = L_CNT + 64;
constexpr int xbuf(int s) { return ((s + 1) % 3) * 16384; }
constexpr int qbuf(int s) { return 49152 + (s % 3) * 12288; }
constexpr int w1buf(int s) { return L_WG + (s % 3) * PART + 8192; }
static_assert(xbuf(7) == 32768 && qbuf(2) + 12288 <= L_WG && L_KV + 12 * KVT <= L_WG && PART >= RD * 2048 && LDS_BYTES <= 163840, "lds map");
constexpr int AW = 6;
constexpr unsigned SPIN_CAP = 1u << 23;

#define MFMA32(a, b, c) __builtin_amdgcn_mfma_f32_32x32x16_bf16(__builtin_bit_cast(bf16x8, a), __builtin_bit_cast(bf16x8, b), c, 0, 0, 0)
#define MFMA16(a, b, c) __builtin_amdgcn_mfma_f32_16x16x32_bf16(__builtin_bit_cast(bf16x8, a), __builtin_bit_cast(bf16x8, b), c, 0, 0, 0)
#define SBAR() __builtin_amdgcn_sched_barrier(0)

DI float asf(unsigned u) { return __builtin_bit_cast(float, u); }
DI unsigned asu(float f) { return __builtin_bit_cast(unsigned, f); }
DI unsigned pk2(float lo, float hi) { return __builtin_bit_cast(unsigned, __builtin_convertvector((f32x2){lo, hi}, bf16x2_t)); }
DI float bflo(unsigned u) { return __builtin_bit_cast(float, u << 16); }
DI float bfhi(unsigned u) { return __builtin_bit_cast(float, u & 0xffff0000u); }
DI v4u make_srd(const void* p, unsigned bytes) { const u64 a = (u64)p; return (v4u){(unsigned)a, (unsigned)(a >> 32) & 0xffffu, bytes, 0x00020000u}; }
DI void dma16(v4u srd, unsigned voff, unsigned soff, unsigned lds) {
  asm volatile("s_mov_b32 m0, %3\n\ts_nop 0\n\tbuffer_load_dwordx4 %0, %1, %2 offen lds" :: "v"(voff), "s"(srd), "s"(soff), "s"(lds) : "memory");
}
DI void dma16_a(v4u srd, unsigned voff, unsigned soff, unsigned lds) { dma16(srd, voff, soff, lds); }
DI void dma16_b(v4u srd, unsigned voff1, unsigned soff) {
  asm volatile("buffer_load_dwordx4 %0, %1, %2 offen offset:1024 lds" :: "v"(voff1), "s"(srd), "s"(soff) : "memory");
}
DI v4u ld16_sc1_async(v4u srd, unsigned voff, unsigned soff) {
  v4u r; asm volatile("buffer_load_dwordx4 %0, %1, %2, %3 offen sc1" : "=v"(r) : "v"(voff), "s"(srd), "s"(soff) : "memory"); return r;
}
DI v4u ld16_async(v4u srd, unsigned voff, unsigned soff) {
  v4u r; asm volatile("buffer_load_dwordx4 %0, %1, %2, %3 offen" : "=v"(r) : "v"(voff), "s"(srd), "s"(soff) : "memory"); return r;
}
DI unsigned ld2_async(v4u srd, unsigned voff, unsigned soff) {
  unsigned r; asm volatile("buffer_load_ushort %0, %1, %2, %3 offen" : "=v"(r) : "v"(voff), "s"(srd), "s"(soff) : "memory"); return r;
}
DI void st16_sc1(v4u v, v4u srd, unsigned voff, unsigned soff) {
  asm volatile("buffer_store_dwordx4 %0, %1, %2, %3 offen sc1\n\ts_nop 1" :: "v"(v), "v"(voff), "s"(srd), "s"(soff) : "memory");
}
template <int N> DI void wait_vm1(v4u& a) { asm volatile("s_waitcnt vmcnt(%1)" : "+v"(a) : "i"(N) : "memory"); }
template <int N> DI void wait_vm2(v4u& a, v4u& b) { asm volatile("s_waitcnt vmcnt(%2)" : "+v"(a), "+v"(b) : "i"(N) : "memory"); }
DI void pin_s(u64& v) { asm volatile("" : "+s"(v)); }
template <class V> DI void pin(V& v) { asm volatile("" : "+v"(v)); }
template <int N> DI void wait_vm() { asm volatile("s_waitcnt vmcnt(%0)" :: "i"(N) : "memory"); }
DI void bar() { asm volatile("s_waitcnt lgkmcnt(0)\n\ts_barrier" ::: "memory"); }
DI v4u lds128(unsigned a) { return *(const LAS v4u*)(size_t)a; }
DI void sts128(unsigned a, v4u v) { *(LAS v4u*)(size_t)a = v; }
DI v2u ldtr(unsigned a) { return __builtin_bit_cast(v2u, __builtin_amdgcn_ds_read_tr16_b64_v4i16((LAS s16x4*)(size_t)a)); }
DI v4u ldtr2(unsigned a, int second) { const v2u lo = ldtr(a), hi = ldtr(a + second); return (v4u){lo.x, lo.y, hi.x, hi.y}; }
template <int CTRL> DI float dpp(float v) { return __builtin_bit_cast(float, __builtin_amdgcn_update_dpp(0, __builtin_bit_cast(int, v), CTRL, 0xf, 0xf, true)); }
DI float rdl(float v, int l) { return __builtin_bit_cast(float, __builtin_amdgcn_readlane(__builtin_bit_cast(int, v), l)); }
DI v4u pack8(const f32x16& a, int s) { return (v4u){pk2(a[8 * s], a[8 * s + 1]), pk2(a[8 * s + 2], a[8 * s + 3]), pk2(a[8 * s + 4], a[8 * s + 5]), pk2(a[8 * s + 6], a[8 * s + 7])}; }
DI void poll_flags(const GAS u64* p, u64 tag) {
#pragma unroll 1
  for (unsigned spin = 0; spin < SPIN_CAP; ++spin) {
    const u64 v = __hip_atomic_load(p, RLX_AGENT);
    if (__all(v == tag)) break;
    __builtin_amdgcn_s_sleep(1);
  }
  asm volatile("" ::: "memory");
}
constexpr u64 upper_mask(int reg) {
  u64 m = 0;
  for (int l = 0; l < 64; ++l) { const int row = (reg & 3) + 8 * (reg >> 2) + 4 * (l >> 5); if (row > (l & 31)) m |= 1ull << l; }
  return m;
}
template <int REG> DI bool upper() { return __builtin_amdgcn_inverse_ballot_w64(upper_mask(REG)); }
template <int I, int N, class Fn> DI void static_for(Fn&& f) { if constexpr (I < N) { f(std::integral_constant<int, I>{}); static_for<I + 1, N>(f); } }
DI float max3(float a, float b, float c) { return __builtin_fmaxf(__builtin_fmaxf(a, b), c); }

constexpr int H1_CK = 16 - AW, H1_LD = H1_CK - 3, O_CK = 28 - AW, O_LD = O_CK - 3;
constexpr int g2_issued(int t) { return ((t >= 1 && t - 1 + NR + RD < 36) ? 2 : 0) + ((t + AW < 28) ? 2 : 0) + (t == H1_LD) + (t == O_LD) + (t == O_CK ? 10 : 0) + (t == 26); }
constexpr int g2_range(int a, int b) { int n = 0; for (int t = a; t < b; ++t) n += g2_issued(t); return n; }
constexpr unsigned CODE_BYTES = 38000;
constexpr int KVPUB = 6;
#define EXP2(v) __builtin_amdgcn_exp2f(v)
#define PIN(v) pin(v)
extern "C" __device__ float mul_unpacked(float, float) __asm("llvm.amdgcn.fmul.legacy");
#define MUL1(a, b) mul_unpacked(a, b)
#define FMA1(a, b, c) __builtin_amdgcn_div_fmasf(a, b, c, false)
DI v2u ld8_sc1_async(v4u srd, unsigned voff) {
  v2u r; asm volatile("buffer_load_dwordx2 %0, %1, %2, 0 offen sc1" : "=v"(r) : "v"(voff), "s"(srd) : "memory"); return r;
}
template <int N> DI void wait_vmf(v2u& a) { asm volatile("s_waitcnt vmcnt(%1)" : "+v"(a) : "i"(N) : "memory"); }
DI void arrive_nowait(unsigned cnt) {
  asm volatile("s_mov_b64 exec, 1\n\tds_add_u32 %0, %1\n\ts_mov_b64 exec, -1" :: "v"(cnt), "v"(1u) : "memory");
}
DI unsigned peek(unsigned cnt) { return __hip_atomic_load((LAS unsigned*)(size_t)cnt, __ATOMIC_RELAXED, __HIP_MEMORY_SCOPE_WORKGROUP); }
DI void await(unsigned cnt, unsigned n) {
#pragma unroll 1
  for (unsigned spin = 0; spin < SPIN_CAP * 32; ++spin) {
    if (__builtin_amdgcn_readfirstlane(peek(cnt)) >= n) break;
    __builtin_amdgcn_s_sleep(1);
  }
  asm volatile("" ::: "memory");
}

__global__ __launch_bounds__(512, 2) void fused_block(const void* __restrict__ x, const void* __restrict__ wqkv, const void* __restrict__ wo,
                                                      const void* __restrict__ w1, const void* __restrict__ b1, const void* __restrict__ w2,
                                                      const void* __restrict__ b2, void* __restrict__ out, char* __restrict__ ws) {
  constexpr unsigned lds0 = 0;
  const int wave = __builtin_amdgcn_readfirstlane(threadIdx.x >> 6);
  const int m = wave & 3;
  const bool isG = wave < 4;
  const int xcd = blockIdx.x & 7, idx = blockIdx.x >> 3;
  const int r = (xcd >> 2) * 8 + (idx >> 2), j = (xcd & 3) * 4 + (idx & 3);
  const int rt = (xcd >> 1) * 8 + (idx >> 2), ct = (xcd & 1) * 4 + (idx & 3);
  int lane = threadIdx.x & 63, r32, h, g, q4, p4;
  asm volatile("" :: "s"(x), "s"(wqkv), "s"(wo), "s"(w1), "s"(b1), "s"(w2), "s"(b2), "s"(out), "s"(ws));

  const v4u sx = make_srd(x, SEQ * DM * 2), sws = make_srd(ws, WS_END);
  const __amdgpu_buffer_rsrc_t rws = __builtin_amdgcn_make_buffer_rsrc(ws, 0, WS_END, 0x00020000);
  const unsigned one2 = 0x3f803f80u;
  const int T = 4 * r + m;
  auto flag_off = [&](int kind, int tile, int jj) { return WS_FLAG + ((kind * 64 + tile) * 16 + jj) * 128; };
  auto flag = [&](int kind, int tile, int jj) { return (GAS u64*)(ws + flag_off(kind, tile, jj)); };
  u64 tag;
  unsigned taglo, tag32;
  auto get_tag = [&]() {
    const v2u tg = *(const LAS v2u*)(size_t)(lds0 + L_CNT + 32);
    taglo = __builtin_amdgcn_readfirstlane(tg[0]); tag32 = __builtin_amdgcn_readfirstlane(tg[1]);
    tag = ((u64)tag32 << 32) | taglo;
  };
  v4u xres, bres, xaug, st;
  float mean, var, rstd, sd;
  v4u sel;
  unsigned xo[4], swz;
  auto lane_setup = [&]() {
    lane = threadIdx.x & 63; PIN(lane);
    r32 = lane & 31; h = lane >> 5; g = lane >> 4; q4 = (lane >> 2) & 3; p4 = lane & 3;
    const unsigned selv = ((lane & 15) == (g & 1)) ? one2 : 0u;
    sel = (v4u){selv, selv, selv, selv};
#pragma unroll
    for (int p = 0; p < 4; ++p) xo[p] = (32 * m + r32) * 128 + (((2 * ((p + m) & 3) + h) ^ ((r32 >> 1) & 7)) * 16);
    swz = ((((lane >> 2) & 1) ^ (g & 1)) * 64) + p4 * 16;
  };

  auto stat_load = [&]() { st = ld16_sc1_async(sws, WS_STATS + (128 * r + 32 * m + r32) * 16, 0); };
  auto res_loads = [&]() {
    int t2 = threadIdx.x; PIN(t2);
    const int row = 64 * rt + (t2 >> 3), col = 64 * ct + (t2 & 7) * 8;
    xres = ld16_async(sx, row * 1024 + col * 2, 0);
    bres = ld16_async(make_srd(b2, DM * 2), col * 2, 0);
  };
  f32x4 lo, hi;
  auto res_sum = [&]() {
    PIN(xres); PIN(bres);
    lo = (f32x4){bflo(xres[0]) + bflo(bres[0]), bfhi(xres[0]) + bfhi(bres[0]), bflo(xres[1]) + bflo(bres[1]), bfhi(xres[1]) + bfhi(bres[1])};
    hi = (f32x4){bflo(xres[2]) + bflo(bres[2]), bfhi(xres[2]) + bfhi(bres[2]), bflo(xres[3]) + bflo(bres[3]), bfhi(xres[3]) + bfhi(bres[3])};
    PIN(lo); PIN(hi);
  };
  auto row_stats = [&]() {
    if (!__all(st[1] == tag32 && st[3] == tag32)) {
#pragma unroll 1
      for (unsigned spin = 0; spin < SPIN_CAP; ++spin) {
        st = ld16_sc1_async(sws, WS_STATS + (128 * r + 32 * m + r32) * 16, 0);
        wait_vm1<0>(st);
        if (__all(st[1] == tag32 && st[3] == tag32)) break;
      }
    }
    mean = asf(st[0]); var = asf(st[2]);
    PIN(mean); PIN(var);
  };
  auto make_xaug = [&]() {
    PIN(mean); PIN(var);
    rstd = __builtin_amdgcn_rsqf(var); sd = var * rstd;
    const unsigned uh = pk2(-mean, sd);
    const unsigned ul = pk2(-mean - bflo(uh), FMA1(bfhi(uh), -1.f, sd));
    xaug = (v4u){(uh & 0xffffu) | (uh << 16), (ul & 0xffffu) | (uh & 0xffff0000u), ul >> 16, 0u};
    if (h) xaug = (v4u){0u, 0u, 0u, 0u};
    PIN(xaug);
  };
  auto cswr = [&](int tile, f32x4 c) {
    if (lane < 16) { LAS float* p = (LAS float*)(size_t)(lds0 + L_CS + (tile * 32 + lane) * 16 + m * 4); p[0] = c[0]; p[64] = c[1]; }
  };
  auto csrd = [&](int tile) { return __builtin_bit_cast(f32x4, lds128(lds0 + L_CS + (tile * 32 + r32) * 16)); };
  auto waug = [&](f32x4 c4, unsigned b16) {
    const float c = ((c4[0] + c4[1]) + c4[2]) + c4[3];
    const unsigned uh = pk2(c, 0.f);
    const unsigned ul = pk2(c - bflo(uh), 0.f);
    return (v4u){uh | (ul << 16), uh | (b16 << 16), b16, 0u};
  };

  const v4u sw1 = make_srd(w1, DM * FF * 2);
  const int hcol0 = 448 * (j >> 2) + 128 * (j & 3);
  const bool heavy = (j & 3) != 3;
  unsigned bias0, bias1;
  auto bias_load = [&](int P) {
    bias0 = ld2_async(make_srd(b1, FF * 2), r32 * 2, (hcol0 + 64 * P) * 2);
    bias1 = ld2_async(make_srd(b1, FF * 2), r32 * 2, (hcol0 + 64 * P + 32) * 2);
  };
  auto w1dma = [&](int P, int c, unsigned dst, int half) {
    const unsigned vw1 = (lane >> 3) * (FF * 2) + swz, vw1b = vw1 + 8 * (FF * 2) - 1024;
    const unsigned so = (64 * ((c + r) & 7) + 16 * m) * (FF * 2) + (hcol0 + 64 * P) * 2;
    if (half == 0) dma16_a(sw1, vw1, so, dst + m * 2048); else dma16_b(sw1, vw1b, so);
  };
  f32x16 hac[2];
  unsigned rk[4][2];
  auto hsetup = [&](int P) {
#pragma unroll
    for (int p = 0; p < 4; ++p)
#pragma unroll
      for (int c = 0; c < 2; ++c) {
        rk[p][c] = lds0 + (P ? L_WA : w1buf(0)) + ((p + m) & 3) * 2048 + h * 1024 + q4 * 128 + ((c ^ (q4 >> 1)) * 64) + (g & 1) * 32 + p4 * 8;
        PIN(rk[p][c]);
      }
  };
  auto hmm = [&](auto pc, const v4u (&xf)[32], auto&& hook) {
    constexpr int P = pc.value;
    constexpr int D = 4;
    const unsigned cnt = lds0 + L_CNT;
    f32x4 cs[2] = {}, c0, c1;
    v4u wf[D][2], au0, au1;
    unsigned seen = 0;
    hac[0] = (f32x16){}; hac[1] = (f32x16){};
    auto rd = [&](int ks, int t) { wf[ks % D][t] = ldtr2(rk[ks & 3][t] + (ks >> 2) * 8192, 512); };
#pragma unroll
    for (int ks = 0; ks < D; ++ks) { rd(ks, 0); rd(ks, 1); }
    SBAR();
    static_for<0, 32>([&](auto kc) {
      constexpr int ks = kc.value;
      hook(kc);
      if constexpr (ks == 10) seen = peek(cnt + 20);
      if constexpr (ks == 29) seen = peek(cnt);
#pragma unroll
      for (int t = 0; t < 2; ++t) {
        hac[t] = MFMA32(wf[ks % D][t], xf[ks], hac[t]);
        if ((ks & 3) == 0 && ks < 28) { cs[t] = MFMA16(sel, wf[ks % D][t], cs[t]); PIN(cs[t]); }
        if (ks == 26) { cs[t] = MFMA16(sel, wf[28 % D][t], cs[t]); PIN(cs[t]); }
        SBAR();
        if constexpr (ks + D < 32) rd(ks + D, t);
        if constexpr (ks == 31) { if (t == 0) { au0 = waug(c0, bias0); PIN(au0); } else { au1 = waug(c1, bias1); PIN(au1); } }
        SBAR();
      }
      if constexpr (ks == 11 || ks == 30) {
        PIN(hac[0]); PIN(hac[1]);
        if (__builtin_amdgcn_readfirstlane(seen) < 4) await(ks == 11 ? cnt + 20 : cnt, 4);
        asm volatile("" ::: "memory");
        if constexpr (ks == 30) { c0 = csrd(2 * P); c1 = csrd(2 * P + 1); }
      }
      if constexpr (ks == 27) { cswr(2 * P, cs[0]); cswr(2 * P + 1, cs[1]); arrive_nowait(cnt); }
      SBAR();
    });
    hac[0] = MFMA32(au0, xaug, hac[0]);
    hac[1] = MFMA32(au1, xaug, hac[1]);
    SBAR();
  };
  auto hgelu = [&](auto pc, auto&& mid) {
    constexpr int P = pc.value;
    constexpr bool PK = P == 1;
    const float C0 = 2.3022081986f, C1 = C0 * 0.044715f;
    const float ga = -C1 * rstd * rstd * rstd, gb = -C0 * rstd;
    auto mul = [&](f32x2 a, f32x2 b) { return PK ? a * b : (f32x2){MUL1(a[0], b[0]), MUL1(a[1], b[1])}; };
    auto fma = [&](f32x2 a, float b, float c) {
      return PK ? __builtin_elementwise_fma(a, (f32x2){b, b}, (f32x2){c, c}) : (f32x2){FMA1(a[0], b, c), FMA1(a[1], b, c)}; };
#pragma unroll
    for (int t = 0; t < 2; ++t) {
      f32x2 v[8], z[8];
#pragma unroll
      for (int e = 0; e < 8; ++e) { v[e] = (f32x2){hac[t][2 * e], hac[t][2 * e + 1]}; z[e] = mul(v[e], v[e]); }
      SBAR();
#pragma unroll
      for (int e = 0; e < 8; ++e) z[e] = fma(z[e], ga, gb);
      SBAR();
#pragma unroll
      for (int e = 0; e < 8; ++e) z[e] = mul(z[e], v[e]);
      SBAR();
#pragma unroll
      for (int e = 0; e < 8; ++e) z[e] = (f32x2){EXP2(z[e][0]), EXP2(z[e][1])};
      SBAR();
#pragma unroll
      for (int e = 0; e < 8; ++e) z[e] = fma(z[e], sd, sd);
      SBAR();
#pragma unroll
      for (int e = 0; e < 8; ++e) z[e] = (f32x2){__builtin_amdgcn_rcpf(z[e][0]), __builtin_amdgcn_rcpf(z[e][1])};
      SBAR();
#pragma unroll
      for (int e = 0; e < 8; ++e) z[e] = mul(z[e], v[e]);
      SBAR();
#pragma unroll
      for (int s = 0; s < 2; ++s) {
        const v4u f = {pk2(z[4 * s][0], z[4 * s][1]), pk2(z[4 * s + 1][0], z[4 * s + 1][1]), pk2(z[4 * s + 2][0], z[4 * s + 2][1]), pk2(z[4 * s + 3][0], z[4 * s + 3][1])};
        st16_sc1(f, sws, lane * 16, WS_AF + (T * AFT + (hcol0 >> 4) + 4 * P + 2 * t + s) * 1024);
      }
      if (t == 0) mid();
      SBAR();
    }
  };

  if (isG) {
    lane_setup();
    const v4u sw2 = make_srd(w2, FF * DM * 2), swo = make_srd(wo, DM * DM * 2);
    const unsigned vw2 = (lane >> 3) * 1024 + swz, vw2b = vw2 + 7 * 1024;
    const int ro3 = rt % 3;
    auto kmap = [&](int kk) {
      if (kk < 16) return 8 * (((kk >> 2) + rt) & 3) + (kk & 3);
      const int p = ((kk - 16) >> 2) + ro3;
      return 8 * (p >= 3 ? p - 3 : p) + 4 + (kk & 3);
    };
    auto omap = [&](int i) { return 2 * ((i + rt) & 3) + (i >> 2); };
    auto wdmah = [&](int kk, int half) {
      const unsigned so = (kk < 28 ? 448 * m + 16 * kmap(kk) : 128 * m + 16 * omap(kk - 28)) * 1024 + 128 * ct;
      if (half == 0) dma16_a(kk < 28 ? sw2 : swo, vw2, so, lds0 + L_WG + m * PART + (kk % RD) * 2048); else dma16_b(kk < 28 ? sw2 : swo, vw2b, so);
    };
    auto wdma = [&](int kk) { wdmah(kk, 0); wdmah(kk, 1); };
    {
      const v4u swq = make_srd(wqkv, DM * 3 * DM * 2);
      const unsigned vx0 = (lane >> 3) * 1024 + (((lane & 7) ^ g) * 16), vx1 = (7 + (lane >> 3)) * 1024 + (((lane & 7) ^ (4 + g)) * 16);
      const unsigned vwq = (lane >> 2) * (3 * DM * 2) + p4 * 16;
      auto piece = [&](int s, int i) {
        const int kc = (s + r) & 7;
        if (i < 4) {
          const unsigned so = (128 * r + 32 * m + 16 * (i >> 1)) * 1024 + kc * 128;
          if (i & 1) dma16_b(sx, vx1, so); else dma16_a(sx, vx0, so, lds0 + xbuf(s) + m * 4096 + (i >> 1) * 2048);
        } else if (i < 7) dma16(swq, vwq, (64 * kc + 16 * m) * (3 * DM * 2) + (512 * (i - 4) + 32 * j) * 2, lds0 + qbuf(s) + ((i - 4) * 4 + m) * 1024);
        else w1dma(0, s, lds0 + w1buf(s), i - 7);
      };
#pragma unroll
      for (int i = 0; i < 18; ++i) { piece(i / 9, i % 9); if (i == 8) __builtin_amdgcn_s_barrier(); }
      __builtin_amdgcn_s_setprio(2);
      hsetup(0);
      f32x4 cs[2] = {};
      v4u xf[4], wf[4][2];
      hac[0] = (f32x16){}; hac[1] = (f32x16){};
      static_for<0, 8>([&](auto sc) {
        constexpr int s = sc.value;
        auto fill = [&](int i) {
          if constexpr (s < 6) piece(s + 2, i);
          if constexpr (s == 6) {
            if (i == 0) { stat_load(); bias_load(0); }
            else if (i < 5) { if (heavy) w1dma(1, (i - 1) >> 1, lds0 + L_WA + ((i - 1) >> 1) * 8192, (i - 1) & 1); }
            else wdmah((i - 5) >> 1, (i - 5) & 1);
          }
          if constexpr (s == 7) {
            if (heavy && i < 4) w1dma(1, 2 + (i >> 1), lds0 + L_WA + (2 + (i >> 1)) * 8192, i & 1);
            if (heavy && i >= 4) { w1dma(1, i, lds0 + L_WA + i * 8192, 0); w1dma(1, i, 0, 1); }
          }
          SBAR();
        };
        auto rd = [&](int p) {
          xf[p] = lds128(xbuf(s) + xo[p]);
          wf[p][0] = ldtr2(rk[p][0] + (s % 3) * PART, 512); wf[p][1] = ldtr2(rk[p][1] + (s % 3) * PART, 512);
          SBAR();
        };
        if constexpr (s < 7) wait_vm<9>(); else { if (heavy) wait_vm<11>(); else wait_vm<7>(); }
        bar();
        rd(0);
        if constexpr (s < 7) fill(0);
        rd(1);
        if constexpr (s < 7) fill(1);
        if constexpr (s == 2) get_tag();
#pragma unroll
        for (int p = 0; p < 4; ++p) {
#pragma unroll
          for (int t = 0; t < 2; ++t) {
            hac[t] = MFMA32(wf[p][t], xf[p], hac[t]);
            if (p == 0) { cs[t] = MFMA16(sel, wf[p][t], cs[t]); PIN(cs[t]); }
            SBAR();
            if (p < 2 && t == 0) rd(p + 2);
            if (s == 7) fill(2 * p + t); else if (2 * p + t < 7) fill(2 + 2 * p + t);
          }
          if (s == 7 && p == 1) {
            cswr(0, cs[0]); cswr(1, cs[1]);
            bar();
          }
        }
        if constexpr (s == 7) {
          const f32x4 c0 = csrd(0), c1 = csrd(1);
          if (heavy) wait_vm<20>(); else wait_vm<4>();
          PIN(st); PIN(bias0); PIN(bias1);
          row_stats(); make_xaug();
          hac[0] = MFMA32(waug(c0, bias0), xaug, hac[0]);
          hac[1] = MFMA32(waug(c1, bias1), xaug, hac[1]);
          SBAR();
        }
      });
    }
    if (heavy) { wait_vm<8>(); arrive_nowait(lds0 + L_CNT + 16); }
    hgelu(std::integral_constant<int, 0>{}, [&]() {
      wait_vm<2>();
      if (heavy) arrive_nowait(lds0 + L_CNT + 20);
    });
    __builtin_amdgcn_s_setprio(0);
    unsigned rb2[2];
#pragma unroll
    for (int c = 0; c < 2; ++c) rb2[c] = lds0 + L_WG + m * PART + h * 512 + q4 * 128 + ((c ^ (q4 >> 1)) * 64) + (g & 1) * 32 + p4 * 8;
    v4u wq[36][2];
    auto wrd = [&](int kk) {
#pragma unroll
      for (int c = 0; c < 2; ++c) wq[kk][c] = ldtr2(rb2[c] + (kk % RD) * 2048, 1024);
    };
    auto wgot = [&](int kk) { PIN(wq[kk][0]); PIN(wq[kk][1]); };
    const int ftile = 2 * rt + ((lane >> 2) & 1);
    v4u af[28][2], of[8][2];
    v2u fl;
    const unsigned a0 = WS_AF + (2 * rt * AFT + 28 * m) * 1024, o0 = WS_AF + (2 * rt * AFT + 112 + 8 * m) * 1024;
    for (int kk = 2; kk < 6; ++kk) wdma(kk);
    wait_vm<8>();
    if (lane == 0) __hip_atomic_store(flag(FL_H0, T, j), tag, RLX_AGENT);
    wdma(6); wdma(7);
    static_for<0, 3>([&](auto ic) {
      constexpr int i = ic.value;
      wait_vm<(i == 0) ? 9 : 8>();
      for (int kk = 4 * i; kk < 4 * i + 4; ++kk) wrd(kk);
      for (int kk = 4 * i; kk < 4 * i + 4; ++kk) wgot(kk);
      if constexpr (i == 2) fl = ld8_sc1_async(sws, flag_off(FL_H0, ftile, 4 * m + p4));
      for (int kk = 4 * i; kk < 4 * i + 4; ++kk) wdma(kk + RD);
    });
    wait_vmf<8>(fl);
    if (!__all(fl[0] == taglo && fl[1] == tag32)) poll_flags(flag(FL_H0, ftile, 4 * m + p4), tag);
#pragma unroll
    for (int kk = 0; kk < AW; ++kk) {
      af[kk][0] = ld16_sc1_async(sws, lane * 16, a0 + kmap(kk) * 1024);
      af[kk][1] = ld16_sc1_async(sws, lane * 16, a0 + (AFT + kmap(kk)) * 1024);
    }
    __builtin_amdgcn_s_setprio(2);
    f32x16 c2[2][2] = {};
    auto mm2 = [&](int kk, const v4u (&a)[2], auto&& fill) {
      SBAR();
#pragma unroll
      for (int i = 0; i < 4; ++i) { c2[i >> 1][i & 1] = MFMA32(wq[kk][i & 1], a[i >> 1], c2[i >> 1][i & 1]); SBAR(); fill(i); SBAR(); }
    };
    static_for<0, 28>([&](auto kc) {
      constexpr int kk = kc.value;
      wait_vm2<(kk < AW) ? 2 * (AW - 1 - kk) + g2_range(0, kk) : g2_range(kk - AW + 1, kk)>(af[kk][0], af[kk][1]);
      if constexpr (kk == H1_CK) {
        wait_vmf<g2_range(H1_LD, H1_CK) - 1>(fl);
        if (!__all(fl[0] == taglo && fl[1] == tag32)) poll_flags(flag(FL_H1, ftile, 4 * m + p4), tag);
      }
      if constexpr (kk == H1_LD) fl = ld8_sc1_async(sws, flag_off(FL_H1, ftile, 4 * m + p4));
      if constexpr (kk == O_LD) fl = ld8_sc1_async(sws, flag_off(FL_O0, ftile, 4 * m + p4));
      if constexpr (kk == 26) fl = ld8_sc1_async(sws, flag_off(FL_O1, ftile, 4 * m + p4));
      constexpr bool dw = kk >= 1 && kk - 1 + NR + RD < 36;
      if constexpr (dw) wgot(kk - 1 + NR);
      mm2(kk, af[kk], [&](int i) {
        if constexpr (dw) { if (i < 2) wdmah(kk - 1 + NR + RD, i); }
        if constexpr (kk + NR < 28) { if (i == 1) wrd(kk + NR); }
        if constexpr (kk == 27) { if (i == 0) wrd(28); }
        if constexpr (kk + AW < 28) { if (i >= 2) af[kk + AW][i - 2] = ld16_sc1_async(sws, lane * 16, a0 + (AFT * (i - 2) + kmap(kk + AW)) * 1024); }
      });
      if constexpr (kk == O_CK) {
        wait_vmf<g2_range(O_LD, O_CK) - 1>(fl);
        if (!__all(fl[0] == taglo && fl[1] == tag32)) poll_flags(flag(FL_O0, ftile, 4 * m + p4), tag);
        res_loads();
#pragma unroll
        for (int i = 0; i < 4; ++i) {
          of[i][0] = ld16_sc1_async(sws, lane * 16, o0 + omap(i) * 1024);
          of[i][1] = ld16_sc1_async(sws, lane * 16, o0 + (AFT + omap(i)) * 1024);
        }
      }
    });
    static_for<0, 4>([&](auto kc) {
      constexpr int kk = kc.value;
      if (kk < 3) wrd(28 + kk + 1);
      if constexpr (kk == 1) {
        res_sum();
        wait_vmf<0>(fl);
        if (!__all(fl[0] == taglo && fl[1] == tag32)) poll_flags(flag(FL_O1, ftile, 4 * m + p4), tag);
#pragma unroll
        for (int i = 0; i < 8; ++i) of[4 + (i & 3)][i >> 2] = ld16_sc1_async(sws, lane * 16, o0 + (AFT * (i >> 2) + omap(4 + (i & 3))) * 1024);
      }
      wait_vm2<(kk == 0) ? 7 : 14 - 2 * kk>(of[kk][0], of[kk][1]);
      mm2(28 + kk, of[kk], [&](int i) { PIN(c2[i >> 1][i & 1]); if (kk == 3) wrd(32 + i); });
    });
    for (int kk = 32; kk < 36; ++kk) wgot(kk);
    unsigned pa = lds0 + L_WG + m * PART + lane * 16;
    PIN(pa);
    auto put = [&](int qn, int qd) {
      const f32x16& c = c2[qn >> 1][qn & 1];
      sts128(pa + (qn * 4 + qd) * 66 * 16, __builtin_bit_cast(v4u, (f32x4){c[4 * qd], c[4 * qd + 1], c[4 * qd + 2], c[4 * qd + 3]}));
    };
    static_for<0, 4>([&](auto qc) {
      constexpr int qn = qc.value;
      if constexpr ((qn & 1) == 0) { wait_vm2<qn ? 0 : 4>(of[4][qn >> 1], of[5][qn >> 1]); PIN(of[6][qn >> 1]); PIN(of[7][qn >> 1]); }
      SBAR();
#pragma unroll
      for (int i = 0; i < 4; ++i) {
        c2[qn >> 1][qn & 1] = MFMA32(wq[32 + i][qn & 1], of[4 + i][qn >> 1], c2[qn >> 1][qn & 1]);
        SBAR();
        if (qn > 0 && i >= 2) { put(qn - 1, 2 * i - 4); put(qn - 1, 2 * i - 3); }
        SBAR();
      }
    });
#pragma unroll
    for (int qd = 0; qd < 4; ++qd) put(3, qd);
  } else {
    __builtin_amdgcn_s_setprio(1);
    lane_setup();
    v4u xr[2];
    xr[0] = ld16_async(sx, lane * 16, (128 * r + 8 * j + 2 * m) * 1024); xr[1] = ld16_async(sx, lane * 16, (128 * r + 8 * j + 2 * m + 1) * 1024);
    unsigned warm;
    {
      u64 pc; asm volatile("s_getpc_b64 %0" : "=s"(pc));
      warm = ld2_async(make_srd((const void*)(pc & ~127ull), CODE_BYTES), lane < 3 ? lane * 128 : 0x40000000u, (idx * 4 + m) * 384);
    }
    __builtin_amdgcn_s_barrier();
    v2u ep = ld8_sc1_async(sws, m == 0 ? WS_EPOCH : 0x40000000u);
    float sval;
    const int wi = blockIdx.x * 4 + m;
    const u64 wbase = wi < 448 ? (u64)w1 + wi * 4096 : wi < 896 ? (u64)w2 + (wi - 448) * 4096 : (u64)wo + (wi - 896) * 4096;
    auto make_stats = [&]() {
      wait_vm2<1>(xr[0], xr[1]);
      PIN(warm);
      float piv[2], a[4];
#pragma unroll
      for (int u = 0; u < 2; ++u) {
        piv[u] = rdl(bflo(xr[u][0]), 0);
        float s1 = 0.f, s2 = 0.f;
#pragma unroll
        for (int i = 0; i < 8; ++i) { const float d = ((i & 1) ? bfhi(xr[u][i >> 1]) : bflo(xr[u][i >> 1])) - piv[u]; s1 += d; s2 = __builtin_fmaf(d, d, s2); }
        a[2 * u] = s1; a[2 * u + 1] = s2;
      }
#pragma unroll
      for (int i = 0; i < 4; ++i) a[i] += dpp<0xB1>(a[i]);
#pragma unroll
      for (int i = 0; i < 4; ++i) a[i] += dpp<0x4E>(a[i]);
      float t = p4 == 0 ? a[0] : p4 == 1 ? a[1] : p4 == 2 ? a[2] : a[3];
      t += dpp<0x114>(t); t += dpp<0x118>(t);
      const auto s16 = __builtin_amdgcn_permlane16_swap(asu(t), asu(t), false, false);
      t = asf(s16[0]) + asf(s16[1]);
      const auto s32 = __builtin_amdgcn_permlane32_swap(asu(t), asu(t), false, false);
      t = (asf(s32[0]) + asf(s32[1])) * (1.f / DM);
      const float e1 = dpp<0xA0>(t);
      sval = (p4 & 1) ? __builtin_fmaxf(__builtin_fmaf(-e1, e1, t), 0.f) + 1e-5f : t + (p4 ? piv[1] : piv[0]);
      PIN(sval);
    };
    const unsigned acnt = lds0 + L_CNT;
    const unsigned vpos = 2048 + (g & 1) * 512 + h * 256 + (lane & 15) * 16;
    v4u qf[2], am0, am1;
    u64 um[16];
    auto masks = [&]() {
      const int dq = r32 - 4 * h;
#pragma unroll
      for (int d = 0; d < 4; ++d) {
        const int row = 2 * (d & 1) + 8 * (d >> 1);
        am0[d] = row > dq ? 0xffffffffu : row == dq ? 0xffff0000u : 0u;
        am1[d] = row + 16 > dq ? 0xffffffffu : row + 16 == dq ? 0xffff0000u : 0u;
      }
      PIN(am0); PIN(am1);
      static_for<0, 16>([&](auto u) { um[u.value] = upper_mask(u.value); pin_s(um[u.value]); });
    };
    {
      v4u xf[32];
      f32x16 acc[3] = {};
      f32x4 cs[3] = {};
      v4u wf[4][3];
      unsigned wb[4];
#pragma unroll
      for (int p = 0; p < 4; ++p) { wb[p] = qbuf(0) + ((p + m) & 3) * 1024 + (8 * h + q4) * 64 + (g & 1) * 32 + p4 * 8; PIN(wb[p]); }
      auto rd = [&](int ks, int t) {
        if (t == 0) xf[ks] = lds128(xbuf(ks >> 2) + xo[ks & 3]);
        wf[ks & 3][t] = ldtr2(wb[ks & 3] + ((ks >> 2) % 3) * 12288 + t * 4096, 256);
      };
      auto mm = [&](int ks, int t) {
        acc[t] = t == 2 ? MFMA32(xf[ks], wf[ks & 3][t], acc[t]) : MFMA32(wf[ks & 3][t], xf[ks], acc[t]);
        if ((ks & 3) == 0) { cs[t] = MFMA16(sel, wf[ks & 3][t], cs[t]); PIN(cs[t]); }
        SBAR();
      };
      make_stats();
      bar();
#pragma unroll
      for (int t = 0; t < 6; ++t) rd(t / 3, t % 3);
      SBAR();
      static_for<0, 8>([&](auto sc) {
        constexpr int s = sc.value;
#pragma unroll
        for (int p = 0; p < 2; ++p) {
#pragma unroll
          for (int t = 0; t < 3; ++t) { mm(4 * s + p, t); rd(4 * s + p + 2, t); SBAR(); }
          if (s == 7 && p == 0) { cswr(4, cs[0]); cswr(5, cs[1]); cswr(6, cs[2]); SBAR(); }
        }
        if constexpr (s == 0) {
          wait_vmf<0>(ep);
          if (m == 0 && lane < 10)
            *(LAS unsigned*)(size_t)(lds0 + L_CNT + lane * 4) = lane == 8 ? ep[0] * 0x9E3779B1u + 0x7F4A7C15u : lane == 9 ? ep[1] * 0x85EBCA77u + 0x165667B1u : 0u;
        }
        if constexpr (s == 1) {
          get_tag();
          if ((lane >> 2) == 3) {
            const v2u gr = {asu(sval), tag32};
            __builtin_amdgcn_raw_buffer_store_b64(gr, rws, WS_STATS + (128 * r + 8 * j + 2 * m) * 16 + p4 * 8, 0, 16);
          }
        }
        if constexpr (s == 1) {
          warm = ld2_async(make_srd((const void*)wbase, 4096), lane * 128, 0);
        }
        if constexpr (s == 5) stat_load();
        if constexpr (s == 6) {
          wait_vm1<0>(st);
          PIN(warm);

          row_stats();
        }
        mm(4 * s + 2, 0); mm(4 * s + 2, 1);
        bar();
        if constexpr (s < 7) {
          mm(4 * s + 2, 2);
          rd(4 * s + 4, 0); rd(4 * s + 4, 1); rd(4 * s + 4, 2); SBAR();
#pragma unroll
          for (int t = 0; t < 3; ++t) { mm(4 * s + 3, t); rd(4 * s + 5, t); SBAR(); }
        }
      });
      __builtin_amdgcn_s_setprio(3);
      const f32x4 c5 = csrd(5), c6 = csrd(6), c4 = csrd(4);
      SBAR();
      mm(30, 2); mm(31, 1);
      make_xaug();
      SBAR();
      mm(31, 2); mm(31, 0);
      if (h == 0) *(LAS float*)(size_t)(lds0 + L_RS + (m * 32 + r32) * 4) = rstd;
      acc[1] = MFMA32(waug(c5, 0u), xaug, acc[1]);
      acc[2] = MFMA32(xaug, waug(c6, 0u), acc[2]);
      acc[0] = MFMA32(waug(c4, 0u), xaug, acc[0]);
      SBAR();
      f32x4 rs[4];
#pragma unroll
      for (int qd = 0; qd < 4; ++qd) rs[qd] = __builtin_bit_cast(f32x4, lds128(lds0 + L_RS + (m * 32 + 8 * qd + 4 * h) * 4));
      const unsigned kvo = WS_KV + (j * 64 + T) * 4096;
#pragma unroll
      for (int e = 0; e < 16; ++e) acc[1][e] = MUL1(acc[1][e], rstd);
#pragma unroll
      for (int e = 0; e < 2; ++e) {
        const v4u f = pack8(acc[1], e);
        st16_sc1(f, sws, lane * 16, kvo + e * 1024);
        sts128(lds0 + L_KV + (8 + m) * KVT + e * 1024 + lane * 16, f);
      }
#pragma unroll
      for (int qd = 0; qd < 4; ++qd)
#pragma unroll
        for (int e = 0; e < 4; ++e) acc[2][4 * qd + e] = MUL1(acc[2][4 * qd + e], rs[qd][e]);
#pragma unroll
      for (int s = 0; s < 2; ++s) {
        const v4u f = pack8(acc[2], s);
        st16_sc1(f, sws, lane * 16, kvo + 2048 + s * 1024);
        sts128(lds0 + L_KV + (8 + m) * KVT + s * VB + vpos, f);
      }
      SBAR();
      const float qs = rstd * (0.25f * 1.4426950408889634f);
#pragma unroll
      for (int e = 0; e < 16; ++e) acc[0][e] = MUL1(acc[0][e], qs);
      qf[0] = pack8(acc[0], 0); qf[1] = pack8(acc[0], 1);
      PIN(qf[0]); PIN(qf[1]);
      const int T0 = 4 * r - 8 + 2 * m;
      const bool live = T0 >= 0;
      auto kv_load = [&](v4u (&kv)[8], int i0, int i1) {
        const unsigned src = WS_KV + (j * 64 + (live ? T0 : 0)) * 4096, vo = live ? lane * 16 : h * 512;
#pragma unroll
        for (int i = i0; i < i1; ++i) kv[i] = ld16_sc1_async(sws, vo, src + (live ? i : (i & 3)) * 1024);
      };
      auto kv_stage = [&](v4u (&kv)[8]) {
        const unsigned on = (lane < 2 || live) ? one2 : 0u;
        if (lane < 6) sts128(lds0 + L_KV + (lane < 2 ? 8 + m : 2 * m + ((lane - 2) >> 1)) * KVT + 2048 + (lane & 1) * VB + 1024, (v4u){on, on, on, on});
        const v4u zz = {};
#pragma unroll
        for (int i = 0; i < 2; ++i) {
          sts128(lds0 + L_KV + (2 * m + i) * KVT + lane * 16, kv[4 * i]);
          sts128(lds0 + L_KV + (2 * m + i) * KVT + 1024 + lane * 16, kv[4 * i + 1]);
          sts128(lds0 + L_KV + (2 * m + i) * KVT + vpos, live ? kv[4 * i + 2] : zz);
          sts128(lds0 + L_KV + (2 * m + i) * KVT + VB + vpos, live ? kv[4 * i + 3] : zz);
        }
        arrive_nowait(acnt);
      };
      auto kv_wait = [&](v4u (&kv)[8]) {
        wait_vm<0>();
#pragma unroll
        for (int i = 0; i < 8; ++i) PIN(kv[i]);
      };
      if (heavy) {
        hsetup(1);
        await(acnt + 16, 4);
        v2u fl;
        v4u kv[8];
        hmm(std::integral_constant<int, 1>{}, xf, [&](auto kc) {
          constexpr int ks = kc.value;
          if constexpr (ks == KVPUB) {
            wait_vm<0>();
            if (lane == 0) __hip_atomic_store(flag(FL_KV, T, j), tag, RLX_AGENT);
          }
          if constexpr (ks == KVPUB + 2) bias_load(1);
          if constexpr (ks == 31) { wait_vm<1>(); PIN(bias0); PIN(bias1); }
          if constexpr (ks == 24) fl = ld8_sc1_async(sws, flag_off(FL_KV, live ? T0 + (lane & 1) : 0, j));
        });
        wait_vmf<0>(fl);
        if (!__all(fl[0] == taglo && fl[1] == tag32)) poll_flags(flag(FL_KV, live ? T0 + (lane & 1) : 0, j), tag);
        kv_load(kv, 0, 8);
        hgelu(std::integral_constant<int, 1>{}, [&]() {});
        masks();
        wait_vm<4>();
#pragma unroll
        for (int i = 0; i < 8; ++i) PIN(kv[i]);
        kv_stage(kv);
      } else {
        wait_vm<0>();
        if (lane == 0) __hip_atomic_store(flag(FL_KV, T, j), tag, RLX_AGENT);
        v4u kv[8];
        poll_flags(flag(FL_KV, live ? T0 + (lane & 1) : 0, j), tag);
        kv_load(kv, 0, 8); masks(); kv_wait(kv); kv_stage(kv);
      }
    }
    const unsigned kb = lds0 + L_KV + m * KVT + lane * 16;
    unsigned vb[2];
#pragma unroll
    for (int e = 0; e < 2; ++e) { vb[e] = lds0 + L_KV + m * KVT + 2048 + ((g & 1) ? 1024 : e * 512 + h * 256 + (lane & 15) * 16); PIN(vb[e]); }
    await(acnt, heavy ? 8 : 4);
    __builtin_amdgcn_s_setprio(3);
    const f32x16 zero = {};
    auto o_store = [&](f32x16& o, int e) {
      const float rl = __builtin_amdgcn_rcpf(o[8]);
#pragma unroll
      for (int u = 0; u < 8; ++u) o[u] = MUL1(o[u], rl);
      st16_sc1(pack8(o, 0), sws, lane * 16, WS_AF + (T * AFT + 112 + 2 * j + e) * 1024);
    };
    int redo = 0;
    {
      f32x16 o[2], sa, sb, su;
      bool pend = false;
      v4u kn, ku, va, vc, vn0, vn1, vu0, vu1, p0, p1, pu;
      auto kaddr = [&](int n) { return kb + ((n & 7) + 1) * KVT + (n >> 3) * 1024; };
      auto vaddr = [&](int n) { return vb[n >> 3] + ((n & 7) + 1) * KVT; };
      kn = lds128(kaddr(0)); vn0 = lds128(vaddr(0)); vn1 = lds128(vaddr(0) + VB);
      sa = MFMA32(kn, qf[0], zero);
      kn = lds128(kaddr(1));
      SBAR();
      auto step = [&](auto nc, f32x16& sc, f32x16& sn) {
        constexpr int n = nc.value, e = n >> 3, i = n & 7;
        if constexpr (n < 15) sn = MFMA32(kn, qf[(n + 1) >> 3], zero);
        if constexpr (n < 14) kn = lds128(kaddr(n + 2));
        if constexpr (i == 5) ku = lds128(kb + e * 1024);
        v4u v0 = vn0, v1 = vn1;
        if constexpr (n < 15) { vn0 = lds128(vaddr(n + 1)); vn1 = lds128(vaddr(n + 1) + VB); }
        if constexpr (i == 6) { vu0 = lds128(vb[e]); vu1 = lds128(vb[e] + VB); }
        SBAR();
        if constexpr (i == 7) {
#pragma unroll
          for (int u = 0; u < 16; ++u) sc[u] = __builtin_amdgcn_inverse_ballot_w64(um[u]) ? su[u] : sc[u];
          PIN(sc); SBAR();
        }
#pragma unroll
        for (int u = 0; u < 6; ++u) sc[u] = EXP2(sc[u]);
        PIN(sc); SBAR();
        if constexpr (n == 8) { o[0] = MFMA32(vc, p1, o[0]); SBAR(); }
        else if constexpr (n > 0) { o[e] = MFMA32(vc, p1, o[e]); SBAR(); }
        sc[6] = EXP2(sc[6]); sc[7] = EXP2(sc[7]);
        p0 = pack8(sc, 0);
        sc[8] = EXP2(sc[8]); sc[9] = EXP2(sc[9]);
        if constexpr (i == 7) { va = p0 & am0; p0 &= ~am0; PIN(va); }
        PIN(sc); PIN(p0); SBAR();
        if constexpr (n == 8) { o[0] = MFMA32(vu1, pu, o[0]); SBAR(); }
        o[e] = MFMA32(v0, p0, i == 0 ? zero : o[e]);
        SBAR();
        if constexpr (i == 6) { su = MFMA32(ku, qf[e], zero); SBAR(); }
#pragma unroll
        for (int u = 10; u < 13; ++u) sc[u] = EXP2(sc[u]);
        if constexpr (i == 7) { PIN(sc); SBAR(); o[e] = MFMA32(vu0, va, o[e]); SBAR(); }
#pragma unroll
        for (int u = 13; u < 16; ++u) sc[u] = EXP2(sc[u]);
        p1 = pack8(sc, 1); vc = v1;
        if constexpr (i == 7) { pu = p1 & am1; p1 &= ~am1; PIN(pu); }
        PIN(p1); SBAR();
        if constexpr (n == 8) {
          if (__all(o[0][8] >= 0x1p-30f && o[0][8] <= 0x1p30f)) o_store(o[0], 0); else redo = 1;
          SBAR();
        }
        if constexpr (i == 2) pend = !redo;
        if constexpr (i == 3 || i == 5 || i == 7) {
          if (pend && (i == 7 || (__builtin_amdgcn_s_getreg(7 | (3 << 11)) & 15) == 0)) {
            wait_vm<0>();
            if (lane == 0) __hip_atomic_store(flag(FL_H1 + e, T, j), tag, RLX_AGENT);
            pend = false;
          }
        }
      };
      static_for<0, 8>([&](auto hc) {
        step(std::integral_constant<int, 2 * hc.value>{}, sa, sb);
        step(std::integral_constant<int, 2 * hc.value + 1>{}, sb, sa);
      });
      o[1] = MFMA32(vc, p1, o[1]);
      o[1] = MFMA32(vu1, pu, o[1]);
      if (__all(o[1][8] >= 0x1p-30f && o[1][8] <= 0x1p30f)) o_store(o[1], 1); else redo |= 2;
    }
    if (redo) {
#pragma unroll 1
      for (int e = 0; e < 2; ++e) {
        if (!((redo >> e) & 1)) continue;
        const v4u q = e ? qf[1] : qf[0];
        const unsigned vbe = e ? vb[1] : vb[0];
        auto merged = [&](const f32x16& c) {
          f32x16 sl = MFMA32(lds128(kb + 8 * KVT + e * 1024), q, c);
          const f32x16 su = MFMA32(lds128(kb + e * 1024), q, c);
          static_for<0, 16>([&](auto u) { sl[u.value] = upper<u.value>() ? su[u.value] : sl[u.value]; });
          return sl;
        };
        float mx = -INFINITY;
#pragma unroll 1
        for (int i = 0; i < 8; ++i) {
          const f32x16 sc = i ? MFMA32(lds128(kb + i * KVT + e * 1024), q, zero) : merged(zero);
#pragma unroll
          for (int u = 0; u < 16; u += 2) mx = max3(mx, sc[u], sc[u + 1]);
        }
        const auto sw = __builtin_amdgcn_permlane32_swap(asu(mx), asu(mx), false, false);
        mx = __builtin_fmaxf(asf(sw[0]), asf(sw[1]));
        f32x16 negm, o;
#pragma unroll
        for (int u = 0; u < 16; ++u) negm[u] = -mx;
        {
          f32x16 sc = merged(negm);
#pragma unroll
          for (int u = 0; u < 16; ++u) sc[u] = EXP2(sc[u]);
          const v4u p0 = pack8(sc, 0), p1 = pack8(sc, 1);
          o = MFMA32(lds128(vbe), p0 & am0, zero);
          o = MFMA32(lds128(vbe + VB), p1 & am1, o);
          o = MFMA32(lds128(vbe + 8 * KVT), p0 & ~am0, o);
          o = MFMA32(lds128(vbe + 8 * KVT + VB), p1 & ~am1, o);
        }
#pragma unroll 1
        for (int i = 1; i < 8; ++i) {
          f32x16 sc = MFMA32(lds128(kb + i * KVT + e * 1024), q, negm);
#pragma unroll
          for (int u = 0; u < 16; ++u) sc[u] = EXP2(sc[u]);
          o = MFMA32(lds128(vbe + i * KVT), pack8(sc, 0), o);
          o = MFMA32(lds128(vbe + i * KVT + VB), pack8(sc, 1), o);
        }
        o_store(o, e);
      }
      wait_vm<0>();
      if (lane == 0) __hip_atomic_store(flag(FL_O0, T, j), tag, RLX_AGENT);
    }
    wait_vm<0>();
    if (lane == 0) __hip_atomic_store(flag(FL_O1, T, j), tag, RLX_AGENT);
    res_loads();
    wait_vm<0>();
    res_sum();
    if (blockIdx.x == 0 && wave == 7) {
#pragma unroll 1
      for (int i = 0; i < 4; ++i) poll_flags(flag(FL_KV, 4 * (4 * i + g), lane & 15), tag);
      if (lane == 0) __hip_atomic_store((GAS u64*)(ws + WS_EPOCH), tag, RLX_AGENT);
    }
  }
  {
    int t2 = threadIdx.x; PIN(t2);
    const int row = t2 >> 3, ch = t2 & 7;
    unsigned a = lds0 + L_WG + (((row >> 5) * 8 + ch) * 66 + (row & 31)) * 16, oo = (64 * rt + row) * 1024 + (64 * ct + ch * 8) * 2;
    PIN(a); PIN(oo);
    bar();
    f32x4 pl[4], ph[4];
#pragma unroll
    for (int w = 0; w < 4; ++w) { pl[w] = __builtin_bit_cast(f32x4, lds128(a + w * PART)); ph[w] = __builtin_bit_cast(f32x4, lds128(a + w * PART + 512)); }
    SBAR();
#pragma unroll
    for (int w = 0; w < 4; ++w) { lo += pl[w]; hi += ph[w]; }
    const v4u o = {pk2(lo[0], lo[1]), pk2(lo[2], lo[3]), pk2(hi[0], hi[1]), pk2(hi[2], hi[3])};
    __builtin_amdgcn_raw_buffer_store_b128(o, __builtin_amdgcn_make_buffer_rsrc(out, 0, SEQ * DM * 2, 0x00020000), oo, 0, 16);
  }
}

extern "C" void kernel_launch(void* const* d_in, const int* in_sizes, int n_in, void* d_out, int out_size, void* d_ws, size_t ws_size,
                              hipStream_t stream) {
  (void)in_sizes; (void)n_in; (void)out_size; (void)ws_size;
  (void)hipFuncSetAttribute((const void*)fused_block, hipFuncAttributeMaxDynamicSharedMemorySize, LDS_BYTES);
  fused_block<<<256, 512, LDS_BYTES, stream>>>(d_in[0], d_in[1], d_in[2], d_in[4], d_in[5], d_in[6], d_in[7], d_out, (char*)d_ws);
}
```

```cpp
#include <hip/hip_runtime.h>
#include <hip/hip_bf16.h>
#include <stdint.h>
#include <type_traits>

typedef unsigned v4u __attribute__((ext_vector_type(4)));
typedef unsigned v2u __attribute__((ext_vector_type(2)));
typedef float f32x2 __attribute__((ext_vector_type(2)));
typedef float f32x4 __attribute__((ext_vector_type(4)));
typedef float f32x16 __attribute__((ext_vector_type(16)));
typedef short bf16x8 __attribute__((ext_vector_type(8)));
typedef short s16x4 __attribute__((ext_vector_type(4)));
typedef __bf16 bf16x2_t __attribute__((ext_vector_type(2)));
typedef unsigned long long u64;
#define GAS __attribute__((address_space(1)))
#define LAS __attribute__((address_space(3)))
#define DI __device__ __forceinline__
#define RLX_AGENT __ATOMIC_RELAXED, __HIP_MEMORY_SCOPE_AGENT

constexpr int SEQ = 2048, DM = 512, FF = 1792;
constexpr unsigned WS_EPOCH = 0;
constexpr unsigned WS_STATS = 32768;
constexpr unsigned WS_FLAG = 65536;
constexpr unsigned WS_KV = WS_FLAG + 5 * 1024 * 128;
constexpr int AFT = 145;
constexpr unsigned WS_AF = WS_KV + 16 * 64 * 4096;
constexpr unsigned WS_END = WS_AF + 64 * AFT * 1024;
constexpr unsigned WS_STAMP = WS_END;
enum { FL_KV = 0, FL_H0 = 1, FL_H1 = 2, FL_O0 = 3, FL_O1 = 4 };
constexpr int L_WA = 0;
constexpr int L_WG = 86016;
constexpr int VB = 1040;
constexpr int KVT = 2048 + 2 * VB;
constexpr int L_KV = 65536 - 8 * KVT;
constexpr int RD = 8, NR = 12;
constexpr int PART = 16 * 66 * 16;
constexpr int L_CS = L_WG + 4 * PART;
constexpr int L_RS = L_CS + 3584;
constexpr int L_CNT = L_RS + 512;
constexpr int LDS_BYTES = L_CNT + 64;
constexpr int xbuf(int s) { return ((s + 1) % 3) * 16384; }
constexpr int qbuf(int s) { return 49152 + (s % 3) * 12288; }
constexpr int w1buf(int s) { return L_WG + (s % 3) * PART + 8192; }
static_assert(xbuf(7) == 32768 && qbuf(2) + 12288 <= L_WG && L_KV + 12 * KVT <= L_WG && PART >= RD * 2048 && LDS_BYTES <= 163840, "lds map");
#define AWIN 6
constexpr int AW = AWIN;
constexpr unsigned SPIN_CAP = 1u << 23;

#define MFMA32(a, b, c) __builtin_amdgcn_mfma_f32_32x32x16_bf16(__builtin_bit_cast(bf16x8, a), __builtin_bit_cast(bf16x8, b), c, 0, 0, 0)
#define MFMA16(a, b, c) __builtin_amdgcn_mfma_f32_16x16x32_bf16(__builtin_bit_cast(bf16x8, a), __builtin_bit_cast(bf16x8, b), c, 0, 0, 0)
#define SBAR() __builtin_amdgcn_sched_barrier(0)

#define STAMP_AT(i) do { } while (0)

DI float asf(unsigned u) { return __builtin_bit_cast(float, u); }
DI unsigned asu(float f) { return __builtin_bit_cast(unsigned, f); }
DI unsigned pk2(float lo, float hi) { return __builtin_bit_cast(unsigned, __builtin_convertvector((f32x2){lo, hi}, bf16x2_t)); }
DI float bflo(unsigned u) { return __builtin_bit_cast(float, u << 16); }
DI float bfhi(unsigned u) { return __builtin_bit_cast(float, u & 0xffff0000u); }
DI v4u make_srd(const void* p, unsigned bytes) { const u64 a = (u64)p; return (v4u){(unsigned)a, (unsigned)(a >> 32) & 0xffffu, bytes, 0x00020000u}; }
DI void dma16(v4u srd, unsigned voff, unsigned soff, unsigned lds) {
  asm volatile("s_mov_b32 m0, %3\n\ts_nop 0\n\tbuffer_load_dwordx4 %0, %1, %2 offen lds" :: "v"(voff), "s"(srd), "s"(soff), "s"(lds) : "memory");
}
DI void dma16_sc1(v4u srd, unsigned voff, unsigned soff, unsigned lds) {
  asm volatile("s_mov_b32 m0, %3\n\ts_nop 0\n\tbuffer_load_dwordx4 %0, %1, %2 offen sc1 lds" :: "v"(voff), "s"(srd), "s"(soff), "s"(lds) : "memory");
}
DI void dma16_a(v4u srd, unsigned voff, unsigned soff, unsigned lds) { dma16(srd, voff, soff, lds); }
DI void dma16_b(v4u srd, unsigned voff1, unsigned soff) {
  asm volatile("buffer_load_dwordx4 %0, %1, %2 offen offset:1024 lds" :: "v"(voff1), "s"(srd), "s"(soff) : "memory");
}
DI v4u ld16_sc1_async(v4u srd, unsigned voff, unsigned soff) {
  v4u r; asm volatile("buffer_load_dwordx4 %0, %1, %2, %3 offen sc1" : "=v"(r) : "v"(voff), "s"(srd), "s"(soff) : "memory"); return r;
}
DI v4u ld16_async(v4u srd, unsigned voff, unsigned soff) {
  v4u r; asm volatile("buffer_load_dwordx4 %0, %1, %2, %3 offen" : "=v"(r) : "v"(voff), "s"(srd), "s"(soff) : "memory"); return r;
}
DI unsigned ld2_async(v4u srd, unsigned voff, unsigned soff) {
  unsigned r; asm volatile("buffer_load_ushort %0, %1, %2, %3 offen" : "=v"(r) : "v"(voff), "s"(srd), "s"(soff) : "memory"); return r;
}
DI void st16_sc1(v4u v, v4u srd, unsigned voff, unsigned soff) {
  asm volatile("buffer_store_dwordx4 %0, %1, %2, %3 offen sc1\n\ts_nop 1" :: "v"(v), "v"(voff), "s"(srd), "s"(soff) : "memory");
}
template <int N> DI void wait_vm1(v4u& a) { asm volatile("s_waitcnt vmcnt(%1)" : "+v"(a) : "i"(N) : "memory"); }
template <int N> DI void wait_vm2(v4u& a, v4u& b) { asm volatile("s_waitcnt vmcnt(%2)" : "+v"(a), "+v"(b) : "i"(N) : "memory"); }
template <int N> DI void wait_vm3(v4u& a, v4u& b, v4u& c) { asm volatile("s_waitcnt vmcnt(%3)" : "+v"(a), "+v"(b), "+v"(c) : "i"(N) : "memory"); }
DI void pin_s(u64& v) { asm volatile("" : "+s"(v)); }
template <class V> DI void pin(V& v) { asm volatile("" : "+v"(v)); }
template <int N> DI void wait_vm() { asm volatile("s_waitcnt vmcnt(%0)" :: "i"(N) : "memory"); }
DI void bar() { asm volatile("s_waitcnt lgkmcnt(0)\n\ts_barrier" ::: "memory"); }
DI v4u lds128(unsigned a) { return *(const LAS v4u*)(size_t)a; }
DI void sts128(unsigned a, v4u v) { *(LAS v4u*)(size_t)a = v; }
DI v2u ldtr(unsigned a) { return __builtin_bit_cast(v2u, __builtin_amdgcn_ds_read_tr16_b64_v4i16((LAS s16x4*)(size_t)a)); }
DI v4u ldtr2(unsigned a, int second) { const v2u lo = ldtr(a), hi = ldtr(a + second); return (v4u){lo.x, lo.y, hi.x, hi.y}; }
template <int CTRL> DI float dpp(float v) { return __builtin_bit_cast(float, __builtin_amdgcn_update_dpp(0, __builtin_bit_cast(int, v), CTRL, 0xf, 0xf, true)); }
DI float rdl(float v, int l) { return __builtin_bit_cast(float, __builtin_amdgcn_readlane(__builtin_bit_cast(int, v), l)); }
DI v4u pack8(const f32x16& a, int s) { return (v4u){pk2(a[8 * s], a[8 * s + 1]), pk2(a[8 * s + 2], a[8 * s + 3]), pk2(a[8 * s + 4], a[8 * s + 5]), pk2(a[8 * s + 6], a[8 * s + 7])}; }
DI void poll_flags(const GAS u64* p, u64 tag) {
#pragma unroll 1
  for (unsigned spin = 0; spin < SPIN_CAP; ++spin) {
    const u64 v = __hip_atomic_load(p, RLX_AGENT);
    if (__all(v == tag)) break;
    __builtin_amdgcn_s_sleep(1);
  }
  asm volatile("" ::: "memory");
}
constexpr u64 upper_mask(int reg) {
  u64 m = 0;
  for (int l = 0; l < 64; ++l) { const int row = (reg & 3) + 8 * (reg >> 2) + 4 * (l >> 5); if (row > (l & 31)) m |= 1ull << l; }
  return m;
}
template <int REG> DI bool upper() { return __builtin_amdgcn_inverse_ballot_w64(upper_mask(REG)); }
template <int I, int N, class Fn> DI void static_for(Fn&& f) { if constexpr (I < N) { f(std::integral_constant<int, I>{}); static_for<I + 1, N>(f); } }
DI float max3(float a, float b, float c) { return __builtin_fmaxf(__builtin_fmaxf(a, b), c); }

constexpr int H1_CK = 16 - AW, H1_LD = H1_CK - 3, O_CK = 28 - AW, O_LD = O_CK - 3;
constexpr int g2_issued(int t) { return ((t >= 1 && t - 1 + NR + RD < 36) ? 2 : 0) + ((t + AW < 28) ? 2 : 0) + (t == H1_LD) + (t == O_LD) + (t == O_CK ? 10 : 0) + (t == 26); }
constexpr int g2_range(int a, int b) { int n = 0; for (int t = a; t < b; ++t) n += g2_issued(t); return n; }
#define GELU_PK(P) (P == 1)
#define PRIO_G2 2
#define PRIO_ATT 3
#define PRIO_G1 2
#define ROT r
#define CODEWARM 38000
#define MIDWARM 1
#define KVPUB 6
#define EXP2(v) __builtin_amdgcn_exp2f(v)
#define PIN(v) pin(v)
extern "C" __device__ float mul_unpacked(float, float) __asm("llvm.amdgcn.fmul.legacy");
#define MUL1(a, b) mul_unpacked(a, b)
#define FMA1(a, b, c) __builtin_amdgcn_div_fmasf(a, b, c, false)
DI v2u ld8_sc1_async(v4u srd, unsigned voff) {
  v2u r; asm volatile("buffer_load_dwordx2 %0, %1, %2, 0 offen sc1" : "=v"(r) : "v"(voff), "s"(srd) : "memory"); return r;
}
template <int N> DI void wait_vmf(v2u& a) { asm volatile("s_waitcnt vmcnt(%1)" : "+v"(a) : "i"(N) : "memory"); }
DI void arrive(unsigned cnt, int lane) {
  asm volatile("s_waitcnt lgkmcnt(0)" ::: "memory");
  if (lane == 0) __hip_atomic_fetch_add((LAS unsigned*)(size_t)cnt, 1u, __ATOMIC_RELAXED, __HIP_MEMORY_SCOPE_WORKGROUP);
}
DI void arrive_nowait(unsigned cnt) {
  asm volatile("s_mov_b64 exec, 1\n\tds_add_u32 %0, %1\n\ts_mov_b64 exec, -1" :: "v"(cnt), "v"(1u) : "memory");
}
DI unsigned peek(unsigned cnt) { return __hip_atomic_load((LAS unsigned*)(size_t)cnt, __ATOMIC_RELAXED, __HIP_MEMORY_SCOPE_WORKGROUP); }
DI void await(unsigned cnt, unsigned n) {
#pragma unroll 1
  for (unsigned spin = 0; spin < SPIN_CAP * 32; ++spin) {
    if (__builtin_amdgcn_readfirstlane(peek(cnt)) >= n) break;
    __builtin_amdgcn_s_sleep(1);
  }
  asm volatile("" ::: "memory");
}

__global__ __launch_bounds__(512, 2) void fused_block(const void* __restrict__ x, const void* __restrict__ wqkv, const void* __restrict__ wo,
                                                      const void* __restrict__ w1, const void* __restrict__ b1, const void* __restrict__ w2,
                                                      const void* __restrict__ b2, void* __restrict__ out, char* __restrict__ ws) {
  constexpr unsigned lds0 = 0;
  const int wave = __builtin_amdgcn_readfirstlane(threadIdx.x >> 6);
  const int m = wave & 3;
  const bool isG = wave < 4;
  const int xcd = blockIdx.x & 7, idx = blockIdx.x >> 3;
  const int r = (xcd >> 2) * 8 + (idx >> 2), j = (xcd & 3) * 4 + (idx & 3);
  const int rt = (xcd >> 1) * 8 + (idx >> 2), ct = (xcd & 1) * 4 + (idx & 3);
  int lane = threadIdx.x & 63, r32, h, g, q4, p4;
  STAMP_AT(0);
  asm volatile("" :: "s"(x), "s"(wqkv), "s"(wo), "s"(w1), "s"(b1), "s"(w2), "s"(b2), "s"(out), "s"(ws));

  const v4u sx = make_srd(x, SEQ * DM * 2), sws = make_srd(ws, WS_END);
  const __amdgpu_buffer_rsrc_t rws = __builtin_amdgcn_make_buffer_rsrc(ws, 0, WS_END, 0x00020000);
  const unsigned one2 = 0x3f803f80u;
  const int T = 4 * r + m;
  const int rot = ROT;
  auto flag_off = [&](int kind, int tile, int jj) { return WS_FLAG + ((kind * 64 + tile) * 16 + jj) * 128; };
  auto flag = [&](int kind, int tile, int jj) { return (GAS u64*)(ws + flag_off(kind, tile, jj)); };
  u64 tag;
  unsigned taglo, tag32;
  auto get_tag = [&]() {
    const v2u tg = *(const LAS v2u*)(size_t)(lds0 + L_CNT + 32);
    taglo = __builtin_amdgcn_readfirstlane(tg[0]); tag32 = __builtin_amdgcn_readfirstlane(tg[1]);
    tag = ((u64)tag32 << 32) | taglo;
  };
  v4u xres, bres, xaug, st;
  float mean, var, rstd, sd;
  v4u sel;
  unsigned xo[4], swz;
  auto lane_setup = [&]() {
    lane = threadIdx.x & 63; PIN(lane);
    r32 = lane & 31; h = lane >> 5; g = lane >> 4; q4 = (lane >> 2) & 3; p4 = lane & 3;
    const unsigned selv = ((lane & 15) == (g & 1)) ? one2 : 0u;
    sel = (v4u){selv, selv, selv, selv};
#pragma unroll
    for (int p = 0; p < 4; ++p) xo[p] = (32 * m + r32) * 128 + (((2 * ((p + m) & 3) + h) ^ ((r32 >> 1) & 7)) * 16);
    swz = ((((lane >> 2) & 1) ^ (g & 1)) * 64) + p4 * 16;
  };

  auto stat_load = [&]() { st = ld16_sc1_async(sws, WS_STATS + (128 * r + 32 * m + r32) * 16, 0); };
  auto res_loads = [&]() {
    int t2 = threadIdx.x; PIN(t2);
    const int row = 64 * rt + (t2 >> 3), col = 64 * ct + (t2 & 7) * 8;
    xres = ld16_async(sx, row * 1024 + col * 2, 0);
    bres = ld16_async(make_srd(b2, DM * 2), col * 2, 0);
  };
  f32x4 lo, hi;
  auto res_sum = [&]() {
    PIN(xres); PIN(bres);
    lo = (f32x4){bflo(xres[0]) + bflo(bres[0]), bfhi(xres[0]) + bfhi(bres[0]), bflo(xres[1]) + bflo(bres[1]), bfhi(xres[1]) + bfhi(bres[1])};
    hi = (f32x4){bflo(xres[2]) + bflo(bres[2]), bfhi(xres[2]) + bfhi(bres[2]), bflo(xres[3]) + bflo(bres[3]), bfhi(xres[3]) + bfhi(bres[3])};
    PIN(lo); PIN(hi);
  };
  auto row_stats = [&]() {
    if (!__all(st[1] == tag32 && st[3] == tag32)) {
#pragma unroll 1
      for (unsigned spin = 0; spin < SPIN_CAP; ++spin) {
        st = ld16_sc1_async(sws, WS_STATS + (128 * r + 32 * m + r32) * 16, 0);
        wait_vm1<0>(st);
        if (__all(st[1] == tag32 && st[3] == tag32)) break;
      }
    }
    mean = asf(st[0]); var = asf(st[2]);
    PIN(mean); PIN(var);
  };
  auto make_xaug = [&]() {
    PIN(mean); PIN(var);
    rstd = __builtin_amdgcn_rsqf(var); sd = var * rstd;
    const unsigned uh = pk2(-mean, sd);
    const unsigned ul = pk2(-mean - bflo(uh), FMA1(bfhi(uh), -1.f, sd));
    xaug = (v4u){(uh & 0xffffu) | (uh << 16), (ul & 0xffffu) | (uh & 0xffff0000u), ul >> 16, 0u};
    if (h) xaug = (v4u){0u, 0u, 0u, 0u};
    PIN(xaug);
  };
  auto cswr = [&](int tile, f32x4 c) {
    if (lane < 16) { LAS float* p = (LAS float*)(size_t)(lds0 + L_CS + (tile * 32 + lane) * 16 + m * 4); p[0] = c[0]; p[64] = c[1]; }
  };
  auto csrd = [&](int tile) { return __builtin_bit_cast(f32x4, lds128(lds0 + L_CS + (tile * 32 + r32) * 16)); };
  auto waug = [&](f32x4 c4, unsigned b16) {
    const float c = ((c4[0] + c4[1]) + c4[2]) + c4[3];
    const unsigned uh = pk2(c, 0.f);
    const unsigned ul = pk2(c - bflo(uh), 0.f);
    return (v4u){uh | (ul << 16), uh | (b16 << 16), b16, 0u};
  };

  const v4u sw1 = make_srd(w1, DM * FF * 2);
  const int hcol0 = 448 * (j >> 2) + 128 * (j & 3);
  const bool heavy = (j & 3) != 3;
  unsigned bias0, bias1;
  auto bias_load = [&](int P) {
    bias0 = ld2_async(make_srd(b1, FF * 2), r32 * 2, (hcol0 + 64 * P) * 2);
    bias1 = ld2_async(make_srd(b1, FF * 2), r32 * 2, (hcol0 + 64 * P + 32) * 2);
  };
  auto w1dma = [&](int P, int c, unsigned dst, int half) {
    const unsigned vw1 = (lane >> 3) * (FF * 2) + swz, vw1b = vw1 + 8 * (FF * 2) - 1024;
    const unsigned so = (64 * ((c + rot) & 7) + 16 * m) * (FF * 2) + (hcol0 + 64 * P) * 2;
    if (half == 0) dma16_a(sw1, vw1, so, dst + m * 2048); else dma16_b(sw1, vw1b, so);
  };
  f32x16 hac[2];
  unsigned rk[4][2];
  auto hsetup = [&](int P) {
#pragma unroll
    for (int p = 0; p < 4; ++p)
#pragma unroll
      for (int c = 0; c < 2; ++c) {
        rk[p][c] = lds0 + (P ? L_WA : w1buf(0)) + ((p + m) & 3) * 2048 + h * 1024 + q4 * 128 + ((c ^ (q4 >> 1)) * 64) + (g & 1) * 32 + p4 * 8;
        PIN(rk[p][c]);
      }
  };
  auto hmm = [&](auto pc, const v4u (&xf)[32], auto&& hook) {
    constexpr int P = pc.value;
    constexpr int D = 4;
    const unsigned cnt = lds0 + L_CNT;
    f32x4 cs[2] = {}, c0, c1;
    v4u wf[D][2], au0, au1;
    unsigned seen = 0;
    hac[0] = (f32x16){}; hac[1] = (f32x16){};
    auto rd = [&](int ks, int t) { wf[ks % D][t] = ldtr2(rk[ks & 3][t] + (ks >> 2) * 8192, 512); };
#pragma unroll
    for (int ks = 0; ks < D; ++ks) { rd(ks, 0); rd(ks, 1); }
    SBAR();
    static_for<0, 32>([&](auto kc) {
      constexpr int ks = kc.value;
      hook(kc);
      if constexpr (ks == 10) seen = peek(cnt + 20);
      if constexpr (ks == 29) seen = peek(cnt);
#pragma unroll
      for (int t = 0; t < 2; ++t) {
        hac[t] = MFMA32(wf[ks % D][t], xf[ks], hac[t]);
        if ((ks & 3) == 0 && ks < 28) { cs[t] = MFMA16(sel, wf[ks % D][t], cs[t]); PIN(cs[t]); }
        if (ks == 26) { cs[t] = MFMA16(sel, wf[28 % D][t], cs[t]); PIN(cs[t]); }
        SBAR();
        if constexpr (ks + D < 32) rd(ks + D, t);
        if constexpr (ks == 31) { if (t == 0) { au0 = waug(c0, bias0); PIN(au0); } else { au1 = waug(c1, bias1); PIN(au1); } }
        SBAR();
      }
      if constexpr (ks == 11 || ks == 30) {
        PIN(hac[0]); PIN(hac[1]);
        if (__builtin_amdgcn_readfirstlane(seen) < 4) await(ks == 11 ? cnt + 20 : cnt, 4);
        asm volatile("" ::: "memory");
        if constexpr (ks == 30) { c0 = csrd(2 * P); c1 = csrd(2 * P + 1); }
      }
      if constexpr (ks == 27) { cswr(2 * P, cs[0]); cswr(2 * P + 1, cs[1]); arrive_nowait(cnt); }
      SBAR();
    });
    hac[0] = MFMA32(au0, xaug, hac[0]);
    hac[1] = MFMA32(au1, xaug, hac[1]);
    SBAR();
  };
  auto hgelu = [&](auto pc, auto&& mid) {
    constexpr int P = pc.value;
    constexpr bool PK = GELU_PK(P);
    const float C0 = 2.3022081986f, C1 = C0 * 0.044715f;
    const float ga = -C1 * rstd * rstd * rstd, gb = -C0 * rstd;
    auto mul = [&](f32x2 a, f32x2 b) { return PK ? a * b : (f32x2){MUL1(a[0], b[0]), MUL1(a[1], b[1])}; };
    auto fma = [&](f32x2 a, float b, float c) {
      return PK ? __builtin_elementwise_fma(a, (f32x2){b, b}, (f32x2){c, c}) : (f32x2){FMA1(a[0], b, c), FMA1(a[1], b, c)}; };
#pragma unroll
    for (int t = 0; t < 2; ++t) {
      f32x2 v[8], z[8];
#pragma unroll
      for (int e = 0; e < 8; ++e) { v[e] = (f32x2){hac[t][2 * e], hac[t][2 * e + 1]}; z[e] = mul(v[e], v[e]); }
      SBAR();
#pragma unroll
      for (int e = 0; e < 8; ++e) z[e] = fma(z[e], ga, gb);
      SBAR();
#pragma unroll
      for (int e = 0; e < 8; ++e) z[e] = mul(z[e], v[e]);
      SBAR();
#pragma unroll
      for (int e = 0; e < 8; ++e) z[e] = (f32x2){EXP2(z[e][0]), EXP2(z[e][1])};
      SBAR();
#pragma unroll
      for (int e = 0; e < 8; ++e) z[e] = fma(z[e], sd, sd);
      SBAR();
#pragma unroll
      for (int e = 0; e < 8; ++e) z[e] = (f32x2){__builtin_amdgcn_rcpf(z[e][0]), __builtin_amdgcn_rcpf(z[e][1])};
      SBAR();
#pragma unroll
      for (int e = 0; e < 8; ++e) z[e] = mul(z[e], v[e]);
      SBAR();
#pragma unroll
      for (int s = 0; s < 2; ++s) {
        const v4u f = {pk2(z[4 * s][0], z[4 * s][1]), pk2(z[4 * s + 1][0], z[4 * s + 1][1]), pk2(z[4 * s + 2][0], z[4 * s + 2][1]), pk2(z[4 * s + 3][0], z[4 * s + 3][1])};
        st16_sc1(f, sws, lane * 16, WS_AF + (T * AFT + (hcol0 >> 4) + 4 * P + 2 * t + s) * 1024);
      }
      if (t == 0) mid();
      SBAR();
    }
  };

  if (isG) {
    lane_setup();
    const v4u sw2 = make_srd(w2, FF * DM * 2), swo = make_srd(wo, DM * DM * 2);
    const unsigned vw2 = (lane >> 3) * 1024 + swz, vw2b = vw2 + 7 * 1024;
    const int ro3 = rt % 3;
    auto kmap = [&](int kk) {
      if (kk < 16) return 8 * (((kk >> 2) + rt) & 3) + (kk & 3);
      const int p = ((kk - 16) >> 2) + ro3;
      return 8 * (p >= 3 ? p - 3 : p) + 4 + (kk & 3);
    };
    auto omap = [&](int i) { return 2 * ((i + rt) & 3) + (i >> 2); };
    auto wdmah = [&](int kk, int half) {
      const unsigned so = (kk < 28 ? 448 * m + 16 * kmap(kk) : 128 * m + 16 * omap(kk - 28)) * 1024 + 128 * ct;
      if (half == 0) dma16_a(kk < 28 ? sw2 : swo, vw2, so, lds0 + L_WG + m * PART + (kk % RD) * 2048); else dma16_b(kk < 28 ? sw2 : swo, vw2b, so);
    };
    auto wdma = [&](int kk) { wdmah(kk, 0); wdmah(kk, 1); };
    {
      const v4u swq = make_srd(wqkv, DM * 3 * DM * 2);
      const unsigned vx0 = (lane >> 3) * 1024 + (((lane & 7) ^ g) * 16), vx1 = (7 + (lane >> 3)) * 1024 + (((lane & 7) ^ (4 + g)) * 16);
      const unsigned vwq = (lane >> 2) * (3 * DM * 2) + p4 * 16;
      auto piece = [&](int s, int i) {
        const int kc = (s + rot) & 7;
        if (i < 4) {
          const unsigned so = (128 * r + 32 * m + 16 * (i >> 1)) * 1024 + kc * 128;
          if (i & 1) dma16_b(sx, vx1, so); else dma16_a(sx, vx0, so, lds0 + xbuf(s) + m * 4096 + (i >> 1) * 2048);
        } else if (i < 7) dma16(swq, vwq, (64 * kc + 16 * m) * (3 * DM * 2) + (512 * (i - 4) + 32 * j) * 2, lds0 + qbuf(s) + ((i - 4) * 4 + m) * 1024);
        else w1dma(0, s, lds0 + w1buf(s), i - 7);
      };
#pragma unroll
      for (int i = 0; i < 18; ++i) { piece(i / 9, i % 9); if (i == 8) __builtin_amdgcn_s_barrier(); }
      __builtin_amdgcn_s_setprio(PRIO_G1);
      hsetup(0);
      f32x4 cs[2] = {};
      v4u xf[4], wf[4][2];
      hac[0] = (f32x16){}; hac[1] = (f32x16){};
      static_for<0, 8>([&](auto sc) {
        constexpr int s = sc.value;
        auto fill = [&](int i) {
          if constexpr (s < 6) piece(s + 2, i);
          if constexpr (s == 6) {
            if (i == 0) { stat_load(); bias_load(0); }
            else if (i < 5) { if (heavy) w1dma(1, (i - 1) >> 1, lds0 + L_WA + ((i - 1) >> 1) * 8192, (i - 1) & 1); }
            else wdmah((i - 5) >> 1, (i - 5) & 1);
          }
          if constexpr (s == 7) {
            if (heavy && i < 4) w1dma(1, 2 + (i >> 1), lds0 + L_WA + (2 + (i >> 1)) * 8192, i & 1);
            if (heavy && i >= 4) { w1dma(1, i, lds0 + L_WA + i * 8192, 0); w1dma(1, i, 0, 1); }
          }
          SBAR();
        };
        auto rd = [&](int p) {
          xf[p] = lds128(xbuf(s) + xo[p]);
          wf[p][0] = ldtr2(rk[p][0] + (s % 3) * PART, 512); wf[p][1] = ldtr2(rk[p][1] + (s % 3) * PART, 512);
          SBAR();
        };
        if constexpr (s < 7) wait_vm<9>(); else { if (heavy) wait_vm<11>(); else wait_vm<7>(); }
        bar();
        STAMP_AT(10 + s);
        rd(0);
        if constexpr (s < 7) fill(0);
        rd(1);
        if constexpr (s < 7) fill(1);
        if constexpr (s == 2) get_tag();
#pragma unroll
        for (int p = 0; p < 4; ++p) {
#pragma unroll
          for (int t = 0; t < 2; ++t) {
            hac[t] = MFMA32(wf[p][t], xf[p], hac[t]);
            if (p == 0) { cs[t] = MFMA16(sel, wf[p][t], cs[t]); PIN(cs[t]); }
            SBAR();
            if (p < 2 && t == 0) rd(p + 2);
            if (s == 7) fill(2 * p + t); else if (2 * p + t < 7) fill(2 + 2 * p + t);
          }
          if (s == 7 && p == 1) {
            cswr(0, cs[0]); cswr(1, cs[1]);
            bar();
            STAMP_AT(1);
          }
        }
        if constexpr (s == 7) {
          const f32x4 c0 = csrd(0), c1 = csrd(1);
          if (heavy) wait_vm<20>(); else wait_vm<4>();
          PIN(st); PIN(bias0); PIN(bias1);
          row_stats(); make_xaug();
          hac[0] = MFMA32(waug(c0, bias0), xaug, hac[0]);
          hac[1] = MFMA32(waug(c1, bias1), xaug, hac[1]);
          SBAR();
        }
      });
    }
    STAMP_AT(18);
    if (heavy) { wait_vm<8>(); arrive_nowait(lds0 + L_CNT + 16); }
    hgelu(std::integral_constant<int, 0>{}, [&]() {
      wait_vm<2>();
      if (heavy) arrive_nowait(lds0 + L_CNT + 20);
    });
    __builtin_amdgcn_s_setprio(0);
    STAMP_AT(19);
    unsigned rb2[2];
#pragma unroll
    for (int c = 0; c < 2; ++c) rb2[c] = lds0 + L_WG + m * PART + h * 512 + q4 * 128 + ((c ^ (q4 >> 1)) * 64) + (g & 1) * 32 + p4 * 8;
    v4u wq[36][2];
    auto wrd = [&](int kk) {
#pragma unroll
      for (int c = 0; c < 2; ++c) wq[kk][c] = ldtr2(rb2[c] + (kk % RD) * 2048, 1024);
    };
    auto wgot = [&](int kk) { PIN(wq[kk][0]); PIN(wq[kk][1]); };
    const int ftile = 2 * rt + ((lane >> 2) & 1);
    v4u af[28][2], of[8][2];
    v2u fl;
    const unsigned a0 = WS_AF + (2 * rt * AFT + 28 * m) * 1024, o0 = WS_AF + (2 * rt * AFT + 112 + 8 * m) * 1024;
    for (int kk = 2; kk < 6; ++kk) wdma(kk);
    wait_vm<8>();
    if (lane == 0) __hip_atomic_store(flag(FL_H0, T, j), tag, RLX_AGENT);
    STAMP_AT(2);
    wdma(6); wdma(7);
    static_for<0, 3>([&](auto ic) {
      constexpr int i = ic.value;
      wait_vm<(i == 0) ? 9 : 8>();
      for (int kk = 4 * i; kk < 4 * i + 4; ++kk) wrd(kk);
      for (int kk = 4 * i; kk < 4 * i + 4; ++kk) wgot(kk);
      if constexpr (i == 2) fl = ld8_sc1_async(sws, flag_off(FL_H0, ftile, 4 * m + p4));
      for (int kk = 4 * i; kk < 4 * i + 4; ++kk) wdma(kk + RD);
    });
    STAMP_AT(3);
    wait_vmf<8>(fl);
    if (!__all(fl[0] == taglo && fl[1] == tag32)) poll_flags(flag(FL_H0, ftile, 4 * m + p4), tag);
    STAMP_AT(4);
#pragma unroll
    for (int kk = 0; kk < AW; ++kk) {
      af[kk][0] = ld16_sc1_async(sws, lane * 16, a0 + kmap(kk) * 1024);
      af[kk][1] = ld16_sc1_async(sws, lane * 16, a0 + (AFT + kmap(kk)) * 1024);
    }
    __builtin_amdgcn_s_setprio(PRIO_G2);
    f32x16 c2[2][2] = {};
    auto mm2 = [&](int kk, const v4u (&a)[2], auto&& fill) {
      SBAR();
#pragma unroll
      for (int i = 0; i < 4; ++i) { c2[i >> 1][i & 1] = MFMA32(wq[kk][i & 1], a[i >> 1], c2[i >> 1][i & 1]); SBAR(); fill(i); SBAR(); }
    };
    static_for<0, 28>([&](auto kc) {
      constexpr int kk = kc.value;
      wait_vm2<(kk < AW) ? 2 * (AW - 1 - kk) + g2_range(0, kk) : g2_range(kk - AW + 1, kk)>(af[kk][0], af[kk][1]);
      if constexpr (kk == H1_CK) {
        STAMP_AT(20);
        wait_vmf<g2_range(H1_LD, H1_CK) - 1>(fl);
        if (!__all(fl[0] == taglo && fl[1] == tag32)) poll_flags(flag(FL_H1, ftile, 4 * m + p4), tag);
        STAMP_AT(21);
      }
      if constexpr (kk == H1_LD) fl = ld8_sc1_async(sws, flag_off(FL_H1, ftile, 4 * m + p4));
      if constexpr (kk == O_LD) fl = ld8_sc1_async(sws, flag_off(FL_O0, ftile, 4 * m + p4));
      if constexpr (kk == 26) fl = ld8_sc1_async(sws, flag_off(FL_O1, ftile, 4 * m + p4));
      constexpr bool dw = kk >= 1 && kk - 1 + NR + RD < 36;
      if constexpr (dw) wgot(kk - 1 + NR);
      mm2(kk, af[kk], [&](int i) {
        if constexpr (dw) { if (i < 2) wdmah(kk - 1 + NR + RD, i); }
        if constexpr (kk + NR < 28) { if (i == 1) wrd(kk + NR); }
        if constexpr (kk == 27) { if (i == 0) wrd(28); }
        if constexpr (kk + AW < 28) { if (i >= 2) af[kk + AW][i - 2] = ld16_sc1_async(sws, lane * 16, a0 + (AFT * (i - 2) + kmap(kk + AW)) * 1024); }
      });
      if constexpr (kk == O_CK) {
        wait_vmf<g2_range(O_LD, O_CK) - 1>(fl);
        if (!__all(fl[0] == taglo && fl[1] == tag32)) poll_flags(flag(FL_O0, ftile, 4 * m + p4), tag);
        res_loads();
#pragma unroll
        for (int i = 0; i < 4; ++i) {
          of[i][0] = ld16_sc1_async(sws, lane * 16, o0 + omap(i) * 1024);
          of[i][1] = ld16_sc1_async(sws, lane * 16, o0 + (AFT + omap(i)) * 1024);
        }
      }
    });
    static_for<0, 4>([&](auto kc) {
      constexpr int kk = kc.value;
      if (kk < 3) wrd(28 + kk + 1);
      if constexpr (kk == 1) {
        res_sum();
        STAMP_AT(5);
        wait_vmf<0>(fl);
        if (!__all(fl[0] == taglo && fl[1] == tag32)) poll_flags(flag(FL_O1, ftile, 4 * m + p4), tag);
        STAMP_AT(6);
#pragma unroll
        for (int i = 0; i < 8; ++i) of[4 + (i & 3)][i >> 2] = ld16_sc1_async(sws, lane * 16, o0 + (AFT * (i >> 2) + omap(4 + (i & 3))) * 1024);
      }
      wait_vm2<(kk == 0) ? 7 : 14 - 2 * kk>(of[kk][0], of[kk][1]);
      mm2(28 + kk, of[kk], [&](int i) { PIN(c2[i >> 1][i & 1]); if (kk == 3) wrd(32 + i); });
    });
    for (int kk = 32; kk < 36; ++kk) wgot(kk);
    unsigned pa = lds0 + L_WG + m * PART + lane * 16;
    PIN(pa);
    auto put = [&](int qn, int qd) {
      const f32x16& c = c2[qn >> 1][qn & 1];
      sts128(pa + (qn * 4 + qd) * 66 * 16, __builtin_bit_cast(v4u, (f32x4){c[4 * qd], c[4 * qd + 1], c[4 * qd + 2], c[4 * qd + 3]}));
    };
    static_for<0, 4>([&](auto qc) {
      constexpr int qn = qc.value;
      if constexpr ((qn & 1) == 0) { wait_vm2<qn ? 0 : 4>(of[4][qn >> 1], of[5][qn >> 1]); PIN(of[6][qn >> 1]); PIN(of[7][qn >> 1]); }
      SBAR();
#pragma unroll
      for (int i = 0; i < 4; ++i) {
        c2[qn >> 1][qn & 1] = MFMA32(wq[32 + i][qn & 1], of[4 + i][qn >> 1], c2[qn >> 1][qn & 1]);
        SBAR();
        if (qn > 0 && i >= 2) { put(qn - 1, 2 * i - 4); put(qn - 1, 2 * i - 3); }
        SBAR();
      }
    });
#pragma unroll
    for (int qd = 0; qd < 4; ++qd) put(3, qd);
    STAMP_AT(7);
  } else {
    __builtin_amdgcn_s_setprio(1);
    lane_setup();
    v4u xr[2];
    xr[0] = ld16_async(sx, lane * 16, (128 * r + 8 * j + 2 * m) * 1024); xr[1] = ld16_async(sx, lane * 16, (128 * r + 8 * j + 2 * m + 1) * 1024);
    unsigned warm;
    {
      u64 pc; asm volatile("s_getpc_b64 %0" : "=s"(pc));
      warm = ld2_async(make_srd((const void*)(pc & ~127ull), CODEWARM), lane < 3 ? lane * 128 : 0x40000000u, (idx * 4 + m) * 384);
    }
    __builtin_amdgcn_s_barrier();
    v2u ep = ld8_sc1_async(sws, m == 0 ? WS_EPOCH : 0x40000000u);
    float sval;
    const int wi = blockIdx.x * 4 + m;
    const u64 wbase = wi < 448 ? (u64)w1 + wi * 4096 : wi < 896 ? (u64)w2 + (wi - 448) * 4096 : (u64)wo + (wi - 896) * 4096;
    auto make_stats = [&]() {
      wait_vm2<1>(xr[0], xr[1]);
      PIN(warm);
      float piv[2], a[4];
#pragma unroll
      for (int u = 0; u < 2; ++u) {
        piv[u] = rdl(bflo(xr[u][0]), 0);
        float s1 = 0.f, s2 = 0.f;
#pragma unroll
        for (int i = 0; i < 8; ++i) { const float d = ((i & 1) ? bfhi(xr[u][i >> 1]) : bflo(xr[u][i >> 1])) - piv[u]; s1 += d; s2 = __builtin_fmaf(d, d, s2); }
        a[2 * u] = s1; a[2 * u + 1] = s2;
      }
#pragma unroll
      for (int i = 0; i < 4; ++i) a[i] += dpp<0xB1>(a[i]);
#pragma unroll
      for (int i = 0; i < 4; ++i) a[i] += dpp<0x4E>(a[i]);
      float t = p4 == 0 ? a[0] : p4 == 1 ? a[1] : p4 == 2 ? a[2] : a[3];
      t += dpp<0x114>(t); t += dpp<0x118>(t);
      const auto s16 = __builtin_amdgcn_permlane16_swap(asu(t), asu(t), false, false);
      t = asf(s16[0]) + asf(s16[1]);
      const auto s32 = __builtin_amdgcn_permlane32_swap(asu(t), asu(t), false, false);
      t = (asf(s32[0]) + asf(s32[1])) * (1.f / DM);
      const float e1 = dpp<0xA0>(t);
      sval = (p4 & 1) ? __builtin_fmaxf(__builtin_fmaf(-e1, e1, t), 0.f) + 1e-5f : t + (p4 ? piv[1] : piv[0]);
      PIN(sval);
    };
    const unsigned acnt = lds0 + L_CNT;
    const unsigned vpos = 2048 + (g & 1) * 512 + h * 256 + (lane & 15) * 16;
    v4u qf[2], am0, am1;
    u64 um[16];
    auto masks = [&]() {
      const int dq = r32 - 4 * h;
#pragma unroll
      for (int u = 0; u < 16; ++u) { um[u] = __builtin_amdgcn_ballot_w64((u & 3) + 8 * (u >> 2) > dq); pin_s(um[u]); }
#pragma unroll
      for (int d = 0; d < 4; ++d) {
        am0[d] = __builtin_amdgcn_inverse_ballot_w64(um[2 * d]) ? 0xffffffffu : __builtin_amdgcn_inverse_ballot_w64(um[2 * d + 1]) ? 0xffff0000u : 0u;
        am1[d] = __builtin_amdgcn_inverse_ballot_w64(um[8 + 2 * d]) ? 0xffffffffu : __builtin_amdgcn_inverse_ballot_w64(um[9 + 2 * d]) ? 0xffff0000u : 0u;
      }
      PIN(am0); PIN(am1);
    };
    {
      v4u xf[32];
      f32x16 acc[3] = {};
      f32x4 cs[3] = {};
      v4u wf[4][3];
      unsigned wb[4];
#pragma unroll
      for (int p = 0; p < 4; ++p) { wb[p] = qbuf(0) + ((p + m) & 3) * 1024 + (8 * h + q4) * 64 + (g & 1) * 32 + p4 * 8; PIN(wb[p]); }
      auto rd = [&](int ks, int t) {
        if (t == 0) xf[ks] = lds128(xbuf(ks >> 2) + xo[ks & 3]);
        wf[ks & 3][t] = ldtr2(wb[ks & 3] + ((ks >> 2) % 3) * 12288 + t * 4096, 256);
      };
      auto mm = [&](int ks, int t) {
        acc[t] = t == 2 ? MFMA32(xf[ks], wf[ks & 3][t], acc[t]) : MFMA32(wf[ks & 3][t], xf[ks], acc[t]);
        if ((ks & 3) == 0) { cs[t] = MFMA16(sel, wf[ks & 3][t], cs[t]); PIN(cs[t]); }
        SBAR();
      };
      make_stats();
      bar();
      STAMP_AT(10);
#pragma unroll
      for (int t = 0; t < 6; ++t) rd(t / 3, t % 3);
      SBAR();
      static_for<0, 8>([&](auto sc) {
        constexpr int s = sc.value;
#pragma unroll
        for (int p = 0; p < 2; ++p) {
#pragma unroll
          for (int t = 0; t < 3; ++t) { mm(4 * s + p, t); rd(4 * s + p + 2, t); SBAR(); }
          if (s == 7 && p == 0) { cswr(4, cs[0]); cswr(5, cs[1]); cswr(6, cs[2]); SBAR(); }
        }
        if constexpr (s == 0) {
          wait_vmf<0>(ep);
          if (m == 0 && lane < 10)
            *(LAS unsigned*)(size_t)(lds0 + L_CNT + lane * 4) = lane == 8 ? ep[0] * 0x9E3779B1u + 0x7F4A7C15u : lane == 9 ? ep[1] * 0x85EBCA77u + 0x165667B1u : 0u;
        }
        if constexpr (s == 1) {
          get_tag();
          if ((lane >> 2) == 3) {
            const v2u gr = {asu(sval), tag32};
            __builtin_amdgcn_raw_buffer_store_b64(gr, rws, WS_STATS + (128 * r + 8 * j + 2 * m) * 16 + p4 * 8, 0, 16);
          }
        }
        if constexpr (s == MIDWARM) {
          warm = ld2_async(make_srd((const void*)wbase, 4096), lane * 128, 0);
        }
        if constexpr (s == 5) stat_load();
        if constexpr (s == 6) {
          wait_vm1<0>(st);
          PIN(warm);

          row_stats();
        }
        mm(4 * s + 2, 0); mm(4 * s + 2, 1);
        bar();
        if constexpr (s < 7) {
          STAMP_AT(11 + s);
          mm(4 * s + 2, 2);
          rd(4 * s + 4, 0); rd(4 * s + 4, 1); rd(4 * s + 4, 2); SBAR();
#pragma unroll
          for (int t = 0; t < 3; ++t) { mm(4 * s + 3, t); rd(4 * s + 5, t); SBAR(); }
        }
      });
      STAMP_AT(1);
      __builtin_amdgcn_s_setprio(3);
      const f32x4 c5 = csrd(5), c6 = csrd(6), c4 = csrd(4);
      SBAR();
      mm(30, 2); mm(31, 1);
      make_xaug();
      SBAR();
      mm(31, 2); mm(31, 0);
      if (h == 0) *(LAS float*)(size_t)(lds0 + L_RS + (m * 32 + r32) * 4) = rstd;
      acc[1] = MFMA32(waug(c5, 0u), xaug, acc[1]);
      acc[2] = MFMA32(xaug, waug(c6, 0u), acc[2]);
      acc[0] = MFMA32(waug(c4, 0u), xaug, acc[0]);
      SBAR();
      f32x4 rs[4];
#pragma unroll
      for (int qd = 0; qd < 4; ++qd) rs[qd] = __builtin_bit_cast(f32x4, lds128(lds0 + L_RS + (m * 32 + 8 * qd + 4 * h) * 4));
      const unsigned kvo = WS_KV + (j * 64 + T) * 4096;
#pragma unroll
      for (int e = 0; e < 16; ++e) acc[1][e] = MUL1(acc[1][e], rstd);
#pragma unroll
      for (int e = 0; e < 2; ++e) {
        const v4u f = pack8(acc[1], e);
        st16_sc1(f, sws, lane * 16, kvo + e * 1024);
        sts128(lds0 + L_KV + (8 + m) * KVT + e * 1024 + lane * 16, f);
      }
#pragma unroll
      for (int qd = 0; qd < 4; ++qd)
#pragma unroll
        for (int e = 0; e < 4; ++e) acc[2][4 * qd + e] = MUL1(acc[2][4 * qd + e], rs[qd][e]);
#pragma unroll
      for (int s = 0; s < 2; ++s) {
        const v4u f = pack8(acc[2], s);
        st16_sc1(f, sws, vpos, kvo + s * 1024);
        sts128(lds0 + L_KV + (8 + m) * KVT + s * VB + vpos, f);
      }
      SBAR();
      const float qs = rstd * (0.25f * 1.4426950408889634f);
#pragma unroll
      for (int e = 0; e < 16; ++e) acc[0][e] = MUL1(acc[0][e], qs);
      qf[0] = pack8(acc[0], 0); qf[1] = pack8(acc[0], 1);
      PIN(qf[0]); PIN(qf[1]);
      STAMP_AT(2);
      const int T0 = 4 * r - 8 + 2 * m;
      const bool live = T0 >= 0;
      auto kv_fetch = [&]() {
        const unsigned src = WS_KV + (j * 64 + (live ? T0 : 0)) * 4096, vk = live ? lane * 16 : h * 512, vv = live ? lane * 16 : 0x40000000u;
#pragma unroll
        for (int i = 0; i < 8; ++i)
          dma16_sc1(sws, (i & 2) ? vv : vk, src + (live ? i : (i & 3)) * 1024, lds0 + L_KV + (2 * m + (i >> 2)) * KVT + (i & 3) * 1024 + ((i & 3) == 3 ? VB - 1024 : 0));
        const unsigned on = (lane < 2 || live) ? one2 : 0u;
        if (lane < 6) sts128(lds0 + L_KV + (lane < 2 ? 8 + m : 2 * m + ((lane - 2) >> 1)) * KVT + 2048 + (lane & 1) * VB + 1024, (v4u){on, on, on, on});
      };
      if (heavy) {
        hsetup(1);
        await(acnt + 16, 4);
        STAMP_AT(3);
        v2u fl;
        hmm(std::integral_constant<int, 1>{}, xf, [&](auto kc) {
          constexpr int ks = kc.value;
          if constexpr (ks == KVPUB) {
            wait_vm<0>();
            if (lane == 0) __hip_atomic_store(flag(FL_KV, T, j), tag, RLX_AGENT);
          }
          if constexpr (ks == KVPUB + 2) bias_load(1);
          if constexpr (ks == 31) { wait_vm<1>(); PIN(bias0); PIN(bias1); }
          if constexpr (ks == 24) fl = ld8_sc1_async(sws, flag_off(FL_KV, live ? T0 + (lane & 1) : 0, j));
        });
        STAMP_AT(18);
        wait_vmf<0>(fl);
        if (!__all(fl[0] == taglo && fl[1] == tag32)) poll_flags(flag(FL_KV, live ? T0 + (lane & 1) : 0, j), tag);
        kv_fetch();
        hgelu(std::integral_constant<int, 1>{}, [&]() {});
        STAMP_AT(4);
        wait_vm<4>();
      } else {
        wait_vm<0>();
        if (lane == 0) __hip_atomic_store(flag(FL_KV, T, j), tag, RLX_AGENT);
        poll_flags(flag(FL_KV, live ? T0 + (lane & 1) : 0, j), tag);
        kv_fetch(); wait_vm<0>();
      }
      arrive_nowait(acnt);
    }
    masks();
    STAMP_AT(19);
    const unsigned kb = lds0 + L_KV + m * KVT + lane * 16;
    unsigned vb[2];
#pragma unroll
    for (int e = 0; e < 2; ++e) { vb[e] = lds0 + L_KV + m * KVT + 2048 + ((g & 1) ? 1024 : e * 512 + h * 256 + (lane & 15) * 16); PIN(vb[e]); }
    await(acnt, heavy ? 8 : 4);
    __builtin_amdgcn_s_setprio(PRIO_ATT);
    STAMP_AT(5);
    const f32x16 zero = {};
    auto o_store = [&](f32x16& o, int e) {
      const float rl = __builtin_amdgcn_rcpf(o[8]);
#pragma unroll
      for (int u = 0; u < 8; ++u) o[u] = MUL1(o[u], rl);
      st16_sc1(pack8(o, 0), sws, lane * 16, WS_AF + (T * AFT + 112 + 2 * j + e) * 1024);
    };
    int redo = 0;
    {
      f32x16 o[2], sa, sb, su;
      bool pend = false;
      v4u kn, ku, va, vc, vn0, vn1, vu0, vu1, p0, p1, pu;
      auto kaddr = [&](int n) { return kb + ((n & 7) + 1) * KVT + (n >> 3) * 1024; };
      auto vaddr = [&](int n) { return vb[n >> 3] + ((n & 7) + 1) * KVT; };
      kn = lds128(kaddr(0)); vn0 = lds128(vaddr(0)); vn1 = lds128(vaddr(0) + VB);
      sa = MFMA32(kn, qf[0], zero);
      kn = lds128(kaddr(1));
      SBAR();
      auto step = [&](auto nc, f32x16& sc, f32x16& sn) {
        constexpr int n = nc.value, e = n >> 3, i = n & 7;
        if constexpr (n < 15) sn = MFMA32(kn, qf[(n + 1) >> 3], zero);
        if constexpr (n < 14) kn = lds128(kaddr(n + 2));
        if constexpr (i == 5) ku = lds128(kb + e * 1024);
        v4u v0 = vn0, v1 = vn1;
        if constexpr (n < 15) { vn0 = lds128(vaddr(n + 1)); vn1 = lds128(vaddr(n + 1) + VB); }
        if constexpr (i == 6) { vu0 = lds128(vb[e]); vu1 = lds128(vb[e] + VB); }
        SBAR();
        if constexpr (i == 7) {
#pragma unroll
          for (int u = 0; u < 16; ++u) sc[u] = __builtin_amdgcn_inverse_ballot_w64(um[u]) ? su[u] : sc[u];
          PIN(sc); SBAR();
        }
#pragma unroll
        for (int u = 0; u < 6; ++u) sc[u] = EXP2(sc[u]);
        PIN(sc); SBAR();
        if constexpr (n == 8) { o[0] = MFMA32(vc, p1, o[0]); SBAR(); }
        else if constexpr (n > 0) { o[e] = MFMA32(vc, p1, o[e]); SBAR(); }
        sc[6] = EXP2(sc[6]); sc[7] = EXP2(sc[7]);
        p0 = pack8(sc, 0);
        sc[8] = EXP2(sc[8]); sc[9] = EXP2(sc[9]);
        if constexpr (i == 7) { va = p0 & am0; p0 &= ~am0; PIN(va); }
        PIN(sc); PIN(p0); SBAR();
        if constexpr (n == 8) { o[0] = MFMA32(vu1, pu, o[0]); SBAR(); }
        o[e] = MFMA32(v0, p0, i == 0 ? zero : o[e]);
        SBAR();
        if constexpr (i == 6) { su = MFMA32(ku, qf[e], zero); SBAR(); }
#pragma unroll
        for (int u = 10; u < 13; ++u) sc[u] = EXP2(sc[u]);
        if constexpr (i == 7) { PIN(sc); SBAR(); o[e] = MFMA32(vu0, va, o[e]); SBAR(); }
#pragma unroll
        for (int u = 13; u < 16; ++u) sc[u] = EXP2(sc[u]);
        p1 = pack8(sc, 1); vc = v1;
        if constexpr (i == 7) { pu = p1 & am1; p1 &= ~am1; PIN(pu); }
        PIN(p1); SBAR();
        if constexpr (n == 8) {
          if (__all(o[0][8] >= 0x1p-30f && o[0][8] <= 0x1p30f)) o_store(o[0], 0); else redo = 1;
          SBAR();
        }
        if constexpr (i == 2) pend = !redo;
        if constexpr (i == 3 || i == 5 || i == 7) {
          if (pend && (i == 7 || (__builtin_amdgcn_s_getreg(7 | (3 << 11)) & 15) == 0)) {
            wait_vm<0>();
            if (lane == 0) __hip_atomic_store(flag(FL_H1 + e, T, j), tag, RLX_AGENT);
            pend = false;
          }
        }
      };
      static_for<0, 8>([&](auto hc) {
        step(std::integral_constant<int, 2 * hc.value>{}, sa, sb);
        step(std::integral_constant<int, 2 * hc.value + 1>{}, sb, sa);
      });
      o[1] = MFMA32(vc, p1, o[1]);
      o[1] = MFMA32(vu1, pu, o[1]);
      if (__all(o[1][8] >= 0x1p-30f && o[1][8] <= 0x1p30f)) o_store(o[1], 1); else redo |= 2;
    }
    if (redo) {
#pragma unroll 1
      for (int e = 0; e < 2; ++e) {
        if (!((redo >> e) & 1)) continue;
        const v4u q = e ? qf[1] : qf[0];
        const unsigned vbe = e ? vb[1] : vb[0];
        auto merged = [&](const f32x16& c) {
          f32x16 sl = MFMA32(lds128(kb + 8 * KVT + e * 1024), q, c);
          const f32x16 su = MFMA32(lds128(kb + e * 1024), q, c);
          static_for<0, 16>([&](auto u) { sl[u.value] = upper<u.value>() ? su[u.value] : sl[u.value]; });
          return sl;
        };
        float mx = -INFINITY;
#pragma unroll 1
        for (int i = 0; i < 8; ++i) {
          const f32x16 sc = i ? MFMA32(lds128(kb + i * KVT + e * 1024), q, zero) : merged(zero);
#pragma unroll
          for (int u = 0; u < 16; u += 2) mx = max3(mx, sc[u], sc[u + 1]);
        }
        const auto sw = __builtin_amdgcn_permlane32_swap(asu(mx), asu(mx), false, false);
        mx = __builtin_fmaxf(asf(sw[0]), asf(sw[1]));
        f32x16 negm, o;
#pragma unroll
        for (int u = 0; u < 16; ++u) negm[u] = -mx;
        {
          f32x16 sc = merged(negm);
#pragma unroll
          for (int u = 0; u < 16; ++u) sc[u] = EXP2(sc[u]);
          const v4u p0 = pack8(sc, 0), p1 = pack8(sc, 1);
          o = MFMA32(lds128(vbe), p0 & am0, zero);
          o = MFMA32(lds128(vbe + VB), p1 & am1, o);
          o = MFMA32(lds128(vbe + 8 * KVT), p0 & ~am0, o);
          o = MFMA32(lds128(vbe + 8 * KVT + VB), p1 & ~am1, o);
        }
#pragma unroll 1
        for (int i = 1; i < 8; ++i) {
          f32x16 sc = MFMA32(lds128(kb + i * KVT + e * 1024), q, negm);
#pragma unroll
          for (int u = 0; u < 16; ++u) sc[u] = EXP2(sc[u]);
          o = MFMA32(lds128(vbe + i * KVT), pack8(sc, 0), o);
          o = MFMA32(lds128(vbe + i * KVT + VB), pack8(sc, 1), o);
        }
        o_store(o, e);
      }
      wait_vm<0>();
      if (lane == 0) __hip_atomic_store(flag(FL_O0, T, j), tag, RLX_AGENT);
    }
    STAMP_AT(6);
    wait_vm<0>();
    if (lane == 0) __hip_atomic_store(flag(FL_O1, T, j), tag, RLX_AGENT);
    STAMP_AT(7);
    res_loads();
    wait_vm<0>();
    res_sum();
    if (blockIdx.x == 0 && wave == 7) {
#pragma unroll 1
      for (int i = 0; i < 4; ++i) poll_flags(flag(FL_KV, 4 * (4 * i + g), lane & 15), tag);
      if (lane == 0) __hip_atomic_store((GAS u64*)(ws + WS_EPOCH), tag, RLX_AGENT);
    }
  }
  {
    int t2 = threadIdx.x; PIN(t2);
    const int row = t2 >> 3, ch = t2 & 7;
    unsigned a = lds0 + L_WG + (((row >> 5) * 8 + ch) * 66 + (row & 31)) * 16, oo = (64 * rt + row) * 1024 + (64 * ct + ch * 8) * 2;
    PIN(a); PIN(oo);
    bar();
    f32x4 pl[4], ph[4];
#pragma unroll
    for (int w = 0; w < 4; ++w) { pl[w] = __builtin_bit_cast(f32x4, lds128(a + w * PART)); ph[w] = __builtin_bit_cast(f32x4, lds128(a + w * PART + 512)); }
    SBAR();
#pragma unroll
    for (int w = 0; w < 4; ++w) { lo += pl[w]; hi += ph[w]; }
    const v4u o = {pk2(lo[0], lo[1]), pk2(lo[2], lo[3]), pk2(hi[0], hi[1]), pk2(hi[2], hi[3])};
    __builtin_amdgcn_raw_buffer_store_b128(o, __builtin_amdgcn_make_buffer_rsrc(out, 0, SEQ * DM * 2, 0x00020000), oo, 0, 16);
  }
  STAMP_AT(8);
}

extern "C" void kernel_launch(void* const* d_in, const int* in_sizes, int n_in, void* d_out, int out_size, void* d_ws, size_t ws_size,
                              hipStream_t stream) {
  (void)in_sizes; (void)n_in; (void)out_size; (void)ws_size;
  (void)hipFuncSetAttribute((const void*)fused_block, hipFuncAttributeMaxDynamicSharedMemorySize, LDS_BYTES);
  fused_block<<<256, 512, LDS_BYTES, stream>>>(d_in[0], d_in[1], d_in[2], d_in[4], d_in[5], d_in[6], d_in[7], d_out, (char*)d_ws);
}
```

```cpp
#include <hip/hip_runtime.h>
#include <hip/hip_bf16.h>
#include <stdint.h>
#include <type_traits>

typedef unsigned v4u __attribute__((ext_vector_type(4)));
typedef unsigned v2u __attribute__((ext_vector_type(2)));
typedef float f32x2 __attribute__((ext_vector_type(2)));
typedef float f32x4 __attribute__((ext_vector_type(4)));
typedef float f32x16 __attribute__((ext_vector_type(16)));
typedef short bf16x8 __attribute__((ext_vector_type(8)));
typedef short s16x4 __attribute__((ext_vector_type(4)));
typedef __bf16 bf16x2_t __attribute__((ext_vector_type(2)));
typedef unsigned long long u64;
#define GAS __attribute__((address_space(1)))
#define LAS __attribute__((address_space(3)))
#define DI __device__ __forceinline__
#define RLX_AGENT __ATOMIC_RELAXED, __HIP_MEMORY_SCOPE_AGENT

constexpr int SEQ = 2048, DM = 512, FF = 1792;
constexpr unsigned WS_EPOCH = 0;
constexpr unsigned WS_STATS = 32768;
constexpr unsigned WS_FLAG = 65536;
constexpr unsigned WS_KV = WS_FLAG + 5 * 1024 * 128;
constexpr int AFT = 145;
constexpr unsigned WS_AF = WS_KV + 16 * 64 * 4096;
constexpr unsigned WS_END = WS_AF + 64 * AFT * 1024;
constexpr unsigned WS_STAMP = WS_END;
enum { FL_KV = 0, FL_H0 = 1, FL_H1 = 2, FL_O0 = 3, FL_O1 = 4 };
constexpr int L_WA = 0;
constexpr int L_WG = 86016;
constexpr int VB = 1040;
constexpr int KVT = 2048 + 2 * VB;
constexpr int L_KV = 65536 - 8 * KVT;
constexpr int RD = 8, NR = 12;
constexpr int PART = 16 * 66 * 16;
constexpr int L_CS = L_WG + 4 * PART;
constexpr int L_RS = L_CS + 3584;
constexpr int L_CNT = L_RS + 512;
constexpr int LDS_BYTES = L_CNT + 64;
constexpr int xbuf(int s) { return ((s + 1) % 3) * 16384; }
constexpr int qbuf(int s) { return 49152 + (s % 3) * 12288; }
constexpr int w1buf(int s) { return L_WG + (s % 3) * PART + 8192; }
static_assert(xbuf(7) == 32768 && qbuf(2) + 12288 <= L_WG && L_KV + 12 * KVT <= L_WG && PART >= RD * 2048 && LDS_BYTES <= 163840, "lds map");
#define AWIN 6
constexpr int AW = AWIN;
constexpr unsigned SPIN_CAP = 1u << 23;

#define MFMA32(a, b, c) __builtin_amdgcn_mfma_f32_32x32x16_bf16(__builtin_bit_cast(bf16x8, a), __builtin_bit_cast(bf16x8, b), c, 0, 0, 0)
#define MFMA16(a, b, c) __builtin_amdgcn_mfma_f32_16x16x32_bf16(__builtin_bit_cast(bf16x8, a), __builtin_bit_cast(bf16x8, b), c, 0, 0, 0)
#define SBAR() __builtin_amdgcn_sched_barrier(0)

#define STAMP_AT(i) do { } while (0)

DI float asf(unsigned u) { return __builtin_bit_cast(float, u); }
DI unsigned asu(float f) { return __builtin_bit_cast(unsigned, f); }
DI unsigned pk2(float lo, float hi) { return __builtin_bit_cast(unsigned, __builtin_convertvector((f32x2){lo, hi}, bf16x2_t)); }
DI float bflo(unsigned u) { return __builtin_bit_cast(float, u << 16); }
DI float bfhi(unsigned u) { return __builtin_bit_cast(float, u & 0xffff0000u); }
DI v4u make_srd(const void* p, unsigned bytes) { const u64 a = (u64)p; return (v4u){(unsigned)a, (unsigned)(a >> 32) & 0xffffu, bytes, 0x00020000u}; }
DI void dma16(v4u srd, unsigned voff, unsigned soff, unsigned lds) {
  asm volatile("s_mov_b32 m0, %3\n\ts_nop 0\n\tbuffer_load_dwordx4 %0, %1, %2 offen lds" :: "v"(voff), "s"(srd), "s"(soff), "s"(lds) : "memory");
}
DI void dma16_sc1(v4u srd, unsigned voff, unsigned soff, unsigned lds) {
  asm volatile("s_mov_b32 m0, %3\n\ts_nop 0\n\tbuffer_load_dwordx4 %0, %1, %2 offen sc1 lds" :: "v"(voff), "s"(srd), "s"(soff), "s"(lds) : "memory");
}
DI void dma16_a(v4u srd, unsigned voff, unsigned soff, unsigned lds) { dma16(srd, voff, soff, lds); }
DI void dma16_b(v4u srd, unsigned voff1, unsigned soff) {
  asm volatile("buffer_load_dwordx4 %0, %1, %2 offen offset:1024 lds" :: "v"(voff1), "s"(srd), "s"(soff) : "memory");
}
DI v4u ld16_sc1_async(v4u srd, unsigned voff, unsigned soff) {
  v4u r; asm volatile("buffer_load_dwordx4 %0, %1, %2, %3 offen sc1" : "=v"(r) : "v"(voff), "s"(srd), "s"(soff) : "memory"); return r;
}
DI v4u ld16_async(v4u srd, unsigned voff, unsigned soff) {
  v4u r; asm volatile("buffer_load_dwordx4 %0, %1, %2, %3 offen" : "=v"(r) : "v"(voff), "s"(srd), "s"(soff) : "memory"); return r;
}
DI unsigned ld2_async(v4u srd, unsigned voff, unsigned soff) {
  unsigned r; asm volatile("buffer_load_ushort %0, %1, %2, %3 offen" : "=v"(r) : "v"(voff), "s"(srd), "s"(soff) : "memory"); return r;
}
DI void st16_sc1(v4u v, v4u srd, unsigned voff, unsigned soff) {
  asm volatile("buffer_store_dwordx4 %0, %1, %2, %3 offen sc1\n\ts_nop 1" :: "v"(v), "v"(voff), "s"(srd), "s"(soff) : "memory");
}
template <int N> DI void wait_vm1(v4u& a) { asm volatile("s_waitcnt vmcnt(%1)" : "+v"(a) : "i"(N) : "memory"); }
template <int N> DI void wait_vm2(v4u& a, v4u& b) { asm volatile("s_waitcnt vmcnt(%2)" : "+v"(a), "+v"(b) : "i"(N) : "memory"); }
template <int N> DI void wait_vm3(v4u& a, v4u& b, v4u& c) { asm volatile("s_waitcnt vmcnt(%3)" : "+v"(a), "+v"(b), "+v"(c) : "i"(N) : "memory"); }
DI void pin_s(u64& v) { asm volatile("" : "+s"(v)); }
template <class V> DI void pin(V& v) { asm volatile("" : "+v"(v)); }
template <int N> DI void wait_vm() { asm volatile("s_waitcnt vmcnt(%0)" :: "i"(N) : "memory"); }
DI void bar() { asm volatile("s_waitcnt lgkmcnt(0)\n\ts_barrier" ::: "memory"); }
DI v4u lds128(unsigned a) { return *(const LAS v4u*)(size_t)a; }
DI void sts128(unsigned a, v4u v) { *(LAS v4u*)(size_t)a = v; }
DI v2u ldtr(unsigned a) { return __builtin_bit_cast(v2u, __builtin_amdgcn_ds_read_tr16_b64_v4i16((LAS s16x4*)(size_t)a)); }
DI v4u ldtr2(unsigned a, int second) { const v2u lo = ldtr(a), hi = ldtr(a + second); return (v4u){lo.x, lo.y, hi.x, hi.y}; }
template <int CTRL> DI float dpp(float v) { return __builtin_bit_cast(float, __builtin_amdgcn_update_dpp(0, __builtin_bit_cast(int, v), CTRL, 0xf, 0xf, true)); }
DI float rdl(float v, int l) { return __builtin_bit_cast(float, __builtin_amdgcn_readlane(__builtin_bit_cast(int, v), l)); }
DI v4u pack8(const f32x16& a, int s) { return (v4u){pk2(a[8 * s], a[8 * s + 1]), pk2(a[8 * s + 2], a[8 * s + 3]), pk2(a[8 * s + 4], a[8 * s + 5]), pk2(a[8 * s + 6], a[8 * s + 7])}; }
DI void poll_flags(const GAS u64* p, u64 tag) {
#pragma unroll 1
  for (unsigned spin = 0; spin < SPIN_CAP; ++spin) {
    const u64 v = __hip_atomic_load(p, RLX_AGENT);
    if (__all(v == tag)) break;
    __builtin_amdgcn_s_sleep(1);
  }
  asm volatile("" ::: "memory");
}
constexpr u64 upper_mask(int reg) {
  u64 m = 0;
  for (int l = 0; l < 64; ++l) { const int row = (reg & 3) + 8 * (reg >> 2) + 4 * (l >> 5); if (row > (l & 31)) m |= 1ull << l; }
  return m;
}
template <int REG> DI bool upper() { return __builtin_amdgcn_inverse_ballot_w64(upper_mask(REG)); }
template <int I, int N, class Fn> DI void static_for(Fn&& f) { if constexpr (I < N) { f(std::integral_constant<int, I>{}); static_for<I + 1, N>(f); } }
DI float max3(float a, float b, float c) { return __builtin_fmaxf(__builtin_fmaxf(a, b), c); }

constexpr int H1_CK = 16 - AW, H1_LD = H1_CK - 3, O_CK = 28 - AW, O_LD = O_CK - 3;
constexpr int g2_issued(int t) { return ((t >= 1 && t - 1 + NR + RD < 36) ? 2 : 0) + ((t + AW < 28) ? 2 : 0) + (t == H1_LD) + (t == O_LD) + (t == O_CK ? 10 : 0) + (t == 26); }
constexpr int g2_range(int a, int b) { int n = 0; for (int t = a; t < b; ++t) n += g2_issued(t); return n; }
#define GELU_PK(P) (P == 1)
#define PRIO_G2 2
#define PRIO_ATT 3
#define PRIO_G1 2
#define ROT r
#define CODEWARM 38000
#define MIDWARM 1
#define KVR 0
#define KVPUB 6
#define EXP2(v) __builtin_amdgcn_exp2f(v)
#define PIN(v) pin(v)
extern "C" __device__ float mul_unpacked(float, float) __asm("llvm.amdgcn.fmul.legacy");
#define MUL1(a, b) mul_unpacked(a, b)
#define FMA1(a, b, c) __builtin_amdgcn_div_fmasf(a, b, c, false)
DI v2u ld8_sc1_async(v4u srd, unsigned voff) {
  v2u r; asm volatile("buffer_load_dwordx2 %0, %1, %2, 0 offen sc1" : "=v"(r) : "v"(voff), "s"(srd) : "memory"); return r;
}
template <int N> DI void wait_vmf(v2u& a) { asm volatile("s_waitcnt vmcnt(%1)" : "+v"(a) : "i"(N) : "memory"); }
DI void arrive(unsigned cnt, int lane) {
  asm volatile("s_waitcnt lgkmcnt(0)" ::: "memory");
  if (lane == 0) __hip_atomic_fetch_add((LAS unsigned*)(size_t)cnt, 1u, __ATOMIC_RELAXED, __HIP_MEMORY_SCOPE_WORKGROUP);
}
DI void arrive_nowait(unsigned cnt) {
  asm volatile("s_mov_b64 exec, 1\n\tds_add_u32 %0, %1\n\ts_mov_b64 exec, -1" :: "v"(cnt), "v"(1u) : "memory");
}
DI unsigned peek(unsigned cnt) { return __hip_atomic_load((LAS unsigned*)(size_t)cnt, __ATOMIC_RELAXED, __HIP_MEMORY_SCOPE_WORKGROUP); }
DI void await(unsigned cnt, unsigned n) {
#pragma unroll 1
  for (unsigned spin = 0; spin < SPIN_CAP * 32; ++spin) {
    if (__builtin_amdgcn_readfirstlane(peek(cnt)) >= n) break;
    __builtin_amdgcn_s_sleep(1);
  }
  asm volatile("" ::: "memory");
}

__global__ __launch_bounds__(512, 2) void fused_block(const void* __restrict__ x, const void* __restrict__ wqkv, const void* __restrict__ wo,
                                                      const void* __restrict__ w1, const void* __restrict__ b1, const void* __restrict__ w2,
                                                      const void* __restrict__ b2, void* __restrict__ out, char* __restrict__ ws) {
  constexpr unsigned lds0 = 0;
  const int wave = __builtin_amdgcn_readfirstlane(threadIdx.x >> 6);
  const int m = wave & 3;
  const bool isG = wave < 4;
  const int xcd = blockIdx.x & 7, idx = blockIdx.x >> 3;
  const int r = (xcd >> 2) * 8 + (idx >> 2), j = (xcd & 3) * 4 + (idx & 3);
  const int rt = (xcd >> 1) * 8 + (idx >> 2), ct = (xcd & 1) * 4 + (idx & 3);
  int lane = threadIdx.x & 63, r32, h, g, q4, p4;
  STAMP_AT(0);
  asm volatile("" :: "s"(x), "s"(wqkv), "s"(wo), "s"(w1), "s"(b1), "s"(w2), "s"(b2), "s"(out), "s"(ws));

  const v4u sx = make_srd(x, SEQ * DM * 2), sws = make_srd(ws, WS_END);
  const __amdgpu_buffer_rsrc_t rws = __builtin_amdgcn_make_buffer_rsrc(ws, 0, WS_END, 0x00020000);
  const unsigned one2 = 0x3f803f80u;
  const int T = 4 * r + m;
  const int rot = ROT;
  auto flag_off = [&](int kind, int tile, int jj) { return WS_FLAG + ((kind * 64 + tile) * 16 + jj) * 128; };
  auto flag = [&](int kind, int tile, int jj) { return (GAS u64*)(ws + flag_off(kind, tile, jj)); };
  u64 tag;
  unsigned taglo, tag32;
  auto get_tag = [&]() {
    const v2u tg = *(const LAS v2u*)(size_t)(lds0 + L_CNT + 32);
    taglo = __builtin_amdgcn_readfirstlane(tg[0]); tag32 = __builtin_amdgcn_readfirstlane(tg[1]);
    tag = ((u64)tag32 << 32) | taglo;
  };
  v4u xres, bres, xaug, st;
  float mean, var, rstd, sd;
  v4u sel;
  unsigned xo[4], swz;
  auto lane_setup = [&]() {
    lane = threadIdx.x & 63; PIN(lane);
    r32 = lane & 31; h = lane >> 5; g = lane >> 4; q4 = (lane >> 2) & 3; p4 = lane & 3;
    const unsigned selv = ((lane & 15) == (g & 1)) ? one2 : 0u;
    sel = (v4u){selv, selv, selv, selv};
#pragma unroll
    for (int p = 0; p < 4; ++p) xo[p] = (32 * m + r32) * 128 + (((2 * ((p + m) & 3) + h) ^ ((r32 >> 1) & 7)) * 16);
    swz = ((((lane >> 2) & 1) ^ (g & 1)) * 64) + p4 * 16;
  };

  auto stat_load = [&]() { st = ld16_sc1_async(sws, WS_STATS + (128 * r + 32 * m + r32) * 16, 0); };
  auto res_loads = [&]() {
    int t2 = threadIdx.x; PIN(t2);
    const int row = 64 * rt + (t2 >> 3), col = 64 * ct + (t2 & 7) * 8;
    xres = ld16_async(sx, row * 1024 + col * 2, 0);
    bres = ld16_async(make_srd(b2, DM * 2), col * 2, 0);
  };
  f32x4 lo, hi;
  auto res_sum = [&]() {
    PIN(xres); PIN(bres);
    lo = (f32x4){bflo(xres[0]) + bflo(bres[0]), bfhi(xres[0]) + bfhi(bres[0]), bflo(xres[1]) + bflo(bres[1]), bfhi(xres[1]) + bfhi(bres[1])};
    hi = (f32x4){bflo(xres[2]) + bflo(bres[2]), bfhi(xres[2]) + bfhi(bres[2]), bflo(xres[3]) + bflo(bres[3]), bfhi(xres[3]) + bfhi(bres[3])};
    PIN(lo); PIN(hi);
  };
  auto row_stats = [&]() {
    if (!__all(st[1] == tag32 && st[3] == tag32)) {
#pragma unroll 1
      for (unsigned spin = 0; spin < SPIN_CAP; ++spin) {
        st = ld16_sc1_async(sws, WS_STATS + (128 * r + 32 * m + r32) * 16, 0);
        wait_vm1<0>(st);
        if (__all(st[1] == tag32 && st[3] == tag32)) break;
      }
    }
    mean = asf(st[0]); var = asf(st[2]);
    PIN(mean); PIN(var);
  };
  auto make_xaug = [&]() {
    PIN(mean); PIN(var);
    rstd = __builtin_amdgcn_rsqf(var); sd = var * rstd;
    const unsigned uh = pk2(-mean, sd);
    const unsigned ul = pk2(-mean - bflo(uh), FMA1(bfhi(uh), -1.f, sd));
    xaug = (v4u){(uh & 0xffffu) | (uh << 16), (ul & 0xffffu) | (uh & 0xffff0000u), ul >> 16, 0u};
    if (h) xaug = (v4u){0u, 0u, 0u, 0u};
    PIN(xaug);
  };
  auto cswr = [&](int tile, f32x4 c) {
    if (lane < 16) { LAS float* p = (LAS float*)(size_t)(lds0 + L_CS + (tile * 32 + lane) * 16 + m * 4); p[0] = c[0]; p[64] = c[1]; }
  };
  auto csrd = [&](int tile) { return __builtin_bit_cast(f32x4, lds128(lds0 + L_CS + (tile * 32 + r32) * 16)); };
  auto waug = [&](f32x4 c4, unsigned b16) {
    const float c = ((c4[0] + c4[1]) + c4[2]) + c4[3];
    const unsigned uh = pk2(c, 0.f);
    const unsigned ul = pk2(c - bflo(uh), 0.f);
    return (v4u){uh | (ul << 16), uh | (b16 << 16), b16, 0u};
  };

  const v4u sw1 = make_srd(w1, DM * FF * 2);
  const int hcol0 = 448 * (j >> 2) + 128 * (j & 3);
  const bool heavy = (j & 3) != 3;
  unsigned bias0, bias1;
  auto bias_load = [&](int P) {
    bias0 = ld2_async(make_srd(b1, FF * 2), r32 * 2, (hcol0 + 64 * P) * 2);
    bias1 = ld2_async(make_srd(b1, FF * 2), r32 * 2, (hcol0 + 64 * P + 32) * 2);
  };
  auto w1dma = [&](int P, int c, unsigned dst, int half) {
    const unsigned vw1 = (lane >> 3) * (FF * 2) + swz, vw1b = vw1 + 8 * (FF * 2) - 1024;
    const unsigned so = (64 * ((c + rot) & 7) + 16 * m) * (FF * 2) + (hcol0 + 64 * P) * 2;
    if (half == 0) dma16_a(sw1, vw1, so, dst + m * 2048); else dma16_b(sw1, vw1b, so);
  };
  f32x16 hac[2];
  unsigned rk[4][2];
  auto hsetup = [&](int P) {
#pragma unroll
    for (int p = 0; p < 4; ++p)
#pragma unroll
      for (int c = 0; c < 2; ++c) {
        rk[p][c] = lds0 + (P ? L_WA : w1buf(0)) + ((p + m) & 3) * 2048 + h * 1024 + q4 * 128 + ((c ^ (q4 >> 1)) * 64) + (g & 1) * 32 + p4 * 8;
        PIN(rk[p][c]);
      }
  };
  auto hmm = [&](auto pc, const v4u (&xf)[32], auto&& hook) {
    constexpr int P = pc.value;
    constexpr int D = 4;
    const unsigned cnt = lds0 + L_CNT + 4;
    f32x4 cs[2] = {}, c0, c1;
    v4u wf[D][2], au0, au1;
    unsigned seen = 0;
    hac[0] = (f32x16){}; hac[1] = (f32x16){};
    auto rd = [&](int ks, int t) { wf[ks % D][t] = ldtr2(rk[ks & 3][t] + (ks >> 2) * 8192, 512); };
#pragma unroll
    for (int ks = 0; ks < D; ++ks) { rd(ks, 0); rd(ks, 1); }
    SBAR();
    static_for<0, 32>([&](auto kc) {
      constexpr int ks = kc.value;
      hook(kc);
      if constexpr (ks == 10) seen = peek(cnt + 16);
      if constexpr (ks == 29) seen = peek(cnt);
#pragma unroll
      for (int t = 0; t < 2; ++t) {
        hac[t] = MFMA32(wf[ks % D][t], xf[ks], hac[t]);
        if ((ks & 3) == 0 && ks < 28) { cs[t] = MFMA16(sel, wf[ks % D][t], cs[t]); PIN(cs[t]); }
        if (ks == 26) { cs[t] = MFMA16(sel, wf[28 % D][t], cs[t]); PIN(cs[t]); }
        SBAR();
        if constexpr (ks + D < 32) rd(ks + D, t);
        if constexpr (ks == 31) { if (t == 0) { au0 = waug(c0, bias0); PIN(au0); } else { au1 = waug(c1, bias1); PIN(au1); } }
        SBAR();
      }
      if constexpr (ks == 11 || ks == 30) {
        PIN(hac[0]); PIN(hac[1]);
        if (__builtin_amdgcn_readfirstlane(seen) < 4) await(ks == 11 ? cnt + 16 : cnt, 4);
        asm volatile("" ::: "memory");
        if constexpr (ks == 30) { c0 = csrd(2 * P); c1 = csrd(2 * P + 1); }
      }
      if constexpr (ks == 27) { cswr(2 * P, cs[0]); cswr(2 * P + 1, cs[1]); arrive_nowait(cnt); }
      SBAR();
    });
    hac[0] = MFMA32(au0, xaug, hac[0]);
    hac[1] = MFMA32(au1, xaug, hac[1]);
    SBAR();
  };
  auto hgelu = [&](auto pc, auto&& mid) {
    constexpr int P = pc.value;
    constexpr bool PK = GELU_PK(P);
    const float C0 = 2.3022081986f, C1 = C0 * 0.044715f;
    const float ga = -C1 * rstd * rstd * rstd, gb = -C0 * rstd;
    auto mul = [&](f32x2 a, f32x2 b) { return PK ? a * b : (f32x2){MUL1(a[0], b[0]), MUL1(a[1], b[1])}; };
    auto fma = [&](f32x2 a, float b, float c) {
      return PK ? __builtin_elementwise_fma(a, (f32x2){b, b}, (f32x2){c, c}) : (f32x2){FMA1(a[0], b, c), FMA1(a[1], b, c)}; };
#pragma unroll
    for (int t = 0; t < 2; ++t) {
      f32x2 v[8], z[8];
#pragma unroll
      for (int e = 0; e < 8; ++e) { v[e] = (f32x2){hac[t][2 * e], hac[t][2 * e + 1]}; z[e] = mul(v[e], v[e]); }
      SBAR();
#pragma unroll
      for (int e = 0; e < 8; ++e) z[e] = fma(z[e], ga, gb);
      SBAR();
#pragma unroll
      for (int e = 0; e < 8; ++e) z[e] = mul(z[e], v[e]);
      SBAR();
#pragma unroll
      for (int e = 0; e < 8; ++e) z[e] = (f32x2){EXP2(z[e][0]), EXP2(z[e][1])};
      SBAR();
#pragma unroll
      for (int e = 0; e < 8; ++e) z[e] = fma(z[e], sd, sd);
      SBAR();
#pragma unroll
      for (int e = 0; e < 8; ++e) z[e] = (f32x2){__builtin_amdgcn_rcpf(z[e][0]), __builtin_amdgcn_rcpf(z[e][1])};
      SBAR();
#pragma unroll
      for (int e = 0; e < 8; ++e) z[e] = mul(z[e], v[e]);
      SBAR();
#pragma unroll
      for (int s = 0; s < 2; ++s) {
        const v4u f = {pk2(z[4 * s][0], z[4 * s][1]), pk2(z[4 * s + 1][0], z[4 * s + 1][1]), pk2(z[4 * s + 2][0], z[4 * s + 2][1]), pk2(z[4 * s + 3][0], z[4 * s + 3][1])};
        st16_sc1(f, sws, lane * 16, WS_AF + (T * AFT + (hcol0 >> 4) + 4 * P + 2 * t + s) * 1024);
      }
      if (t == 0) mid();
      SBAR();
    }
  };

  if (isG) {
    lane_setup();
    const v4u sw2 = make_srd(w2, FF * DM * 2), swo = make_srd(wo, DM * DM * 2);
    const unsigned vw2 = (lane >> 3) * 1024 + swz, vw2b = vw2 + 7 * 1024;
    const int ro3 = rt % 3;
    auto kmap = [&](int kk) {
      if (kk < 16) return 8 * (((kk >> 2) + rt) & 3) + (kk & 3);
      const int p = ((kk - 16) >> 2) + ro3;
      return 8 * (p >= 3 ? p - 3 : p) + 4 + (kk & 3);
    };
    auto omap = [&](int i) { return 2 * ((i + rt) & 3) + (i >> 2); };
    auto wdmah = [&](int kk, int half) {
      const unsigned so = (kk < 28 ? 448 * m + 16 * kmap(kk) : 128 * m + 16 * omap(kk - 28)) * 1024 + 128 * ct;
      if (half == 0) dma16_a(kk < 28 ? sw2 : swo, vw2, so, lds0 + L_WG + m * PART + (kk % RD) * 2048); else dma16_b(kk < 28 ? sw2 : swo, vw2b, so);
    };
    auto wdma = [&](int kk) { wdmah(kk, 0); wdmah(kk, 1); };
    {
      const v4u swq = make_srd(wqkv, DM * 3 * DM * 2);
      const unsigned vx0 = (lane >> 3) * 1024 + (((lane & 7) ^ g) * 16), vx1 = (7 + (lane >> 3)) * 1024 + (((lane & 7) ^ (4 + g)) * 16);
      const unsigned vwq = (lane >> 2) * (3 * DM * 2) + p4 * 16;
      auto piece = [&](int s, int i) {
        const int kc = (s + rot) & 7;
        if (i < 4) {
          const unsigned so = (128 * r + 32 * m + 16 * (i >> 1)) * 1024 + kc * 128;
          if (i & 1) dma16_b(sx, vx1, so); else dma16_a(sx, vx0, so, lds0 + xbuf(s) + m * 4096 + (i >> 1) * 2048);
        } else if (i < 7) dma16(swq, vwq, (64 * kc + 16 * m) * (3 * DM * 2) + (512 * (i - 4) + 32 * j) * 2, lds0 + qbuf(s) + ((i - 4) * 4 + m) * 1024);
        else w1dma(0, s, lds0 + w1buf(s), i - 7);
      };
#pragma unroll
      for (int i = 0; i < 18; ++i) { piece(i / 9, i % 9); if (i == 8) __builtin_amdgcn_s_barrier(); }
      __builtin_amdgcn_s_setprio(PRIO_G1);
      hsetup(0);
      f32x4 cs[2] = {};
      v4u xf[4], wf[4][2];
      hac[0] = (f32x16){}; hac[1] = (f32x16){};
      static_for<0, 8>([&](auto sc) {
        constexpr int s = sc.value;
        auto fill = [&](int i) {
          if constexpr (s < 6) piece(s + 2, i);
          if constexpr (s == 6) {
            if (i == 0) { stat_load(); bias_load(0); }
            else if (i < 5) { if (heavy) w1dma(1, (i - 1) >> 1, lds0 + L_WA + ((i - 1) >> 1) * 8192, (i - 1) & 1); }
            else wdmah((i - 5) >> 1, (i - 5) & 1);
          }
          if constexpr (s == 7) {
            if (heavy && i < 4) w1dma(1, 2 + (i >> 1), lds0 + L_WA + (2 + (i >> 1)) * 8192, i & 1);
            if (heavy && i >= 4) { w1dma(1, i, lds0 + L_WA + i * 8192, 0); w1dma(1, i, 0, 1); }
          }
          SBAR();
        };
        auto rd = [&](int p) {
          xf[p] = lds128(xbuf(s) + xo[p]);
          wf[p][0] = ldtr2(rk[p][0] + (s % 3) * PART, 512); wf[p][1] = ldtr2(rk[p][1] + (s % 3) * PART, 512);
          SBAR();
        };
        if constexpr (s < 7) wait_vm<9>(); else { if (heavy) wait_vm<11>(); else wait_vm<7>(); }
        bar();
        STAMP_AT(10 + s);
        rd(0);
        if constexpr (s < 7) fill(0);
        rd(1);
        if constexpr (s < 7) fill(1);
        if constexpr (s == 2) get_tag();
#pragma unroll
        for (int p = 0; p < 4; ++p) {
#pragma unroll
          for (int t = 0; t < 2; ++t) {
            hac[t] = MFMA32(wf[p][t], xf[p], hac[t]);
            if (p == 0) { cs[t] = MFMA16(sel, wf[p][t], cs[t]); PIN(cs[t]); }
            SBAR();
            if (p < 2 && t == 0) rd(p + 2);
            if (s == 7) fill(2 * p + t); else if (2 * p + t < 7) fill(2 + 2 * p + t);
          }
          if (s == 7 && p == 1) {
            cswr(0, cs[0]); cswr(1, cs[1]);
            bar();
            STAMP_AT(1);
          }
        }
        if constexpr (s == 7) {
          const f32x4 c0 = csrd(0), c1 = csrd(1);
          if (heavy) wait_vm<20>(); else wait_vm<4>();
          PIN(st); PIN(bias0); PIN(bias1);
          row_stats(); make_xaug();
          hac[0] = MFMA32(waug(c0, bias0), xaug, hac[0]);
          hac[1] = MFMA32(waug(c1, bias1), xaug, hac[1]);
          SBAR();
        }
      });
    }
    STAMP_AT(18);
    if (heavy) { wait_vm<8>(); arrive_nowait(lds0 + L_CNT + 16); }
    hgelu(std::integral_constant<int, 0>{}, [&]() {
      wait_vm<2>();
      if (heavy) arrive_nowait(lds0 + L_CNT + 20);
    });
    __builtin_amdgcn_s_setprio(0);
    STAMP_AT(19);
    unsigned rb2[2];
#pragma unroll
    for (int c = 0; c < 2; ++c) rb2[c] = lds0 + L_WG + m * PART + h * 512 + q4 * 128 + ((c ^ (q4 >> 1)) * 64) + (g & 1) * 32 + p4 * 8;
    v4u wq[36][2];
    auto wrd = [&](int kk) {
#pragma unroll
      for (int c = 0; c < 2; ++c) wq[kk][c] = ldtr2(rb2[c] + (kk % RD) * 2048, 1024);
    };
    auto wgot = [&](int kk) { PIN(wq[kk][0]); PIN(wq[kk][1]); };
    const int ftile = 2 * rt + ((lane >> 2) & 1);
    const int T0 = 4 * r - 8 + 2 * m;
    const bool live = T0 >= 0;
    auto kv_fetch = [&]() {
      const unsigned src = WS_KV + (j * 64 + (live ? T0 : 0)) * 4096, vk = live ? lane * 16 : h * 512, vv = live ? lane * 16 : 0x40000000u;
#pragma unroll
      for (int i = 0; i < 8; ++i)
        dma16_sc1(sws, (i & 2) ? vv : vk, src + (live ? i : (i & 3)) * 1024, lds0 + L_KV + (2 * m + (i >> 2)) * KVT + (i & 3) * 1024 + ((i & 3) == 3 ? VB - 1024 : 0));
      const unsigned on = (lane < 2 || live) ? one2 : 0u;
      if (lane < 6) sts128(lds0 + L_KV + (lane < 2 ? 8 + m : 2 * m + ((lane - 2) >> 1)) * KVT + 2048 + (lane & 1) * VB + 1024, (v4u){on, on, on, on});
    };
    v4u af[28][2], of[8][2];
    v2u fl;
    const unsigned a0 = WS_AF + (2 * rt * AFT + 28 * m) * 1024, o0 = WS_AF + (2 * rt * AFT + 112 + 8 * m) * 1024;
    for (int kk = 2; kk < 6; ++kk) wdma(kk);
    wait_vm<8>();
    if (lane == 0) __hip_atomic_store(flag(FL_H0, T, j), tag, RLX_AGENT);
    STAMP_AT(2);
    wdma(6); wdma(7);
    static_for<0, 3>([&](auto ic) {
      constexpr int i = ic.value;
      if constexpr (i != KVR + 1) wait_vm<(i == 0) ? 9 : 8>();
      if constexpr (i == KVR + 2) arrive_nowait(lds0 + L_CNT);
      for (int kk = 4 * i; kk < 4 * i + 4; ++kk) wrd(kk);
      for (int kk = 4 * i; kk < 4 * i + 4; ++kk) wgot(kk);
      if constexpr (i == KVR) fl = ld8_sc1_async(sws, flag_off(FL_KV, live ? T0 + (lane & 1) : 0, j));
      if constexpr (i == 2) fl = ld8_sc1_async(sws, flag_off(FL_H0, ftile, 4 * m + p4));
      for (int kk = 4 * i; kk < 4 * i + 4; ++kk) wdma(kk + RD);
      if constexpr (i == KVR) {
        wait_vmf<8>(fl);
        if (!__all(fl[0] == taglo && fl[1] == tag32)) poll_flags(flag(FL_KV, live ? T0 + (lane & 1) : 0, j), tag);
        if (heavy) await(lds0 + L_CNT + 4, 4);
        kv_fetch();
      }
    });
    STAMP_AT(3);
    wait_vmf<8>(fl);
    if constexpr (KVR == 1) arrive_nowait(lds0 + L_CNT);
    if (!__all(fl[0] == taglo && fl[1] == tag32)) poll_flags(flag(FL_H0, ftile, 4 * m + p4), tag);
    STAMP_AT(4);
#pragma unroll
    for (int kk = 0; kk < AW; ++kk) {
      af[kk][0] = ld16_sc1_async(sws, lane * 16, a0 + kmap(kk) * 1024);
      af[kk][1] = ld16_sc1_async(sws, lane * 16, a0 + (AFT + kmap(kk)) * 1024);
    }
    __builtin_amdgcn_s_setprio(PRIO_G2);
    f32x16 c2[2][2] = {};
    auto mm2 = [&](int kk, const v4u (&a)[2], auto&& fill) {
      SBAR();
#pragma unroll
      for (int i = 0; i < 4; ++i) { c2[i >> 1][i & 1] = MFMA32(wq[kk][i & 1], a[i >> 1], c2[i >> 1][i & 1]); SBAR(); fill(i); SBAR(); }
    };
    static_for<0, 28>([&](auto kc) {
      constexpr int kk = kc.value;
      wait_vm2<(kk < AW) ? 2 * (AW - 1 - kk) + g2_range(0, kk) : g2_range(kk - AW + 1, kk)>(af[kk][0], af[kk][1]);
      if constexpr (kk == H1_CK) {
        STAMP_AT(20);
        wait_vmf<g2_range(H1_LD, H1_CK) - 1>(fl);
        if (!__all(fl[0] == taglo && fl[1] == tag32)) poll_flags(flag(FL_H1, ftile, 4 * m + p4), tag);
        STAMP_AT(21);
      }
      if constexpr (kk == H1_LD) fl = ld8_sc1_async(sws, flag_off(FL_H1, ftile, 4 * m + p4));
      if constexpr (kk == O_LD) fl = ld8_sc1_async(sws, flag_off(FL_O0, ftile, 4 * m + p4));
      if constexpr (kk == 26) fl = ld8_sc1_async(sws, flag_off(FL_O1, ftile, 4 * m + p4));
      constexpr bool dw = kk >= 1 && kk - 1 + NR + RD < 36;
      if constexpr (dw) wgot(kk - 1 + NR);
      mm2(kk, af[kk], [&](int i) {
        if constexpr (dw) { if (i < 2) wdmah(kk - 1 + NR + RD, i); }
        if constexpr (kk + NR < 28) { if (i == 1) wrd(kk + NR); }
        if constexpr (kk == 27) { if (i == 0) wrd(28); }
        if constexpr (kk + AW < 28) { if (i >= 2) af[kk + AW][i - 2] = ld16_sc1_async(sws, lane * 16, a0 + (AFT * (i - 2) + kmap(kk + AW)) * 1024); }
      });
      if constexpr (kk == O_CK) {
        wait_vmf<g2_range(O_LD, O_CK) - 1>(fl);
        if (!__all(fl[0] == taglo && fl[1] == tag32)) poll_flags(flag(FL_O0, ftile, 4 * m + p4), tag);
        res_loads();
#pragma unroll
        for (int i = 0; i < 4; ++i) {
          of[i][0] = ld16_sc1_async(sws, lane * 16, o0 + omap(i) * 1024);
          of[i][1] = ld16_sc1_async(sws, lane * 16, o0 + (AFT + omap(i)) * 1024);
        }
      }
    });
    static_for<0, 4>([&](auto kc) {
      constexpr int kk = kc.value;
      if (kk < 3) wrd(28 + kk + 1);
      if constexpr (kk == 1) {
        res_sum();
        STAMP_AT(5);
        wait_vmf<0>(fl);
        if (!__all(fl[0] == taglo && fl[1] == tag32)) poll_flags(flag(FL_O1, ftile, 4 * m + p4), tag);
        STAMP_AT(6);
#pragma unroll
        for (int i = 0; i < 8; ++i) of[4 + (i & 3)][i >> 2] = ld16_sc1_async(sws, lane * 16, o0 + (AFT * (i >> 2) + omap(4 + (i & 3))) * 1024);
      }
      wait_vm2<(kk == 0) ? 7 : 14 - 2 * kk>(of[kk][0], of[kk][1]);
      mm2(28 + kk, of[kk], [&](int i) { PIN(c2[i >> 1][i & 1]); if (kk == 3) wrd(32 + i); });
    });
    for (int kk = 32; kk < 36; ++kk) wgot(kk);
    unsigned pa = lds0 + L_WG + m * PART + lane * 16;
    PIN(pa);
    auto put = [&](int qn, int qd) {
      const f32x16& c = c2[qn >> 1][qn & 1];
      sts128(pa + (qn * 4 + qd) * 66 * 16, __builtin_bit_cast(v4u, (f32x4){c[4 * qd], c[4 * qd + 1], c[4 * qd + 2], c[4 * qd + 3]}));
    };
    static_for<0, 4>([&](auto qc) {
      constexpr int qn = qc.value;
      if constexpr ((qn & 1) == 0) { wait_vm2<qn ? 0 : 4>(of[4][qn >> 1], of[5][qn >> 1]); PIN(of[6][qn >> 1]); PIN(of[7][qn >> 1]); }
      SBAR();
#pragma unroll
      for (int i = 0; i < 4; ++i) {
        c2[qn >> 1][qn & 1] = MFMA32(wq[32 + i][qn & 1], of[4 + i][qn >> 1], c2[qn >> 1][qn & 1]);
        SBAR();
        if (qn > 0 && i >= 2) { put(qn - 1, 2 * i - 4); put(qn - 1, 2 * i - 3); }
        SBAR();
      }
    });
#pragma unroll
    for (int qd = 0; qd < 4; ++qd) put(3, qd);
    STAMP_AT(7);
  } else {
    __builtin_amdgcn_s_setprio(1);
    lane_setup();
    v4u xr[2];
    xr[0] = ld16_async(sx, lane * 16, (128 * r + 8 * j + 2 * m) * 1024); xr[1] = ld16_async(sx, lane * 16, (128 * r + 8 * j + 2 * m + 1) * 1024);
    unsigned warm;
    {
      u64 pc; asm volatile("s_getpc_b64 %0" : "=s"(pc));
      warm = ld2_async(make_srd((const void*)(pc & ~127ull), CODEWARM), lane < 3 ? lane * 128 : 0x40000000u, (idx * 4 + m) * 384);
    }
    __builtin_amdgcn_s_barrier();
    v2u ep = ld8_sc1_async(sws, m == 0 ? WS_EPOCH : 0x40000000u);
    float sval;
    const int wi = blockIdx.x * 4 + m;
    const u64 wbase = wi < 448 ? (u64)w1 + wi * 4096 : wi < 896 ? (u64)w2 + (wi - 448) * 4096 : (u64)wo + (wi - 896) * 4096;
    auto make_stats = [&]() {
      wait_vm2<1>(xr[0], xr[1]);
      PIN(warm);
      float piv[2], a[4];
#pragma unroll
      for (int u = 0; u < 2; ++u) {
        piv[u] = rdl(bflo(xr[u][0]), 0);
        float s1 = 0.f, s2 = 0.f;
#pragma unroll
        for (int i = 0; i < 8; ++i) { const float d = ((i & 1) ? bfhi(xr[u][i >> 1]) : bflo(xr[u][i >> 1])) - piv[u]; s1 += d; s2 = __builtin_fmaf(d, d, s2); }
        a[2 * u] = s1; a[2 * u + 1] = s2;
      }
#pragma unroll
      for (int i = 0; i < 4; ++i) a[i] += dpp<0xB1>(a[i]);
#pragma unroll
      for (int i = 0; i < 4; ++i) a[i] += dpp<0x4E>(a[i]);
      float t = p4 == 0 ? a[0] : p4 == 1 ? a[1] : p4 == 2 ? a[2] : a[3];
      t += dpp<0x114>(t); t += dpp<0x118>(t);
      const auto s16 = __builtin_amdgcn_permlane16_swap(asu(t), asu(t), false, false);
      t = asf(s16[0]) + asf(s16[1]);
      const auto s32 = __builtin_amdgcn_permlane32_swap(asu(t), asu(t), false, false);
      t = (asf(s32[0]) + asf(s32[1])) * (1.f / DM);
      const float e1 = dpp<0xA0>(t);
      sval = (p4 & 1) ? __builtin_fmaxf(__builtin_fmaf(-e1, e1, t), 0.f) + 1e-5f : t + (p4 ? piv[1] : piv[0]);
      PIN(sval);
    };
    const unsigned acnt = lds0 + L_CNT;
    const unsigned vpos = 2048 + (g & 1) * 512 + h * 256 + (lane & 15) * 16;
    v4u qf[2], am0, am1;
    u64 um[16];
    auto masks = [&]() {
      const int dq = r32 - 4 * h;
#pragma unroll
      for (int u = 0; u < 16; ++u) { um[u] = __builtin_amdgcn_ballot_w64((u & 3) + 8 * (u >> 2) > dq); pin_s(um[u]); }
#pragma unroll
      for (int d = 0; d < 4; ++d) {
        am0[d] = __builtin_amdgcn_inverse_ballot_w64(um[2 * d]) ? 0xffffffffu : __builtin_amdgcn_inverse_ballot_w64(um[2 * d + 1]) ? 0xffff0000u : 0u;
        am1[d] = __builtin_amdgcn_inverse_ballot_w64(um[8 + 2 * d]) ? 0xffffffffu : __builtin_amdgcn_inverse_ballot_w64(um[9 + 2 * d]) ? 0xffff0000u : 0u;
      }
      PIN(am0); PIN(am1);
    };
    {
      v4u xf[32];
      f32x16 acc[3] = {};
      f32x4 cs[3] = {};
      v4u wf[4][3];
      unsigned wb[4];
#pragma unroll
      for (int p = 0; p < 4; ++p) { wb[p] = qbuf(0) + ((p + m) & 3) * 1024 + (8 * h + q4) * 64 + (g & 1) * 32 + p4 * 8; PIN(wb[p]); }
      auto rd = [&](int ks, int t) {
        if (t == 0) xf[ks] = lds128(xbuf(ks >> 2) + xo[ks & 3]);
        wf[ks & 3][t] = ldtr2(wb[ks & 3] + ((ks >> 2) % 3) * 12288 + t * 4096, 256);
      };
      auto mm = [&](int ks, int t) {
        acc[t] = t == 2 ? MFMA32(xf[ks], wf[ks & 3][t], acc[t]) : MFMA32(wf[ks & 3][t], xf[ks], acc[t]);
        if ((ks & 3) == 0) { cs[t] = MFMA16(sel, wf[ks & 3][t], cs[t]); PIN(cs[t]); }
        SBAR();
      };
      make_stats();
      bar();
      STAMP_AT(10);
#pragma unroll
      for (int t = 0; t < 6; ++t) rd(t / 3, t % 3);
      SBAR();
      static_for<0, 8>([&](auto sc) {
        constexpr int s = sc.value;
#pragma unroll
        for (int p = 0; p < 2; ++p) {
#pragma unroll
          for (int t = 0; t < 3; ++t) { mm(4 * s + p, t); rd(4 * s + p + 2, t); SBAR(); }
          if (s == 7 && p == 0) { cswr(4, cs[0]); cswr(5, cs[1]); cswr(6, cs[2]); SBAR(); }
        }
        if constexpr (s == 0) {
          wait_vmf<0>(ep);
          if (m == 0 && lane < 10)
            *(LAS unsigned*)(size_t)(lds0 + L_CNT + lane * 4) = lane == 8 ? ep[0] * 0x9E3779B1u + 0x7F4A7C15u : lane == 9 ? ep[1] * 0x85EBCA77u + 0x165667B1u : 0u;
        }
        if constexpr (s == 1) {
          get_tag();
          if ((lane >> 2) == 3) {
            const v2u gr = {asu(sval), tag32};
            __builtin_amdgcn_raw_buffer_store_b64(gr, rws, WS_STATS + (128 * r + 8 * j + 2 * m) * 16 + p4 * 8, 0, 16);
          }
        }
        if constexpr (s == MIDWARM) {
          warm = ld2_async(make_srd((const void*)wbase, 4096), lane * 128, 0);
        }
        if constexpr (s == 5) stat_load();
        if constexpr (s == 6) {
          wait_vm1<0>(st);
          PIN(warm);

          row_stats();
        }
        mm(4 * s + 2, 0); mm(4 * s + 2, 1);
        bar();
        if constexpr (s < 7) {
          STAMP_AT(11 + s);
          mm(4 * s + 2, 2);
          rd(4 * s + 4, 0); rd(4 * s + 4, 1); rd(4 * s + 4, 2); SBAR();
#pragma unroll
          for (int t = 0; t < 3; ++t) { mm(4 * s + 3, t); rd(4 * s + 5, t); SBAR(); }
        }
      });
      STAMP_AT(1);
      __builtin_amdgcn_s_setprio(3);
      const f32x4 c5 = csrd(5), c6 = csrd(6), c4 = csrd(4);
      SBAR();
      mm(30, 2); mm(31, 1);
      make_xaug();
      SBAR();
      mm(31, 2); mm(31, 0);
      if (h == 0) *(LAS float*)(size_t)(lds0 + L_RS + (m * 32 + r32) * 4) = rstd;
      acc[1] = MFMA32(waug(c5, 0u), xaug, acc[1]);
      acc[2] = MFMA32(xaug, waug(c6, 0u), acc[2]);
      acc[0] = MFMA32(waug(c4, 0u), xaug, acc[0]);
      SBAR();
      f32x4 rs[4];
#pragma unroll
      for (int qd = 0; qd < 4; ++qd) rs[qd] = __builtin_bit_cast(f32x4, lds128(lds0 + L_RS + (m * 32 + 8 * qd + 4 * h) * 4));
      const unsigned kvo = WS_KV + (j * 64 + T) * 4096;
#pragma unroll
      for (int e = 0; e < 16; ++e) acc[1][e] = MUL1(acc[1][e], rstd);
#pragma unroll
      for (int e = 0; e < 2; ++e) {
        const v4u f = pack8(acc[1], e);
        st16_sc1(f, sws, lane * 16, kvo + e * 1024);
        sts128(lds0 + L_KV + (8 + m) * KVT + e * 1024 + lane * 16, f);
      }
#pragma unroll
      for (int qd = 0; qd < 4; ++qd)
#pragma unroll
        for (int e = 0; e < 4; ++e) acc[2][4 * qd + e] = MUL1(acc[2][4 * qd + e], rs[qd][e]);
#pragma unroll
      for (int s = 0; s < 2; ++s) {
        const v4u f = pack8(acc[2], s);
        st16_sc1(f, sws, vpos, kvo + s * 1024);
        sts128(lds0 + L_KV + (8 + m) * KVT + s * VB + vpos, f);
      }
      SBAR();
      const float qs = rstd * (0.25f * 1.4426950408889634f);
#pragma unroll
      for (int e = 0; e < 16; ++e) acc[0][e] = MUL1(acc[0][e], qs);
      qf[0] = pack8(acc[0], 0); qf[1] = pack8(acc[0], 1);
      PIN(qf[0]); PIN(qf[1]);
      arrive_nowait(acnt);
      STAMP_AT(2);
      if (heavy) {
        hsetup(1);
        await(acnt + 16, 4);
        STAMP_AT(3);
        hmm(std::integral_constant<int, 1>{}, xf, [&](auto kc) {
          constexpr int ks = kc.value;
          if constexpr (ks == KVPUB) {
            wait_vm<0>();
            if (lane == 0) __hip_atomic_store(flag(FL_KV, T, j), tag, RLX_AGENT);
          }
          if constexpr (ks == KVPUB + 2) bias_load(1);
          if constexpr (ks == 31) { wait_vm<0>(); PIN(bias0); PIN(bias1); }
        });
        STAMP_AT(18);
        hgelu(std::integral_constant<int, 1>{}, [&]() {});
        STAMP_AT(4);
      } else {
        wait_vm<0>();
        if (lane == 0) __hip_atomic_store(flag(FL_KV, T, j), tag, RLX_AGENT);
      }
    }
    masks();
    STAMP_AT(19);
    const unsigned kb = lds0 + L_KV + m * KVT + lane * 16;
    unsigned vb[2];
#pragma unroll
    for (int e = 0; e < 2; ++e) { vb[e] = lds0 + L_KV + m * KVT + 2048 + ((g & 1) ? 1024 : e * 512 + h * 256 + (lane & 15) * 16); PIN(vb[e]); }
    await(acnt, 8);
    __builtin_amdgcn_s_setprio(PRIO_ATT);
    STAMP_AT(5);
    const f32x16 zero = {};
    auto o_store = [&](f32x16& o, int e) {
      const float rl = __builtin_amdgcn_rcpf(o[8]);
#pragma unroll
      for (int u = 0; u < 8; ++u) o[u] = MUL1(o[u], rl);
      st16_sc1(pack8(o, 0), sws, lane * 16, WS_AF + (T * AFT + 112 + 2 * j + e) * 1024);
    };
    int redo = 0;
    {
      f32x16 o[2], sa, sb, su;
      bool pend = false;
      v4u kn, ku, va, vc, vn0, vn1, vu0, vu1, p0, p1, pu;
      auto kaddr = [&](int n) { return kb + ((n & 7) + 1) * KVT + (n >> 3) * 1024; };
      auto vaddr = [&](int n) { return vb[n >> 3] + ((n & 7) + 1) * KVT; };
      kn = lds128(kaddr(0)); vn0 = lds128(vaddr(0)); vn1 = lds128(vaddr(0) + VB);
      sa = MFMA32(kn, qf[0], zero);
      kn = lds128(kaddr(1));
      SBAR();
      auto step = [&](auto nc, f32x16& sc, f32x16& sn) {
        constexpr int n = nc.value, e = n >> 3, i = n & 7;
        if constexpr (n < 15) sn = MFMA32(kn, qf[(n + 1) >> 3], zero);
        if constexpr (n < 14) kn = lds128(kaddr(n + 2));
        if constexpr (i == 5) ku = lds128(kb + e * 1024);
        v4u v0 = vn0, v1 = vn1;
        if constexpr (n < 15) { vn0 = lds128(vaddr(n + 1)); vn1 = lds128(vaddr(n + 1) + VB); }
        if constexpr (i == 6) { vu0 = lds128(vb[e]); vu1 = lds128(vb[e] + VB); }
        SBAR();
        if constexpr (i == 7) {
#pragma unroll
          for (int u = 0; u < 16; ++u) sc[u] = __builtin_amdgcn_inverse_ballot_w64(um[u]) ? su[u] : sc[u];
          PIN(sc); SBAR();
        }
#pragma unroll
        for (int u = 0; u < 6; ++u) sc[u] = EXP2(sc[u]);
        PIN(sc); SBAR();
        if constexpr (n == 8) { o[0] = MFMA32(vc, p1, o[0]); SBAR(); }
        else if constexpr (n > 0) { o[e] = MFMA32(vc, p1, o[e]); SBAR(); }
        sc[6] = EXP2(sc[6]); sc[7] = EXP2(sc[7]);
        p0 = pack8(sc, 0);
        sc[8] = EXP2(sc[8]); sc[9] = EXP2(sc[9]);
        if constexpr (i == 7) { va = p0 & am0; p0 &= ~am0; PIN(va); }
        PIN(sc); PIN(p0); SBAR();
        if constexpr (n == 8) { o[0] = MFMA32(vu1, pu, o[0]); SBAR(); }
        o[e] = MFMA32(v0, p0, i == 0 ? zero : o[e]);
        SBAR();
        if constexpr (i == 6) { su = MFMA32(ku, qf[e], zero); SBAR(); }
#pragma unroll
        for (int u = 10; u < 13; ++u) sc[u] = EXP2(sc[u]);
        if constexpr (i == 7) { PIN(sc); SBAR(); o[e] = MFMA32(vu0, va, o[e]); SBAR(); }
#pragma unroll
        for (int u = 13; u < 16; ++u) sc[u] = EXP2(sc[u]);
        p1 = pack8(sc, 1); vc = v1;
        if constexpr (i == 7) { pu = p1 & am1; p1 &= ~am1; PIN(pu); }
        PIN(p1); SBAR();
        if constexpr (n == 8) {
          if (__all(o[0][8] >= 0x1p-30f && o[0][8] <= 0x1p30f)) o_store(o[0], 0); else redo = 1;
          SBAR();
        }
        if constexpr (i == 2) pend = !redo;
        if constexpr (i == 3 || i == 5 || i == 7) {
          if (pend && (i == 7 || (__builtin_amdgcn_s_getreg(7 | (3 << 11)) & 15) == 0)) {
            wait_vm<0>();
            if (lane == 0) __hip_atomic_store(flag(FL_H1 + e, T, j), tag, RLX_AGENT);
            pend = false;
          }
        }
      };
      static_for<0, 8>([&](auto hc) {
        step(std::integral_constant<int, 2 * hc.value>{}, sa, sb);
        step(std::integral_constant<int, 2 * hc.value + 1>{}, sb, sa);
      });
      o[1] = MFMA32(vc, p1, o[1]);
      o[1] = MFMA32(vu1, pu, o[1]);
      if (__all(o[1][8] >= 0x1p-30f && o[1][8] <= 0x1p30f)) o_store(o[1], 1); else redo |= 2;
    }
    if (redo) {
#pragma unroll 1
      for (int e = 0; e < 2; ++e) {
        if (!((redo >> e) & 1)) continue;
        const v4u q = e ? qf[1] : qf[0];
        const unsigned vbe = e ? vb[1] : vb[0];
        auto merged = [&](const f32x16& c) {
          f32x16 sl = MFMA32(lds128(kb + 8 * KVT + e * 1024), q, c);
          const f32x16 su = MFMA32(lds128(kb + e * 1024), q, c);
          static_for<0, 16>([&](auto u) { sl[u.value] = upper<u.value>() ? su[u.value] : sl[u.value]; });
          return sl;
        };
        float mx = -INFINITY;
#pragma unroll 1
        for (int i = 0; i < 8; ++i) {
          const f32x16 sc = i ? MFMA32(lds128(kb + i * KVT + e * 1024), q, zero) : merged(zero);
#pragma unroll
          for (int u = 0; u < 16; u += 2) mx = max3(mx, sc[u], sc[u + 1]);
        }
        const auto sw = __builtin_amdgcn_permlane32_swap(asu(mx), asu(mx), false, false);
        mx = __builtin_fmaxf(asf(sw[0]), asf(sw[1]));
        f32x16 negm, o;
#pragma unroll
        for (int u = 0; u < 16; ++u) negm[u] = -mx;
        {
          f32x16 sc = merged(negm);
#pragma unroll
          for (int u = 0; u < 16; ++u) sc[u] = EXP2(sc[u]);
          const v4u p0 = pack8(sc, 0), p1 = pack8(sc, 1);
          o = MFMA32(lds128(vbe), p0 & am0, zero);
          o = MFMA32(lds128(vbe + VB), p1 & am1, o);
          o = MFMA32(lds128(vbe + 8 * KVT), p0 & ~am0, o);
          o = MFMA32(lds128(vbe + 8 * KVT + VB), p1 & ~am1, o);
        }
#pragma unroll 1
        for (int i = 1; i < 8; ++i) {
          f32x16 sc = MFMA32(lds128(kb + i * KVT + e * 1024), q, negm);
#pragma unroll
          for (int u = 0; u < 16; ++u) sc[u] = EXP2(sc[u]);
          o = MFMA32(lds128(vbe + i * KVT), pack8(sc, 0), o);
          o = MFMA32(lds128(vbe + i * KVT + VB), pack8(sc, 1), o);
        }
        o_store(o, e);
      }
      wait_vm<0>();
      if (lane == 0) __hip_atomic_store(flag(FL_O0, T, j), tag, RLX_AGENT);
    }
    STAMP_AT(6);
    wait_vm<0>();
    if (lane == 0) __hip_atomic_store(flag(FL_O1, T, j), tag, RLX_AGENT);
    STAMP_AT(7);
    res_loads();
    wait_vm<0>();
    res_sum();
    if (blockIdx.x == 0 && wave == 7) {
#pragma unroll 1
      for (int i = 0; i < 4; ++i) poll_flags(flag(FL_KV, 4 * (4 * i + g), lane & 15), tag);
      if (lane == 0) __hip_atomic_store((GAS u64*)(ws + WS_EPOCH), tag, RLX_AGENT);
    }
  }
  {
    int t2 = threadIdx.x; PIN(t2);
    const int row = t2 >> 3, ch = t2 & 7;
    unsigned a = lds0 + L_WG + (((row >> 5) * 8 + ch) * 66 + (row & 31)) * 16, oo = (64 * rt + row) * 1024 + (64 * ct + ch * 8) * 2;
    PIN(a); PIN(oo);
    bar();
    f32x4 pl[4], ph[4];
#pragma unroll
    for (int w = 0; w < 4; ++w) { pl[w] = __builtin_bit_cast(f32x4, lds128(a + w * PART)); ph[w] = __builtin_bit_cast(f32x4, lds128(a + w * PART + 512)); }
    SBAR();
#pragma unroll
    for (int w = 0; w < 4; ++w) { lo += pl[w]; hi += ph[w]; }
    const v4u o = {pk2(lo[0], lo[1]), pk2(lo[2], lo[3]), pk2(hi[0], hi[1]), pk2(hi[2], hi[3])};
    __builtin_amdgcn_raw_buffer_store_b128(o, __builtin_amdgcn_make_buffer_rsrc(out, 0, SEQ * DM * 2, 0x00020000), oo, 0, 16);
  }
  STAMP_AT(8);
}

extern "C" void kernel_launch(void* const* d_in, const int* in_sizes, int n_in, void* d_out, int out_size, void* d_ws, size_t ws_size,
                              hipStream_t stream) {
  (void)in_sizes; (void)n_in; (void)out_size; (void)ws_size;
  (void)hipFuncSetAttribute((const void*)fused_block, hipFuncAttributeMaxDynamicSharedMemorySize, LDS_BYTES);
  fused_block<<<256, 512, LDS_BYTES, stream>>>(d_in[0], d_in[1], d_in[2], d_in[4], d_in[5], d_in[6], d_in[7], d_out, (char*)d_ws);
}
```

```cpp
#include <hip/hip_runtime.h>
#include <hip/hip_bf16.h>
#include <stdint.h>
#include <type_traits>

typedef unsigned v4u __attribute__((ext_vector_type(4)));
typedef unsigned v2u __attribute__((ext_vector_type(2)));
typedef float f32x2 __attribute__((ext_vector_type(2)));
typedef float f32x4 __attribute__((ext_vector_type(4)));
typedef float f32x16 __attribute__((ext_vector_type(16)));
typedef short bf16x8 __attribute__((ext_vector_type(8)));
typedef short s16x4 __attribute__((ext_vector_type(4)));
typedef __bf16 bf16x2_t __attribute__((ext_vector_type(2)));
typedef unsigned long long u64;
#define GAS __attribute__((address_space(1)))
#define LAS __attribute__((address_space(3)))
#define DI __device__ __forceinline__
#define RLX_AGENT __ATOMIC_RELAXED, __HIP_MEMORY_SCOPE_AGENT

constexpr int SEQ = 2048, DM = 512, FF = 1792;
constexpr unsigned WS_EPOCH = 0;
constexpr unsigned WS_STATS = 32768;
constexpr unsigned WS_FLAG = 65536;
constexpr unsigned WS_KV = WS_FLAG + 5 * 1024 * 128;
constexpr int AFT = 145;
constexpr unsigned WS_AF = WS_KV + 16 * 64 * 4096;
constexpr unsigned WS_END = WS_AF + 64 * AFT * 1024;
constexpr unsigned WS_STAMP = WS_END;
enum { FL_KV = 0, FL_H0 = 1, FL_H1 = 2, FL_O0 = 3, FL_O1 = 4 };
constexpr int L_WA = 0;
constexpr int L_WG = 86016;
constexpr int VB = 1040;
constexpr int KVT = 2048 + 2 * VB;
constexpr int L_KV = 32768;
constexpr int RD = 8, NR = 12;
constexpr int PART = 16 * 66 * 16;
constexpr int L_CS = L_WG + 4 * PART;
constexpr int L_RS = L_CS + 3584;
constexpr int L_CNT = L_RS + 512;
constexpr int LDS_BYTES = L_CNT + 64;
constexpr int xbuf(int s) { return ((s + 1) % 3) * 16384; }
constexpr int qbuf(int s) { return 49152 + (s % 3) * 12288; }
constexpr int w1buf(int s) { return L_WG + (s % 3) * PART + 8192; }
static_assert(L_KV + 8 * KVT >= 65536 && xbuf(7) == 32768 && qbuf(2) + 12288 <= L_WG && L_KV + 12 * KVT <= L_WG && PART >= RD * 2048 && LDS_BYTES <= 163840, "lds map");
#define AWIN 6
constexpr int AW = AWIN;
constexpr unsigned SPIN_CAP = 1u << 23;

#define MFMA32(a, b, c) __builtin_amdgcn_mfma_f32_32x32x16_bf16(__builtin_bit_cast(bf16x8, a), __builtin_bit_cast(bf16x8, b), c, 0, 0, 0)
#define MFMA16(a, b, c) __builtin_amdgcn_mfma_f32_16x16x32_bf16(__builtin_bit_cast(bf16x8, a), __builtin_bit_cast(bf16x8, b), c, 0, 0, 0)
#define SBAR() __builtin_amdgcn_sched_barrier(0)

#define STAMP_AT(i) do { } while (0)

DI float asf(unsigned u) { return __builtin_bit_cast(float, u); }
DI unsigned asu(float f) { return __builtin_bit_cast(unsigned, f); }
DI unsigned pk2(float lo, float hi) { return __builtin_bit_cast(unsigned, __builtin_convertvector((f32x2){lo, hi}, bf16x2_t)); }
DI float bflo(unsigned u) { return __builtin_bit_cast(float, u << 16); }
DI float bfhi(unsigned u) { return __builtin_bit_cast(float, u & 0xffff0000u); }
DI v4u make_srd(const void* p, unsigned bytes) { const u64 a = (u64)p; return (v4u){(unsigned)a, (unsigned)(a >> 32) & 0xffffu, bytes, 0x00020000u}; }
DI void dma16(v4u srd, unsigned voff, unsigned soff, unsigned lds) {
  asm volatile("s_mov_b32 m0, %3\n\ts_nop 0\n\tbuffer_load_dwordx4 %0, %1, %2 offen lds" :: "v"(voff), "s"(srd), "s"(soff), "s"(lds) : "memory");
}
DI void dma16_sc1(v4u srd, unsigned voff, unsigned soff, unsigned lds) {
  asm volatile("s_mov_b32 m0, %3\n\ts_nop 0\n\tbuffer_load_dwordx4 %0, %1, %2 offen sc1 lds" :: "v"(voff), "s"(srd), "s"(soff), "s"(lds) : "memory");
}
DI void dma16_a(v4u srd, unsigned voff, unsigned soff, unsigned lds) { dma16(srd, voff, soff, lds); }
DI void dma16_b(v4u srd, unsigned voff1, unsigned soff) {
  asm volatile("buffer_load_dwordx4 %0, %1, %2 offen offset:1024 lds" :: "v"(voff1), "s"(srd), "s"(soff) : "memory");
}
DI v4u ld16_sc1_async(v4u srd, unsigned voff, unsigned soff) {
  v4u r; asm volatile("buffer_load_dwordx4 %0, %1, %2, %3 offen sc1" : "=v"(r) : "v"(voff), "s"(srd), "s"(soff) : "memory"); return r;
}
DI v4u ld16_async(v4u srd, unsigned voff, unsigned soff) {
  v4u r; asm volatile("buffer_load_dwordx4 %0, %1, %2, %3 offen" : "=v"(r) : "v"(voff), "s"(srd), "s"(soff) : "memory"); return r;
}
DI unsigned ld2_async(v4u srd, unsigned voff, unsigned soff) {
  unsigned r; asm volatile("buffer_load_ushort %0, %1, %2, %3 offen" : "=v"(r) : "v"(voff), "s"(srd), "s"(soff) : "memory"); return r;
}
DI void st16_sc1(v4u v, v4u srd, unsigned voff, unsigned soff) {
  asm volatile("buffer_store_dwordx4 %0, %1, %2, %3 offen sc1\n\ts_nop 1" :: "v"(v), "v"(voff), "s"(srd), "s"(soff) : "memory");
}
template <int N> DI void wait_vm1(v4u& a) { asm volatile("s_waitcnt vmcnt(%1)" : "+v"(a) : "i"(N) : "memory"); }
template <int N> DI void wait_vm2(v4u& a, v4u& b) { asm volatile("s_waitcnt vmcnt(%2)" : "+v"(a), "+v"(b) : "i"(N) : "memory"); }
template <int N> DI void wait_vm3(v4u& a, v4u& b, v4u& c) { asm volatile("s_waitcnt vmcnt(%3)" : "+v"(a), "+v"(b), "+v"(c) : "i"(N) : "memory"); }
DI void pin_s(u64& v) { asm volatile("" : "+s"(v)); }
template <class V> DI void pin(V& v) { asm volatile("" : "+v"(v)); }
template <int N> DI void wait_vm() { asm volatile("s_waitcnt vmcnt(%0)" :: "i"(N) : "memory"); }
DI void bar() { asm volatile("s_waitcnt lgkmcnt(0)\n\ts_barrier" ::: "memory"); }
DI v4u lds128(unsigned a) { return *(const LAS v4u*)(size_t)a; }
DI void sts128(unsigned a, v4u v) { *(LAS v4u*)(size_t)a = v; }
DI v2u ldtr(unsigned a) { return __builtin_bit_cast(v2u, __builtin_amdgcn_ds_read_tr16_b64_v4i16((LAS s16x4*)(size_t)a)); }
DI v4u ldtr2(unsigned a, int second) { const v2u lo = ldtr(a), hi = ldtr(a + second); return (v4u){lo.x, lo.y, hi.x, hi.y}; }
template <int CTRL> DI float dpp(float v) { return __builtin_bit_cast(float, __builtin_amdgcn_update_dpp(0, __builtin_bit_cast(int, v), CTRL, 0xf, 0xf, true)); }
DI float rdl(float v, int l) { return __builtin_bit_cast(float, __builtin_amdgcn_readlane(__builtin_bit_cast(int, v), l)); }
DI v4u pack8(const f32x16& a, int s) { return (v4u){pk2(a[8 * s], a[8 * s + 1]), pk2(a[8 * s + 2], a[8 * s + 3]), pk2(a[8 * s + 4], a[8 * s + 5]), pk2(a[8 * s + 6], a[8 * s + 7])}; }
DI void poll_flags(const GAS u64* p, u64 tag) {
#pragma unroll 1
  for (unsigned spin = 0; spin < SPIN_CAP; ++spin) {
    const u64 v = __hip_atomic_load(p, RLX_AGENT);
    if (__all(v == tag)) break;
    __builtin_amdgcn_s_sleep(1);
  }
  asm volatile("" ::: "memory");
}
constexpr u64 upper_mask(int reg) {
  u64 m = 0;
  for (int l = 0; l < 64; ++l) { const int row = (reg & 3) + 8 * (reg >> 2) + 4 * (l >> 5); if (row > (l & 31)) m |= 1ull << l; }
  return m;
}
template <int REG> DI bool upper() { return __builtin_amdgcn_inverse_ballot_w64(upper_mask(REG)); }
template <int I, int N, class Fn> DI void static_for(Fn&& f) { if constexpr (I < N) { f(std::integral_constant<int, I>{}); static_for<I + 1, N>(f); } }
DI float max3(float a, float b, float c) { return __builtin_fmaxf(__builtin_fmaxf(a, b), c); }

constexpr int H1_CK = 16 - AW, H1_LD = H1_CK - 3, O_CK = 28 - AW, O_LD = O_CK - 3;
constexpr int g2_issued(int t) { return ((t >= 1 && t - 1 + NR + RD < 36) ? 2 : 0) + ((t + AW < 28) ? 2 : 0) + (t == H1_LD) + (t == O_LD) + (t == O_CK ? 10 : 0) + (t == 26); }
constexpr int g2_range(int a, int b) { int n = 0; for (int t = a; t < b; ++t) n += g2_issued(t); return n; }
#define GELU_PK(P) (P == 1)
#define PRIO_G2 2
#define PRIO_ATT 3
#define PRIO_G1 2
#define ROT r
#define CODEWARM 38000
#define MIDWARM 1
#define KVPUB 2
#define EXP2(v) __builtin_amdgcn_exp2f(v)
#define PIN(v) pin(v)
extern "C" __device__ float mul_unpacked(float, float) __asm("llvm.amdgcn.fmul.legacy");
#define MUL1(a, b) mul_unpacked(a, b)
#define FMA1(a, b, c) __builtin_amdgcn_div_fmasf(a, b, c, false)
DI v2u ld8_sc1_async(v4u srd, unsigned voff) {
  v2u r; asm volatile("buffer_load_dwordx2 %0, %1, %2, 0 offen sc1" : "=v"(r) : "v"(voff), "s"(srd) : "memory"); return r;
}
template <int N> DI void wait_vmf(v2u& a) { asm volatile("s_waitcnt vmcnt(%1)" : "+v"(a) : "i"(N) : "memory"); }
DI void arrive(unsigned cnt, int lane) {
  asm volatile("s_waitcnt lgkmcnt(0)" ::: "memory");
  if (lane == 0) __hip_atomic_fetch_add((LAS unsigned*)(size_t)cnt, 1u, __ATOMIC_RELAXED, __HIP_MEMORY_SCOPE_WORKGROUP);
}
DI void arrive_nowait(unsigned cnt) {
  asm volatile("s_mov_b64 exec, 1\n\tds_add_u32 %0, %1\n\ts_mov_b64 exec, -1" :: "v"(cnt), "v"(1u) : "memory");
}
DI unsigned peek(unsigned cnt) { return __hip_atomic_load((LAS unsigned*)(size_t)cnt, __ATOMIC_RELAXED, __HIP_MEMORY_SCOPE_WORKGROUP); }
DI void await(unsigned cnt, unsigned n) {
#pragma unroll 1
  for (unsigned spin = 0; spin < SPIN_CAP * 32; ++spin) {
    if (__builtin_amdgcn_readfirstlane(peek(cnt)) >= n) break;
    __builtin_amdgcn_s_sleep(1);
  }
  asm volatile("" ::: "memory");
}

__global__ __launch_bounds__(512, 2) void fused_block(const void* __restrict__ x, const void* __restrict__ wqkv, const void* __restrict__ wo,
                                                      const void* __restrict__ w1, const void* __restrict__ b1, const void* __restrict__ w2,
                                                      const void* __restrict__ b2, void* __restrict__ out, char* __restrict__ ws) {
  constexpr unsigned lds0 = 0;
  const int wave = __builtin_amdgcn_readfirstlane(threadIdx.x >> 6);
  const int m = wave & 3;
  const bool isG = wave < 4;
  const int xcd = blockIdx.x & 7, idx = blockIdx.x >> 3;
  const int r = (xcd >> 2) * 8 + (idx >> 2), j = (xcd & 3) * 4 + (idx & 3);
  const int rt = (xcd >> 1) * 8 + (idx >> 2), ct = (xcd & 1) * 4 + (idx & 3);
  int lane = threadIdx.x & 63, r32, h, g, q4, p4;
  STAMP_AT(0);
  asm volatile("" :: "s"(x), "s"(wqkv), "s"(wo), "s"(w1), "s"(b1), "s"(w2), "s"(b2), "s"(out), "s"(ws));

  const v4u sx = make_srd(x, SEQ * DM * 2), sws = make_srd(ws, WS_END);
  const __amdgpu_buffer_rsrc_t rws = __builtin_amdgcn_make_buffer_rsrc(ws, 0, WS_END, 0x00020000);
  const unsigned one2 = 0x3f803f80u;
  const int T = 4 * r + m;
  const int rot = ROT;
  auto flag_off = [&](int kind, int tile, int jj) { return WS_FLAG + ((kind * 64 + tile) * 16 + jj) * 128; };
  auto flag = [&](int kind, int tile, int jj) { return (GAS u64*)(ws + flag_off(kind, tile, jj)); };
  u64 tag;
  unsigned taglo, tag32;
  auto get_tag = [&]() {
    const v2u tg = *(const LAS v2u*)(size_t)(lds0 + L_CNT + 32);
    taglo = __builtin_amdgcn_readfirstlane(tg[0]); tag32 = __builtin_amdgcn_readfirstlane(tg[1]);
    tag = ((u64)tag32 << 32) | taglo;
  };
  v4u xres, bres, xaug, st;
  float mean, var, rstd, sd;
  v4u sel;
  unsigned xo[4], swz;
  auto lane_setup = [&]() {
    lane = threadIdx.x & 63; PIN(lane);
    r32 = lane & 31; h = lane >> 5; g = lane >> 4; q4 = (lane >> 2) & 3; p4 = lane & 3;
    const unsigned selv = ((lane & 15) == (g & 1)) ? one2 : 0u;
    sel = (v4u){selv, selv, selv, selv};
#pragma unroll
    for (int p = 0; p < 4; ++p) xo[p] = (32 * m + r32) * 128 + (((2 * ((p + m) & 3) + h) ^ ((r32 >> 1) & 7)) * 16);
    swz = ((((lane >> 2) & 1) ^ (g & 1)) * 64) + p4 * 16;
  };

  auto stat_load = [&]() { st = ld16_sc1_async(sws, WS_STATS + (128 * r + 32 * m + r32) * 16, 0); };
  auto res_loads = [&]() {
    int t2 = threadIdx.x; PIN(t2);
    const int row = 64 * rt + (t2 >> 3), col = 64 * ct + (t2 & 7) * 8;
    xres = ld16_async(sx, row * 1024 + col * 2, 0);
    bres = ld16_async(make_srd(b2, DM * 2), col * 2, 0);
  };
  f32x4 lo, hi;
  auto res_sum = [&]() {
    PIN(xres); PIN(bres);
    lo = (f32x4){bflo(xres[0]) + bflo(bres[0]), bfhi(xres[0]) + bfhi(bres[0]), bflo(xres[1]) + bflo(bres[1]), bfhi(xres[1]) + bfhi(bres[1])};
    hi = (f32x4){bflo(xres[2]) + bflo(bres[2]), bfhi(xres[2]) + bfhi(bres[2]), bflo(xres[3]) + bflo(bres[3]), bfhi(xres[3]) + bfhi(bres[3])};
    PIN(lo); PIN(hi);
  };
  auto row_stats = [&]() {
    if (!__all(st[1] == tag32 && st[3] == tag32)) {
#pragma unroll 1
      for (unsigned spin = 0; spin < SPIN_CAP; ++spin) {
        st = ld16_sc1_async(sws, WS_STATS + (128 * r + 32 * m + r32) * 16, 0);
        wait_vm1<0>(st);
        if (__all(st[1] == tag32 && st[3] == tag32)) break;
      }
    }
    mean = asf(st[0]); var = asf(st[2]);
    PIN(mean); PIN(var);
  };
  auto make_xaug = [&]() {
    PIN(mean); PIN(var);
    rstd = __builtin_amdgcn_rsqf(var); sd = var * rstd;
    const unsigned uh = pk2(-mean, sd);
    const unsigned ul = pk2(-mean - bflo(uh), FMA1(bfhi(uh), -1.f, sd));
    xaug = (v4u){(uh & 0xffffu) | (uh << 16), (ul & 0xffffu) | (uh & 0xffff0000u), ul >> 16, 0u};
    if (h) xaug = (v4u){0u, 0u, 0u, 0u};
    PIN(xaug);
  };
  auto cswr = [&](int tile, f32x4 c) {
    if (lane < 16) { LAS float* p = (LAS float*)(size_t)(lds0 + L_CS + (tile * 32 + lane) * 16 + m * 4); p[0] = c[0]; p[64] = c[1]; }
  };
  auto csrd = [&](int tile) { return __builtin_bit_cast(f32x4, lds128(lds0 + L_CS + (tile * 32 + r32) * 16)); };
  auto waug = [&](f32x4 c4, unsigned b16) {
    const float c = ((c4[0] + c4[1]) + c4[2]) + c4[3];
    const unsigned uh = pk2(c, 0.f);
    const unsigned ul = pk2(c - bflo(uh), 0.f);
    return (v4u){uh | (ul << 16), uh | (b16 << 16), b16, 0u};
  };

  const v4u sw1 = make_srd(w1, DM * FF * 2);
  const int hcol0 = 448 * (j >> 2) + 128 * (j & 3);
  const bool heavy = (j & 3) != 3;
  unsigned bias0, bias1;
  auto bias_load = [&](int P) {
    bias0 = ld2_async(make_srd(b1, FF * 2), r32 * 2, (hcol0 + 64 * P) * 2);
    bias1 = ld2_async(make_srd(b1, FF * 2), r32 * 2, (hcol0 + 64 * P + 32) * 2);
  };
  auto w1dma = [&](int P, int c, unsigned dst, int half) {
    const unsigned vw1 = (lane >> 3) * (FF * 2) + swz, vw1b = vw1 + 8 * (FF * 2) - 1024;
    const unsigned so = (64 * ((c + rot) & 7) + 16 * m) * (FF * 2) + (hcol0 + 64 * P) * 2;
    if (half == 0) dma16_a(sw1, vw1, so, dst + m * 2048); else dma16_b(sw1, vw1b, so);
  };
  f32x16 hac[2];
  unsigned rk[4][2];
  auto hsetup = [&](int P) {
#pragma unroll
    for (int p = 0; p < 4; ++p)
#pragma unroll
      for (int c = 0; c < 2; ++c) {
        rk[p][c] = lds0 + (P ? L_WA : w1buf(0)) + ((p + m) & 3) * 2048 + h * 1024 + q4 * 128 + ((c ^ (q4 >> 1)) * 64) + (g & 1) * 32 + p4 * 8;
        PIN(rk[p][c]);
      }
  };
  auto hmm = [&](auto pc, const v4u (&xf)[32], auto&& hook) {
    constexpr int P = pc.value;
    constexpr int D = 4;
    const unsigned cnt = lds0 + L_CNT + 4;
    f32x4 cs[2] = {}, c0, c1;
    v4u wf[D][2], au0, au1;
    unsigned seen = 0;
    hac[0] = (f32x16){}; hac[1] = (f32x16){};
    auto rd = [&](int ks, int t) { wf[ks % D][t] = ldtr2(rk[ks & 3][t] + ((ks >> 2) ^ 4) * 8192, 512); };
#pragma unroll
    for (int ks = 0; ks < D; ++ks) { rd(ks, 0); rd(ks, 1); }
    SBAR();
    static_for<0, 32>([&](auto kc) {
      constexpr int ks = kc.value;
      hook(kc);
      if constexpr (ks == 12) arrive_nowait(cnt + 16);
      if constexpr (ks == 29) seen = peek(cnt);
#pragma unroll
      for (int t = 0; t < 2; ++t) {
        hac[t] = MFMA32(wf[ks % D][t], xf[ks ^ 16], hac[t]);
        if ((ks & 3) == 0 && ks < 28) { cs[t] = MFMA16(sel, wf[ks % D][t], cs[t]); PIN(cs[t]); }
        if (ks == 26) { cs[t] = MFMA16(sel, wf[28 % D][t], cs[t]); PIN(cs[t]); }
        SBAR();
        if constexpr (ks + D < 32) rd(ks + D, t);
        if constexpr (ks == 31) { if (t == 0) { au0 = waug(c0, bias0); PIN(au0); } else { au1 = waug(c1, bias1); PIN(au1); } }
        SBAR();
      }
      if constexpr (ks == 30) {
        PIN(hac[0]); PIN(hac[1]);
        if (__builtin_amdgcn_readfirstlane(seen) < 4) await(cnt, 4);
        asm volatile("" ::: "memory");
        if constexpr (ks == 30) { c0 = csrd(2 * P); c1 = csrd(2 * P + 1); }
      }
      if constexpr (ks == 27) { cswr(2 * P, cs[0]); cswr(2 * P + 1, cs[1]); arrive_nowait(cnt); }
      SBAR();
    });
    hac[0] = MFMA32(au0, xaug, hac[0]);
    hac[1] = MFMA32(au1, xaug, hac[1]);
    SBAR();
  };
  auto hgelu = [&](auto pc, auto&& mid) {
    constexpr int P = pc.value;
    constexpr bool PK = GELU_PK(P);
    const float C0 = 2.3022081986f, C1 = C0 * 0.044715f;
    const float ga = -C1 * rstd * rstd * rstd, gb = -C0 * rstd;
    auto mul = [&](f32x2 a, f32x2 b) { return PK ? a * b : (f32x2){MUL1(a[0], b[0]), MUL1(a[1], b[1])}; };
    auto fma = [&](f32x2 a, float b, float c) {
      return PK ? __builtin_elementwise_fma(a, (f32x2){b, b}, (f32x2){c, c}) : (f32x2){FMA1(a[0], b, c), FMA1(a[1], b, c)}; };
#pragma unroll
    for (int t = 0; t < 2; ++t) {
      f32x2 v[8], z[8];
#pragma unroll
      for (int e = 0; e < 8; ++e) { v[e] = (f32x2){hac[t][2 * e], hac[t][2 * e + 1]}; z[e] = mul(v[e], v[e]); }
      SBAR();
#pragma unroll
      for (int e = 0; e < 8; ++e) z[e] = fma(z[e], ga, gb);
      SBAR();
#pragma unroll
      for (int e = 0; e < 8; ++e) z[e] = mul(z[e], v[e]);
      SBAR();
#pragma unroll
      for (int e = 0; e < 8; ++e) z[e] = (f32x2){EXP2(z[e][0]), EXP2(z[e][1])};
      SBAR();
#pragma unroll
      for (int e = 0; e < 8; ++e) z[e] = fma(z[e], sd, sd);
      SBAR();
#pragma unroll
      for (int e = 0; e < 8; ++e) z[e] = (f32x2){__builtin_amdgcn_rcpf(z[e][0]), __builtin_amdgcn_rcpf(z[e][1])};
      SBAR();
#pragma unroll
      for (int e = 0; e < 8; ++e) z[e] = mul(z[e], v[e]);
      SBAR();
#pragma unroll
      for (int s = 0; s < 2; ++s) {
        const v4u f = {pk2(z[4 * s][0], z[4 * s][1]), pk2(z[4 * s + 1][0], z[4 * s + 1][1]), pk2(z[4 * s + 2][0], z[4 * s + 2][1]), pk2(z[4 * s + 3][0], z[4 * s + 3][1])};
        st16_sc1(f, sws, lane * 16, WS_AF + (T * AFT + (hcol0 >> 4) + 4 * P + 2 * t + s) * 1024);
      }
      if (t == 0) mid();
      SBAR();
    }
  };

  if (isG) {
    lane_setup();
    const v4u sw2 = make_srd(w2, FF * DM * 2), swo = make_srd(wo, DM * DM * 2);
    const unsigned vw2 = (lane >> 3) * 1024 + swz, vw2b = vw2 + 7 * 1024;
    const int ro3 = rt % 3;
    auto kmap = [&](int kk) {
      if (kk < 16) return 8 * (((kk >> 2) + rt) & 3) + (kk & 3);
      const int p = ((kk - 16) >> 2) + ro3;
      return 8 * (p >= 3 ? p - 3 : p) + 4 + (kk & 3);
    };
    auto omap = [&](int i) { return 2 * ((i + rt) & 3) + (i >> 2); };
    auto wdmah = [&](int kk, int half) {
      const unsigned so = (kk < 28 ? 448 * m + 16 * kmap(kk) : 128 * m + 16 * omap(kk - 28)) * 1024 + 128 * ct;
      if (half == 0) dma16_a(kk < 28 ? sw2 : swo, vw2, so, lds0 + L_WG + m * PART + (kk % RD) * 2048); else dma16_b(kk < 28 ? sw2 : swo, vw2b, so);
    };
    auto wdma = [&](int kk) { wdmah(kk, 0); wdmah(kk, 1); };
    {
      const v4u swq = make_srd(wqkv, DM * 3 * DM * 2);
      const unsigned vx0 = (lane >> 3) * 1024 + (((lane & 7) ^ g) * 16), vx1 = (7 + (lane >> 3)) * 1024 + (((lane & 7) ^ (4 + g)) * 16);
      const unsigned vwq = (lane >> 2) * (3 * DM * 2) + p4 * 16;
      auto piece = [&](int s, int i) {
        const int kc = (s + rot) & 7;
        if (i < 4) {
          const unsigned so = (128 * r + 32 * m + 16 * (i >> 1)) * 1024 + kc * 128;
          if (i & 1) dma16_b(sx, vx1, so); else dma16_a(sx, vx0, so, lds0 + xbuf(s) + m * 4096 + (i >> 1) * 2048);
        } else if (i < 7) dma16(swq, vwq, (64 * kc + 16 * m) * (3 * DM * 2) + (512 * (i - 4) + 32 * j) * 2, lds0 + qbuf(s) + ((i - 4) * 4 + m) * 1024);
        else w1dma(0, s, lds0 + w1buf(s), i - 7);
      };
#pragma unroll
      for (int i = 0; i < 18; ++i) { piece(i / 9, i % 9); if (i == 8) __builtin_amdgcn_s_barrier(); }
      __builtin_amdgcn_s_setprio(PRIO_G1);
      hsetup(0);
      f32x4 cs[2] = {};
      v4u xf[4], wf[4][2];
      hac[0] = (f32x16){}; hac[1] = (f32x16){};
      static_for<0, 8>([&](auto sc) {
        constexpr int s = sc.value;
        auto fill = [&](int i) {
          if constexpr (s < 6) piece(s + 2, i);
          if constexpr (s == 6) {
            if (i == 0) { stat_load(); bias_load(0); }
            else if (i < 5) { if (heavy) w1dma(1, (i - 1) >> 1, lds0 + L_WA + ((i - 1) >> 1) * 8192, (i - 1) & 1); }
            else wdmah((i - 5) >> 1, (i - 5) & 1);
          }
          if constexpr (s == 7) {
            if (heavy && i < 4) w1dma(1, 2 + (i >> 1), lds0 + L_WA + (2 + (i >> 1)) * 8192, i & 1);
            if (heavy && i >= 4) { w1dma(1, i, lds0 + L_WA + i * 8192, 0); w1dma(1, i, 0, 1); }
          }
          SBAR();
        };
        auto rd = [&](int p) {
          xf[p] = lds128(xbuf(s) + xo[p]);
          wf[p][0] = ldtr2(rk[p][0] + (s % 3) * PART, 512); wf[p][1] = ldtr2(rk[p][1] + (s % 3) * PART, 512);
          SBAR();
        };
        if constexpr (s < 7) wait_vm<9>(); else { if (heavy) wait_vm<11>(); else wait_vm<7>(); }
        bar();
        STAMP_AT(10 + s);
        rd(0);
        if constexpr (s < 7) fill(0);
        rd(1);
        if constexpr (s < 7) fill(1);
        if constexpr (s == 2) get_tag();
#pragma unroll
        for (int p = 0; p < 4; ++p) {
#pragma unroll
          for (int t = 0; t < 2; ++t) {
            hac[t] = MFMA32(wf[p][t], xf[p], hac[t]);
            if (p == 0) { cs[t] = MFMA16(sel, wf[p][t], cs[t]); PIN(cs[t]); }
            SBAR();
            if (p < 2 && t == 0) rd(p + 2);
            if (s == 7) fill(2 * p + t); else if (2 * p + t < 7) fill(2 + 2 * p + t);
          }
          if (s == 7 && p == 1) {
            cswr(0, cs[0]); cswr(1, cs[1]);
            bar();
            STAMP_AT(1);
          }
        }
        if constexpr (s == 7) {
          const f32x4 c0 = csrd(0), c1 = csrd(1);
          if (heavy) wait_vm<20>(); else wait_vm<4>();
          PIN(st); PIN(bias0); PIN(bias1);
          row_stats(); make_xaug();
          hac[0] = MFMA32(waug(c0, bias0), xaug, hac[0]);
          hac[1] = MFMA32(waug(c1, bias1), xaug, hac[1]);
          SBAR();
        }
      });
    }
    STAMP_AT(18);
    if (heavy) { wait_vm<0>(); arrive_nowait(lds0 + L_CNT + 16); }
    hgelu(std::integral_constant<int, 0>{}, [&]() {});
    __builtin_amdgcn_s_setprio(0);
    STAMP_AT(19);
    unsigned rb2[2];
#pragma unroll
    for (int c = 0; c < 2; ++c) rb2[c] = lds0 + L_WG + m * PART + h * 512 + q4 * 128 + ((c ^ (q4 >> 1)) * 64) + (g & 1) * 32 + p4 * 8;
    v4u wq[36][2];
    auto wrd = [&](int kk) {
#pragma unroll
      for (int c = 0; c < 2; ++c) wq[kk][c] = ldtr2(rb2[c] + (kk % RD) * 2048, 1024);
    };
    auto wgot = [&](int kk) { PIN(wq[kk][0]); PIN(wq[kk][1]); };
    const int ftile = 2 * rt + ((lane >> 2) & 1);
    const int T0 = 4 * r - 8 + 2 * m;
    const bool live = T0 >= 0;
    auto kv_fetch = [&]() {
      const unsigned src = WS_KV + (j * 64 + (live ? T0 : 0)) * 4096, vk = live ? lane * 16 : h * 512, vv = live ? lane * 16 : 0x40000000u;
#pragma unroll
      for (int i = 0; i < 8; ++i)
        dma16_sc1(sws, (i & 2) ? vv : vk, src + (live ? i : (i & 3)) * 1024, lds0 + L_KV + (2 * m + (i >> 2)) * KVT + (i & 3) * 1024 + ((i & 3) == 3 ? VB - 1024 : 0));
      const unsigned on = (lane < 2 || live) ? one2 : 0u;
      if (lane < 6) sts128(lds0 + L_KV + (lane < 2 ? 8 + m : 2 * m + ((lane - 2) >> 1)) * KVT + 2048 + (lane & 1) * VB + 1024, (v4u){on, on, on, on});
    };
    v4u af[28][2], of[8][2];
    v2u fl;
    const unsigned a0 = WS_AF + (2 * rt * AFT + 28 * m) * 1024, o0 = WS_AF + (2 * rt * AFT + 112 + 8 * m) * 1024;
    fl = ld8_sc1_async(sws, flag_off(FL_KV, live ? T0 + (lane & 1) : 0, j));
    for (int kk = 2; kk < 6; ++kk) wdma(kk);
    wait_vmf<8>(fl);
    if (lane == 0) __hip_atomic_store(flag(FL_H0, T, j), tag, RLX_AGENT);
    STAMP_AT(2);
    if (!__all(fl[0] == taglo && fl[1] == tag32)) poll_flags(flag(FL_KV, live ? T0 + (lane & 1) : 0, j), tag);
    if (heavy) await(lds0 + L_CNT + 20, 4);
    kv_fetch();
    wdma(6); wdma(7);
    static_for<0, 3>([&](auto ic) {
      constexpr int i = ic.value;
      wait_vm<(i == 0) ? 17 : 8>();
      for (int kk = 4 * i; kk < 4 * i + 4; ++kk) wrd(kk);
      for (int kk = 4 * i; kk < 4 * i + 4; ++kk) wgot(kk);
      if constexpr (i == 0) { wait_vm<4>(); arrive_nowait(lds0 + L_CNT); }
      if constexpr (i == 2) fl = ld8_sc1_async(sws, flag_off(FL_H0, ftile, 4 * m + p4));
      for (int kk = 4 * i; kk < 4 * i + 4; ++kk) wdma(kk + RD);
    });
    STAMP_AT(3);
    wait_vmf<8>(fl);
    if (!__all(fl[0] == taglo && fl[1] == tag32)) poll_flags(flag(FL_H0, ftile, 4 * m + p4), tag);
    STAMP_AT(4);
#pragma unroll
    for (int kk = 0; kk < AW; ++kk) {
      af[kk][0] = ld16_sc1_async(sws, lane * 16, a0 + kmap(kk) * 1024);
      af[kk][1] = ld16_sc1_async(sws, lane * 16, a0 + (AFT + kmap(kk)) * 1024);
    }
    __builtin_amdgcn_s_setprio(PRIO_G2);
    f32x16 c2[2][2] = {};
    auto mm2 = [&](int kk, const v4u (&a)[2], auto&& fill) {
      SBAR();
#pragma unroll
      for (int i = 0; i < 4; ++i) { c2[i >> 1][i & 1] = MFMA32(wq[kk][i & 1], a[i >> 1], c2[i >> 1][i & 1]); SBAR(); fill(i); SBAR(); }
    };
    static_for<0, 28>([&](auto kc) {
      constexpr int kk = kc.value;
      wait_vm2<(kk < AW) ? 2 * (AW - 1 - kk) + g2_range(0, kk) : g2_range(kk - AW + 1, kk)>(af[kk][0], af[kk][1]);
      if constexpr (kk == H1_CK) {
        STAMP_AT(20);
        wait_vmf<g2_range(H1_LD, H1_CK) - 1>(fl);
        if (!__all(fl[0] == taglo && fl[1] == tag32)) poll_flags(flag(FL_H1, ftile, 4 * m + p4), tag);
        STAMP_AT(21);
      }
      if constexpr (kk == H1_LD) fl = ld8_sc1_async(sws, flag_off(FL_H1, ftile, 4 * m + p4));
      if constexpr (kk == O_LD) fl = ld8_sc1_async(sws, flag_off(FL_O0, ftile, 4 * m + p4));
      if constexpr (kk == 26) fl = ld8_sc1_async(sws, flag_off(FL_O1, ftile, 4 * m + p4));
      constexpr bool dw = kk >= 1 && kk - 1 + NR + RD < 36;
      if constexpr (dw) wgot(kk - 1 + NR);
      mm2(kk, af[kk], [&](int i) {
        if constexpr (dw) { if (i < 2) wdmah(kk - 1 + NR + RD, i); }
        if constexpr (kk + NR < 28) { if (i == 1) wrd(kk + NR); }
        if constexpr (kk == 27) { if (i == 0) wrd(28); }
        if constexpr (kk + AW < 28) { if (i >= 2) af[kk + AW][i - 2] = ld16_sc1_async(sws, lane * 16, a0 + (AFT * (i - 2) + kmap(kk + AW)) * 1024); }
      });
      if constexpr (kk == O_CK) {
        wait_vmf<g2_range(O_LD, O_CK) - 1>(fl);
        if (!__all(fl[0] == taglo && fl[1] == tag32)) poll_flags(flag(FL_O0, ftile, 4 * m + p4), tag);
        res_loads();
#pragma unroll
        for (int i = 0; i < 4; ++i) {
          of[i][0] = ld16_sc1_async(sws, lane * 16, o0 + omap(i) * 1024);
          of[i][1] = ld16_sc1_async(sws, lane * 16, o0 + (AFT + omap(i)) * 1024);
        }
      }
    });
    static_for<0, 4>([&](auto kc) {
      constexpr int kk = kc.value;
      if (kk < 3) wrd(28 + kk + 1);
      if constexpr (kk == 1) {
        res_sum();
        STAMP_AT(5);
        wait_vmf<0>(fl);
        if (!__all(fl[0] == taglo && fl[1] == tag32)) poll_flags(flag(FL_O1, ftile, 4 * m + p4), tag);
        STAMP_AT(6);
#pragma unroll
        for (int i = 0; i < 8; ++i) of[4 + (i & 3)][i >> 2] = ld16_sc1_async(sws, lane * 16, o0 + (AFT * (i >> 2) + omap(4 + (i & 3))) * 1024);
      }
      wait_vm2<(kk == 0) ? 7 : 14 - 2 * kk>(of[kk][0], of[kk][1]);
      mm2(28 + kk, of[kk], [&](int i) { PIN(c2[i >> 1][i & 1]); if (kk == 3) wrd(32 + i); });
    });
    for (int kk = 32; kk < 36; ++kk) wgot(kk);
    unsigned pa = lds0 + L_WG + m * PART + lane * 16;
    PIN(pa);
    auto put = [&](int qn, int qd) {
      const f32x16& c = c2[qn >> 1][qn & 1];
      sts128(pa + (qn * 4 + qd) * 66 * 16, __builtin_bit_cast(v4u, (f32x4){c[4 * qd], c[4 * qd + 1], c[4 * qd + 2], c[4 * qd + 3]}));
    };
    static_for<0, 4>([&](auto qc) {
      constexpr int qn = qc.value;
      if constexpr ((qn & 1) == 0) { wait_vm2<qn ? 0 : 4>(of[4][qn >> 1], of[5][qn >> 1]); PIN(of[6][qn >> 1]); PIN(of[7][qn >> 1]); }
      SBAR();
#pragma unroll
      for (int i = 0; i < 4; ++i) {
        c2[qn >> 1][qn & 1] = MFMA32(wq[32 + i][qn & 1], of[4 + i][qn >> 1], c2[qn >> 1][qn & 1]);
        SBAR();
        if (qn > 0 && i >= 2) { put(qn - 1, 2 * i - 4); put(qn - 1, 2 * i - 3); }
        SBAR();
      }
    });
#pragma unroll
    for (int qd = 0; qd < 4; ++qd) put(3, qd);
    STAMP_AT(7);
  } else {
    __builtin_amdgcn_s_setprio(1);
    lane_setup();
    v4u xr[2];
    xr[0] = ld16_async(sx, lane * 16, (128 * r + 8 * j + 2 * m) * 1024); xr[1] = ld16_async(sx, lane * 16, (128 * r + 8 * j + 2 * m + 1) * 1024);
    unsigned warm;
    {
      u64 pc; asm volatile("s_getpc_b64 %0" : "=s"(pc));
      warm = ld2_async(make_srd((const void*)(pc & ~127ull), CODEWARM), lane < 3 ? lane * 128 : 0x40000000u, (idx * 4 + m) * 384);
    }
    __builtin_amdgcn_s_barrier();
    v2u ep = ld8_sc1_async(sws, m == 0 ? WS_EPOCH : 0x40000000u);
    float sval;
    const int wi = blockIdx.x * 4 + m;
    const u64 wbase = wi < 448 ? (u64)w1 + wi * 4096 : wi < 896 ? (u64)w2 + (wi - 448) * 4096 : (u64)wo + (wi - 896) * 4096;
    auto make_stats = [&]() {
      wait_vm2<1>(xr[0], xr[1]);
      PIN(warm);
      float piv[2], a[4];
#pragma unroll
      for (int u = 0; u < 2; ++u) {
        piv[u] = rdl(bflo(xr[u][0]), 0);
        float s1 = 0.f, s2 = 0.f;
#pragma unroll
        for (int i = 0; i < 8; ++i) { const float d = ((i & 1) ? bfhi(xr[u][i >> 1]) : bflo(xr[u][i >> 1])) - piv[u]; s1 += d; s2 = __builtin_fmaf(d, d, s2); }
        a[2 * u] = s1; a[2 * u + 1] = s2;
      }
#pragma unroll
      for (int i = 0; i < 4; ++i) a[i] += dpp<0xB1>(a[i]);
#pragma unroll
      for (int i = 0; i < 4; ++i) a[i] += dpp<0x4E>(a[i]);
      float t = p4 == 0 ? a[0] : p4 == 1 ? a[1] : p4 == 2 ? a[2] : a[3];
      t += dpp<0x114>(t); t += dpp<0x118>(t);
      const auto s16 = __builtin_amdgcn_permlane16_swap(asu(t), asu(t), false, false);
      t = asf(s16[0]) + asf(s16[1]);
      const auto s32 = __builtin_amdgcn_permlane32_swap(asu(t), asu(t), false, false);
      t = (asf(s32[0]) + asf(s32[1])) * (1.f / DM);
      const float e1 = dpp<0xA0>(t);
      sval = (p4 & 1) ? __builtin_fmaxf(__builtin_fmaf(-e1, e1, t), 0.f) + 1e-5f : t + (p4 ? piv[1] : piv[0]);
      PIN(sval);
    };
    const unsigned acnt = lds0 + L_CNT;
    const unsigned vpos = 2048 + (g & 1) * 512 + h * 256 + (lane & 15) * 16;
    v4u qf[2], am0, am1;
    u64 um[16];
    auto masks = [&]() {
      const int dq = r32 - 4 * h;
#pragma unroll
      for (int u = 0; u < 16; ++u) { um[u] = __builtin_amdgcn_ballot_w64((u & 3) + 8 * (u >> 2) > dq); pin_s(um[u]); }
#pragma unroll
      for (int d = 0; d < 4; ++d) {
        am0[d] = __builtin_amdgcn_inverse_ballot_w64(um[2 * d]) ? 0xffffffffu : __builtin_amdgcn_inverse_ballot_w64(um[2 * d + 1]) ? 0xffff0000u : 0u;
        am1[d] = __builtin_amdgcn_inverse_ballot_w64(um[8 + 2 * d]) ? 0xffffffffu : __builtin_amdgcn_inverse_ballot_w64(um[9 + 2 * d]) ? 0xffff0000u : 0u;
      }
      PIN(am0); PIN(am1);
    };
    {
      v4u xf[32];
      f32x16 acc[3] = {};
      f32x4 cs[3] = {};
      v4u wf[4][3];
      unsigned wb[4];
#pragma unroll
      for (int p = 0; p < 4; ++p) { wb[p] = qbuf(0) + ((p + m) & 3) * 1024 + (8 * h + q4) * 64 + (g & 1) * 32 + p4 * 8; PIN(wb[p]); }
      auto rd = [&](int ks, int t) {
        if (t == 0) xf[ks] = lds128(xbuf(ks >> 2) + xo[ks & 3]);
        wf[ks & 3][t] = ldtr2(wb[ks & 3] + ((ks >> 2) % 3) * 12288 + t * 4096, 256);
      };
      auto mm = [&](int ks, int t) {
        acc[t] = t == 2 ? MFMA32(xf[ks], wf[ks & 3][t], acc[t]) : MFMA32(wf[ks & 3][t], xf[ks], acc[t]);
        if ((ks & 3) == 0) { cs[t] = MFMA16(sel, wf[ks & 3][t], cs[t]); PIN(cs[t]); }
        SBAR();
      };
      make_stats();
      bar();
      STAMP_AT(10);
#pragma unroll
      for (int t = 0; t < 6; ++t) rd(t / 3, t % 3);
      SBAR();
      static_for<0, 8>([&](auto sc) {
        constexpr int s = sc.value;
#pragma unroll
        for (int p = 0; p < 2; ++p) {
#pragma unroll
          for (int t = 0; t < 3; ++t) { mm(4 * s + p, t); rd(4 * s + p + 2, t); SBAR(); }
          if (s == 7 && p == 0) { cswr(4, cs[0]); cswr(5, cs[1]); cswr(6, cs[2]); SBAR(); }
        }
        if constexpr (s == 0) {
          wait_vmf<0>(ep);
          if (m == 0 && lane < 10)
            *(LAS unsigned*)(size_t)(lds0 + L_CNT + lane * 4) = lane == 8 ? ep[0] * 0x9E3779B1u + 0x7F4A7C15u : lane == 9 ? ep[1] * 0x85EBCA77u + 0x165667B1u : 0u;
        }
        if constexpr (s == 1) {
          get_tag();
          if ((lane >> 2) == 3) {
            const v2u gr = {asu(sval), tag32};
            __builtin_amdgcn_raw_buffer_store_b64(gr, rws, WS_STATS + (128 * r + 8 * j + 2 * m) * 16 + p4 * 8, 0, 16);
          }
        }
        if constexpr (s == MIDWARM) {
          warm = ld2_async(make_srd((const void*)wbase, 4096), lane * 128, 0);
        }
        if constexpr (s == 5) stat_load();
        if constexpr (s == 6) {
          wait_vm1<0>(st);
          PIN(warm);

          row_stats();
        }
        mm(4 * s + 2, 0); mm(4 * s + 2, 1);
        bar();
        if constexpr (s < 7) {
          STAMP_AT(11 + s);
          mm(4 * s + 2, 2);
          rd(4 * s + 4, 0); rd(4 * s + 4, 1); rd(4 * s + 4, 2); SBAR();
#pragma unroll
          for (int t = 0; t < 3; ++t) { mm(4 * s + 3, t); rd(4 * s + 5, t); SBAR(); }
        }
      });
      STAMP_AT(1);
      __builtin_amdgcn_s_setprio(3);
      const f32x4 c5 = csrd(5), c6 = csrd(6), c4 = csrd(4);
      SBAR();
      mm(30, 2); mm(31, 1);
      make_xaug();
      SBAR();
      mm(31, 2); mm(31, 0);
      if (h == 0) *(LAS float*)(size_t)(lds0 + L_RS + (m * 32 + r32) * 4) = rstd;
      acc[1] = MFMA32(waug(c5, 0u), xaug, acc[1]);
      acc[2] = MFMA32(xaug, waug(c6, 0u), acc[2]);
      acc[0] = MFMA32(waug(c4, 0u), xaug, acc[0]);
      SBAR();
      f32x4 rs[4];
#pragma unroll
      for (int qd = 0; qd < 4; ++qd) rs[qd] = __builtin_bit_cast(f32x4, lds128(lds0 + L_RS + (m * 32 + 8 * qd + 4 * h) * 4));
      const unsigned kvo = WS_KV + (j * 64 + T) * 4096;
#pragma unroll
      for (int e = 0; e < 16; ++e) acc[1][e] = MUL1(acc[1][e], rstd);
#pragma unroll
      for (int e = 0; e < 2; ++e) {
        const v4u f = pack8(acc[1], e);
        st16_sc1(f, sws, lane * 16, kvo + e * 1024);
        sts128(lds0 + L_KV + (8 + m) * KVT + e * 1024 + lane * 16, f);
      }
#pragma unroll
      for (int qd = 0; qd < 4; ++qd)
#pragma unroll
        for (int e = 0; e < 4; ++e) acc[2][4 * qd + e] = MUL1(acc[2][4 * qd + e], rs[qd][e]);
#pragma unroll
      for (int s = 0; s < 2; ++s) {
        const v4u f = pack8(acc[2], s);
        st16_sc1(f, sws, vpos, kvo + s * 1024);
        sts128(lds0 + L_KV + (8 + m) * KVT + s * VB + vpos, f);
      }
      SBAR();
      const float qs = rstd * (0.25f * 1.4426950408889634f);
#pragma unroll
      for (int e = 0; e < 16; ++e) acc[0][e] = MUL1(acc[0][e], qs);
      qf[0] = pack8(acc[0], 0); qf[1] = pack8(acc[0], 1);
      PIN(qf[0]); PIN(qf[1]);
      arrive_nowait(acnt);
      STAMP_AT(2);
      if (heavy) {
        hsetup(1);
        await(acnt + 16, 4);
        STAMP_AT(3);
        hmm(std::integral_constant<int, 1>{}, xf, [&](auto kc) {
          constexpr int ks = kc.value;
          if constexpr (ks == KVPUB) {
            wait_vm<0>();
            if (lane == 0) __hip_atomic_store(flag(FL_KV, T, j), tag, RLX_AGENT);
          }
          if constexpr (ks == KVPUB + 2) bias_load(1);
          if constexpr (ks == 31) { wait_vm<0>(); PIN(bias0); PIN(bias1); }
        });
        STAMP_AT(18);
        hgelu(std::integral_constant<int, 1>{}, [&]() {});
        STAMP_AT(4);
      } else {
        wait_vm<0>();
        if (lane == 0) __hip_atomic_store(flag(FL_KV, T, j), tag, RLX_AGENT);
      }
    }
    masks();
    STAMP_AT(19);
    const unsigned kb = lds0 + L_KV + m * KVT + lane * 16;
    unsigned vb[2];
#pragma unroll
    for (int e = 0; e < 2; ++e) { vb[e] = lds0 + L_KV + m * KVT + 2048 + ((g & 1) ? 1024 : e * 512 + h * 256 + (lane & 15) * 16); PIN(vb[e]); }
    await(acnt, 8);
    __builtin_amdgcn_s_setprio(PRIO_ATT);
    STAMP_AT(5);
    const f32x16 zero = {};
    auto o_store = [&](f32x16& o, int e) {
      const float rl = __builtin_amdgcn_rcpf(o[8]);
#pragma unroll
      for (int u = 0; u < 8; ++u) o[u] = MUL1(o[u], rl);
      st16_sc1(pack8(o, 0), sws, lane * 16, WS_AF + (T * AFT + 112 + 2 * j + e) * 1024);
    };
    int redo = 0;
    {
      f32x16 o[2], sa, sb, su;
      bool pend = false;
      v4u kn, ku, va, vc, vn0, vn1, vu0, vu1, p0, p1, pu;
      auto kaddr = [&](int n) { return kb + ((n & 7) + 1) * KVT + (n >> 3) * 1024; };
      auto vaddr = [&](int n) { return vb[n >> 3] + ((n & 7) + 1) * KVT; };
      kn = lds128(kaddr(0)); vn0 = lds128(vaddr(0)); vn1 = lds128(vaddr(0) + VB);
      sa = MFMA32(kn, qf[0], zero);
      kn = lds128(kaddr(1));
      SBAR();
      auto step = [&](auto nc, f32x16& sc, f32x16& sn) {
        constexpr int n = nc.value, e = n >> 3, i = n & 7;
        if constexpr (n < 15) sn = MFMA32(kn, qf[(n + 1) >> 3], zero);
        if constexpr (n < 14) kn = lds128(kaddr(n + 2));
        if constexpr (i == 5) ku = lds128(kb + e * 1024);
        v4u v0 = vn0, v1 = vn1;
        if constexpr (n < 15) { vn0 = lds128(vaddr(n + 1)); vn1 = lds128(vaddr(n + 1) + VB); }
        if constexpr (i == 6) { vu0 = lds128(vb[e]); vu1 = lds128(vb[e] + VB); }
        SBAR();
        if constexpr (i == 7) {
#pragma unroll
          for (int u = 0; u < 16; ++u) sc[u] = __builtin_amdgcn_inverse_ballot_w64(um[u]) ? su[u] : sc[u];
          PIN(sc); SBAR();
        }
#pragma unroll
        for (int u = 0; u < 6; ++u) sc[u] = EXP2(sc[u]);
        PIN(sc); SBAR();
        if constexpr (n == 8) { o[0] = MFMA32(vc, p1, o[0]); SBAR(); }
        else if constexpr (n > 0) { o[e] = MFMA32(vc, p1, o[e]); SBAR(); }
        sc[6] = EXP2(sc[6]); sc[7] = EXP2(sc[7]);
        p0 = pack8(sc, 0);
        sc[8] = EXP2(sc[8]); sc[9] = EXP2(sc[9]);
        if constexpr (i == 7) { va = p0 & am0; p0 &= ~am0; PIN(va); }
        PIN(sc); PIN(p0); SBAR();
        if constexpr (n == 8) { o[0] = MFMA32(vu1, pu, o[0]); SBAR(); }
        o[e] = MFMA32(v0, p0, i == 0 ? zero : o[e]);
        SBAR();
        if constexpr (i == 6) { su = MFMA32(ku, qf[e], zero); SBAR(); }
#pragma unroll
        for (int u = 10; u < 13; ++u) sc[u] = EXP2(sc[u]);
        if constexpr (i == 7) { PIN(sc); SBAR(); o[e] = MFMA32(vu0, va, o[e]); SBAR(); }
#pragma unroll
        for (int u = 13; u < 16; ++u) sc[u] = EXP2(sc[u]);
        p1 = pack8(sc, 1); vc = v1;
        if constexpr (i == 7) { pu = p1 & am1; p1 &= ~am1; PIN(pu); }
        PIN(p1); SBAR();
        if constexpr (n == 8) {
          if (__all(o[0][8] >= 0x1p-30f && o[0][8] <= 0x1p30f)) o_store(o[0], 0); else redo = 1;
          SBAR();
        }
        if constexpr (i == 2) pend = !redo;
        if constexpr (i == 3 || i == 5 || i == 7) {
          if (pend && (i == 7 || (__builtin_amdgcn_s_getreg(7 | (3 << 11)) & 15) == 0)) {
            wait_vm<0>();
            if (lane == 0) __hip_atomic_store(flag(FL_H1 + e, T, j), tag, RLX_AGENT);
            pend = false;
          }
        }
      };
      static_for<0, 8>([&](auto hc) {
        step(std::integral_constant<int, 2 * hc.value>{}, sa, sb);
        step(std::integral_constant<int, 2 * hc.value + 1>{}, sb, sa);
      });
      o[1] = MFMA32(vc, p1, o[1]);
      o[1] = MFMA32(vu1, pu, o[1]);
      if (__all(o[1][8] >= 0x1p-30f && o[1][8] <= 0x1p30f)) o_store(o[1], 1); else redo |= 2;
    }
    if (redo) {
#pragma unroll 1
      for (int e = 0; e < 2; ++e) {
        if (!((redo >> e) & 1)) continue;
        const v4u q = e ? qf[1] : qf[0];
        const unsigned vbe = e ? vb[1] : vb[0];
        auto merged = [&](const f32x16& c) {
          f32x16 sl = MFMA32(lds128(kb + 8 * KVT + e * 1024), q, c);
          const f32x16 su = MFMA32(lds128(kb + e * 1024), q, c);
          static_for<0, 16>([&](auto u) { sl[u.value] = upper<u.value>() ? su[u.value] : sl[u.value]; });
          return sl;
        };
        float mx = -INFINITY;
#pragma unroll 1
        for (int i = 0; i < 8; ++i) {
          const f32x16 sc = i ? MFMA32(lds128(kb + i * KVT + e * 1024), q, zero) : merged(zero);
#pragma unroll
          for (int u = 0; u < 16; u += 2) mx = max3(mx, sc[u], sc[u + 1]);
        }
        const auto sw = __builtin_amdgcn_permlane32_swap(asu(mx), asu(mx), false, false);
        mx = __builtin_fmaxf(asf(sw[0]), asf(sw[1]));
        f32x16 negm, o;
#pragma unroll
        for (int u = 0; u < 16; ++u) negm[u] = -mx;
        {
          f32x16 sc = merged(negm);
#pragma unroll
          for (int u = 0; u < 16; ++u) sc[u] = EXP2(sc[u]);
          const v4u p0 = pack8(sc, 0), p1 = pack8(sc, 1);
          o = MFMA32(lds128(vbe), p0 & am0, zero);
          o = MFMA32(lds128(vbe + VB), p1 & am1, o);
          o = MFMA32(lds128(vbe + 8 * KVT), p0 & ~am0, o);
          o = MFMA32(lds128(vbe + 8 * KVT + VB), p1 & ~am1, o);
        }
#pragma unroll 1
        for (int i = 1; i < 8; ++i) {
          f32x16 sc = MFMA32(lds128(kb + i * KVT + e * 1024), q, negm);
#pragma unroll
          for (int u = 0; u < 16; ++u) sc[u] = EXP2(sc[u]);
          o = MFMA32(lds128(vbe + i * KVT), pack8(sc, 0), o);
          o = MFMA32(lds128(vbe + i * KVT + VB), pack8(sc, 1), o);
        }
        o_store(o, e);
      }
      wait_vm<0>();
      if (lane == 0) __hip_atomic_store(flag(FL_O0, T, j), tag, RLX_AGENT);
    }
    STAMP_AT(6);
    wait_vm<0>();
    if (lane == 0) __hip_atomic_store(flag(FL_O1, T, j), tag, RLX_AGENT);
    STAMP_AT(7);
    res_loads();
    wait_vm<0>();
    res_sum();
    if (blockIdx.x == 0 && wave == 7) {
#pragma unroll 1
      for (int i = 0; i < 4; ++i) poll_flags(flag(FL_KV, 4 * (4 * i + g), lane & 15), tag);
      if (lane == 0) __hip_atomic_store((GAS u64*)(ws + WS_EPOCH), tag, RLX_AGENT);
    }
  }
  {
    int t2 = threadIdx.x; PIN(t2);
    const int row = t2 >> 3, ch = t2 & 7;
    unsigned a = lds0 + L_WG + (((row >> 5) * 8 + ch) * 66 + (row & 31)) * 16, oo = (64 * rt + row) * 1024 + (64 * ct + ch * 8) * 2;
    PIN(a); PIN(oo);
    bar();
    f32x4 pl[4], ph[4];
#pragma unroll
    for (int w = 0; w < 4; ++w) { pl[w] = __builtin_bit_cast(f32x4, lds128(a + w * PART)); ph[w] = __builtin_bit_cast(f32x4, lds128(a + w * PART + 512)); }
    SBAR();
#pragma unroll
    for (int w = 0; w < 4; ++w) { lo += pl[w]; hi += ph[w]; }
    const v4u o = {pk2(lo[0], lo[1]), pk2(lo[2], lo[3]), pk2(hi[0], hi[1]), pk2(hi[2], hi[3])};
    __builtin_amdgcn_raw_buffer_store_b128(o, __builtin_amdgcn_make_buffer_rsrc(out, 0, SEQ * DM * 2, 0x00020000), oo, 0, 16);
  }
  STAMP_AT(8);
}

extern "C" void kernel_launch(void* const* d_in, const int* in_sizes, int n_in, void* d_out, int out_size, void* d_ws, size_t ws_size,
                              hipStream_t stream) {
  (void)in_sizes; (void)n_in; (void)out_size; (void)ws_size;
  (void)hipFuncSetAttribute((const void*)fused_block, hipFuncAttributeMaxDynamicSharedMemorySize, LDS_BYTES);
  fused_block<<<256, 512, LDS_BYTES, stream>>>(d_in[0], d_in[1], d_in[2], d_in[4], d_in[5], d_in[6], d_in[7], d_out, (char*)d_ws);
}
```

```cpp
#include <hip/hip_runtime.h>
#include <hip/hip_bf16.h>
#include <stdint.h>
#include <type_traits>

typedef unsigned v4u __attribute__((ext_vector_type(4)));
typedef unsigned v2u __attribute__((ext_vector_type(2)));
typedef float f32x2 __attribute__((ext_vector_type(2)));
typedef float f32x4 __attribute__((ext_vector_type(4)));
typedef float f32x16 __attribute__((ext_vector_type(16)));
typedef short bf16x8 __attribute__((ext_vector_type(8)));
typedef short s16x4 __attribute__((ext_vector_type(4)));
typedef __bf16 bf16x2_t __attribute__((ext_vector_type(2)));
typedef unsigned long long u64;
#define GAS __attribute__((address_space(1)))
#define LAS __attribute__((address_space(3)))
#define DI __device__ __forceinline__
#define RLX_AGENT __ATOMIC_RELAXED, __HIP_MEMORY_SCOPE_AGENT

constexpr int SEQ = 2048, DM = 512, FF = 1792;
constexpr unsigned WS_EPOCH = 0;
constexpr unsigned WS_STATS = 32768;
constexpr unsigned WS_FLAG = 65536;
constexpr unsigned WS_KV = WS_FLAG + 5 * 1024 * 128;
constexpr int AFT = 145;
constexpr unsigned WS_AF = WS_KV + 16 * 64 * 4096;
constexpr unsigned WS_END = WS_AF + 64 * AFT * 1024;
constexpr unsigned WS_STAMP = WS_END;
enum { FL_KV = 0, FL_H0 = 1, FL_H1 = 2, FL_O0 = 3, FL_O1 = 4 };
constexpr int L_WA = 0;
constexpr int L_WG = 86016;
constexpr int VB = 1040;
constexpr int KVT = 2048 + 2 * VB;
constexpr int L_KV = 32768;
constexpr int RD = 8, NR = 12;
constexpr int PART = 16 * 66 * 16;
constexpr int L_CS = L_WG + 4 * PART;
constexpr int L_RS = L_CS + 3584;
constexpr int L_CNT = L_RS + 512;
constexpr int LDS_BYTES = L_CNT + 64;
constexpr int xbuf(int s) { return ((s + 1) % 3) * 16384; }
constexpr int qbuf(int s) { return 49152 + (s % 3) * 12288; }
constexpr int w1buf(int s) { return L_WG + (s % 3) * PART + 8192; }
static_assert(L_KV + 8 * KVT >= 65536 && xbuf(7) == 32768 && qbuf(2) + 12288 <= L_WG && L_KV + 12 * KVT <= L_WG && PART >= RD * 2048 && LDS_BYTES <= 163840, "lds map");
#define AWIN 6
constexpr int AW = AWIN;
constexpr unsigned SPIN_CAP = 1u << 23;

#define MFMA32(a, b, c) __builtin_amdgcn_mfma_f32_32x32x16_bf16(__builtin_bit_cast(bf16x8, a), __builtin_bit_cast(bf16x8, b), c, 0, 0, 0)
#define MFMA16(a, b, c) __builtin_amdgcn_mfma_f32_16x16x32_bf16(__builtin_bit_cast(bf16x8, a), __builtin_bit_cast(bf16x8, b), c, 0, 0, 0)
#define SBAR() __builtin_amdgcn_sched_barrier(0)

#define STAMP_AT(i) do { } while (0)

DI float asf(unsigned u) { return __builtin_bit_cast(float, u); }
DI unsigned asu(float f) { return __builtin_bit_cast(unsigned, f); }
DI unsigned pk2(float lo, float hi) { return __builtin_bit_cast(unsigned, __builtin_convertvector((f32x2){lo, hi}, bf16x2_t)); }
DI float bflo(unsigned u) { return __builtin_bit_cast(float, u << 16); }
DI float bfhi(unsigned u) { return __builtin_bit_cast(float, u & 0xffff0000u); }
DI v4u make_srd(const void* p, unsigned bytes) { const u64 a = (u64)p; return (v4u){(unsigned)a, (unsigned)(a >> 32) & 0xffffu, bytes, 0x00020000u}; }
DI void dma16(v4u srd, unsigned voff, unsigned soff, unsigned lds) {
  asm volatile("s_mov_b32 m0, %3\n\ts_nop 0\n\tbuffer_load_dwordx4 %0, %1, %2 offen lds" :: "v"(voff), "s"(srd), "s"(soff), "s"(lds) : "memory");
}
DI void dma16_sc1(v4u srd, unsigned voff, unsigned soff, unsigned lds) {
  asm volatile("s_mov_b32 m0, %3\n\ts_nop 0\n\tbuffer_load_dwordx4 %0, %1, %2 offen sc1 lds" :: "v"(voff), "s"(srd), "s"(soff), "s"(lds) : "memory");
}
DI void dma16_a(v4u srd, unsigned voff, unsigned soff, unsigned lds) { dma16(srd, voff, soff, lds); }
DI void dma16_b(v4u srd, unsigned voff1, unsigned soff) {
  asm volatile("buffer_load_dwordx4 %0, %1, %2 offen offset:1024 lds" :: "v"(voff1), "s"(srd), "s"(soff) : "memory");
}
DI v4u ld16_sc1_async(v4u srd, unsigned voff, unsigned soff) {
  v4u r; asm volatile("buffer_load_dwordx4 %0, %1, %2, %3 offen sc1" : "=v"(r) : "v"(voff), "s"(srd), "s"(soff) : "memory"); return r;
}
DI v4u ld16_async(v4u srd, unsigned voff, unsigned soff) {
  v4u r; asm volatile("buffer_load_dwordx4 %0, %1, %2, %3 offen" : "=v"(r) : "v"(voff), "s"(srd), "s"(soff) : "memory"); return r;
}
DI unsigned ld2_async(v4u srd, unsigned voff, unsigned soff) {
  unsigned r; asm volatile("buffer_load_ushort %0, %1, %2, %3 offen" : "=v"(r) : "v"(voff), "s"(srd), "s"(soff) : "memory"); return r;
}
DI void st16_sc1(v4u v, v4u srd, unsigned voff, unsigned soff) {
  asm volatile("buffer_store_dwordx4 %0, %1, %2, %3 offen sc1\n\ts_nop 1" :: "v"(v), "v"(voff), "s"(srd), "s"(soff) : "memory");
}
template <int N> DI void wait_vm1(v4u& a) { asm volatile("s_waitcnt vmcnt(%1)" : "+v"(a) : "i"(N) : "memory"); }
template <int N> DI void wait_vm2(v4u& a, v4u& b) { asm volatile("s_waitcnt vmcnt(%2)" : "+v"(a), "+v"(b) : "i"(N) : "memory"); }
template <int N> DI void wait_vm3(v4u& a, v4u& b, v4u& c) { asm volatile("s_waitcnt vmcnt(%3)" : "+v"(a), "+v"(b), "+v"(c) : "i"(N) : "memory"); }
DI void pin_s(u64& v) { asm volatile("" : "+s"(v)); }
template <class V> DI void pin(V& v) { asm volatile("" : "+v"(v)); }
template <int N> DI void wait_vm() { asm volatile("s_waitcnt vmcnt(%0)" :: "i"(N) : "memory"); }
DI void bar() { asm volatile("s_waitcnt lgkmcnt(0)\n\ts_barrier" ::: "memory"); }
DI v4u lds128(unsigned a) { return *(const LAS v4u*)(size_t)a; }
DI void sts128(unsigned a, v4u v) { *(LAS v4u*)(size_t)a = v; }
DI v2u ldtr(unsigned a) { return __builtin_bit_cast(v2u, __builtin_amdgcn_ds_read_tr16_b64_v4i16((LAS s16x4*)(size_t)a)); }
DI v4u ldtr2(unsigned a, int second) { const v2u lo = ldtr(a), hi = ldtr(a + second); return (v4u){lo.x, lo.y, hi.x, hi.y}; }
template <int CTRL> DI float dpp(float v) { return __builtin_bit_cast(float, __builtin_amdgcn_update_dpp(0, __builtin_bit_cast(int, v), CTRL, 0xf, 0xf, true)); }
DI float rdl(float v, int l) { return __builtin_bit_cast(float, __builtin_amdgcn_readlane(__builtin_bit_cast(int, v), l)); }
DI v4u pack8(const f32x16& a, int s) { return (v4u){pk2(a[8 * s], a[8 * s + 1]), pk2(a[8 * s + 2], a[8 * s + 3]), pk2(a[8 * s + 4], a[8 * s + 5]), pk2(a[8 * s + 6], a[8 * s + 7])}; }
DI void poll_flags(const GAS u64* p, u64 tag) {
#pragma unroll 1
  for (unsigned spin = 0; spin < SPIN_CAP; ++spin) {
    const u64 v = __hip_atomic_load(p, RLX_AGENT);
    if (__all(v == tag)) break;
    __builtin_amdgcn_s_sleep(1);
  }
  asm volatile("" ::: "memory");
}
constexpr u64 upper_mask(int reg) {
  u64 m = 0;
  for (int l = 0; l < 64; ++l) { const int row = (reg & 3) + 8 * (reg >> 2) + 4 * (l >> 5); if (row > (l & 31)) m |= 1ull << l; }
  return m;
}
template <int REG> DI bool upper() { return __builtin_amdgcn_inverse_ballot_w64(upper_mask(REG)); }
template <int I, int N, class Fn> DI void static_for(Fn&& f) { if constexpr (I < N) { f(std::integral_constant<int, I>{}); static_for<I + 1, N>(f); } }
DI float max3(float a, float b, float c) { return __builtin_fmaxf(__builtin_fmaxf(a, b), c); }

constexpr int H1_CK = 16 - AW, H1_LD = H1_CK - 3, O_CK = 28 - AW, O_LD = O_CK - 3;
constexpr int g2_issued(int t) { return ((t >= 1 && t - 1 + NR + RD < 36) ? 2 : 0) + ((t + AW < 28) ? 2 : 0) + (t == H1_LD) + (t == O_LD) + (t == O_CK ? 10 : 0) + (t == 26); }
constexpr int g2_range(int a, int b) { int n = 0; for (int t = a; t < b; ++t) n += g2_issued(t); return n; }
#define GELU_PK(P) (P == 1)
#define PRIO_G2 2
#define PRIO_ATT 3
#define PRIO_G1 2
#define ROT r
#define CODEWARM 38000
#define MIDWARM 1
#define KVPUB 2
#define EXP2(v) __builtin_amdgcn_exp2f(v)
#define PIN(v) pin(v)
extern "C" __device__ float mul_unpacked(float, float) __asm("llvm.amdgcn.fmul.legacy");
#define MUL1(a, b) mul_unpacked(a, b)
#define FMA1(a, b, c) __builtin_amdgcn_div_fmasf(a, b, c, false)
DI v2u ld8_sc1_async(v4u srd, unsigned voff) {
  v2u r; asm volatile("buffer_load_dwordx2 %0, %1, %2, 0 offen sc1" : "=v"(r) : "v"(voff), "s"(srd) : "memory"); return r;
}
template <int N> DI void wait_vmf(v2u& a) { asm volatile("s_waitcnt vmcnt(%1)" : "+v"(a) : "i"(N) : "memory"); }
DI void arrive(unsigned cnt, int lane) {
  asm volatile("s_waitcnt lgkmcnt(0)" ::: "memory");
  if (lane == 0) __hip_atomic_fetch_add((LAS unsigned*)(size_t)cnt, 1u, __ATOMIC_RELAXED, __HIP_MEMORY_SCOPE_WORKGROUP);
}
DI void arrive_nowait(unsigned cnt) {
  asm volatile("s_mov_b64 exec, 1\n\tds_add_u32 %0, %1\n\ts_mov_b64 exec, -1" :: "v"(cnt), "v"(1u) : "memory");
}
DI unsigned peek(unsigned cnt) { return __hip_atomic_load((LAS unsigned*)(size_t)cnt, __ATOMIC_RELAXED, __HIP_MEMORY_SCOPE_WORKGROUP); }
DI void await(unsigned cnt, unsigned n) {
#pragma unroll 1
  for (unsigned spin = 0; spin < SPIN_CAP * 32; ++spin) {
    if (__builtin_amdgcn_readfirstlane(peek(cnt)) >= n) break;
    __builtin_amdgcn_s_sleep(1);
  }
  asm volatile("" ::: "memory");
}

__global__ __launch_bounds__(512, 2) void fused_block(const void* __restrict__ x, const void* __restrict__ wqkv, const void* __restrict__ wo,
                                                      const void* __restrict__ w1, const void* __restrict__ b1, const void* __restrict__ w2,
                                                      const void* __restrict__ b2, void* __restrict__ out, char* __restrict__ ws) {
  constexpr unsigned lds0 = 0;
  const int wave = __builtin_amdgcn_readfirstlane(threadIdx.x >> 6);
  const int m = wave & 3;
  const bool isG = wave < 4;
  const int xcd = blockIdx.x & 7, idx = blockIdx.x >> 3;
  const int r = (xcd >> 2) * 8 + (idx >> 2), j = (xcd & 3) * 4 + (idx & 3);
  const int rt = (xcd >> 1) * 8 + (idx >> 2), ct = (xcd & 1) * 4 + (idx & 3);
  int lane = threadIdx.x & 63, r32, h, g, q4, p4;
  STAMP_AT(0);
  asm volatile("" :: "s"(x), "s"(wqkv), "s"(wo), "s"(w1), "s"(b1), "s"(w2), "s"(b2), "s"(out), "s"(ws));

  const v4u sx = make_srd(x, SEQ * DM * 2), sws = make_srd(ws, WS_END);
  const __amdgpu_buffer_rsrc_t rws = __builtin_amdgcn_make_buffer_rsrc(ws, 0, WS_END, 0x00020000);
  const unsigned one2 = 0x3f803f80u;
  const int T = 4 * r + m;
  const int rot = ROT;
  auto flag_off = [&](int kind, int tile, int jj) { return WS_FLAG + ((kind * 64 + tile) * 16 + jj) * 128; };
  auto flag = [&](int kind, int tile, int jj) { return (GAS u64*)(ws + flag_off(kind, tile, jj)); };
  u64 tag;
  unsigned taglo, tag32;
  auto get_tag = [&]() {
    const v2u tg = *(const LAS v2u*)(size_t)(lds0 + L_CNT + 32);
    taglo = __builtin_amdgcn_readfirstlane(tg[0]); tag32 = __builtin_amdgcn_readfirstlane(tg[1]);
    tag = ((u64)tag32 << 32) | taglo;
  };
  v4u xres, bres, xaug, st;
  float mean, var, rstd, sd;
  v4u sel;
  unsigned xo[4], swz;
  auto lane_setup = [&]() {
    lane = threadIdx.x & 63; PIN(lane);
    r32 = lane & 31; h = lane >> 5; g = lane >> 4; q4 = (lane >> 2) & 3; p4 = lane & 3;
    const unsigned selv = ((lane & 15) == (g & 1)) ? one2 : 0u;
    sel = (v4u){selv, selv, selv, selv};
#pragma unroll
    for (int p = 0; p < 4; ++p) xo[p] = (32 * m + r32) * 128 + (((2 * ((p + m) & 3) + h) ^ ((r32 >> 1) & 7)) * 16);
    swz = ((((lane >> 2) & 1) ^ (g & 1)) * 64) + p4 * 16;
  };

  auto stat_load = [&]() { st = ld16_sc1_async(sws, WS_STATS + (128 * r + 32 * m + r32) * 16, 0); };
  auto res_loads = [&]() {
    int t2 = threadIdx.x; PIN(t2);
    const int row = 64 * rt + (t2 >> 3), col = 64 * ct + (t2 & 7) * 8;
    xres = ld16_async(sx, row * 1024 + col * 2, 0);
    bres = ld16_async(make_srd(b2, DM * 2), col * 2, 0);
  };
  f32x4 lo, hi;
  auto res_sum = [&]() {
    PIN(xres); PIN(bres);
    lo = (f32x4){bflo(xres[0]) + bflo(bres[0]), bfhi(xres[0]) + bfhi(bres[0]), bflo(xres[1]) + bflo(bres[1]), bfhi(xres[1]) + bfhi(bres[1])};
    hi = (f32x4){bflo(xres[2]) + bflo(bres[2]), bfhi(xres[2]) + bfhi(bres[2]), bflo(xres[3]) + bflo(bres[3]), bfhi(xres[3]) + bfhi(bres[3])};
    PIN(lo); PIN(hi);
  };
  auto row_stats = [&]() {
    if (!__all(st[1] == tag32 && st[3] == tag32)) {
#pragma unroll 1
      for (unsigned spin = 0; spin < SPIN_CAP; ++spin) {
        st = ld16_sc1_async(sws, WS_STATS + (128 * r + 32 * m + r32) * 16, 0);
        wait_vm1<0>(st);
        if (__all(st[1] == tag32 && st[3] == tag32)) break;
      }
    }
    mean = asf(st[0]); var = asf(st[2]);
    PIN(mean); PIN(var);
  };
  auto make_xaug = [&]() {
    PIN(mean); PIN(var);
    rstd = __builtin_amdgcn_rsqf(var); sd = var * rstd;
    const unsigned uh = pk2(-mean, sd);
    const unsigned ul = pk2(-mean - bflo(uh), FMA1(bfhi(uh), -1.f, sd));
    xaug = (v4u){(uh & 0xffffu) | (uh << 16), (ul & 0xffffu) | (uh & 0xffff0000u), ul >> 16, 0u};
    if (h) xaug = (v4u){0u, 0u, 0u, 0u};
    PIN(xaug);
  };
  auto cswr = [&](int tile, f32x4 c) {
    if (lane < 16) { LAS float* p = (LAS float*)(size_t)(lds0 + L_CS + (tile * 32 + lane) * 16 + m * 4); p[0] = c[0]; p[64] = c[1]; }
  };
  auto csrd = [&](int tile) { return __builtin_bit_cast(f32x4, lds128(lds0 + L_CS + (tile * 32 + r32) * 16)); };
  auto waug = [&](f32x4 c4, unsigned b16) {
    const float c = ((c4[0] + c4[1]) + c4[2]) + c4[3];
    const unsigned uh = pk2(c, 0.f);
    const unsigned ul = pk2(c - bflo(uh), 0.f);
    return (v4u){uh | (ul << 16), uh | (b16 << 16), b16, 0u};
  };

  const v4u sw1 = make_srd(w1, DM * FF * 2);
  const int hcol0 = 448 * (j >> 2) + 128 * (j & 3);
  const bool heavy = (j & 3) != 3;
  unsigned bias0, bias1;
  auto bias_load = [&](int P) {
    bias0 = ld2_async(make_srd(b1, FF * 2), r32 * 2, (hcol0 + 64 * P) * 2);
    bias1 = ld2_async(make_srd(b1, FF * 2), r32 * 2, (hcol0 + 64 * P + 32) * 2);
  };
  auto w1dma = [&](int P, int c, unsigned dst, int half) {
    const unsigned vw1 = (lane >> 3) * (FF * 2) + swz, vw1b = vw1 + 8 * (FF * 2) - 1024;
    const unsigned so = (64 * ((c + rot) & 7) + 16 * m) * (FF * 2) + (hcol0 + 64 * P) * 2;
    if (half == 0) dma16_a(sw1, vw1, so, dst + m * 2048); else dma16_b(sw1, vw1b, so);
  };
  f32x16 hac[2];
  unsigned rk[4][2];
  auto hsetup = [&](int P) {
#pragma unroll
    for (int p = 0; p < 4; ++p)
#pragma unroll
      for (int c = 0; c < 2; ++c) {
        rk[p][c] = lds0 + (P ? L_WA : w1buf(0)) + ((p + m) & 3) * 2048 + h * 1024 + q4 * 128 + ((c ^ (q4 >> 1)) * 64) + (g & 1) * 32 + p4 * 8;
        PIN(rk[p][c]);
      }
  };
  auto hmm = [&](auto pc, const v4u (&xf)[32], auto&& hook) {
    constexpr int P = pc.value;
    constexpr int D = 4;
    const unsigned cnt = lds0 + L_CNT + 4;
    f32x4 cs[2] = {}, c0, c1;
    v4u wf[D][2], au0, au1;
    unsigned seen = 0;
    hac[0] = (f32x16){}; hac[1] = (f32x16){};
    auto rd = [&](int ks, int t) { wf[ks % D][t] = ldtr2(rk[ks & 3][t] + ((ks >> 2) ^ 4) * 8192, 512); };
#pragma unroll
    for (int ks = 0; ks < D; ++ks) { rd(ks, 0); rd(ks, 1); }
    SBAR();
    static_for<0, 32>([&](auto kc) {
      constexpr int ks = kc.value;
      hook(kc);
      if constexpr (ks == 12) arrive_nowait(cnt + 16);
      if constexpr (ks == 29) seen = peek(cnt);
#pragma unroll
      for (int t = 0; t < 2; ++t) {
        hac[t] = MFMA32(wf[ks % D][t], xf[ks ^ 16], hac[t]);
        if ((ks & 3) == 0 && ks < 28) { cs[t] = MFMA16(sel, wf[ks % D][t], cs[t]); PIN(cs[t]); }
        if (ks == 26) { cs[t] = MFMA16(sel, wf[28 % D][t], cs[t]); PIN(cs[t]); }
        SBAR();
        if constexpr (ks + D < 32) rd(ks + D, t);
        if constexpr (ks == 31) { if (t == 0) { au0 = waug(c0, bias0); PIN(au0); } else { au1 = waug(c1, bias1); PIN(au1); } }
        SBAR();
      }
      if constexpr (ks == 30) {
        PIN(hac[0]); PIN(hac[1]);
        if (__builtin_amdgcn_readfirstlane(seen) < 4) await(cnt, 4);
        asm volatile("" ::: "memory");
        if constexpr (ks == 30) { c0 = csrd(2 * P); c1 = csrd(2 * P + 1); }
      }
      if constexpr (ks == 27) { cswr(2 * P, cs[0]); cswr(2 * P + 1, cs[1]); arrive_nowait(cnt); }
      SBAR();
    });
    hac[0] = MFMA32(au0, xaug, hac[0]);
    hac[1] = MFMA32(au1, xaug, hac[1]);
    SBAR();
  };
  auto hgelu = [&](auto pc, auto&& mid) {
    constexpr int P = pc.value;
    constexpr bool PK = GELU_PK(P);
    const float C0 = 2.3022081986f, C1 = C0 * 0.044715f;
    const float ga = -C1 * rstd * rstd * rstd, gb = -C0 * rstd;
    auto mul = [&](f32x2 a, f32x2 b) { return PK ? a * b : (f32x2){MUL1(a[0], b[0]), MUL1(a[1], b[1])}; };
    auto fma = [&](f32x2 a, float b, float c) {
      return PK ? __builtin_elementwise_fma(a, (f32x2){b, b}, (f32x2){c, c}) : (f32x2){FMA1(a[0], b, c), FMA1(a[1], b, c)}; };
#pragma unroll
    for (int t = 0; t < 2; ++t) {
      f32x2 v[8], z[8];
#pragma unroll
      for (int e = 0; e < 8; ++e) { v[e] = (f32x2){hac[t][2 * e], hac[t][2 * e + 1]}; z[e] = mul(v[e], v[e]); }
      SBAR();
#pragma unroll
      for (int e = 0; e < 8; ++e) z[e] = fma(z[e], ga, gb);
      SBAR();
#pragma unroll
      for (int e = 0; e < 8; ++e) z[e] = mul(z[e], v[e]);
      SBAR();
#pragma unroll
      for (int e = 0; e < 8; ++e) z[e] = (f32x2){EXP2(z[e][0]), EXP2(z[e][1])};
      SBAR();
#pragma unroll
      for (int e = 0; e < 8; ++e) z[e] = fma(z[e], sd, sd);
      SBAR();
#pragma unroll
      for (int e = 0; e < 8; ++e) z[e] = (f32x2){__builtin_amdgcn_rcpf(z[e][0]), __builtin_amdgcn_rcpf(z[e][1])};
      SBAR();
#pragma unroll
      for (int e = 0; e < 8; ++e) z[e] = mul(z[e], v[e]);
      SBAR();
#pragma unroll
      for (int s = 0; s < 2; ++s) {
        const v4u f = {pk2(z[4 * s][0], z[4 * s][1]), pk2(z[4 * s + 1][0], z[4 * s + 1][1]), pk2(z[4 * s + 2][0], z[4 * s + 2][1]), pk2(z[4 * s + 3][0], z[4 * s + 3][1])};
        st16_sc1(f, sws, lane * 16, WS_AF + (T * AFT + (hcol0 >> 4) + 4 * P + 2 * t + s) * 1024);
      }
      if (t == 0) mid();
      SBAR();
    }
  };

  if (isG) {
    lane_setup();
    const v4u sw2 = make_srd(w2, FF * DM * 2), swo = make_srd(wo, DM * DM * 2);
    const unsigned vw2 = (lane >> 3) * 1024 + swz, vw2b = vw2 + 7 * 1024;
    const int ro3 = rt % 3;
    auto kmap = [&](int kk) {
      if (kk < 16) return 8 * (((kk >> 2) + rt) & 3) + (kk & 3);
      const int p = ((kk - 16) >> 2) + ro3;
      return 8 * (p >= 3 ? p - 3 : p) + 4 + (kk & 3);
    };
    auto omap = [&](int i) { return 2 * ((i + rt) & 3) + (i >> 2); };
    auto wdmah = [&](int kk, int half) {
      const unsigned so = (kk < 28 ? 448 * m + 16 * kmap(kk) : 128 * m + 16 * omap(kk - 28)) * 1024 + 128 * ct;
      if (half == 0) dma16_a(kk < 28 ? sw2 : swo, vw2, so, lds0 + L_WG + m * PART + (kk % RD) * 2048); else dma16_b(kk < 28 ? sw2 : swo, vw2b, so);
    };
    auto wdma = [&](int kk) { wdmah(kk, 0); wdmah(kk, 1); };
    {
      const v4u swq = make_srd(wqkv, DM * 3 * DM * 2);
      const unsigned vx0 = (lane >> 3) * 1024 + (((lane & 7) ^ g) * 16), vx1 = (7 + (lane >> 3)) * 1024 + (((lane & 7) ^ (4 + g)) * 16);
      const unsigned vwq = (lane >> 2) * (3 * DM * 2) + p4 * 16;
      auto piece = [&](int s, int i) {
        const int kc = (s + rot) & 7;
        if (i < 4) {
          const unsigned so = (128 * r + 32 * m + 16 * (i >> 1)) * 1024 + kc * 128;
          if (i & 1) dma16_b(sx, vx1, so); else dma16_a(sx, vx0, so, lds0 + xbuf(s) + m * 4096 + (i >> 1) * 2048);
        } else if (i < 7) dma16(swq, vwq, (64 * kc + 16 * m) * (3 * DM * 2) + (512 * (i - 4) + 32 * j) * 2, lds0 + qbuf(s) + ((i - 4) * 4 + m) * 1024);
        else w1dma(0, s, lds0 + w1buf(s), i - 7);
      };
#pragma unroll
      for (int i = 0; i < 18; ++i) { piece(i / 9, i % 9); if (i == 8) __builtin_amdgcn_s_barrier(); }
      __builtin_amdgcn_s_setprio(PRIO_G1);
      hsetup(0);
      f32x4 cs[2] = {};
      v4u xf[4], wf[4][2];
      hac[0] = (f32x16){}; hac[1] = (f32x16){};
      static_for<0, 8>([&](auto sc) {
        constexpr int s = sc.value;
        auto fill = [&](int i) {
          if constexpr (s < 6) piece(s + 2, i);
          if constexpr (s == 6) {
            if (i == 0) { stat_load(); bias_load(0); }
            else if (i < 5) { if (heavy) w1dma(1, (i - 1) >> 1, lds0 + L_WA + ((i - 1) >> 1) * 8192, (i - 1) & 1); }
            else wdmah((i - 5) >> 1, (i - 5) & 1);
          }
          if constexpr (s == 7) {
            if (heavy && i < 4) w1dma(1, 2 + (i >> 1), lds0 + L_WA + (2 + (i >> 1)) * 8192, i & 1);
            if (heavy && i >= 4) { w1dma(1, i, lds0 + L_WA + i * 8192, 0); w1dma(1, i, 0, 1); }
          }
          SBAR();
        };
        auto rd = [&](int p) {
          xf[p] = lds128(xbuf(s) + xo[p]);
          wf[p][0] = ldtr2(rk[p][0] + (s % 3) * PART, 512); wf[p][1] = ldtr2(rk[p][1] + (s % 3) * PART, 512);
          SBAR();
        };
        if constexpr (s < 7) wait_vm<9>(); else { if (heavy) wait_vm<11>(); else wait_vm<7>(); }
        bar();
        STAMP_AT(10 + s);
        rd(0);
        if constexpr (s < 7) fill(0);
        rd(1);
        if constexpr (s < 7) fill(1);
        if constexpr (s == 2) get_tag();
#pragma unroll
        for (int p = 0; p < 4; ++p) {
#pragma unroll
          for (int t = 0; t < 2; ++t) {
            hac[t] = MFMA32(wf[p][t], xf[p], hac[t]);
            if (p == 0) { cs[t] = MFMA16(sel, wf[p][t], cs[t]); PIN(cs[t]); }
            SBAR();
            if (p < 2 && t == 0) rd(p + 2);
            if (s == 7) fill(2 * p + t); else if (2 * p + t < 7) fill(2 + 2 * p + t);
          }
          if (s == 7 && p == 1) {
            cswr(0, cs[0]); cswr(1, cs[1]);
            bar();
            STAMP_AT(1);
          }
        }
        if constexpr (s == 7) {
          const f32x4 c0 = csrd(0), c1 = csrd(1);
          if (heavy) wait_vm<20>(); else wait_vm<4>();
          PIN(st); PIN(bias0); PIN(bias1);
          row_stats(); make_xaug();
          hac[0] = MFMA32(waug(c0, bias0), xaug, hac[0]);
          hac[1] = MFMA32(waug(c1, bias1), xaug, hac[1]);
          SBAR();
        }
      });
    }
    STAMP_AT(18);
    if (heavy) { wait_vm<0>(); arrive_nowait(lds0 + L_CNT + 16); }
    hgelu(std::integral_constant<int, 0>{}, [&]() {});
    __builtin_amdgcn_s_setprio(0);
    STAMP_AT(19);
    unsigned rb2[2];
#pragma unroll
    for (int c = 0; c < 2; ++c) rb2[c] = lds0 + L_WG + m * PART + h * 512 + q4 * 128 + ((c ^ (q4 >> 1)) * 64) + (g & 1) * 32 + p4 * 8;
    v4u wq[36][2];
    auto wrd = [&](int kk) {
#pragma unroll
      for (int c = 0; c < 2; ++c) wq[kk][c] = ldtr2(rb2[c] + (kk % RD) * 2048, 1024);
    };
    auto wgot = [&](int kk) { PIN(wq[kk][0]); PIN(wq[kk][1]); };
    const int ftile = 2 * rt + ((lane >> 2) & 1);
    const int T0 = 4 * r - 8 + 2 * m;
    const bool live = T0 >= 0;
    auto kv_fetch = [&]() {
      const unsigned src = WS_KV + (j * 64 + (live ? T0 : 0)) * 4096, vk = live ? lane * 16 : h * 512, vv = live ? lane * 16 : 0x40000000u;
#pragma unroll
      for (int i = 0; i < 8; ++i)
        dma16_sc1(sws, (i & 2) ? vv : vk, src + (live ? i : (i & 3)) * 1024, lds0 + L_KV + (2 * m + (i >> 2)) * KVT + (i & 3) * 1024 + ((i & 3) == 3 ? VB - 1024 : 0));
      const unsigned on = (lane < 2 || live) ? one2 : 0u;
      if (lane < 6) sts128(lds0 + L_KV + (lane < 2 ? 8 + m : 2 * m + ((lane - 2) >> 1)) * KVT + 2048 + (lane & 1) * VB + 1024, (v4u){on, on, on, on});
    };
    v4u af[28][2], of[8][2];
    v2u fl;
    const unsigned a0 = WS_AF + (2 * rt * AFT + 28 * m) * 1024, o0 = WS_AF + (2 * rt * AFT + 112 + 8 * m) * 1024;
    fl = ld8_sc1_async(sws, flag_off(FL_KV, live ? T0 + (lane & 1) : 0, j));
    for (int kk = 2; kk < 6; ++kk) wdma(kk);
    wait_vmf<8>(fl);
    if (lane == 0) __hip_atomic_store(flag(FL_H0, T, j), tag, RLX_AGENT);
    STAMP_AT(2);
    if (!__all(fl[0] == taglo && fl[1] == tag32)) poll_flags(flag(FL_KV, live ? T0 + (lane & 1) : 0, j), tag);
    if (heavy) await(lds0 + L_CNT + 20, 4);
    kv_fetch();
    wdma(6); wdma(7);
    static_for<0, 3>([&](auto ic) {
      constexpr int i = ic.value;
      wait_vm<(i == 0) ? 17 : 8>();
      for (int kk = 4 * i; kk < 4 * i + 4; ++kk) wrd(kk);
      for (int kk = 4 * i; kk < 4 * i + 4; ++kk) wgot(kk);
      if constexpr (i == 0) { wait_vm<4>(); arrive_nowait(lds0 + L_CNT); }
      if constexpr (i == 2) fl = ld8_sc1_async(sws, flag_off(FL_H0, ftile, 4 * m + p4));
      for (int kk = 4 * i; kk < 4 * i + 4; ++kk) wdma(kk + RD);
    });
    STAMP_AT(3);
    wait_vmf<8>(fl);
    if (!__all(fl[0] == taglo && fl[1] == tag32)) poll_flags(flag(FL_H0, ftile, 4 * m + p4), tag);
    STAMP_AT(4);
#pragma unroll
    for (int kk = 0; kk < AW; ++kk) {
      af[kk][0] = ld16_sc1_async(sws, lane * 16, a0 + kmap(kk) * 1024);
      af[kk][1] = ld16_sc1_async(sws, lane * 16, a0 + (AFT + kmap(kk)) * 1024);
    }
    __builtin_amdgcn_s_setprio(PRIO_G2);
    f32x16 c2[2][2] = {};
    auto mm2 = [&](int kk, const v4u (&a)[2], auto&& fill) {
      SBAR();
#pragma unroll
      for (int i = 0; i < 4; ++i) { c2[i >> 1][i & 1] = MFMA32(wq[kk][i & 1], a[i >> 1], c2[i >> 1][i & 1]); SBAR(); fill(i); SBAR(); }
    };
    static_for<0, 28>([&](auto kc) {
      constexpr int kk = kc.value;
      wait_vm2<(kk < AW) ? 2 * (AW - 1 - kk) + g2_range(0, kk) : g2_range(kk - AW + 1, kk)>(af[kk][0], af[kk][1]);
      if constexpr (kk == H1_CK) {
        STAMP_AT(20);
        wait_vmf<g2_range(H1_LD, H1_CK) - 1>(fl);
        if (!__all(fl[0] == taglo && fl[1] == tag32)) poll_flags(flag(FL_H1, ftile, 4 * m + p4), tag);
        STAMP_AT(21);
      }
      if constexpr (kk == H1_LD) fl = ld8_sc1_async(sws, flag_off(FL_H1, ftile, 4 * m + p4));
      if constexpr (kk == O_LD) fl = ld8_sc1_async(sws, flag_off(FL_O0, ftile, 4 * m + p4));
      if constexpr (kk == 26) fl = ld8_sc1_async(sws, flag_off(FL_O1, ftile, 4 * m + p4));
      constexpr bool dw = kk >= 1 && kk - 1 + NR + RD < 36;
      if constexpr (dw) wgot(kk - 1 + NR);
      mm2(kk, af[kk], [&](int i) {
        if constexpr (dw) { if (i < 2) wdmah(kk - 1 + NR + RD, i); }
        if constexpr (kk + NR < 28) { if (i == 1) wrd(kk + NR); }
        if constexpr (kk == 27) { if (i == 0) wrd(28); }
        if constexpr (kk + AW < 28) { if (i >= 2) af[kk + AW][i - 2] = ld16_sc1_async(sws, lane * 16, a0 + (AFT * (i - 2) + kmap(kk + AW)) * 1024); }
      });
      if constexpr (kk == O_CK) {
        wait_vmf<g2_range(O_LD, O_CK) - 1>(fl);
        if (!__all(fl[0] == taglo && fl[1] == tag32)) poll_flags(flag(FL_O0, ftile, 4 * m + p4), tag);
        res_loads();
#pragma unroll
        for (int i = 0; i < 4; ++i) {
          of[i][0] = ld16_sc1_async(sws, lane * 16, o0 + omap(i) * 1024);
          of[i][1] = ld16_sc1_async(sws, lane * 16, o0 + (AFT + omap(i)) * 1024);
        }
      }
    });
    static_for<0, 4>([&](auto kc) {
      constexpr int kk = kc.value;
      if (kk < 3) wrd(28 + kk + 1);
      if constexpr (kk == 1) {
        res_sum();
        STAMP_AT(5);
        wait_vmf<0>(fl);
        if (!__all(fl[0] == taglo && fl[1] == tag32)) poll_flags(flag(FL_O1, ftile, 4 * m + p4), tag);
        STAMP_AT(6);
#pragma unroll
        for (int i = 0; i < 8; ++i) of[4 + (i & 3)][i >> 2] = ld16_sc1_async(sws, lane * 16, o0 + (AFT * (i >> 2) + omap(4 + (i & 3))) * 1024);
      }
      wait_vm2<(kk == 0) ? 7 : 14 - 2 * kk>(of[kk][0], of[kk][1]);
      mm2(28 + kk, of[kk], [&](int i) { PIN(c2[i >> 1][i & 1]); if (kk == 3) wrd(32 + i); });
    });
    for (int kk = 32; kk < 36; ++kk) wgot(kk);
    unsigned pa = lds0 + L_WG + m * PART + lane * 16;
    PIN(pa);
    auto put = [&](int qn, int qd) {
      const f32x16& c = c2[qn >> 1][qn & 1];
      sts128(pa + (qn * 4 + qd) * 66 * 16, __builtin_bit_cast(v4u, (f32x4){c[4 * qd], c[4 * qd + 1], c[4 * qd + 2], c[4 * qd + 3]}));
    };
    static_for<0, 4>([&](auto qc) {
      constexpr int qn = qc.value;
      if constexpr ((qn & 1) == 0) { wait_vm2<qn ? 0 : 4>(of[4][qn >> 1], of[5][qn >> 1]); PIN(of[6][qn >> 1]); PIN(of[7][qn >> 1]); }
      SBAR();
#pragma unroll
      for (int i = 0; i < 4; ++i) {
        c2[qn >> 1][qn & 1] = MFMA32(wq[32 + i][qn & 1], of[4 + i][qn >> 1], c2[qn >> 1][qn & 1]);
        SBAR();
        if (qn > 0 && i >= 2) { put(qn - 1, 2 * i - 4); put(qn - 1, 2 * i - 3); }
        SBAR();
      }
    });
#pragma unroll
    for (int qd = 0; qd < 4; ++qd) put(3, qd);
    STAMP_AT(7);
  } else {
    __builtin_amdgcn_s_setprio(1);
    lane_setup();
    v4u xr[2];
    xr[0] = ld16_async(sx, lane * 16, (128 * r + 8 * j + 2 * m) * 1024); xr[1] = ld16_async(sx, lane * 16, (128 * r + 8 * j + 2 * m + 1) * 1024);
    unsigned warm;
    {
      u64 pc; asm volatile("s_getpc_b64 %0" : "=s"(pc));
      warm = ld2_async(make_srd((const void*)(pc & ~127ull), CODEWARM), lane < 3 ? lane * 128 : 0x40000000u, (idx * 4 + m) * 384);
    }
    __builtin_amdgcn_s_barrier();
    v2u ep = ld8_sc1_async(sws, m == 0 ? WS_EPOCH : 0x40000000u);
    float sval;
    const int wi = blockIdx.x * 4 + m;
    const u64 wbase = wi < 448 ? (u64)w1 + wi * 4096 : wi < 896 ? (u64)w2 + (wi - 448) * 4096 : (u64)wo + (wi - 896) * 4096;
    auto make_stats = [&]() {
      wait_vm2<1>(xr[0], xr[1]);
      PIN(warm);
      float piv[2], a[4];
#pragma unroll
      for (int u = 0; u < 2; ++u) {
        piv[u] = rdl(bflo(xr[u][0]), 0);
        float s1 = 0.f, s2 = 0.f;
#pragma unroll
        for (int i = 0; i < 8; ++i) { const float d = ((i & 1) ? bfhi(xr[u][i >> 1]) : bflo(xr[u][i >> 1])) - piv[u]; s1 += d; s2 = __builtin_fmaf(d, d, s2); }
        a[2 * u] = s1; a[2 * u + 1] = s2;
      }
#pragma unroll
      for (int i = 0; i < 4; ++i) a[i] += dpp<0xB1>(a[i]);
#pragma unroll
      for (int i = 0; i < 4; ++i) a[i] += dpp<0x4E>(a[i]);
      float t = p4 == 0 ? a[0] : p4 == 1 ? a[1] : p4 == 2 ? a[2] : a[3];
      t += dpp<0x114>(t); t += dpp<0x118>(t);
      const auto s16 = __builtin_amdgcn_permlane16_swap(asu(t), asu(t), false, false);
      t = asf(s16[0]) + asf(s16[1]);
      const auto s32 = __builtin_amdgcn_permlane32_swap(asu(t), asu(t), false, false);
      t = (asf(s32[0]) + asf(s32[1])) * (1.f / DM);
      const float e1 = dpp<0xA0>(t);
      sval = (p4 & 1) ? __builtin_fmaxf(__builtin_fmaf(-e1, e1, t), 0.f) + 1e-5f : t + (p4 ? piv[1] : piv[0]);
      PIN(sval);
    };
    const unsigned acnt = lds0 + L_CNT;
    const unsigned vpos = 2048 + (g & 1) * 512 + h * 256 + (lane & 15) * 16;
    v4u qf[2], am0, am1;
    u64 um[16];
    auto masks = [&]() {
      const int dq = r32 - 4 * h;
#pragma unroll
      for (int u = 0; u < 16; ++u) { um[u] = __builtin_amdgcn_ballot_w64((u & 3) + 8 * (u >> 2) > dq); pin_s(um[u]); }
#pragma unroll
      for (int d = 0; d < 4; ++d) {
        am0[d] = __builtin_amdgcn_inverse_ballot_w64(um[2 * d]) ? 0xffffffffu : __builtin_amdgcn_inverse_ballot_w64(um[2 * d + 1]) ? 0xffff0000u : 0u;
        am1[d] = __builtin_amdgcn_inverse_ballot_w64(um[8 + 2 * d]) ? 0xffffffffu : __builtin_amdgcn_inverse_ballot_w64(um[9 + 2 * d]) ? 0xffff0000u : 0u;
      }
      PIN(am0); PIN(am1);
    };
    {
      v4u xf[32];
      f32x16 acc[3] = {};
      f32x4 cs[3] = {};
      v4u wf[4][3];
      unsigned wb[4];
#pragma unroll
      for (int p = 0; p < 4; ++p) { wb[p] = qbuf(0) + ((p + m) & 3) * 1024 + (8 * h + q4) * 64 + (g & 1) * 32 + p4 * 8; PIN(wb[p]); }
      auto rd = [&](int ks, int t) {
        if (t == 0) xf[ks] = lds128(xbuf(ks >> 2) + xo[ks & 3]);
        wf[ks & 3][t] = ldtr2(wb[ks & 3] + ((ks >> 2) % 3) * 12288 + t * 4096, 256);
      };
      auto mm = [&](int ks, int t) {
        acc[t] = t == 2 ? MFMA32(xf[ks], wf[ks & 3][t], acc[t]) : MFMA32(wf[ks & 3][t], xf[ks], acc[t]);
        if ((ks & 3) == 0) { cs[t] = MFMA16(sel, wf[ks & 3][t], cs[t]); PIN(cs[t]); }
        SBAR();
      };
      make_stats();
      bar();
      STAMP_AT(10);
#pragma unroll
      for (int t = 0; t < 6; ++t) rd(t / 3, t % 3);
      SBAR();
      static_for<0, 8>([&](auto sc) {
        constexpr int s = sc.value;
#pragma unroll
        for (int p = 0; p < 2; ++p) {
#pragma unroll
          for (int t = 0; t < 3; ++t) { mm(4 * s + p, t); rd(4 * s + p + 2, t); SBAR(); }
          if (s == 7 && p == 0) { cswr(4, cs[0]); cswr(5, cs[1]); cswr(6, cs[2]); SBAR(); }
        }
        if constexpr (s == 0) {
          wait_vmf<0>(ep);
          if (m == 0 && lane < 10)
            *(LAS unsigned*)(size_t)(lds0 + L_CNT + lane * 4) = lane == 8 ? ep[0] * 0x9E3779B1u + 0x7F4A7C15u : lane == 9 ? ep[1] * 0x85EBCA77u + 0x165667B1u : 0u;
        }
        if constexpr (s == 1) {
          get_tag();
          if ((lane >> 2) == 3) {
            const v2u gr = {asu(sval), tag32};
            __builtin_amdgcn_raw_buffer_store_b64(gr, rws, WS_STATS + (128 * r + 8 * j + 2 * m) * 16 + p4 * 8, 0, 16);
          }
        }
        if constexpr (s == MIDWARM) {
          warm = ld2_async(make_srd((const void*)wbase, 4096), lane * 128, 0);
        }
        if constexpr (s == 5) stat_load();
        if constexpr (s == 6) {
          wait_vm1<0>(st);
          PIN(warm);

          row_stats();
        }
        mm(4 * s + 2, 0); mm(4 * s + 2, 1);
        bar();
        if constexpr (s < 7) {
          STAMP_AT(11 + s);
          mm(4 * s + 2, 2);
          rd(4 * s + 4, 0); rd(4 * s + 4, 1); rd(4 * s + 4, 2); SBAR();
#pragma unroll
          for (int t = 0; t < 3; ++t) { mm(4 * s + 3, t); rd(4 * s + 5, t); SBAR(); }
        }
      });
      STAMP_AT(1);
      __builtin_amdgcn_s_setprio(3);
      const f32x4 c5 = csrd(5), c6 = csrd(6), c4 = csrd(4);
      SBAR();
      mm(30, 2); mm(31, 1);
      make_xaug();
      SBAR();
      mm(31, 2); mm(31, 0);
      if (h == 0) *(LAS float*)(size_t)(lds0 + L_RS + (m * 32 + r32) * 4) = rstd;
      acc[1] = MFMA32(waug(c5, 0u), xaug, acc[1]);
      acc[2] = MFMA32(xaug, waug(c6, 0u), acc[2]);
      acc[0] = MFMA32(waug(c4, 0u), xaug, acc[0]);
      SBAR();
      f32x4 rs[4];
#pragma unroll
      for (int qd = 0; qd < 4; ++qd) rs[qd] = __builtin_bit_cast(f32x4, lds128(lds0 + L_RS + (m * 32 + 8 * qd + 4 * h) * 4));
      const unsigned kvo = WS_KV + (j * 64 + T) * 4096;
#pragma unroll
      for (int e = 0; e < 16; ++e) acc[1][e] = MUL1(acc[1][e], rstd);
#pragma unroll
      for (int e = 0; e < 2; ++e) {
        const v4u f = pack8(acc[1], e);
        st16_sc1(f, sws, lane * 16, kvo + e * 1024);
        sts128(lds0 + L_KV + (8 + m) * KVT + e * 1024 + lane * 16, f);
      }
#pragma unroll
      for (int qd = 0; qd < 4; ++qd)
#pragma unroll
        for (int e = 0; e < 4; ++e) acc[2][4 * qd + e] = MUL1(acc[2][4 * qd + e], rs[qd][e]);
#pragma unroll
      for (int s = 0; s < 2; ++s) {
        const v4u f = pack8(acc[2], s);
        st16_sc1(f, sws, vpos, kvo + s * 1024);
        sts128(lds0 + L_KV + (8 + m) * KVT + s * VB + vpos, f);
      }
      SBAR();
      const float qs = rstd * (0.25f * 1.4426950408889634f);
#pragma unroll
      for (int e = 0; e < 16; ++e) acc[0][e] = MUL1(acc[0][e], qs);
      qf[0] = pack8(acc[0], 0); qf[1] = pack8(acc[0], 1);
      PIN(qf[0]); PIN(qf[1]);
      arrive_nowait(acnt);
      STAMP_AT(2);
      if (heavy) {
        hsetup(1);
        await(acnt + 16, 4);
        STAMP_AT(3);
        hmm(std::integral_constant<int, 1>{}, xf, [&](auto kc) {
          constexpr int ks = kc.value;
          if constexpr (ks == KVPUB) {
            wait_vm<0>();
            if (lane == 0) __hip_atomic_store(flag(FL_KV, T, j), tag, RLX_AGENT);
          }
          if constexpr (ks == KVPUB + 2) bias_load(1);
          if constexpr (ks == 31) { wait_vm<0>(); PIN(bias0); PIN(bias1); }
        });
        STAMP_AT(18);
        hgelu(std::integral_constant<int, 1>{}, [&]() {});
        STAMP_AT(4);
      } else {
        wait_vm<0>();
        if (lane == 0) __hip_atomic_store(flag(FL_KV, T, j), tag, RLX_AGENT);
      }
    }
    masks();
    STAMP_AT(19);
    const unsigned kb = lds0 + L_KV + m * KVT + lane * 16;
    unsigned vb[2];
#pragma unroll
    for (int e = 0; e < 2; ++e) { vb[e] = lds0 + L_KV + m * KVT + 2048 + ((g & 1) ? 1024 : e * 512 + h * 256 + (lane & 15) * 16); PIN(vb[e]); }
    bool pend = true;
    if (__builtin_amdgcn_readfirstlane(peek(acnt)) < 8) {
      wait_vm<0>();
      if (lane == 0) __hip_atomic_store(flag(FL_H1, T, j), tag, RLX_AGENT);
      pend = false;
      await(acnt, 8);
    }
    asm volatile("" ::: "memory");
    __builtin_amdgcn_s_setprio(PRIO_ATT);
    STAMP_AT(5);
    const f32x16 zero = {};
    auto o_store = [&](f32x16& o, int e) {
      const float rl = __builtin_amdgcn_rcpf(o[8]);
#pragma unroll
      for (int u = 0; u < 8; ++u) o[u] = MUL1(o[u], rl);
      st16_sc1(pack8(o, 0), sws, lane * 16, WS_AF + (T * AFT + 112 + 2 * j + e) * 1024);
    };
    int redo = 0;
    {
      f32x16 o[2], sa, sb, su;
      v4u kn, ku, va, vc, vn0, vn1, vu0, vu1, p0, p1, pu;
      auto kaddr = [&](int n) { return kb + ((n & 7) + 1) * KVT + (n >> 3) * 1024; };
      auto vaddr = [&](int n) { return vb[n >> 3] + ((n & 7) + 1) * KVT; };
      kn = lds128(kaddr(0)); vn0 = lds128(vaddr(0)); vn1 = lds128(vaddr(0) + VB);
      sa = MFMA32(kn, qf[0], zero);
      kn = lds128(kaddr(1));
      SBAR();
      auto step = [&](auto nc, f32x16& sc, f32x16& sn) {
        constexpr int n = nc.value, e = n >> 3, i = n & 7;
        if constexpr (n < 15) sn = MFMA32(kn, qf[(n + 1) >> 3], zero);
        if constexpr (n < 14) kn = lds128(kaddr(n + 2));
        if constexpr (i == 5) ku = lds128(kb + e * 1024);
        v4u v0 = vn0, v1 = vn1;
        if constexpr (n < 15) { vn0 = lds128(vaddr(n + 1)); vn1 = lds128(vaddr(n + 1) + VB); }
        if constexpr (i == 6) { vu0 = lds128(vb[e]); vu1 = lds128(vb[e] + VB); }
        SBAR();
        if constexpr (i == 7) {
#pragma unroll
          for (int u = 0; u < 16; ++u) sc[u] = __builtin_amdgcn_inverse_ballot_w64(um[u]) ? su[u] : sc[u];
          PIN(sc); SBAR();
        }
#pragma unroll
        for (int u = 0; u < 6; ++u) sc[u] = EXP2(sc[u]);
        PIN(sc); SBAR();
        if constexpr (n == 8) { o[0] = MFMA32(vc, p1, o[0]); SBAR(); }
        else if constexpr (n > 0) { o[e] = MFMA32(vc, p1, o[e]); SBAR(); }
        sc[6] = EXP2(sc[6]); sc[7] = EXP2(sc[7]);
        p0 = pack8(sc, 0);
        sc[8] = EXP2(sc[8]); sc[9] = EXP2(sc[9]);
        if constexpr (i == 7) { va = p0 & am0; p0 &= ~am0; PIN(va); }
        PIN(sc); PIN(p0); SBAR();
        if constexpr (n == 8) { o[0] = MFMA32(vu1, pu, o[0]); SBAR(); }
        o[e] = MFMA32(v0, p0, i == 0 ? zero : o[e]);
        SBAR();
        if constexpr (i == 6) { su = MFMA32(ku, qf[e], zero); SBAR(); }
#pragma unroll
        for (int u = 10; u < 13; ++u) sc[u] = EXP2(sc[u]);
        if constexpr (i == 7) { PIN(sc); SBAR(); o[e] = MFMA32(vu0, va, o[e]); SBAR(); }
#pragma unroll
        for (int u = 13; u < 16; ++u) sc[u] = EXP2(sc[u]);
        p1 = pack8(sc, 1); vc = v1;
        if constexpr (i == 7) { pu = p1 & am1; p1 &= ~am1; PIN(pu); }
        PIN(p1); SBAR();
        if constexpr (n == 8) {
          if (__all(o[0][8] >= 0x1p-30f && o[0][8] <= 0x1p30f)) o_store(o[0], 0); else redo = 1;
          SBAR();
        }
        if constexpr (n == 10) pend = !redo;
        if constexpr (i == 3 || i == 5 || i == 7) {
          if (pend && (i == 7 || (__builtin_amdgcn_s_getreg(7 | (3 << 11)) & 15) == 0)) {
            wait_vm<0>();
            if (lane == 0) __hip_atomic_store(flag(FL_H1 + e, T, j), tag, RLX_AGENT);
            pend = false;
          }
        }
      };
      static_for<0, 8>([&](auto hc) {
        step(std::integral_constant<int, 2 * hc.value>{}, sa, sb);
        step(std::integral_constant<int, 2 * hc.value + 1>{}, sb, sa);
      });
      o[1] = MFMA32(vc, p1, o[1]);
      o[1] = MFMA32(vu1, pu, o[1]);
      if (__all(o[1][8] >= 0x1p-30f && o[1][8] <= 0x1p30f)) o_store(o[1], 1); else redo |= 2;
    }
    if (redo) {
#pragma unroll 1
      for (int e = 0; e < 2; ++e) {
        if (!((redo >> e) & 1)) continue;
        const v4u q = e ? qf[1] : qf[0];
        const unsigned vbe = e ? vb[1] : vb[0];
        auto merged = [&](const f32x16& c) {
          f32x16 sl = MFMA32(lds128(kb + 8 * KVT + e * 1024), q, c);
          const f32x16 su = MFMA32(lds128(kb + e * 1024), q, c);
          static_for<0, 16>([&](auto u) { sl[u.value] = upper<u.value>() ? su[u.value] : sl[u.value]; });
          return sl;
        };
        float mx = -INFINITY;
#pragma unroll 1
        for (int i = 0; i < 8; ++i) {
          const f32x16 sc = i ? MFMA32(lds128(kb + i * KVT + e * 1024), q, zero) : merged(zero);
#pragma unroll
          for (int u = 0; u < 16; u += 2) mx = max3(mx, sc[u], sc[u + 1]);
        }
        const auto sw = __builtin_amdgcn_permlane32_swap(asu(mx), asu(mx), false, false);
        mx = __builtin_fmaxf(asf(sw[0]), asf(sw[1]));
        f32x16 negm, o;
#pragma unroll
        for (int u = 0; u < 16; ++u) negm[u] = -mx;
        {
          f32x16 sc = merged(negm);
#pragma unroll
          for (int u = 0; u < 16; ++u) sc[u] = EXP2(sc[u]);
          const v4u p0 = pack8(sc, 0), p1 = pack8(sc, 1);
          o = MFMA32(lds128(vbe), p0 & am0, zero);
          o = MFMA32(lds128(vbe + VB), p1 & am1, o);
          o = MFMA32(lds128(vbe + 8 * KVT), p0 & ~am0, o);
          o = MFMA32(lds128(vbe + 8 * KVT + VB), p1 & ~am1, o);
        }
#pragma unroll 1
        for (int i = 1; i < 8; ++i) {
          f32x16 sc = MFMA32(lds128(kb + i * KVT + e * 1024), q, negm);
#pragma unroll
          for (int u = 0; u < 16; ++u) sc[u] = EXP2(sc[u]);
          o = MFMA32(lds128(vbe + i * KVT), pack8(sc, 0), o);
          o = MFMA32(lds128(vbe + i * KVT + VB), pack8(sc, 1), o);
        }
        o_store(o, e);
      }
      wait_vm<0>();
      if (lane == 0) __hip_atomic_store(flag(FL_O0, T, j), tag, RLX_AGENT);
    }
    STAMP_AT(6);
    wait_vm<0>();
    if (lane == 0) __hip_atomic_store(flag(FL_O1, T, j), tag, RLX_AGENT);
    STAMP_AT(7);
    res_loads();
    wait_vm<0>();
    res_sum();
    if (blockIdx.x == 0 && wave == 7) {
#pragma unroll 1
      for (int i = 0; i < 4; ++i) poll_flags(flag(FL_KV, 4 * (4 * i + g), lane & 15), tag);
      if (lane == 0) __hip_atomic_store((GAS u64*)(ws + WS_EPOCH), tag, RLX_AGENT);
    }
  }
  {
    int t2 = threadIdx.x; PIN(t2);
    const int row = t2 >> 3, ch = t2 & 7;
    unsigned a = lds0 + L_WG + (((row >> 5) * 8 + ch) * 66 + (row & 31)) * 16, oo = (64 * rt + row) * 1024 + (64 * ct + ch * 8) * 2;
    PIN(a); PIN(oo);
    bar();
    f32x4 pl[4], ph[4];
#pragma unroll
    for (int w = 0; w < 4; ++w) { pl[w] = __builtin_bit_cast(f32x4, lds128(a + w * PART)); ph[w] = __builtin_bit_cast(f32x4, lds128(a + w * PART + 512)); }
    SBAR();
#pragma unroll
    for (int w = 0; w < 4; ++w) { lo += pl[w]; hi += ph[w]; }
    const v4u o = {pk2(lo[0], lo[1]), pk2(lo[2], lo[3]), pk2(hi[0], hi[1]), pk2(hi[2], hi[3])};
    __builtin_amdgcn_raw_buffer_store_b128(o, __builtin_amdgcn_make_buffer_rsrc(out, 0, SEQ * DM * 2, 0x00020000), oo, 0, 16);
  }
  STAMP_AT(8);
}

extern "C" void kernel_launch(void* const* d_in, const int* in_sizes, int n_in, void* d_out, int out_size, void* d_ws, size_t ws_size,
                              hipStream_t stream) {
  (void)in_sizes; (void)n_in; (void)out_size; (void)ws_size;
  (void)hipFuncSetAttribute((const void*)fused_block, hipFuncAttributeMaxDynamicSharedMemorySize, LDS_BYTES);
  fused_block<<<256, 512, LDS_BYTES, stream>>>(d_in[0], d_in[1], d_in[2], d_in[4], d_in[5], d_in[6], d_in[7], d_out, (char*)d_ws);
}
```

```cpp
#define PROBE 1
#define PROLE 0
#include <hip/hip_runtime.h>
#include <hip/hip_bf16.h>
#include <stdint.h>
#include <type_traits>

typedef unsigned v4u __attribute__((ext_vector_type(4)));
typedef unsigned v2u __attribute__((ext_vector_type(2)));
typedef float f32x2 __attribute__((ext_vector_type(2)));
typedef float f32x4 __attribute__((ext_vector_type(4)));
typedef float f32x16 __attribute__((ext_vector_type(16)));
typedef short bf16x8 __attribute__((ext_vector_type(8)));
typedef short s16x4 __attribute__((ext_vector_type(4)));
typedef __bf16 bf16x2_t __attribute__((ext_vector_type(2)));
typedef unsigned long long u64;
#define GAS __attribute__((address_space(1)))
#define LAS __attribute__((address_space(3)))
#define DI __device__ __forceinline__
#define RLX_AGENT __ATOMIC_RELAXED, __HIP_MEMORY_SCOPE_AGENT

constexpr int SEQ = 2048, DM = 512, FF = 1792;
constexpr unsigned WS_EPOCH = 0;
constexpr unsigned WS_STATS = 32768;
constexpr unsigned WS_FLAG = 65536;
constexpr unsigned WS_KV = WS_FLAG + 5 * 1024 * 128;
constexpr int AFT = 145;
constexpr unsigned WS_AF = WS_KV + 16 * 64 * 4096;
constexpr unsigned WS_END = WS_AF + 64 * AFT * 1024;
constexpr unsigned WS_STAMP = WS_END;
enum { FL_KV = 0, FL_H0 = 1, FL_H1 = 2, FL_O0 = 3, FL_O1 = 4 };
constexpr int L_WA = 0;
constexpr int L_WG = 86016;
constexpr int VB = 1040;
constexpr int KVT = 2048 + 2 * VB;
constexpr int L_KV = 32768;
constexpr int RD = 8, NR = 12;
constexpr int PART = 16 * 66 * 16;
constexpr int L_CS = L_WG + 4 * PART;
constexpr int L_RS = L_CS + 3584;
constexpr int L_CNT = L_RS + 512;
constexpr int LDS_BYTES = L_CNT + 64;
constexpr int xbuf(int s) { return ((s + 1) % 3) * 16384; }
constexpr int qbuf(int s) { return 49152 + (s % 3) * 12288; }
constexpr int w1buf(int s) { return L_WG + (s % 3) * PART + 8192; }
static_assert(L_KV + 8 * KVT >= 65536 && xbuf(7) == 32768 && qbuf(2) + 12288 <= L_WG && L_KV + 12 * KVT <= L_WG && PART >= RD * 2048 && LDS_BYTES <= 163840, "lds map");
#define AWIN 6
constexpr int AW = AWIN;
constexpr unsigned SPIN_CAP = 1u << 23;

#define MFMA32(a, b, c) __builtin_amdgcn_mfma_f32_32x32x16_bf16(__builtin_bit_cast(bf16x8, a), __builtin_bit_cast(bf16x8, b), c, 0, 0, 0)
#define MFMA16(a, b, c) __builtin_amdgcn_mfma_f32_16x16x32_bf16(__builtin_bit_cast(bf16x8, a), __builtin_bit_cast(bf16x8, b), c, 0, 0, 0)
#define SBAR() __builtin_amdgcn_sched_barrier(0)

#define STAMP_AT(i) do { if ((i) == PROBE && (isG ? 0 : 1) == PROLE) tstart = now_t(); } while (0)

DI u64 now_t() { u64 t; asm volatile("s_memrealtime %0\n\ts_waitcnt lgkmcnt(0)" : "=s"(t) :: "memory"); return t; }
DI float asf(unsigned u) { return __builtin_bit_cast(float, u); }
DI unsigned asu(float f) { return __builtin_bit_cast(unsigned, f); }
DI unsigned pk2(float lo, float hi) { return __builtin_bit_cast(unsigned, __builtin_convertvector((f32x2){lo, hi}, bf16x2_t)); }
DI float bflo(unsigned u) { return __builtin_bit_cast(float, u << 16); }
DI float bfhi(unsigned u) { return __builtin_bit_cast(float, u & 0xffff0000u); }
DI v4u make_srd(const void* p, unsigned bytes) { const u64 a = (u64)p; return (v4u){(unsigned)a, (unsigned)(a >> 32) & 0xffffu, bytes, 0x00020000u}; }
DI void dma16(v4u srd, unsigned voff, unsigned soff, unsigned lds) {
  asm volatile("s_mov_b32 m0, %3\n\ts_nop 0\n\tbuffer_load_dwordx4 %0, %1, %2 offen lds" :: "v"(voff), "s"(srd), "s"(soff), "s"(lds) : "memory");
}
DI void dma16_sc1(v4u srd, unsigned voff, unsigned soff, unsigned lds) {
  asm volatile("s_mov_b32 m0, %3\n\ts_nop 0\n\tbuffer_load_dwordx4 %0, %1, %2 offen sc1 lds" :: "v"(voff), "s"(srd), "s"(soff), "s"(lds) : "memory");
}
DI void dma16_a(v4u srd, unsigned voff, unsigned soff, unsigned lds) { dma16(srd, voff, soff, lds); }
DI void dma16_b(v4u srd, unsigned voff1, unsigned soff) {
  asm volatile("buffer_load_dwordx4 %0, %1, %2 offen offset:1024 lds" :: "v"(voff1), "s"(srd), "s"(soff) : "memory");
}
DI v4u ld16_sc1_async(v4u srd, unsigned voff, unsigned soff) {
  v4u r; asm volatile("buffer_load_dwordx4 %0, %1, %2, %3 offen sc1" : "=v"(r) : "v"(voff), "s"(srd), "s"(soff) : "memory"); return r;
}
DI v4u ld16_async(v4u srd, unsigned voff, unsigned soff) {
  v4u r; asm volatile("buffer_load_dwordx4 %0, %1, %2, %3 offen" : "=v"(r) : "v"(voff), "s"(srd), "s"(soff) : "memory"); return r;
}
DI unsigned ld2_async(v4u srd, unsigned voff, unsigned soff) {
  unsigned r; asm volatile("buffer_load_ushort %0, %1, %2, %3 offen" : "=v"(r) : "v"(voff), "s"(srd), "s"(soff) : "memory"); return r;
}
DI void st16_sc1(v4u v, v4u srd, unsigned voff, unsigned soff) {
  asm volatile("buffer_store_dwordx4 %0, %1, %2, %3 offen sc1\n\ts_nop 1" :: "v"(v), "v"(voff), "s"(srd), "s"(soff) : "memory");
}
template <int N> DI void wait_vm1(v4u& a) { asm volatile("s_waitcnt vmcnt(%1)" : "+v"(a) : "i"(N) : "memory"); }
template <int N> DI void wait_vm2(v4u& a, v4u& b) { asm volatile("s_waitcnt vmcnt(%2)" : "+v"(a), "+v"(b) : "i"(N) : "memory"); }
template <int N> DI void wait_vm3(v4u& a, v4u& b, v4u& c) { asm volatile("s_waitcnt vmcnt(%3)" : "+v"(a), "+v"(b), "+v"(c) : "i"(N) : "memory"); }
DI void pin_s(u64& v) { asm volatile("" : "+s"(v)); }
template <class V> DI void pin(V& v) { asm volatile("" : "+v"(v)); }
template <int N> DI void wait_vm() { asm volatile("s_waitcnt vmcnt(%0)" :: "i"(N) : "memory"); }
DI void bar() { asm volatile("s_waitcnt lgkmcnt(0)\n\ts_barrier" ::: "memory"); }
DI v4u lds128(unsigned a) { return *(const LAS v4u*)(size_t)a; }
DI void sts128(unsigned a, v4u v) { *(LAS v4u*)(size_t)a = v; }
DI v2u ldtr(unsigned a) { return __builtin_bit_cast(v2u, __builtin_amdgcn_ds_read_tr16_b64_v4i16((LAS s16x4*)(size_t)a)); }
DI v4u ldtr2(unsigned a, int second) { const v2u lo = ldtr(a), hi = ldtr(a + second); return (v4u){lo.x, lo.y, hi.x, hi.y}; }
template <int CTRL> DI float dpp(float v) { return __builtin_bit_cast(float, __builtin_amdgcn_update_dpp(0, __builtin_bit_cast(int, v), CTRL, 0xf, 0xf, true)); }
DI float rdl(float v, int l) { return __builtin_bit_cast(float, __builtin_amdgcn_readlane(__builtin_bit_cast(int, v), l)); }
DI v4u pack8(const f32x16& a, int s) { return (v4u){pk2(a[8 * s], a[8 * s + 1]), pk2(a[8 * s + 2], a[8 * s + 3]), pk2(a[8 * s + 4], a[8 * s + 5]), pk2(a[8 * s + 6], a[8 * s + 7])}; }
DI void poll_flags(const GAS u64* p, u64 tag) {
#pragma unroll 1
  for (unsigned spin = 0; spin < SPIN_CAP; ++spin) {
    const u64 v = __hip_atomic_load(p, RLX_AGENT);
    if (__all(v == tag)) break;
    __builtin_amdgcn_s_sleep(1);
  }
  asm volatile("" ::: "memory");
}
constexpr u64 upper_mask(int reg) {
  u64 m = 0;
  for (int l = 0; l < 64; ++l) { const int row = (reg & 3) + 8 * (reg >> 2) + 4 * (l >> 5); if (row > (l & 31)) m |= 1ull << l; }
  return m;
}
template <int REG> DI bool upper() { return __builtin_amdgcn_inverse_ballot_w64(upper_mask(REG)); }
template <int I, int N, class Fn> DI void static_for(Fn&& f) { if constexpr (I < N) { f(std::integral_constant<int, I>{}); static_for<I + 1, N>(f); } }
DI float max3(float a, float b, float c) { return __builtin_fmaxf(__builtin_fmaxf(a, b), c); }

constexpr int H1_CK = 16 - AW, H1_LD = H1_CK - 3, O_CK = 28 - AW, O_LD = O_CK - 3;
constexpr int g2_issued(int t) { return ((t >= 1 && t - 1 + NR + RD < 36) ? 2 : 0) + ((t + AW < 28) ? 2 : 0) + (t == H1_LD) + (t == O_LD) + (t == O_CK ? 10 : 0) + (t == 26); }
constexpr int g2_range(int a, int b) { int n = 0; for (int t = a; t < b; ++t) n += g2_issued(t); return n; }
#define GELU_PK(P) (P == 1)
#define PRIO_G2 2
#define PRIO_ATT 3
#define PRIO_G1 2
#define ROT r
#define CODEWARM 38000
#define MIDWARM 1
#define KVPUB 2
#define EXP2(v) __builtin_amdgcn_exp2f(v)
#define PIN(v) pin(v)
extern "C" __device__ float mul_unpacked(float, float) __asm("llvm.amdgcn.fmul.legacy");
#define MUL1(a, b) mul_unpacked(a, b)
#define FMA1(a, b, c) __builtin_amdgcn_div_fmasf(a, b, c, false)
DI v2u ld8_sc1_async(v4u srd, unsigned voff) {
  v2u r; asm volatile("buffer_load_dwordx2 %0, %1, %2, 0 offen sc1" : "=v"(r) : "v"(voff), "s"(srd) : "memory"); return r;
}
template <int N> DI void wait_vmf(v2u& a) { asm volatile("s_waitcnt vmcnt(%1)" : "+v"(a) : "i"(N) : "memory"); }
DI void arrive(unsigned cnt, int lane) {
  asm volatile("s_waitcnt lgkmcnt(0)" ::: "memory");
  if (lane == 0) __hip_atomic_fetch_add((LAS unsigned*)(size_t)cnt, 1u, __ATOMIC_RELAXED, __HIP_MEMORY_SCOPE_WORKGROUP);
}
DI void arrive_nowait(unsigned cnt) {
  asm volatile("s_mov_b64 exec, 1\n\tds_add_u32 %0, %1\n\ts_mov_b64 exec, -1" :: "v"(cnt), "v"(1u) : "memory");
}
DI unsigned peek(unsigned cnt) { return __hip_atomic_load((LAS unsigned*)(size_t)cnt, __ATOMIC_RELAXED, __HIP_MEMORY_SCOPE_WORKGROUP); }
DI void await(unsigned cnt, unsigned n) {
#pragma unroll 1
  for (unsigned spin = 0; spin < SPIN_CAP * 32; ++spin) {
    if (__builtin_amdgcn_readfirstlane(peek(cnt)) >= n) break;
    __builtin_amdgcn_s_sleep(1);
  }
  asm volatile("" ::: "memory");
}

__global__ __launch_bounds__(512, 2) void fused_block(const void* __restrict__ x, const void* __restrict__ wqkv, const void* __restrict__ wo,
                                                      const void* __restrict__ w1, const void* __restrict__ b1, const void* __restrict__ w2,
                                                      const void* __restrict__ b2, void* __restrict__ out, char* __restrict__ ws) {
  constexpr unsigned lds0 = 0;
  const int wave = __builtin_amdgcn_readfirstlane(threadIdx.x >> 6);
  const int m = wave & 3;
  const bool isG = wave < 4;
  const int xcd = blockIdx.x & 7, idx = blockIdx.x >> 3;
  const int r = (xcd >> 2) * 8 + (idx >> 2), j = (xcd & 3) * 4 + (idx & 3);
  const int rt = (xcd >> 1) * 8 + (idx >> 2), ct = (xcd & 1) * 4 + (idx & 3);
  int lane = threadIdx.x & 63, r32, h, g, q4, p4;
  u64 tstart = 0;
  STAMP_AT(0);
  asm volatile("" :: "s"(x), "s"(wqkv), "s"(wo), "s"(w1), "s"(b1), "s"(w2), "s"(b2), "s"(out), "s"(ws));

  const v4u sx = make_srd(x, SEQ * DM * 2), sws = make_srd(ws, WS_END);
  const __amdgpu_buffer_rsrc_t rws = __builtin_amdgcn_make_buffer_rsrc(ws, 0, WS_END, 0x00020000);
  const unsigned one2 = 0x3f803f80u;
  const int T = 4 * r + m;
  const int rot = ROT;
  auto flag_off = [&](int kind, int tile, int jj) { return WS_FLAG + ((kind * 64 + tile) * 16 + jj) * 128; };
  auto flag = [&](int kind, int tile, int jj) { return (GAS u64*)(ws + flag_off(kind, tile, jj)); };
  u64 tag;
  unsigned taglo, tag32;
  auto get_tag = [&]() {
    const v2u tg = *(const LAS v2u*)(size_t)(lds0 + L_CNT + 32);
    taglo = __builtin_amdgcn_readfirstlane(tg[0]); tag32 = __builtin_amdgcn_readfirstlane(tg[1]);
    tag = ((u64)tag32 << 32) | taglo;
  };
  v4u xres, bres, xaug, st;
  float mean, var, rstd, sd;
  v4u sel;
  unsigned xo[4], swz;
  auto lane_setup = [&]() {
    lane = threadIdx.x & 63; PIN(lane);
    r32 = lane & 31; h = lane >> 5; g = lane >> 4; q4 = (lane >> 2) & 3; p4 = lane & 3;
    const unsigned selv = ((lane & 15) == (g & 1)) ? one2 : 0u;
    sel = (v4u){selv, selv, selv, selv};
#pragma unroll
    for (int p = 0; p < 4; ++p) xo[p] = (32 * m + r32) * 128 + (((2 * ((p + m) & 3) + h) ^ ((r32 >> 1) & 7)) * 16);
    swz = ((((lane >> 2) & 1) ^ (g & 1)) * 64) + p4 * 16;
  };

  auto stat_load = [&]() { st = ld16_sc1_async(sws, WS_STATS + (128 * r + 32 * m + r32) * 16, 0); };
  auto res_loads = [&]() {
    int t2 = threadIdx.x; PIN(t2);
    const int row = 64 * rt + (t2 >> 3), col = 64 * ct + (t2 & 7) * 8;
    xres = ld16_async(sx, row * 1024 + col * 2, 0);
    bres = ld16_async(make_srd(b2, DM * 2), col * 2, 0);
  };
  f32x4 lo, hi;
  auto res_sum = [&]() {
    PIN(xres); PIN(bres);
    lo = (f32x4){bflo(xres[0]) + bflo(bres[0]), bfhi(xres[0]) + bfhi(bres[0]), bflo(xres[1]) + bflo(bres[1]), bfhi(xres[1]) + bfhi(bres[1])};
    hi = (f32x4){bflo(xres[2]) + bflo(bres[2]), bfhi(xres[2]) + bfhi(bres[2]), bflo(xres[3]) + bflo(bres[3]), bfhi(xres[3]) + bfhi(bres[3])};
    PIN(lo); PIN(hi);
  };
  auto row_stats = [&]() {
    if (!__all(st[1] == tag32 && st[3] == tag32)) {
#pragma unroll 1
      for (unsigned spin = 0; spin < SPIN_CAP; ++spin) {
        st = ld16_sc1_async(sws, WS_STATS + (128 * r + 32 * m + r32) * 16, 0);
        wait_vm1<0>(st);
        if (__all(st[1] == tag32 && st[3] == tag32)) break;
      }
    }
    mean = asf(st[0]); var = asf(st[2]);
    PIN(mean); PIN(var);
  };
  auto make_xaug = [&]() {
    PIN(mean); PIN(var);
    rstd = __builtin_amdgcn_rsqf(var); sd = var * rstd;
    const unsigned uh = pk2(-mean, sd);
    const unsigned ul = pk2(-mean - bflo(uh), FMA1(bfhi(uh), -1.f, sd));
    xaug = (v4u){(uh & 0xffffu) | (uh << 16), (ul & 0xffffu) | (uh & 0xffff0000u), ul >> 16, 0u};
    if (h) xaug = (v4u){0u, 0u, 0u, 0u};
    PIN(xaug);
  };
  auto cswr = [&](int tile, f32x4 c) {
    if (lane < 16) { LAS float* p = (LAS float*)(size_t)(lds0 + L_CS + (tile * 32 + lane) * 16 + m * 4); p[0] = c[0]; p[64] = c[1]; }
  };
  auto csrd = [&](int tile) { return __builtin_bit_cast(f32x4, lds128(lds0 + L_CS + (tile * 32 + r32) * 16)); };
  auto waug = [&](f32x4 c4, unsigned b16) {
    const float c = ((c4[0] + c4[1]) + c4[2]) + c4[3];
    const unsigned uh = pk2(c, 0.f);
    const unsigned ul = pk2(c - bflo(uh), 0.f);
    return (v4u){uh | (ul << 16), uh | (b16 << 16), b16, 0u};
  };

  const v4u sw1 = make_srd(w1, DM * FF * 2);
  const int hcol0 = 448 * (j >> 2) + 128 * (j & 3);
  const bool heavy = (j & 3) != 3;
  unsigned bias0, bias1;
  auto bias_load = [&](int P) {
    bias0 = ld2_async(make_srd(b1, FF * 2), r32 * 2, (hcol0 + 64 * P) * 2);
    bias1 = ld2_async(make_srd(b1, FF * 2), r32 * 2, (hcol0 + 64 * P + 32) * 2);
  };
  auto w1dma = [&](int P, int c, unsigned dst, int half) {
    const unsigned vw1 = (lane >> 3) * (FF * 2) + swz, vw1b = vw1 + 8 * (FF * 2) - 1024;
    const unsigned so = (64 * ((c + rot) & 7) + 16 * m) * (FF * 2) + (hcol0 + 64 * P) * 2;
    if (half == 0) dma16_a(sw1, vw1, so, dst + m * 2048); else dma16_b(sw1, vw1b, so);
  };
  f32x16 hac[2];
  unsigned rk[4][2];
  auto hsetup = [&](int P) {
#pragma unroll
    for (int p = 0; p < 4; ++p)
#pragma unroll
      for (int c = 0; c < 2; ++c) {
        rk[p][c] = lds0 + (P ? L_WA : w1buf(0)) + ((p + m) & 3) * 2048 + h * 1024 + q4 * 128 + ((c ^ (q4 >> 1)) * 64) + (g & 1) * 32 + p4 * 8;
        PIN(rk[p][c]);
      }
  };
  auto hmm = [&](auto pc, const v4u (&xf)[32], auto&& hook) {
    constexpr int P = pc.value;
    constexpr int D = 4;
    const unsigned cnt = lds0 + L_CNT + 4;
    f32x4 cs[2] = {}, c0, c1;
    v4u wf[D][2], au0, au1;
    unsigned seen = 0;
    hac[0] = (f32x16){}; hac[1] = (f32x16){};
    auto rd = [&](int ks, int t) { wf[ks % D][t] = ldtr2(rk[ks & 3][t] + ((ks >> 2) ^ 4) * 8192, 512); };
#pragma unroll
    for (int ks = 0; ks < D; ++ks) { rd(ks, 0); rd(ks, 1); }
    SBAR();
    static_for<0, 32>([&](auto kc) {
      constexpr int ks = kc.value;
      hook(kc);
      if constexpr (ks == 12) arrive_nowait(cnt + 16);
      if constexpr (ks == 29) seen = peek(cnt);
#pragma unroll
      for (int t = 0; t < 2; ++t) {
        hac[t] = MFMA32(wf[ks % D][t], xf[ks ^ 16], hac[t]);
        if ((ks & 3) == 0 && ks < 28) { cs[t] = MFMA16(sel, wf[ks % D][t], cs[t]); PIN(cs[t]); }
        if (ks == 26) { cs[t] = MFMA16(sel, wf[28 % D][t], cs[t]); PIN(cs[t]); }
        SBAR();
        if constexpr (ks + D < 32) rd(ks + D, t);
        if constexpr (ks == 31) { if (t == 0) { au0 = waug(c0, bias0); PIN(au0); } else { au1 = waug(c1, bias1); PIN(au1); } }
        SBAR();
      }
      if constexpr (ks == 30) {
        PIN(hac[0]); PIN(hac[1]);
        if (__builtin_amdgcn_readfirstlane(seen) < 4) await(cnt, 4);
        asm volatile("" ::: "memory");
        if constexpr (ks == 30) { c0 = csrd(2 * P); c1 = csrd(2 * P + 1); }
      }
      if constexpr (ks == 27) { cswr(2 * P, cs[0]); cswr(2 * P + 1, cs[1]); arrive_nowait(cnt); }
      SBAR();
    });
    hac[0] = MFMA32(au0, xaug, hac[0]);
    hac[1] = MFMA32(au1, xaug, hac[1]);
    SBAR();
  };
  auto hgelu = [&](auto pc, auto&& mid) {
    constexpr int P = pc.value;
    constexpr bool PK = GELU_PK(P);
    const float C0 = 2.3022081986f, C1 = C0 * 0.044715f;
    const float ga = -C1 * rstd * rstd * rstd, gb = -C0 * rstd;
    auto mul = [&](f32x2 a, f32x2 b) { return PK ? a * b : (f32x2){MUL1(a[0], b[0]), MUL1(a[1], b[1])}; };
    auto fma = [&](f32x2 a, float b, float c) {
      return PK ? __builtin_elementwise_fma(a, (f32x2){b, b}, (f32x2){c, c}) : (f32x2){FMA1(a[0], b, c), FMA1(a[1], b, c)}; };
#pragma unroll
    for (int t = 0; t < 2; ++t) {
      f32x2 v[8], z[8];
#pragma unroll
      for (int e = 0; e < 8; ++e) { v[e] = (f32x2){hac[t][2 * e], hac[t][2 * e + 1]}; z[e] = mul(v[e], v[e]); }
      SBAR();
#pragma unroll
      for (int e = 0; e < 8; ++e) z[e] = fma(z[e], ga, gb);
      SBAR();
#pragma unroll
      for (int e = 0; e < 8; ++e) z[e] = mul(z[e], v[e]);
      SBAR();
#pragma unroll
      for (int e = 0; e < 8; ++e) z[e] = (f32x2){EXP2(z[e][0]), EXP2(z[e][1])};
      SBAR();
#pragma unroll
      for (int e = 0; e < 8; ++e) z[e] = fma(z[e], sd, sd);
      SBAR();
#pragma unroll
      for (int e = 0; e < 8; ++e) z[e] = (f32x2){__builtin_amdgcn_rcpf(z[e][0]), __builtin_amdgcn_rcpf(z[e][1])};
      SBAR();
#pragma unroll
      for (int e = 0; e < 8; ++e) z[e] = mul(z[e], v[e]);
      SBAR();
#pragma unroll
      for (int s = 0; s < 2; ++s) {
        const v4u f = {pk2(z[4 * s][0], z[4 * s][1]), pk2(z[4 * s + 1][0], z[4 * s + 1][1]), pk2(z[4 * s + 2][0], z[4 * s + 2][1]), pk2(z[4 * s + 3][0], z[4 * s + 3][1])};
        st16_sc1(f, sws, lane * 16, WS_AF + (T * AFT + (hcol0 >> 4) + 4 * P + 2 * t + s) * 1024);
      }
      if (t == 0) mid();
      SBAR();
    }
  };

  if (isG) {
    lane_setup();
    const v4u sw2 = make_srd(w2, FF * DM * 2), swo = make_srd(wo, DM * DM * 2);
    const unsigned vw2 = (lane >> 3) * 1024 + swz, vw2b = vw2 + 7 * 1024;
    const int ro3 = rt % 3;
    auto kmap = [&](int kk) {
      if (kk < 16) return 8 * (((kk >> 2) + rt) & 3) + (kk & 3);
      const int p = ((kk - 16) >> 2) + ro3;
      return 8 * (p >= 3 ? p - 3 : p) + 4 + (kk & 3);
    };
    auto omap = [&](int i) { return 2 * ((i + rt) & 3) + (i >> 2); };
    auto wdmah = [&](int kk, int half) {
      const unsigned so = (kk < 28 ? 448 * m + 16 * kmap(kk) : 128 * m + 16 * omap(kk - 28)) * 1024 + 128 * ct;
      if (half == 0) dma16_a(kk < 28 ? sw2 : swo, vw2, so, lds0 + L_WG + m * PART + (kk % RD) * 2048); else dma16_b(kk < 28 ? sw2 : swo, vw2b, so);
    };
    auto wdma = [&](int kk) { wdmah(kk, 0); wdmah(kk, 1); };
    {
      const v4u swq = make_srd(wqkv, DM * 3 * DM * 2);
      const unsigned vx0 = (lane >> 3) * 1024 + (((lane & 7) ^ g) * 16), vx1 = (7 + (lane >> 3)) * 1024 + (((lane & 7) ^ (4 + g)) * 16);
      const unsigned vwq = (lane >> 2) * (3 * DM * 2) + p4 * 16;
      auto piece = [&](int s, int i) {
        const int kc = (s + rot) & 7;
        if (i < 4) {
          const unsigned so = (128 * r + 32 * m + 16 * (i >> 1)) * 1024 + kc * 128;
          if (i & 1) dma16_b(sx, vx1, so); else dma16_a(sx, vx0, so, lds0 + xbuf(s) + m * 4096 + (i >> 1) * 2048);
        } else if (i < 7) dma16(swq, vwq, (64 * kc + 16 * m) * (3 * DM * 2) + (512 * (i - 4) + 32 * j) * 2, lds0 + qbuf(s) + ((i - 4) * 4 + m) * 1024);
        else w1dma(0, s, lds0 + w1buf(s), i - 7);
      };
#pragma unroll
      for (int i = 0; i < 18; ++i) { piece(i / 9, i % 9); if (i == 8) __builtin_amdgcn_s_barrier(); }
      __builtin_amdgcn_s_setprio(PRIO_G1);
      hsetup(0);
      f32x4 cs[2] = {};
      v4u xf[4], wf[4][2];
      hac[0] = (f32x16){}; hac[1] = (f32x16){};
      static_for<0, 8>([&](auto sc) {
        constexpr int s = sc.value;
        auto fill = [&](int i) {
          if constexpr (s < 6) piece(s + 2, i);
          if constexpr (s == 6) {
            if (i == 0) { stat_load(); bias_load(0); }
            else if (i < 5) { if (heavy) w1dma(1, (i - 1) >> 1, lds0 + L_WA + ((i - 1) >> 1) * 8192, (i - 1) & 1); }
            else wdmah((i - 5) >> 1, (i - 5) & 1);
          }
          if constexpr (s == 7) {
            if (heavy && i < 4) w1dma(1, 2 + (i >> 1), lds0 + L_WA + (2 + (i >> 1)) * 8192, i & 1);
            if (heavy && i >= 4) { w1dma(1, i, lds0 + L_WA + i * 8192, 0); w1dma(1, i, 0, 1); }
          }
          SBAR();
        };
        auto rd = [&](int p) {
          xf[p] = lds128(xbuf(s) + xo[p]);
          wf[p][0] = ldtr2(rk[p][0] + (s % 3) * PART, 512); wf[p][1] = ldtr2(rk[p][1] + (s % 3) * PART, 512);
          SBAR();
        };
        if constexpr (s < 7) wait_vm<9>(); else { if (heavy) wait_vm<11>(); else wait_vm<7>(); }
        bar();
        STAMP_AT(10 + s);
        rd(0);
        if constexpr (s < 7) fill(0);
        rd(1);
        if constexpr (s < 7) fill(1);
        if constexpr (s == 2) get_tag();
#pragma unroll
        for (int p = 0; p < 4; ++p) {
#pragma unroll
          for (int t = 0; t < 2; ++t) {
            hac[t] = MFMA32(wf[p][t], xf[p], hac[t]);
            if (p == 0) { cs[t] = MFMA16(sel, wf[p][t], cs[t]); PIN(cs[t]); }
            SBAR();
            if (p < 2 && t == 0) rd(p + 2);
            if (s == 7) fill(2 * p + t); else if (2 * p + t < 7) fill(2 + 2 * p + t);
          }
          if (s == 7 && p == 1) {
            cswr(0, cs[0]); cswr(1, cs[1]);
            bar();
            STAMP_AT(1);
          }
        }
        if constexpr (s == 7) {
          const f32x4 c0 = csrd(0), c1 = csrd(1);
          if (heavy) wait_vm<20>(); else wait_vm<4>();
          PIN(st); PIN(bias0); PIN(bias1);
          row_stats(); make_xaug();
          hac[0] = MFMA32(waug(c0, bias0), xaug, hac[0]);
          hac[1] = MFMA32(waug(c1, bias1), xaug, hac[1]);
          SBAR();
        }
      });
    }
    STAMP_AT(18);
    if (heavy) { wait_vm<0>(); arrive_nowait(lds0 + L_CNT + 16); }
    hgelu(std::integral_constant<int, 0>{}, [&]() {});
    __builtin_amdgcn_s_setprio(0);
    STAMP_AT(19);
    unsigned rb2[2];
#pragma unroll
    for (int c = 0; c < 2; ++c) rb2[c] = lds0 + L_WG + m * PART + h * 512 + q4 * 128 + ((c ^ (q4 >> 1)) * 64) + (g & 1) * 32 + p4 * 8;
    v4u wq[36][2];
    auto wrd = [&](int kk) {
#pragma unroll
      for (int c = 0; c < 2; ++c) wq[kk][c] = ldtr2(rb2[c] + (kk % RD) * 2048, 1024);
    };
    auto wgot = [&](int kk) { PIN(wq[kk][0]); PIN(wq[kk][1]); };
    const int ftile = 2 * rt + ((lane >> 2) & 1);
    const int T0 = 4 * r - 8 + 2 * m;
    const bool live = T0 >= 0;
    auto kv_fetch = [&]() {
      const unsigned src = WS_KV + (j * 64 + (live ? T0 : 0)) * 4096, vk = live ? lane * 16 : h * 512, vv = live ? lane * 16 : 0x40000000u;
#pragma unroll
      for (int i = 0; i < 8; ++i)
        dma16_sc1(sws, (i & 2) ? vv : vk, src + (live ? i : (i & 3)) * 1024, lds0 + L_KV + (2 * m + (i >> 2)) * KVT + (i & 3) * 1024 + ((i & 3) == 3 ? VB - 1024 : 0));
      const unsigned on = (lane < 2 || live) ? one2 : 0u;
      if (lane < 6) sts128(lds0 + L_KV + (lane < 2 ? 8 + m : 2 * m + ((lane - 2) >> 1)) * KVT + 2048 + (lane & 1) * VB + 1024, (v4u){on, on, on, on});
    };
    v4u af[28][2], of[8][2];
    v2u fl;
    const unsigned a0 = WS_AF + (2 * rt * AFT + 28 * m) * 1024, o0 = WS_AF + (2 * rt * AFT + 112 + 8 * m) * 1024;
    fl = ld8_sc1_async(sws, flag_off(FL_KV, live ? T0 + (lane & 1) : 0, j));
    for (int kk = 2; kk < 6; ++kk) wdma(kk);
    wait_vmf<8>(fl);
    if (lane == 0) __hip_atomic_store(flag(FL_H0, T, j), tag, RLX_AGENT);
    STAMP_AT(2);
    if (!__all(fl[0] == taglo && fl[1] == tag32)) poll_flags(flag(FL_KV, live ? T0 + (lane & 1) : 0, j), tag);
    if (heavy) await(lds0 + L_CNT + 20, 4);
    kv_fetch();
    wdma(6); wdma(7);
    static_for<0, 3>([&](auto ic) {
      constexpr int i = ic.value;
      wait_vm<(i == 0) ? 17 : 8>();
      for (int kk = 4 * i; kk < 4 * i + 4; ++kk) wrd(kk);
      for (int kk = 4 * i; kk < 4 * i + 4; ++kk) wgot(kk);
      if constexpr (i == 0) { wait_vm<4>(); arrive_nowait(lds0 + L_CNT); }
      if constexpr (i == 2) fl = ld8_sc1_async(sws, flag_off(FL_H0, ftile, 4 * m + p4));
      for (int kk = 4 * i; kk < 4 * i + 4; ++kk) wdma(kk + RD);
    });
    STAMP_AT(3);
    wait_vmf<8>(fl);
    if (!__all(fl[0] == taglo && fl[1] == tag32)) poll_flags(flag(FL_H0, ftile, 4 * m + p4), tag);
    STAMP_AT(4);
#pragma unroll
    for (int kk = 0; kk < AW; ++kk) {
      af[kk][0] = ld16_sc1_async(sws, lane * 16, a0 + kmap(kk) * 1024);
      af[kk][1] = ld16_sc1_async(sws, lane * 16, a0 + (AFT + kmap(kk)) * 1024);
    }
    __builtin_amdgcn_s_setprio(PRIO_G2);
    f32x16 c2[2][2] = {};
    auto mm2 = [&](int kk, const v4u (&a)[2], auto&& fill) {
      SBAR();
#pragma unroll
      for (int i = 0; i < 4; ++i) { c2[i >> 1][i & 1] = MFMA32(wq[kk][i & 1], a[i >> 1], c2[i >> 1][i & 1]); SBAR(); fill(i); SBAR(); }
    };
    static_for<0, 28>([&](auto kc) {
      constexpr int kk = kc.value;
      wait_vm2<(kk < AW) ? 2 * (AW - 1 - kk) + g2_range(0, kk) : g2_range(kk - AW + 1, kk)>(af[kk][0], af[kk][1]);
      if constexpr (kk == H1_CK) {
        STAMP_AT(20);
        wait_vmf<g2_range(H1_LD, H1_CK) - 1>(fl);
        if (!__all(fl[0] == taglo && fl[1] == tag32)) poll_flags(flag(FL_H1, ftile, 4 * m + p4), tag);
        STAMP_AT(21);
      }
      if constexpr (kk == H1_LD) fl = ld8_sc1_async(sws, flag_off(FL_H1, ftile, 4 * m + p4));
      if constexpr (kk == O_LD) fl = ld8_sc1_async(sws, flag_off(FL_O0, ftile, 4 * m + p4));
      if constexpr (kk == 26) fl = ld8_sc1_async(sws, flag_off(FL_O1, ftile, 4 * m + p4));
      constexpr bool dw = kk >= 1 && kk - 1 + NR + RD < 36;
      if constexpr (dw) wgot(kk - 1 + NR);
      mm2(kk, af[kk], [&](int i) {
        if constexpr (dw) { if (i < 2) wdmah(kk - 1 + NR + RD, i); }
        if constexpr (kk + NR < 28) { if (i == 1) wrd(kk + NR); }
        if constexpr (kk == 27) { if (i == 0) wrd(28); }
        if constexpr (kk + AW < 28) { if (i >= 2) af[kk + AW][i - 2] = ld16_sc1_async(sws, lane * 16, a0 + (AFT * (i - 2) + kmap(kk + AW)) * 1024); }
      });
      if constexpr (kk == O_CK) {
        wait_vmf<g2_range(O_LD, O_CK) - 1>(fl);
        if (!__all(fl[0] == taglo && fl[1] == tag32)) poll_flags(flag(FL_O0, ftile, 4 * m + p4), tag);
        res_loads();
#pragma unroll
        for (int i = 0; i < 4; ++i) {
          of[i][0] = ld16_sc1_async(sws, lane * 16, o0 + omap(i) * 1024);
          of[i][1] = ld16_sc1_async(sws, lane * 16, o0 + (AFT + omap(i)) * 1024);
        }
      }
    });
    static_for<0, 4>([&](auto kc) {
      constexpr int kk = kc.value;
      if (kk < 3) wrd(28 + kk + 1);
      if constexpr (kk == 1) {
        res_sum();
        STAMP_AT(5);
        wait_vmf<0>(fl);
        if (!__all(fl[0] == taglo && fl[1] == tag32)) poll_flags(flag(FL_O1, ftile, 4 * m + p4), tag);
        STAMP_AT(6);
#pragma unroll
        for (int i = 0; i < 8; ++i) of[4 + (i & 3)][i >> 2] = ld16_sc1_async(sws, lane * 16, o0 + (AFT * (i >> 2) + omap(4 + (i & 3))) * 1024);
      }
      wait_vm2<(kk == 0) ? 7 : 14 - 2 * kk>(of[kk][0], of[kk][1]);
      mm2(28 + kk, of[kk], [&](int i) { PIN(c2[i >> 1][i & 1]); if (kk == 3) wrd(32 + i); });
    });
    for (int kk = 32; kk < 36; ++kk) wgot(kk);
    unsigned pa = lds0 + L_WG + m * PART + lane * 16;
    PIN(pa);
    auto put = [&](int qn, int qd) {
      const f32x16& c = c2[qn >> 1][qn & 1];
      sts128(pa + (qn * 4 + qd) * 66 * 16, __builtin_bit_cast(v4u, (f32x4){c[4 * qd], c[4 * qd + 1], c[4 * qd + 2], c[4 * qd + 3]}));
    };
    static_for<0, 4>([&](auto qc) {
      constexpr int qn = qc.value;
      if constexpr ((qn & 1) == 0) { wait_vm2<qn ? 0 : 4>(of[4][qn >> 1], of[5][qn >> 1]); PIN(of[6][qn >> 1]); PIN(of[7][qn >> 1]); }
      SBAR();
#pragma unroll
      for (int i = 0; i < 4; ++i) {
        c2[qn >> 1][qn & 1] = MFMA32(wq[32 + i][qn & 1], of[4 + i][qn >> 1], c2[qn >> 1][qn & 1]);
        SBAR();
        if (qn > 0 && i >= 2) { put(qn - 1, 2 * i - 4); put(qn - 1, 2 * i - 3); }
        SBAR();
      }
    });
#pragma unroll
    for (int qd = 0; qd < 4; ++qd) put(3, qd);
    STAMP_AT(7);
  } else {
    __builtin_amdgcn_s_setprio(1);
    lane_setup();
    v4u xr[2];
    xr[0] = ld16_async(sx, lane * 16, (128 * r + 8 * j + 2 * m) * 1024); xr[1] = ld16_async(sx, lane * 16, (128 * r + 8 * j + 2 * m + 1) * 1024);
    unsigned warm;
    {
      u64 pc; asm volatile("s_getpc_b64 %0" : "=s"(pc));
      warm = ld2_async(make_srd((const void*)(pc & ~127ull), CODEWARM), lane < 3 ? lane * 128 : 0x40000000u, (idx * 4 + m) * 384);
    }
    __builtin_amdgcn_s_barrier();
    v2u ep = ld8_sc1_async(sws, m == 0 ? WS_EPOCH : 0x40000000u);
    float sval;
    const int wi = blockIdx.x * 4 + m;
    const u64 wbase = wi < 448 ? (u64)w1 + wi * 4096 : wi < 896 ? (u64)w2 + (wi - 448) * 4096 : (u64)wo + (wi - 896) * 4096;
    auto make_stats = [&]() {
      wait_vm2<1>(xr[0], xr[1]);
      PIN(warm);
      float piv[2], a[4];
#pragma unroll
      for (int u = 0; u < 2; ++u) {
        piv[u] = rdl(bflo(xr[u][0]), 0);
        float s1 = 0.f, s2 = 0.f;
#pragma unroll
        for (int i = 0; i < 8; ++i) { const float d = ((i & 1) ? bfhi(xr[u][i >> 1]) : bflo(xr[u][i >> 1])) - piv[u]; s1 += d; s2 = __builtin_fmaf(d, d, s2); }
        a[2 * u] = s1; a[2 * u + 1] = s2;
      }
#pragma unroll
      for (int i = 0; i < 4; ++i) a[i] += dpp<0xB1>(a[i]);
#pragma unroll
      for (int i = 0; i < 4; ++i) a[i] += dpp<0x4E>(a[i]);
      float t = p4 == 0 ? a[0] : p4 == 1 ? a[1] : p4 == 2 ? a[2] : a[3];
      t += dpp<0x114>(t); t += dpp<0x118>(t);
      const auto s16 = __builtin_amdgcn_permlane16_swap(asu(t), asu(t), false, false);
      t = asf(s16[0]) + asf(s16[1]);
      const auto s32 = __builtin_amdgcn_permlane32_swap(asu(t), asu(t), false, false);
      t = (asf(s32[0]) + asf(s32[1])) * (1.f / DM);
      const float e1 = dpp<0xA0>(t);
      sval = (p4 & 1) ? __builtin_fmaxf(__builtin_fmaf(-e1, e1, t), 0.f) + 1e-5f : t + (p4 ? piv[1] : piv[0]);
      PIN(sval);
    };
    const unsigned acnt = lds0 + L_CNT;
    const unsigned vpos = 2048 + (g & 1) * 512 + h * 256 + (lane & 15) * 16;
    v4u qf[2], am0, am1;
    u64 um[16];
    auto masks = [&]() {
      const int dq = r32 - 4 * h;
#pragma unroll
      for (int u = 0; u < 16; ++u) { um[u] = __builtin_amdgcn_ballot_w64((u & 3) + 8 * (u >> 2) > dq); pin_s(um[u]); }
#pragma unroll
      for (int d = 0; d < 4; ++d) {
        am0[d] = __builtin_amdgcn_inverse_ballot_w64(um[2 * d]) ? 0xffffffffu : __builtin_amdgcn_inverse_ballot_w64(um[2 * d + 1]) ? 0xffff0000u : 0u;
        am1[d] = __builtin_amdgcn_inverse_ballot_w64(um[8 + 2 * d]) ? 0xffffffffu : __builtin_amdgcn_inverse_ballot_w64(um[9 + 2 * d]) ? 0xffff0000u : 0u;
      }
      PIN(am0); PIN(am1);
    };
    {
      v4u xf[32];
      f32x16 acc[3] = {};
      f32x4 cs[3] = {};
      v4u wf[4][3];
      unsigned wb[4];
#pragma unroll
      for (int p = 0; p < 4; ++p) { wb[p] = qbuf(0) + ((p + m) & 3) * 1024 + (8 * h + q4) * 64 + (g & 1) * 32 + p4 * 8; PIN(wb[p]); }
      auto rd = [&](int ks, int t) {
        if (t == 0) xf[ks] = lds128(xbuf(ks >> 2) + xo[ks & 3]);
        wf[ks & 3][t] = ldtr2(wb[ks & 3] + ((ks >> 2) % 3) * 12288 + t * 4096, 256);
      };
      auto mm = [&](int ks, int t) {
        acc[t] = t == 2 ? MFMA32(xf[ks], wf[ks & 3][t], acc[t]) : MFMA32(wf[ks & 3][t], xf[ks], acc[t]);
        if ((ks & 3) == 0) { cs[t] = MFMA16(sel, wf[ks & 3][t], cs[t]); PIN(cs[t]); }
        SBAR();
      };
      make_stats();
      bar();
      STAMP_AT(10);
#pragma unroll
      for (int t = 0; t < 6; ++t) rd(t / 3, t % 3);
      SBAR();
      static_for<0, 8>([&](auto sc) {
        constexpr int s = sc.value;
#pragma unroll
        for (int p = 0; p < 2; ++p) {
#pragma unroll
          for (int t = 0; t < 3; ++t) { mm(4 * s + p, t); rd(4 * s + p + 2, t); SBAR(); }
          if (s == 7 && p == 0) { cswr(4, cs[0]); cswr(5, cs[1]); cswr(6, cs[2]); SBAR(); }
        }
        if constexpr (s == 0) {
          wait_vmf<0>(ep);
          if (m == 0 && lane < 10)
            *(LAS unsigned*)(size_t)(lds0 + L_CNT + lane * 4) = lane == 8 ? ep[0] * 0x9E3779B1u + 0x7F4A7C15u : lane == 9 ? ep[1] * 0x85EBCA77u + 0x165667B1u : 0u;
        }
        if constexpr (s == 1) {
          get_tag();
          if ((lane >> 2) == 3) {
            const v2u gr = {asu(sval), tag32};
            __builtin_amdgcn_raw_buffer_store_b64(gr, rws, WS_STATS + (128 * r + 8 * j + 2 * m) * 16 + p4 * 8, 0, 16);
          }
        }
        if constexpr (s == MIDWARM) {
          warm = ld2_async(make_srd((const void*)wbase, 4096), lane * 128, 0);
        }
        if constexpr (s == 5) stat_load();
        if constexpr (s == 6) {
          wait_vm1<0>(st);
          PIN(warm);

          row_stats();
        }
        mm(4 * s + 2, 0); mm(4 * s + 2, 1);
        bar();
        if constexpr (s < 7) {
          STAMP_AT(11 + s);
          mm(4 * s + 2, 2);
          rd(4 * s + 4, 0); rd(4 * s + 4, 1); rd(4 * s + 4, 2); SBAR();
#pragma unroll
          for (int t = 0; t < 3; ++t) { mm(4 * s + 3, t); rd(4 * s + 5, t); SBAR(); }
        }
      });
      STAMP_AT(1);
      __builtin_amdgcn_s_setprio(3);
      const f32x4 c5 = csrd(5), c6 = csrd(6), c4 = csrd(4);
      SBAR();
      mm(30, 2); mm(31, 1);
      make_xaug();
      SBAR();
      mm(31, 2); mm(31, 0);
      if (h == 0) *(LAS float*)(size_t)(lds0 + L_RS + (m * 32 + r32) * 4) = rstd;
      acc[1] = MFMA32(waug(c5, 0u), xaug, acc[1]);
      acc[2] = MFMA32(xaug, waug(c6, 0u), acc[2]);
      acc[0] = MFMA32(waug(c4, 0u), xaug, acc[0]);
      SBAR();
      f32x4 rs[4];
#pragma unroll
      for (int qd = 0; qd < 4; ++qd) rs[qd] = __builtin_bit_cast(f32x4, lds128(lds0 + L_RS + (m * 32 + 8 * qd + 4 * h) * 4));
      const unsigned kvo = WS_KV + (j * 64 + T) * 4096;
#pragma unroll
      for (int e = 0; e < 16; ++e) acc[1][e] = MUL1(acc[1][e], rstd);
#pragma unroll
      for (int e = 0; e < 2; ++e) {
        const v4u f = pack8(acc[1], e);
        st16_sc1(f, sws, lane * 16, kvo + e * 1024);
        sts128(lds0 + L_KV + (8 + m) * KVT + e * 1024 + lane * 16, f);
      }
#pragma unroll
      for (int qd = 0; qd < 4; ++qd)
#pragma unroll
        for (int e = 0; e < 4; ++e) acc[2][4 * qd + e] = MUL1(acc[2][4 * qd + e], rs[qd][e]);
#pragma unroll
      for (int s = 0; s < 2; ++s) {
        const v4u f = pack8(acc[2], s);
        st16_sc1(f, sws, vpos, kvo + s * 1024);
        sts128(lds0 + L_KV + (8 + m) * KVT + s * VB + vpos, f);
      }
      SBAR();
      const float qs = rstd * (0.25f * 1.4426950408889634f);
#pragma unroll
      for (int e = 0; e < 16; ++e) acc[0][e] = MUL1(acc[0][e], qs);
      qf[0] = pack8(acc[0], 0); qf[1] = pack8(acc[0], 1);
      PIN(qf[0]); PIN(qf[1]);
      arrive_nowait(acnt);
      STAMP_AT(2);
      if (heavy) {
        hsetup(1);
        await(acnt + 16, 4);
        STAMP_AT(3);
        hmm(std::integral_constant<int, 1>{}, xf, [&](auto kc) {
          constexpr int ks = kc.value;
          if constexpr (ks == KVPUB) {
            wait_vm<0>();
            if (lane == 0) __hip_atomic_store(flag(FL_KV, T, j), tag, RLX_AGENT);
          }
          if constexpr (ks == KVPUB + 2) bias_load(1);
          if constexpr (ks == 31) { wait_vm<0>(); PIN(bias0); PIN(bias1); }
        });
        STAMP_AT(18);
        hgelu(std::integral_constant<int, 1>{}, [&]() {});
        STAMP_AT(4);
      } else {
        wait_vm<0>();
        if (lane == 0) __hip_atomic_store(flag(FL_KV, T, j), tag, RLX_AGENT);
      }
    }
    masks();
    STAMP_AT(19);
    const unsigned kb = lds0 + L_KV + m * KVT + lane * 16;
    unsigned vb[2];
#pragma unroll
    for (int e = 0; e < 2; ++e) { vb[e] = lds0 + L_KV + m * KVT + 2048 + ((g & 1) ? 1024 : e * 512 + h * 256 + (lane & 15) * 16); PIN(vb[e]); }
    bool pend = true;
    if (__builtin_amdgcn_readfirstlane(peek(acnt)) < 8) {
      wait_vm<0>();
      if (lane == 0) __hip_atomic_store(flag(FL_H1, T, j), tag, RLX_AGENT);
      pend = false;
      await(acnt, 8);
    }
    asm volatile("" ::: "memory");
    __builtin_amdgcn_s_setprio(PRIO_ATT);
    STAMP_AT(5);
    const f32x16 zero = {};
    auto o_store = [&](f32x16& o, int e) {
      const float rl = __builtin_amdgcn_rcpf(o[8]);
#pragma unroll
      for (int u = 0; u < 8; ++u) o[u] = MUL1(o[u], rl);
      st16_sc1(pack8(o, 0), sws, lane * 16, WS_AF + (T * AFT + 112 + 2 * j + e) * 1024);
    };
    int redo = 0;
    {
      f32x16 o[2], sa, sb, su;
      v4u kn, ku, va, vc, vn0, vn1, vu0, vu1, p0, p1, pu;
      auto kaddr = [&](int n) { return kb + ((n & 7) + 1) * KVT + (n >> 3) * 1024; };
      auto vaddr = [&](int n) { return vb[n >> 3] + ((n & 7) + 1) * KVT; };
      kn = lds128(kaddr(0)); vn0 = lds128(vaddr(0)); vn1 = lds128(vaddr(0) + VB);
      sa = MFMA32(kn, qf[0], zero);
      kn = lds128(kaddr(1));
      SBAR();
      auto step = [&](auto nc, f32x16& sc, f32x16& sn) {
        constexpr int n = nc.value, e = n >> 3, i = n & 7;
        if constexpr (n < 15) sn = MFMA32(kn, qf[(n + 1) >> 3], zero);
        if constexpr (n < 14) kn = lds128(kaddr(n + 2));
        if constexpr (i == 5) ku = lds128(kb + e * 1024);
        v4u v0 = vn0, v1 = vn1;
        if constexpr (n < 15) { vn0 = lds128(vaddr(n + 1)); vn1 = lds128(vaddr(n + 1) + VB); }
        if constexpr (i == 6) { vu0 = lds128(vb[e]); vu1 = lds128(vb[e] + VB); }
        SBAR();
        if constexpr (i == 7) {
#pragma unroll
          for (int u = 0; u < 16; ++u) sc[u] = __builtin_amdgcn_inverse_ballot_w64(um[u]) ? su[u] : sc[u];
          PIN(sc); SBAR();
        }
#pragma unroll
        for (int u = 0; u < 6; ++u) sc[u] = EXP2(sc[u]);
        PIN(sc); SBAR();
        if constexpr (n == 8) { o[0] = MFMA32(vc, p1, o[0]); SBAR(); }
        else if constexpr (n > 0) { o[e] = MFMA32(vc, p1, o[e]); SBAR(); }
        sc[6] = EXP2(sc[6]); sc[7] = EXP2(sc[7]);
        p0 = pack8(sc, 0);
        sc[8] = EXP2(sc[8]); sc[9] = EXP2(sc[9]);
        if constexpr (i == 7) { va = p0 & am0; p0 &= ~am0; PIN(va); }
        PIN(sc); PIN(p0); SBAR();
        if constexpr (n == 8) { o[0] = MFMA32(vu1, pu, o[0]); SBAR(); }
        o[e] = MFMA32(v0, p0, i == 0 ? zero : o[e]);
        SBAR();
        if constexpr (i == 6) { su = MFMA32(ku, qf[e], zero); SBAR(); }
#pragma unroll
        for (int u = 10; u < 13; ++u) sc[u] = EXP2(sc[u]);
        if constexpr (i == 7) { PIN(sc); SBAR(); o[e] = MFMA32(vu0, va, o[e]); SBAR(); }
#pragma unroll
        for (int u = 13; u < 16; ++u) sc[u] = EXP2(sc[u]);
        p1 = pack8(sc, 1); vc = v1;
        if constexpr (i == 7) { pu = p1 & am1; p1 &= ~am1; PIN(pu); }
        PIN(p1); SBAR();
        if constexpr (n == 8) {
          if (__all(o[0][8] >= 0x1p-30f && o[0][8] <= 0x1p30f)) o_store(o[0], 0); else redo = 1;
          SBAR();
        }
        if constexpr (n == 10) pend = !redo;
        if constexpr (i == 3 || i == 5 || i == 7) {
          if (pend && (i == 7 || (__builtin_amdgcn_s_getreg(7 | (3 << 11)) & 15) == 0)) {
            wait_vm<0>();
            if (lane == 0) __hip_atomic_store(flag(FL_H1 + e, T, j), tag, RLX_AGENT);
            pend = false;
          }
        }
      };
      static_for<0, 8>([&](auto hc) {
        step(std::integral_constant<int, 2 * hc.value>{}, sa, sb);
        step(std::integral_constant<int, 2 * hc.value + 1>{}, sb, sa);
      });
      o[1] = MFMA32(vc, p1, o[1]);
      o[1] = MFMA32(vu1, pu, o[1]);
      if (__all(o[1][8] >= 0x1p-30f && o[1][8] <= 0x1p30f)) o_store(o[1], 1); else redo |= 2;
    }
    if (redo) {
#pragma unroll 1
      for (int e = 0; e < 2; ++e) {
        if (!((redo >> e) & 1)) continue;
        const v4u q = e ? qf[1] : qf[0];
        const unsigned vbe = e ? vb[1] : vb[0];
        auto merged = [&](const f32x16& c) {
          f32x16 sl = MFMA32(lds128(kb + 8 * KVT + e * 1024), q, c);
          const f32x16 su = MFMA32(lds128(kb + e * 1024), q, c);
          static_for<0, 16>([&](auto u) { sl[u.value] = upper<u.value>() ? su[u.value] : sl[u.value]; });
          return sl;
        };
        float mx = -INFINITY;
#pragma unroll 1
        for (int i = 0; i < 8; ++i) {
          const f32x16 sc = i ? MFMA32(lds128(kb + i * KVT + e * 1024), q, zero) : merged(zero);
#pragma unroll
          for (int u = 0; u < 16; u += 2) mx = max3(mx, sc[u], sc[u + 1]);
        }
        const auto sw = __builtin_amdgcn_permlane32_swap(asu(mx), asu(mx), false, false);
        mx = __builtin_fmaxf(asf(sw[0]), asf(sw[1]));
        f32x16 negm, o;
#pragma unroll
        for (int u = 0; u < 16; ++u) negm[u] = -mx;
        {
          f32x16 sc = merged(negm);
#pragma unroll
          for (int u = 0; u < 16; ++u) sc[u] = EXP2(sc[u]);
          const v4u p0 = pack8(sc, 0), p1 = pack8(sc, 1);
          o = MFMA32(lds128(vbe), p0 & am0, zero);
          o = MFMA32(lds128(vbe + VB), p1 & am1, o);
          o = MFMA32(lds128(vbe + 8 * KVT), p0 & ~am0, o);
          o = MFMA32(lds128(vbe + 8 * KVT + VB), p1 & ~am1, o);
        }
#pragma unroll 1
        for (int i = 1; i < 8; ++i) {
          f32x16 sc = MFMA32(lds128(kb + i * KVT + e * 1024), q, negm);
#pragma unroll
          for (int u = 0; u < 16; ++u) sc[u] = EXP2(sc[u]);
          o = MFMA32(lds128(vbe + i * KVT), pack8(sc, 0), o);
          o = MFMA32(lds128(vbe + i * KVT + VB), pack8(sc, 1), o);
        }
        o_store(o, e);
      }
      wait_vm<0>();
      if (lane == 0) __hip_atomic_store(flag(FL_O0, T, j), tag, RLX_AGENT);
    }
    STAMP_AT(6);
    wait_vm<0>();
    if (lane == 0) __hip_atomic_store(flag(FL_O1, T, j), tag, RLX_AGENT);
    STAMP_AT(7);
    res_loads();
    wait_vm<0>();
    res_sum();
    if (blockIdx.x == 0 && wave == 7) {
#pragma unroll 1
      for (int i = 0; i < 4; ++i) poll_flags(flag(FL_KV, 4 * (4 * i + g), lane & 15), tag);
      if (lane == 0) __hip_atomic_store((GAS u64*)(ws + WS_EPOCH), tag, RLX_AGENT);
    }
  }
  {
    int t2 = threadIdx.x; PIN(t2);
    const int row = t2 >> 3, ch = t2 & 7;
    unsigned a = lds0 + L_WG + (((row >> 5) * 8 + ch) * 66 + (row & 31)) * 16, oo = (64 * rt + row) * 1024 + (64 * ct + ch * 8) * 2;
    PIN(a); PIN(oo);
    bar();
    f32x4 pl[4], ph[4];
#pragma unroll
    for (int w = 0; w < 4; ++w) { pl[w] = __builtin_bit_cast(f32x4, lds128(a + w * PART)); ph[w] = __builtin_bit_cast(f32x4, lds128(a + w * PART + 512)); }
    SBAR();
#pragma unroll
    for (int w = 0; w < 4; ++w) { lo += pl[w]; hi += ph[w]; }
    const v4u o = {pk2(lo[0], lo[1]), pk2(lo[2], lo[3]), pk2(hi[0], hi[1]), pk2(hi[2], hi[3])};
    __builtin_amdgcn_raw_buffer_store_b128(o, __builtin_amdgcn_make_buffer_rsrc(out, 0, SEQ * DM * 2, 0x00020000), oo, 0, 16);
  }
  STAMP_AT(8);
  if (tstart) while (now_t() - tstart < 4000) __builtin_amdgcn_s_sleep(1);
}

extern "C" void kernel_launch(void* const* d_in, const int* in_sizes, int n_in, void* d_out, int out_size, void* d_ws, size_t ws_size,
                              hipStream_t stream) {
  (void)in_sizes; (void)n_in; (void)out_size; (void)ws_size;
  (void)hipFuncSetAttribute((const void*)fused_block, hipFuncAttributeMaxDynamicSharedMemorySize, LDS_BYTES);
  fused_block<<<256, 512, LDS_BYTES, stream>>>(d_in[0], d_in[1], d_in[2], d_in[4], d_in[5], d_in[6], d_in[7], d_out, (char*)d_ws);
}
```

```cpp
#include <hip/hip_runtime.h>
#include <hip/hip_bf16.h>
#include <stdint.h>
#include <type_traits>

typedef unsigned v4u __attribute__((ext_vector_type(4)));
typedef unsigned v2u __attribute__((ext_vector_type(2)));
typedef float f32x2 __attribute__((ext_vector_type(2)));
typedef float f32x4 __attribute__((ext_vector_type(4)));
typedef float f32x16 __attribute__((ext_vector_type(16)));
typedef short bf16x8 __attribute__((ext_vector_type(8)));
typedef short s16x4 __attribute__((ext_vector_type(4)));
typedef __bf16 bf16x2_t __attribute__((ext_vector_type(2)));
typedef unsigned long long u64;
#define GAS __attribute__((address_space(1)))
#define LAS __attribute__((address_space(3)))
#define DI __device__ __forceinline__
#define RLX_AGENT __ATOMIC_RELAXED, __HIP_MEMORY_SCOPE_AGENT

constexpr int SEQ = 2048, DM = 512, FF = 1792;
constexpr unsigned WS_EPOCH = 0;
constexpr unsigned WS_STATS = 32768;
constexpr unsigned WS_FLAG = 65536;
constexpr unsigned WS_KV = WS_FLAG + 5 * 1024 * 128;
constexpr int AFT = 145;
constexpr unsigned WS_AF = WS_KV + 16 * 64 * 4096;
constexpr unsigned WS_END = WS_AF + 64 * AFT * 1024;
constexpr unsigned WS_STAMP = WS_END;
enum { FL_KV = 0, FL_H0 = 1, FL_H1 = 2, FL_O0 = 3, FL_O1 = 4 };
constexpr int L_WA = 0;
constexpr int L_WG = 86016;
constexpr int VB = 1040;
constexpr int KVT = 2048 + 2 * VB;
constexpr int L_KV = 32768;
constexpr int RD = 8, NR = 12;
constexpr int PART = 16 * 66 * 16;
constexpr int L_CS = L_WG + 4 * PART;
constexpr int L_RS = L_CS + 3584;
constexpr int L_CNT = L_RS + 512;
constexpr int LDS_BYTES = L_CNT + 64;
constexpr int xbuf(int s) { return ((s + 1) % 3) * 16384; }
constexpr int qbuf(int s) { return 49152 + (s % 3) * 12288; }
constexpr int w1buf(int s) { return L_WG + (s % 3) * PART + 8192; }
static_assert(L_KV + 8 * KVT >= 65536 && xbuf(7) == 32768 && qbuf(2) + 12288 <= L_WG && L_KV + 12 * KVT <= L_WG && PART >= RD * 2048 && LDS_BYTES <= 163840, "lds map");
#define AWIN 6
constexpr int AW = AWIN;
constexpr unsigned SPIN_CAP = 1u << 23;

#define MFMA32(a, b, c) __builtin_amdgcn_mfma_f32_32x32x16_bf16(__builtin_bit_cast(bf16x8, a), __builtin_bit_cast(bf16x8, b), c, 0, 0, 0)
#define MFMA16(a, b, c) __builtin_amdgcn_mfma_f32_16x16x32_bf16(__builtin_bit_cast(bf16x8, a), __builtin_bit_cast(bf16x8, b), c, 0, 0, 0)
#define SBAR() __builtin_amdgcn_sched_barrier(0)

#define STAMP_AT(i) do { } while (0)

DI float asf(unsigned u) { return __builtin_bit_cast(float, u); }
DI unsigned asu(float f) { return __builtin_bit_cast(unsigned, f); }
DI unsigned pk2(float lo, float hi) { return __builtin_bit_cast(unsigned, __builtin_convertvector((f32x2){lo, hi}, bf16x2_t)); }
DI float bflo(unsigned u) { return __builtin_bit_cast(float, u << 16); }
DI float bfhi(unsigned u) { return __builtin_bit_cast(float, u & 0xffff0000u); }
DI v4u make_srd(const void* p, unsigned bytes) { const u64 a = (u64)p; return (v4u){(unsigned)a, (unsigned)(a >> 32) & 0xffffu, bytes, 0x00020000u}; }
DI void dma16(v4u srd, unsigned voff, unsigned soff, unsigned lds) {
  asm volatile("s_mov_b32 m0, %3\n\ts_nop 0\n\tbuffer_load_dwordx4 %0, %1, %2 offen lds" :: "v"(voff), "s"(srd), "s"(soff), "s"(lds) : "memory");
}
DI void dma16_sc1(v4u srd, unsigned voff, unsigned soff, unsigned lds) {
  asm volatile("s_mov_b32 m0, %3\n\ts_nop 0\n\tbuffer_load_dwordx4 %0, %1, %2 offen sc1 lds" :: "v"(voff), "s"(srd), "s"(soff), "s"(lds) : "memory");
}
DI void dma16_a(v4u srd, unsigned voff, unsigned soff, unsigned lds) { dma16(srd, voff, soff, lds); }
DI void dma16_b(v4u srd, unsigned voff1, unsigned soff) {
  asm volatile("buffer_load_dwordx4 %0, %1, %2 offen offset:1024 lds" :: "v"(voff1), "s"(srd), "s"(soff) : "memory");
}
DI v4u ld16_sc1_async(v4u srd, unsigned voff, unsigned soff) {
  v4u r; asm volatile("buffer_load_dwordx4 %0, %1, %2, %3 offen sc1" : "=v"(r) : "v"(voff), "s"(srd), "s"(soff) : "memory"); return r;
}
DI v4u ld16_async(v4u srd, unsigned voff, unsigned soff) {
  v4u r; asm volatile("buffer_load_dwordx4 %0, %1, %2, %3 offen" : "=v"(r) : "v"(voff), "s"(srd), "s"(soff) : "memory"); return r;
}
DI unsigned ld2_async(v4u srd, unsigned voff, unsigned soff) {
  unsigned r; asm volatile("buffer_load_ushort %0, %1, %2, %3 offen" : "=v"(r) : "v"(voff), "s"(srd), "s"(soff) : "memory"); return r;
}
DI void st16_sc1(v4u v, v4u srd, unsigned voff, unsigned soff) {
  asm volatile("buffer_store_dwordx4 %0, %1, %2, %3 offen sc1\n\ts_nop 1" :: "v"(v), "v"(voff), "s"(srd), "s"(soff) : "memory");
}
template <int N> DI void wait_vm1(v4u& a) { asm volatile("s_waitcnt vmcnt(%1)" : "+v"(a) : "i"(N) : "memory"); }
template <int N> DI void wait_vm2(v4u& a, v4u& b) { asm volatile("s_waitcnt vmcnt(%2)" : "+v"(a), "+v"(b) : "i"(N) : "memory"); }
template <int N> DI void wait_vm3(v4u& a, v4u& b, v4u& c) { asm volatile("s_waitcnt vmcnt(%3)" : "+v"(a), "+v"(b), "+v"(c) : "i"(N) : "memory"); }
DI void pin_s(u64& v) { asm volatile("" : "+s"(v)); }
template <class V> DI void pin(V& v) { asm volatile("" : "+v"(v)); }
template <int N> DI void wait_vm() { asm volatile("s_waitcnt vmcnt(%0)" :: "i"(N) : "memory"); }
DI void bar() { asm volatile("s_waitcnt lgkmcnt(0)\n\ts_barrier" ::: "memory"); }
DI v4u lds128(unsigned a) { return *(const LAS v4u*)(size_t)a; }
DI void sts128(unsigned a, v4u v) { *(LAS v4u*)(size_t)a = v; }
DI v2u ldtr(unsigned a) { return __builtin_bit_cast(v2u, __builtin_amdgcn_ds_read_tr16_b64_v4i16((LAS s16x4*)(size_t)a)); }
DI v4u ldtr2(unsigned a, int second) { const v2u lo = ldtr(a), hi = ldtr(a + second); return (v4u){lo.x, lo.y, hi.x, hi.y}; }
template <int CTRL> DI float dpp(float v) { return __builtin_bit_cast(float, __builtin_amdgcn_update_dpp(0, __builtin_bit_cast(int, v), CTRL, 0xf, 0xf, true)); }
DI float rdl(float v, int l) { return __builtin_bit_cast(float, __builtin_amdgcn_readlane(__builtin_bit_cast(int, v), l)); }
DI v4u pack8(const f32x16& a, int s) { return (v4u){pk2(a[8 * s], a[8 * s + 1]), pk2(a[8 * s + 2], a[8 * s + 3]), pk2(a[8 * s + 4], a[8 * s + 5]), pk2(a[8 * s + 6], a[8 * s + 7])}; }
DI void poll_flags(const GAS u64* p, u64 tag) {
#pragma unroll 1
  for (unsigned spin = 0; spin < SPIN_CAP; ++spin) {
    const u64 v = __hip_atomic_load(p, RLX_AGENT);
    if (__all(v == tag)) break;
    __builtin_amdgcn_s_sleep(1);
  }
  asm volatile("" ::: "memory");
}
constexpr u64 upper_mask(int reg) {
  u64 m = 0;
  for (int l = 0; l < 64; ++l) { const int row = (reg & 3) + 8 * (reg >> 2) + 4 * (l >> 5); if (row > (l & 31)) m |= 1ull << l; }
  return m;
}
template <int REG> DI bool upper() { return __builtin_amdgcn_inverse_ballot_w64(upper_mask(REG)); }
template <int I, int N, class Fn> DI void static_for(Fn&& f) { if constexpr (I < N) { f(std::integral_constant<int, I>{}); static_for<I + 1, N>(f); } }
DI float max3(float a, float b, float c) { return __builtin_fmaxf(__builtin_fmaxf(a, b), c); }

constexpr int H1_CK = 16 - AW, H1_LD = H1_CK - 3, O_CK = 28 - AW, O_LD = O_CK - 3;
constexpr int g2_issued(int t) { return ((t >= 1 && t - 1 + NR + RD < 36) ? 2 : 0) + ((t + AW < 28) ? 2 : 0) + (t == H1_LD) + (t == O_LD) + (t == O_CK ? 10 : 0) + (t == 26); }
constexpr int g2_range(int a, int b) { int n = 0; for (int t = a; t < b; ++t) n += g2_issued(t); return n; }
#define GELU_PK(P) (P == 1)
#define PRIO_G2 2
#define PRIO_ATT 3
#define PRIO_G1 2
#define ROT r
#define CODEWARM 38000
#define MIDWARM 1
#define PK0 1
#define KVPUB 2
#define EXP2(v) __builtin_amdgcn_exp2f(v)
#define PIN(v) pin(v)
extern "C" __device__ float mul_unpacked(float, float) __asm("llvm.amdgcn.fmul.legacy");
#define MUL1(a, b) mul_unpacked(a, b)
#define FMA1(a, b, c) __builtin_amdgcn_div_fmasf(a, b, c, false)
DI v2u ld8_sc1_async(v4u srd, unsigned voff) {
  v2u r; asm volatile("buffer_load_dwordx2 %0, %1, %2, 0 offen sc1" : "=v"(r) : "v"(voff), "s"(srd) : "memory"); return r;
}
template <int N> DI void wait_vmf(v2u& a) { asm volatile("s_waitcnt vmcnt(%1)" : "+v"(a) : "i"(N) : "memory"); }
DI void arrive(unsigned cnt, int lane) {
  asm volatile("s_waitcnt lgkmcnt(0)" ::: "memory");
  if (lane == 0) __hip_atomic_fetch_add((LAS unsigned*)(size_t)cnt, 1u, __ATOMIC_RELAXED, __HIP_MEMORY_SCOPE_WORKGROUP);
}
DI void arrive_nowait(unsigned cnt) {
  asm volatile("s_mov_b64 exec, 1\n\tds_add_u32 %0, %1\n\ts_mov_b64 exec, -1" :: "v"(cnt), "v"(1u) : "memory");
}
DI unsigned peek(unsigned cnt) { return __hip_atomic_load((LAS unsigned*)(size_t)cnt, __ATOMIC_RELAXED, __HIP_MEMORY_SCOPE_WORKGROUP); }
DI void await(unsigned cnt, unsigned n) {
#pragma unroll 1
  for (unsigned spin = 0; spin < SPIN_CAP * 32; ++spin) {
    if (__builtin_amdgcn_readfirstlane(peek(cnt)) >= n) break;
    __builtin_amdgcn_s_sleep(1);
  }
  asm volatile("" ::: "memory");
}

__global__ __launch_bounds__(512, 2) void fused_block(const void* __restrict__ x, const void* __restrict__ wqkv, const void* __restrict__ wo,
                                                      const void* __restrict__ w1, const void* __restrict__ b1, const void* __restrict__ w2,
                                                      const void* __restrict__ b2, void* __restrict__ out, char* __restrict__ ws) {
  constexpr unsigned lds0 = 0;
  const int wave = __builtin_amdgcn_readfirstlane(threadIdx.x >> 6);
  const int m = wave & 3;
  const bool isG = wave < 4;
  const int xcd = blockIdx.x & 7, idx = blockIdx.x >> 3;
  const int r = (xcd >> 2) * 8 + (idx >> 2), j = (xcd & 3) * 4 + (idx & 3);
  const int rt = (xcd >> 1) * 8 + (idx >> 2), ct = (xcd & 1) * 4 + (idx & 3);
  int lane = threadIdx.x & 63, r32, h, g, q4, p4;
  STAMP_AT(0);
  asm volatile("" :: "s"(x), "s"(wqkv), "s"(wo), "s"(w1), "s"(b1), "s"(w2), "s"(b2), "s"(out), "s"(ws));

  const v4u sx = make_srd(x, SEQ * DM * 2), sws = make_srd(ws, WS_END);
  const __amdgpu_buffer_rsrc_t rws = __builtin_amdgcn_make_buffer_rsrc(ws, 0, WS_END, 0x00020000);
  const unsigned one2 = 0x3f803f80u;
  const int T = 4 * r + m;
  const int rot = ROT;
  auto flag_off = [&](int kind, int tile, int jj) { return WS_FLAG + ((kind * 64 + tile) * 16 + jj) * 128; };
  auto flag = [&](int kind, int tile, int jj) { return (GAS u64*)(ws + flag_off(kind, tile, jj)); };
  u64 tag;
  unsigned taglo, tag32;
  auto get_tag = [&]() {
    const v2u tg = *(const LAS v2u*)(size_t)(lds0 + L_CNT + 32);
    taglo = __builtin_amdgcn_readfirstlane(tg[0]); tag32 = __builtin_amdgcn_readfirstlane(tg[1]);
    tag = ((u64)tag32 << 32) | taglo;
  };
  v4u xres, bres, xaug, st;
  float mean, var, rstd, sd;
  v4u sel;
  unsigned xo[4], swz;
  auto lane_setup = [&]() {
    lane = threadIdx.x & 63; PIN(lane);
    r32 = lane & 31; h = lane >> 5; g = lane >> 4; q4 = (lane >> 2) & 3; p4 = lane & 3;
    const unsigned selv = ((lane & 15) == (g & 1)) ? one2 : 0u;
    sel = (v4u){selv, selv, selv, selv};
#pragma unroll
    for (int p = 0; p < 4; ++p) xo[p] = (32 * m + r32) * 128 + (((2 * ((p + m) & 3) + h) ^ ((r32 >> 1) & 7)) * 16);
    swz = ((((lane >> 2) & 1) ^ (g & 1)) * 64) + p4 * 16;
  };

  auto stat_load = [&]() { st = ld16_sc1_async(sws, WS_STATS + (128 * r + 32 * m + r32) * 16, 0); };
  auto res_loads = [&]() {
    int t2 = threadIdx.x; PIN(t2);
    const int row = 64 * rt + (t2 >> 3), col = 64 * ct + (t2 & 7) * 8;
    xres = ld16_async(sx, row * 1024 + col * 2, 0);
    bres = ld16_async(make_srd(b2, DM * 2), col * 2, 0);
  };
  f32x4 lo, hi;
  auto res_sum = [&]() {
    PIN(xres); PIN(bres);
    lo = (f32x4){bflo(xres[0]) + bflo(bres[0]), bfhi(xres[0]) + bfhi(bres[0]), bflo(xres[1]) + bflo(bres[1]), bfhi(xres[1]) + bfhi(bres[1])};
    hi = (f32x4){bflo(xres[2]) + bflo(bres[2]), bfhi(xres[2]) + bfhi(bres[2]), bflo(xres[3]) + bflo(bres[3]), bfhi(xres[3]) + bfhi(bres[3])};
    PIN(lo); PIN(hi);
  };
  auto row_stats = [&]() {
    if (!__all(st[1] == tag32 && st[3] == tag32)) {
#pragma unroll 1
      for (unsigned spin = 0; spin < SPIN_CAP; ++spin) {
        st = ld16_sc1_async(sws, WS_STATS + (128 * r + 32 * m + r32) * 16, 0);
        wait_vm1<0>(st);
        if (__all(st[1] == tag32 && st[3] == tag32)) break;
      }
    }
    mean = asf(st[0]); var = asf(st[2]);
    PIN(mean); PIN(var);
  };
  auto make_xaug = [&]() {
    PIN(mean); PIN(var);
    rstd = __builtin_amdgcn_rsqf(var); sd = var * rstd;
    const unsigned uh = pk2(-mean, sd);
    const unsigned ul = pk2(-mean - bflo(uh), FMA1(bfhi(uh), -1.f, sd));
    xaug = (v4u){(uh & 0xffffu) | (uh << 16), (ul & 0xffffu) | (uh & 0xffff0000u), ul >> 16, 0u};
    if (h) xaug = (v4u){0u, 0u, 0u, 0u};
    PIN(xaug);
  };
  auto cswr = [&](int tile, f32x4 c) {
    if (lane < 16) { LAS float* p = (LAS float*)(size_t)(lds0 + L_CS + (tile * 32 + lane) * 16 + m * 4); p[0] = c[0]; p[64] = c[1]; }
  };
  auto csrd = [&](int tile) { return __builtin_bit_cast(f32x4, lds128(lds0 + L_CS + (tile * 32 + r32) * 16)); };
  auto waug = [&](f32x4 c4, unsigned b16) {
    const float c = ((c4[0] + c4[1]) + c4[2]) + c4[3];
    const unsigned uh = pk2(c, 0.f);
    const unsigned ul = pk2(c - bflo(uh), 0.f);
    return (v4u){uh | (ul << 16), uh | (b16 << 16), b16, 0u};
  };

  const v4u sw1 = make_srd(w1, DM * FF * 2);
  const int hcol0 = 448 * (j >> 2) + 128 * (j & 3);
  const bool heavy = (j & 3) != 3;
  unsigned bias0, bias1;
  auto bias_load = [&](int P) {
    bias0 = ld2_async(make_srd(b1, FF * 2), r32 * 2, (hcol0 + 64 * P) * 2);
    bias1 = ld2_async(make_srd(b1, FF * 2), r32 * 2, (hcol0 + 64 * P + 32) * 2);
  };
  auto w1dma = [&](int P, int c, unsigned dst, int half) {
    const unsigned vw1 = (lane >> 3) * (FF * 2) + swz, vw1b = vw1 + 8 * (FF * 2) - 1024;
    const unsigned so = (64 * ((c + rot) & 7) + 16 * m) * (FF * 2) + (hcol0 + 64 * P) * 2;
    if (half == 0) dma16_a(sw1, vw1, so, dst + m * 2048); else dma16_b(sw1, vw1b, so);
  };
  f32x16 hac[2];
  unsigned rk[4][2];
  auto hsetup = [&](int P) {
#pragma unroll
    for (int p = 0; p < 4; ++p)
#pragma unroll
      for (int c = 0; c < 2; ++c) {
        rk[p][c] = lds0 + (P ? L_WA : w1buf(0)) + ((p + m) & 3) * 2048 + h * 1024 + q4 * 128 + ((c ^ (q4 >> 1)) * 64) + (g & 1) * 32 + p4 * 8;
        PIN(rk[p][c]);
      }
  };
  auto hmm = [&](auto pc, const v4u (&xf)[32], auto&& hook) {
    constexpr int P = pc.value;
    constexpr int D = 4;
    const unsigned cnt = lds0 + L_CNT + 4;
    f32x4 cs[2] = {}, c0, c1;
    v4u wf[D][2], au0, au1;
    unsigned seen = 0;
    hac[0] = (f32x16){}; hac[1] = (f32x16){};
    auto rd = [&](int ks, int t) { wf[ks % D][t] = ldtr2(rk[ks & 3][t] + ((ks >> 2) ^ 4) * 8192, 512); };
#pragma unroll
    for (int ks = 0; ks < D; ++ks) { rd(ks, 0); rd(ks, 1); }
    SBAR();
    static_for<0, 32>([&](auto kc) {
      constexpr int ks = kc.value;
      hook(kc);
      if constexpr (ks == 12) arrive_nowait(cnt + 16);
      if constexpr (ks == 29) seen = peek(cnt);
#pragma unroll
      for (int t = 0; t < 2; ++t) {
        hac[t] = MFMA32(wf[ks % D][t], xf[ks ^ 16], hac[t]);
        if ((ks & 3) == 0 && ks < 28) { cs[t] = MFMA16(sel, wf[ks % D][t], cs[t]); PIN(cs[t]); }
        if (ks == 26) { cs[t] = MFMA16(sel, wf[28 % D][t], cs[t]); PIN(cs[t]); }
        SBAR();
        if constexpr (ks + D < 32) rd(ks + D, t);
        if constexpr (ks == 31) { if (t == 0) { au0 = waug(c0, bias0); PIN(au0); } else { au1 = waug(c1, bias1); PIN(au1); } }
        SBAR();
      }
      if constexpr (ks == 30) {
        PIN(hac[0]); PIN(hac[1]);
        if (__builtin_amdgcn_readfirstlane(seen) < 4) await(cnt, 4);
        asm volatile("" ::: "memory");
        if constexpr (ks == 30) { c0 = csrd(2 * P); c1 = csrd(2 * P + 1); }
      }
      if constexpr (ks == 27) { cswr(2 * P, cs[0]); cswr(2 * P + 1, cs[1]); arrive_nowait(cnt); }
      SBAR();
    });
    hac[0] = MFMA32(au0, xaug, hac[0]);
    hac[1] = MFMA32(au1, xaug, hac[1]);
    SBAR();
  };
  auto hgelu = [&](auto pc, auto&& mid) {
    constexpr int P = pc.value;
    const float C0 = 2.3022081986f, C1 = C0 * 0.044715f;
    const float ga = -C1 * rstd * rstd * rstd, gb = -C0 * rstd;
#pragma unroll
    for (int t = 0; t < 2; ++t) {
      const bool PK = P == 1 || (PK0 && t == 0);
      auto mul = [&](f32x2 a, f32x2 b) { return PK ? a * b : (f32x2){MUL1(a[0], b[0]), MUL1(a[1], b[1])}; };
      auto fma = [&](f32x2 a, float b, float c) {
        return PK ? __builtin_elementwise_fma(a, (f32x2){b, b}, (f32x2){c, c}) : (f32x2){FMA1(a[0], b, c), FMA1(a[1], b, c)}; };
      f32x2 v[8], z[8];
#pragma unroll
      for (int e = 0; e < 8; ++e) { v[e] = (f32x2){hac[t][2 * e], hac[t][2 * e + 1]}; z[e] = mul(v[e], v[e]); }
      SBAR();
#pragma unroll
      for (int e = 0; e < 8; ++e) z[e] = fma(z[e], ga, gb);
      SBAR();
#pragma unroll
      for (int e = 0; e < 8; ++e) z[e] = mul(z[e], v[e]);
      SBAR();
#pragma unroll
      for (int e = 0; e < 8; ++e) z[e] = (f32x2){EXP2(z[e][0]), EXP2(z[e][1])};
      SBAR();
      if (t == 0) { for (int e = 0; e < 8; ++e) PIN(z[e]); mid(); SBAR(); }
#pragma unroll
      for (int e = 0; e < 8; ++e) z[e] = fma(z[e], sd, sd);
      SBAR();
#pragma unroll
      for (int e = 0; e < 8; ++e) z[e] = (f32x2){__builtin_amdgcn_rcpf(z[e][0]), __builtin_amdgcn_rcpf(z[e][1])};
      SBAR();
#pragma unroll
      for (int e = 0; e < 8; ++e) z[e] = mul(z[e], v[e]);
      SBAR();
#pragma unroll
      for (int s = 0; s < 2; ++s) {
        const v4u f = {pk2(z[4 * s][0], z[4 * s][1]), pk2(z[4 * s + 1][0], z[4 * s + 1][1]), pk2(z[4 * s + 2][0], z[4 * s + 2][1]), pk2(z[4 * s + 3][0], z[4 * s + 3][1])};
        st16_sc1(f, sws, lane * 16, WS_AF + (T * AFT + (hcol0 >> 4) + 4 * P + 2 * t + s) * 1024);
      }
      SBAR();
    }
  };

  if (isG) {
    lane_setup();
    const v4u sw2 = make_srd(w2, FF * DM * 2), swo = make_srd(wo, DM * DM * 2);
    const unsigned vw2 = (lane >> 3) * 1024 + swz, vw2b = vw2 + 7 * 1024;
    const int ro3 = rt % 3;
    auto kmap = [&](int kk) {
      if (kk < 16) return 8 * (((kk >> 2) + rt) & 3) + (kk & 3);
      const int p = ((kk - 16) >> 2) + ro3;
      return 8 * (p >= 3 ? p - 3 : p) + 4 + (kk & 3);
    };
    auto omap = [&](int i) { return 2 * ((i + rt) & 3) + (i >> 2); };
    auto wdmah = [&](int kk, int half) {
      const unsigned so = (kk < 28 ? 448 * m + 16 * kmap(kk) : 128 * m + 16 * omap(kk - 28)) * 1024 + 128 * ct;
      if (half == 0) dma16_a(kk < 28 ? sw2 : swo, vw2, so, lds0 + L_WG + m * PART + (kk % RD) * 2048); else dma16_b(kk < 28 ? sw2 : swo, vw2b, so);
    };
    auto wdma = [&](int kk) { wdmah(kk, 0); wdmah(kk, 1); };
    {
      const v4u swq = make_srd(wqkv, DM * 3 * DM * 2);
      const unsigned vx0 = (lane >> 3) * 1024 + (((lane & 7) ^ g) * 16), vx1 = (7 + (lane >> 3)) * 1024 + (((lane & 7) ^ (4 + g)) * 16);
      const unsigned vwq = (lane >> 2) * (3 * DM * 2) + p4 * 16;
      auto piece = [&](int s, int i) {
        const int kc = (s + rot) & 7;
        if (i < 4) {
          const unsigned so = (128 * r + 32 * m + 16 * (i >> 1)) * 1024 + kc * 128;
          if (i & 1) dma16_b(sx, vx1, so); else dma16_a(sx, vx0, so, lds0 + xbuf(s) + m * 4096 + (i >> 1) * 2048);
        } else if (i < 7) dma16(swq, vwq, (64 * kc + 16 * m) * (3 * DM * 2) + (512 * (i - 4) + 32 * j) * 2, lds0 + qbuf(s) + ((i - 4) * 4 + m) * 1024);
        else w1dma(0, s, lds0 + w1buf(s), i - 7);
      };
#pragma unroll
      for (int i = 0; i < 18; ++i) { piece(i / 9, i % 9); if (i == 8) __builtin_amdgcn_s_barrier(); }
      __builtin_amdgcn_s_setprio(PRIO_G1);
      hsetup(0);
      f32x4 cs[2] = {};
      v4u xf[4], wf[4][2];
      hac[0] = (f32x16){}; hac[1] = (f32x16){};
      static_for<0, 8>([&](auto sc) {
        constexpr int s = sc.value;
        auto fill = [&](int i) {
          if constexpr (s < 6) piece(s + 2, i);
          if constexpr (s == 6) {
            if (i == 0) { stat_load(); bias_load(0); }
            else if (i < 5) { if (heavy) w1dma(1, (i - 1) >> 1, lds0 + L_WA + ((i - 1) >> 1) * 8192, (i - 1) & 1); }
            else wdmah((i - 5) >> 1, (i - 5) & 1);
          }
          if constexpr (s == 7) {
            if (heavy && i < 4) w1dma(1, 2 + (i >> 1), lds0 + L_WA + (2 + (i >> 1)) * 8192, i & 1);
            if (heavy && i >= 4) { w1dma(1, i, lds0 + L_WA + i * 8192, 0); w1dma(1, i, 0, 1); }
          }
          SBAR();
        };
        auto rd = [&](int p) {
          xf[p] = lds128(xbuf(s) + xo[p]);
          wf[p][0] = ldtr2(rk[p][0] + (s % 3) * PART, 512); wf[p][1] = ldtr2(rk[p][1] + (s % 3) * PART, 512);
          SBAR();
        };
        if constexpr (s < 7) wait_vm<9>(); else { if (heavy) wait_vm<11>(); else wait_vm<7>(); }
        bar();
        STAMP_AT(10 + s);
        rd(0);
        if constexpr (s < 7) fill(0);
        rd(1);
        if constexpr (s < 7) fill(1);
        if constexpr (s == 2) get_tag();
#pragma unroll
        for (int p = 0; p < 4; ++p) {
#pragma unroll
          for (int t = 0; t < 2; ++t) {
            hac[t] = MFMA32(wf[p][t], xf[p], hac[t]);
            if (p == 0) { cs[t] = MFMA16(sel, wf[p][t], cs[t]); PIN(cs[t]); }
            SBAR();
            if (p < 2 && t == 0) rd(p + 2);
            if (s == 7) fill(2 * p + t); else if (2 * p + t < 7) fill(2 + 2 * p + t);
          }
          if (s == 7 && p == 1) {
            cswr(0, cs[0]); cswr(1, cs[1]);
            bar();
            STAMP_AT(1);
          }
        }
        if constexpr (s == 7) {
          const f32x4 c0 = csrd(0), c1 = csrd(1);
          if (heavy) wait_vm<20>(); else wait_vm<4>();
          PIN(st); PIN(bias0); PIN(bias1);
          row_stats(); make_xaug();
          hac[0] = MFMA32(waug(c0, bias0), xaug, hac[0]);
          hac[1] = MFMA32(waug(c1, bias1), xaug, hac[1]);
          SBAR();
        }
      });
    }
    STAMP_AT(18);
    hgelu(std::integral_constant<int, 0>{}, [&]() { if (heavy) { wait_vm<0>(); arrive_nowait(lds0 + L_CNT + 16); } });
    __builtin_amdgcn_s_setprio(0);
    STAMP_AT(19);
    unsigned rb2[2];
#pragma unroll
    for (int c = 0; c < 2; ++c) rb2[c] = lds0 + L_WG + m * PART + h * 512 + q4 * 128 + ((c ^ (q4 >> 1)) * 64) + (g & 1) * 32 + p4 * 8;
    v4u wq[36][2];
    auto wrd = [&](int kk) {
#pragma unroll
      for (int c = 0; c < 2; ++c) wq[kk][c] = ldtr2(rb2[c] + (kk % RD) * 2048, 1024);
    };
    auto wgot = [&](int kk) { PIN(wq[kk][0]); PIN(wq[kk][1]); };
    const int ftile = 2 * rt + ((lane >> 2) & 1);
    const int T0 = 4 * r - 8 + 2 * m;
    const bool live = T0 >= 0;
    auto kv_fetch = [&]() {
      const unsigned src = WS_KV + (j * 64 + (live ? T0 : 0)) * 4096, vk = live ? lane * 16 : h * 512, vv = live ? lane * 16 : 0x40000000u;
#pragma unroll
      for (int i = 0; i < 8; ++i)
        dma16_sc1(sws, (i & 2) ? vv : vk, src + (live ? i : (i & 3)) * 1024, lds0 + L_KV + (2 * m + (i >> 2)) * KVT + (i & 3) * 1024 + ((i & 3) == 3 ? VB - 1024 : 0));
      const unsigned on = (lane < 2 || live) ? one2 : 0u;
      if (lane < 6) sts128(lds0 + L_KV + (lane < 2 ? 8 + m : 2 * m + ((lane - 2) >> 1)) * KVT + 2048 + (lane & 1) * VB + 1024, (v4u){on, on, on, on});
    };
    v4u af[28][2], of[8][2];
    v2u fl;
    const unsigned a0 = WS_AF + (2 * rt * AFT + 28 * m) * 1024, o0 = WS_AF + (2 * rt * AFT + 112 + 8 * m) * 1024;
    fl = ld8_sc1_async(sws, flag_off(FL_KV, live ? T0 + (lane & 1) : 0, j));
    for (int kk = 2; kk < 6; ++kk) wdma(kk);
    wait_vmf<8>(fl);
    if (lane == 0) __hip_atomic_store(flag(FL_H0, T, j), tag, RLX_AGENT);
    STAMP_AT(2);
    if (!__all(fl[0] == taglo && fl[1] == tag32)) poll_flags(flag(FL_KV, live ? T0 + (lane & 1) : 0, j), tag);
    if (heavy) await(lds0 + L_CNT + 20, 4);
    kv_fetch();
    wdma(6); wdma(7);
    static_for<0, 3>([&](auto ic) {
      constexpr int i = ic.value;
      wait_vm<(i == 0) ? 17 : 8>();
      for (int kk = 4 * i; kk < 4 * i + 4; ++kk) wrd(kk);
      for (int kk = 4 * i; kk < 4 * i + 4; ++kk) wgot(kk);
      if constexpr (i == 0) { wait_vm<4>(); arrive_nowait(lds0 + L_CNT); }
      if constexpr (i == 2) fl = ld8_sc1_async(sws, flag_off(FL_H0, ftile, 4 * m + p4));
      for (int kk = 4 * i; kk < 4 * i + 4; ++kk) wdma(kk + RD);
    });
    STAMP_AT(3);
    wait_vmf<8>(fl);
    if (!__all(fl[0] == taglo && fl[1] == tag32)) poll_flags(flag(FL_H0, ftile, 4 * m + p4), tag);
    STAMP_AT(4);
#pragma unroll
    for (int kk = 0; kk < AW; ++kk) {
      af[kk][0] = ld16_sc1_async(sws, lane * 16, a0 + kmap(kk) * 1024);
      af[kk][1] = ld16_sc1_async(sws, lane * 16, a0 + (AFT + kmap(kk)) * 1024);
    }
    __builtin_amdgcn_s_setprio(PRIO_G2);
    f32x16 c2[2][2] = {};
    auto mm2 = [&](int kk, const v4u (&a)[2], auto&& fill) {
      SBAR();
#pragma unroll
      for (int i = 0; i < 4; ++i) { c2[i >> 1][i & 1] = MFMA32(wq[kk][i & 1], a[i >> 1], c2[i >> 1][i & 1]); SBAR(); fill(i); SBAR(); }
    };
    static_for<0, 28>([&](auto kc) {
      constexpr int kk = kc.value;
      wait_vm2<(kk < AW) ? 2 * (AW - 1 - kk) + g2_range(0, kk) : g2_range(kk - AW + 1, kk)>(af[kk][0], af[kk][1]);
      if constexpr (kk == H1_CK) {
        STAMP_AT(20);
        wait_vmf<g2_range(H1_LD, H1_CK) - 1>(fl);
        if (!__all(fl[0] == taglo && fl[1] == tag32)) poll_flags(flag(FL_H1, ftile, 4 * m + p4), tag);
        STAMP_AT(21);
      }
      if constexpr (kk == H1_LD) fl = ld8_sc1_async(sws, flag_off(FL_H1, ftile, 4 * m + p4));
      if constexpr (kk == O_LD) fl = ld8_sc1_async(sws, flag_off(FL_O0, ftile, 4 * m + p4));
      if constexpr (kk == 26) fl = ld8_sc1_async(sws, flag_off(FL_O1, ftile, 4 * m + p4));
      constexpr bool dw = kk >= 1 && kk - 1 + NR + RD < 36;
      if constexpr (dw) wgot(kk - 1 + NR);
      mm2(kk, af[kk], [&](int i) {
        if constexpr (dw) { if (i < 2) wdmah(kk - 1 + NR + RD, i); }
        if constexpr (kk + NR < 28) { if (i == 1) wrd(kk + NR); }
        if constexpr (kk == 27) { if (i == 0) wrd(28); }
        if constexpr (kk + AW < 28) { if (i >= 2) af[kk + AW][i - 2] = ld16_sc1_async(sws, lane * 16, a0 + (AFT * (i - 2) + kmap(kk + AW)) * 1024); }
      });
      if constexpr (kk == O_CK) {
        wait_vmf<g2_range(O_LD, O_CK) - 1>(fl);
        if (!__all(fl[0] == taglo && fl[1] == tag32)) poll_flags(flag(FL_O0, ftile, 4 * m + p4), tag);
        res_loads();
#pragma unroll
        for (int i = 0; i < 4; ++i) {
          of[i][0] = ld16_sc1_async(sws, lane * 16, o0 + omap(i) * 1024);
          of[i][1] = ld16_sc1_async(sws, lane * 16, o0 + (AFT + omap(i)) * 1024);
        }
      }
    });
    static_for<0, 4>([&](auto kc) {
      constexpr int kk = kc.value;
      if (kk < 3) wrd(28 + kk + 1);
      if constexpr (kk == 1) {
        res_sum();
        STAMP_AT(5);
        wait_vmf<0>(fl);
        if (!__all(fl[0] == taglo && fl[1] == tag32)) poll_flags(flag(FL_O1, ftile, 4 * m + p4), tag);
        STAMP_AT(6);
#pragma unroll
        for (int i = 0; i < 8; ++i) of[4 + (i & 3)][i >> 2] = ld16_sc1_async(sws, lane * 16, o0 + (AFT * (i >> 2) + omap(4 + (i & 3))) * 1024);
      }
      wait_vm2<(kk == 0) ? 7 : 14 - 2 * kk>(of[kk][0], of[kk][1]);
      mm2(28 + kk, of[kk], [&](int i) { PIN(c2[i >> 1][i & 1]); if (kk == 3) wrd(32 + i); });
    });
    for (int kk = 32; kk < 36; ++kk) wgot(kk);
    unsigned pa = lds0 + L_WG + m * PART + lane * 16;
    PIN(pa);
    auto put = [&](int qn, int qd) {
      const f32x16& c = c2[qn >> 1][qn & 1];
      sts128(pa + (qn * 4 + qd) * 66 * 16, __builtin_bit_cast(v4u, (f32x4){c[4 * qd], c[4 * qd + 1], c[4 * qd + 2], c[4 * qd + 3]}));
    };
    static_for<0, 4>([&](auto qc) {
      constexpr int qn = qc.value;
      if constexpr ((qn & 1) == 0) { wait_vm2<qn ? 0 : 4>(of[4][qn >> 1], of[5][qn >> 1]); PIN(of[6][qn >> 1]); PIN(of[7][qn >> 1]); }
      SBAR();
#pragma unroll
      for (int i = 0; i < 4; ++i) {
        c2[qn >> 1][qn & 1] = MFMA32(wq[32 + i][qn & 1], of[4 + i][qn >> 1], c2[qn >> 1][qn & 1]);
        SBAR();
        if (qn > 0 && i >= 2) { put(qn - 1, 2 * i - 4); put(qn - 1, 2 * i - 3); }
        SBAR();
      }
    });
#pragma unroll
    for (int qd = 0; qd < 4; ++qd) put(3, qd);
    STAMP_AT(7);
  } else {
    __builtin_amdgcn_s_setprio(1);
    lane_setup();
    v4u xr[2];
    xr[0] = ld16_async(sx, lane * 16, (128 * r + 8 * j + 2 * m) * 1024); xr[1] = ld16_async(sx, lane * 16, (128 * r + 8 * j + 2 * m + 1) * 1024);
    unsigned warm;
    {
      u64 pc; asm volatile("s_getpc_b64 %0" : "=s"(pc));
      warm = ld2_async(make_srd((const void*)(pc & ~127ull), CODEWARM), lane < 3 ? lane * 128 : 0x40000000u, (idx * 4 + m) * 384);
    }
    __builtin_amdgcn_s_barrier();
    v2u ep = ld8_sc1_async(sws, m == 0 ? WS_EPOCH : 0x40000000u);
    float sval;
    const int wi = blockIdx.x * 4 + m;
    const u64 wbase = wi < 448 ? (u64)w1 + wi * 4096 : wi < 896 ? (u64)w2 + (wi - 448) * 4096 : (u64)wo + (wi - 896) * 4096;
    auto make_stats = [&]() {
      wait_vm2<1>(xr[0], xr[1]);
      PIN(warm);
      float piv[2], a[4];
#pragma unroll
      for (int u = 0; u < 2; ++u) {
        piv[u] = rdl(bflo(xr[u][0]), 0);
        float s1 = 0.f, s2 = 0.f;
#pragma unroll
        for (int i = 0; i < 8; ++i) { const float d = ((i & 1) ? bfhi(xr[u][i >> 1]) : bflo(xr[u][i >> 1])) - piv[u]; s1 += d; s2 = __builtin_fmaf(d, d, s2); }
        a[2 * u] = s1; a[2 * u + 1] = s2;
      }
#pragma unroll
      for (int i = 0; i < 4; ++i) a[i] += dpp<0xB1>(a[i]);
#pragma unroll
      for (int i = 0; i < 4; ++i) a[i] += dpp<0x4E>(a[i]);
      float t = p4 == 0 ? a[0] : p4 == 1 ? a[1] : p4 == 2 ? a[2] : a[3];
      t += dpp<0x114>(t); t += dpp<0x118>(t);
      const auto s16 = __builtin_amdgcn_permlane16_swap(asu(t), asu(t), false, false);
      t = asf(s16[0]) + asf(s16[1]);
      const auto s32 = __builtin_amdgcn_permlane32_swap(asu(t), asu(t), false, false);
      t = (asf(s32[0]) + asf(s32[1])) * (1.f / DM);
      const float e1 = dpp<0xA0>(t);
      sval = (p4 & 1) ? __builtin_fmaxf(__builtin_fmaf(-e1, e1, t), 0.f) + 1e-5f : t + (p4 ? piv[1] : piv[0]);
      PIN(sval);
    };
    const unsigned acnt = lds0 + L_CNT;
    const unsigned vpos = 2048 + (g & 1) * 512 + h * 256 + (lane & 15) * 16;
    v4u qf[2], am0, am1;
    u64 um[16];
    auto masks = [&]() {
      const int dq = r32 - 4 * h;
#pragma unroll
      for (int u = 0; u < 16; ++u) { um[u] = __builtin_amdgcn_ballot_w64((u & 3) + 8 * (u >> 2) > dq); pin_s(um[u]); }
#pragma unroll
      for (int d = 0; d < 4; ++d) {
        am0[d] = __builtin_amdgcn_inverse_ballot_w64(um[2 * d]) ? 0xffffffffu : __builtin_amdgcn_inverse_ballot_w64(um[2 * d + 1]) ? 0xffff0000u : 0u;
        am1[d] = __builtin_amdgcn_inverse_ballot_w64(um[8 + 2 * d]) ? 0xffffffffu : __builtin_amdgcn_inverse_ballot_w64(um[9 + 2 * d]) ? 0xffff0000u : 0u;
      }
      PIN(am0); PIN(am1);
    };
    {
      v4u xf[32];
      f32x16 acc[3] = {};
      f32x4 cs[3] = {};
      v4u wf[4][3];
      unsigned wb[4];
#pragma unroll
      for (int p = 0; p < 4; ++p) { wb[p] = qbuf(0) + ((p + m) & 3) * 1024 + (8 * h + q4) * 64 + (g & 1) * 32 + p4 * 8; PIN(wb[p]); }
      auto rd = [&](int ks, int t) {
        if (t == 0) xf[ks] = lds128(xbuf(ks >> 2) + xo[ks & 3]);
        wf[ks & 3][t] = ldtr2(wb[ks & 3] + ((ks >> 2) % 3) * 12288 + t * 4096, 256);
      };
      auto mm = [&](int ks, int t) {
        acc[t] = t == 2 ? MFMA32(xf[ks], wf[ks & 3][t], acc[t]) : MFMA32(wf[ks & 3][t], xf[ks], acc[t]);
        if ((ks & 3) == 0) { cs[t] = MFMA16(sel, wf[ks & 3][t], cs[t]); PIN(cs[t]); }
        SBAR();
      };
      make_stats();
      bar();
      STAMP_AT(10);
#pragma unroll
      for (int t = 0; t < 6; ++t) rd(t / 3, t % 3);
      SBAR();
      static_for<0, 8>([&](auto sc) {
        constexpr int s = sc.value;
#pragma unroll
        for (int p = 0; p < 2; ++p) {
#pragma unroll
          for (int t = 0; t < 3; ++t) { mm(4 * s + p, t); rd(4 * s + p + 2, t); SBAR(); }
          if (s == 7 && p == 0) { cswr(4, cs[0]); cswr(5, cs[1]); cswr(6, cs[2]); SBAR(); }
        }
        if constexpr (s == 0) {
          wait_vmf<0>(ep);
          if (m == 0 && lane < 10)
            *(LAS unsigned*)(size_t)(lds0 + L_CNT + lane * 4) = lane == 8 ? ep[0] * 0x9E3779B1u + 0x7F4A7C15u : lane == 9 ? ep[1] * 0x85EBCA77u + 0x165667B1u : 0u;
        }
        if constexpr (s == 1) {
          get_tag();
          if ((lane >> 2) == 3) {
            const v2u gr = {asu(sval), tag32};
            __builtin_amdgcn_raw_buffer_store_b64(gr, rws, WS_STATS + (128 * r + 8 * j + 2 * m) * 16 + p4 * 8, 0, 16);
          }
        }
        if constexpr (s == MIDWARM) {
          warm = ld2_async(make_srd((const void*)wbase, 4096), lane * 128, 0);
        }
        if constexpr (s == 5) stat_load();
        if constexpr (s == 6) {
          wait_vm1<0>(st);
          PIN(warm);

          row_stats();
        }
        mm(4 * s + 2, 0); mm(4 * s + 2, 1);
        bar();
        if constexpr (s < 7) {
          STAMP_AT(11 + s);
          mm(4 * s + 2, 2);
          rd(4 * s + 4, 0); rd(4 * s + 4, 1); rd(4 * s + 4, 2); SBAR();
#pragma unroll
          for (int t = 0; t < 3; ++t) { mm(4 * s + 3, t); rd(4 * s + 5, t); SBAR(); }
        }
      });
      STAMP_AT(1);
      __builtin_amdgcn_s_setprio(3);
      const f32x4 c5 = csrd(5), c6 = csrd(6), c4 = csrd(4);
      SBAR();
      mm(30, 2); mm(31, 1);
      make_xaug();
      SBAR();
      mm(31, 2); mm(31, 0);
      if (h == 0) *(LAS float*)(size_t)(lds0 + L_RS + (m * 32 + r32) * 4) = rstd;
      acc[1] = MFMA32(waug(c5, 0u), xaug, acc[1]);
      acc[2] = MFMA32(xaug, waug(c6, 0u), acc[2]);
      acc[0] = MFMA32(waug(c4, 0u), xaug, acc[0]);
      SBAR();
      f32x4 rs[4];
#pragma unroll
      for (int qd = 0; qd < 4; ++qd) rs[qd] = __builtin_bit_cast(f32x4, lds128(lds0 + L_RS + (m * 32 + 8 * qd + 4 * h) * 4));
      const unsigned kvo = WS_KV + (j * 64 + T) * 4096;
#pragma unroll
      for (int e = 0; e < 16; ++e) acc[1][e] = MUL1(acc[1][e], rstd);
#pragma unroll
      for (int e = 0; e < 2; ++e) {
        const v4u f = pack8(acc[1], e);
        st16_sc1(f, sws, lane * 16, kvo + e * 1024);
        sts128(lds0 + L_KV + (8 + m) * KVT + e * 1024 + lane * 16, f);
      }
#pragma unroll
      for (int qd = 0; qd < 4; ++qd)
#pragma unroll
        for (int e = 0; e < 4; ++e) acc[2][4 * qd + e] = MUL1(acc[2][4 * qd + e], rs[qd][e]);
#pragma unroll
      for (int s = 0; s < 2; ++s) {
        const v4u f = pack8(acc[2], s);
        st16_sc1(f, sws, vpos, kvo + s * 1024);
        sts128(lds0 + L_KV + (8 + m) * KVT + s * VB + vpos, f);
      }
      SBAR();
      const float qs = rstd * (0.25f * 1.4426950408889634f);
#pragma unroll
      for (int e = 0; e < 16; ++e) acc[0][e] = MUL1(acc[0][e], qs);
      qf[0] = pack8(acc[0], 0); qf[1] = pack8(acc[0], 1);
      PIN(qf[0]); PIN(qf[1]);
      arrive_nowait(acnt);
      STAMP_AT(2);
      if (heavy) {
        hsetup(1);
        await(acnt + 16, 4);
        STAMP_AT(3);
        hmm(std::integral_constant<int, 1>{}, xf, [&](auto kc) {
          constexpr int ks = kc.value;
          if constexpr (ks == KVPUB) {
            wait_vm<0>();
            if (lane == 0) __hip_atomic_store(flag(FL_KV, T, j), tag, RLX_AGENT);
          }
          if constexpr (ks == KVPUB + 2) bias_load(1);
          if constexpr (ks == 31) { wait_vm<0>(); PIN(bias0); PIN(bias1); }
        });
        STAMP_AT(18);
        hgelu(std::integral_constant<int, 1>{}, [&]() {});
        STAMP_AT(4);
      } else {
        wait_vm<0>();
        if (lane == 0) __hip_atomic_store(flag(FL_KV, T, j), tag, RLX_AGENT);
      }
    }
    masks();
    STAMP_AT(19);
    const unsigned kb = lds0 + L_KV + m * KVT + lane * 16;
    unsigned vb[2];
#pragma unroll
    for (int e = 0; e < 2; ++e) { vb[e] = lds0 + L_KV + m * KVT + 2048 + ((g & 1) ? 1024 : e * 512 + h * 256 + (lane & 15) * 16); PIN(vb[e]); }
    bool pend = true;
    if (__builtin_amdgcn_readfirstlane(peek(acnt)) < 8) {
      wait_vm<0>();
      if (lane == 0) __hip_atomic_store(flag(FL_H1, T, j), tag, RLX_AGENT);
      pend = false;
      await(acnt, 8);
    }
    asm volatile("" ::: "memory");
    __builtin_amdgcn_s_setprio(PRIO_ATT);
    STAMP_AT(5);
    const f32x16 zero = {};
    auto o_store = [&](f32x16& o, int e) {
      const float rl = __builtin_amdgcn_rcpf(o[8]);
#pragma unroll
      for (int u = 0; u < 8; ++u) o[u] = MUL1(o[u], rl);
      st16_sc1(pack8(o, 0), sws, lane * 16, WS_AF + (T * AFT + 112 + 2 * j + e) * 1024);
    };
    int redo = 0;
    {
      f32x16 o[2], sa, sb, su;
      v4u kn, ku, va, vc, vn0, vn1, vu0, vu1, p0, p1, pu;
      auto kaddr = [&](int n) { return kb + ((n & 7) + 1) * KVT + (n >> 3) * 1024; };
      auto vaddr = [&](int n) { return vb[n >> 3] + ((n & 7) + 1) * KVT; };
      kn = lds128(kaddr(0)); vn0 = lds128(vaddr(0)); vn1 = lds128(vaddr(0) + VB);
      sa = MFMA32(kn, qf[0], zero);
      kn = lds128(kaddr(1));
      SBAR();
      auto step = [&](auto nc, f32x16& sc, f32x16& sn) {
        constexpr int n = nc.value, e = n >> 3, i = n & 7;
        if constexpr (n < 15) sn = MFMA32(kn, qf[(n + 1) >> 3], zero);
        if constexpr (n < 14) kn = lds128(kaddr(n + 2));
        if constexpr (i == 5) ku = lds128(kb + e * 1024);
        v4u v0 = vn0, v1 = vn1;
        if constexpr (n < 15) { vn0 = lds128(vaddr(n + 1)); vn1 = lds128(vaddr(n + 1) + VB); }
        if constexpr (i == 6) { vu0 = lds128(vb[e]); vu1 = lds128(vb[e] + VB); }
        SBAR();
        if constexpr (i == 7) {
#pragma unroll
          for (int u = 0; u < 16; ++u) sc[u] = __builtin_amdgcn_inverse_ballot_w64(um[u]) ? su[u] : sc[u];
          PIN(sc); SBAR();
        }
#pragma unroll
        for (int u = 0; u < 6; ++u) sc[u] = EXP2(sc[u]);
        PIN(sc); SBAR();
        if constexpr (n == 8) { o[0] = MFMA32(vc, p1, o[0]); SBAR(); }
        else if constexpr (n > 0) { o[e] = MFMA32(vc, p1, o[e]); SBAR(); }
        sc[6] = EXP2(sc[6]); sc[7] = EXP2(sc[7]);
        p0 = pack8(sc, 0);
        sc[8] = EXP2(sc[8]); sc[9] = EXP2(sc[9]);
        if constexpr (i == 7) { va = p0 & am0; p0 &= ~am0; PIN(va); }
        PIN(sc); PIN(p0); SBAR();
        if constexpr (n == 8) { o[0] = MFMA32(vu1, pu, o[0]); SBAR(); }
        o[e] = MFMA32(v0, p0, i == 0 ? zero : o[e]);
        SBAR();
        if constexpr (i == 6) { su = MFMA32(ku, qf[e], zero); SBAR(); }
#pragma unroll
        for (int u = 10; u < 13; ++u) sc[u] = EXP2(sc[u]);
        if constexpr (i == 7) { PIN(sc); SBAR(); o[e] = MFMA32(vu0, va, o[e]); SBAR(); }
#pragma unroll
        for (int u = 13; u < 16; ++u) sc[u] = EXP2(sc[u]);
        p1 = pack8(sc, 1); vc = v1;
        if constexpr (i == 7) { pu = p1 & am1; p1 &= ~am1; PIN(pu); }
        PIN(p1); SBAR();
        if constexpr (n == 8) {
          if (__all(o[0][8] >= 0x1p-30f && o[0][8] <= 0x1p30f)) o_store(o[0], 0); else redo = 1;
          SBAR();
        }
        if constexpr (n == 10) pend = !redo;
        if constexpr (i == 3 || i == 5 || i == 7) {
          if (pend && (i == 7 || (__builtin_amdgcn_s_getreg(7 | (3 << 11)) & 15) == 0)) {
            wait_vm<0>();
            if (lane == 0) __hip_atomic_store(flag(FL_H1 + e, T, j), tag, RLX_AGENT);
            pend = false;
          }
        }
      };
      static_for<0, 8>([&](auto hc) {
        step(std::integral_constant<int, 2 * hc.value>{}, sa, sb);
        step(std::integral_constant<int, 2 * hc.value + 1>{}, sb, sa);
      });
      o[1] = MFMA32(vc, p1, o[1]);
      o[1] = MFMA32(vu1, pu, o[1]);
      if (__all(o[1][8] >= 0x1p-30f && o[1][8] <= 0x1p30f)) o_store(o[1], 1); else redo |= 2;
    }
    if (redo) {
#pragma unroll 1
      for (int e = 0; e < 2; ++e) {
        if (!((redo >> e) & 1)) continue;
        const v4u q = e ? qf[1] : qf[0];
        const unsigned vbe = e ? vb[1] : vb[0];
        auto merged = [&](const f32x16& c) {
          f32x16 sl = MFMA32(lds128(kb + 8 * KVT + e * 1024), q, c);
          const f32x16 su = MFMA32(lds128(kb + e * 1024), q, c);
          static_for<0, 16>([&](auto u) { sl[u.value] = upper<u.value>() ? su[u.value] : sl[u.value]; });
          return sl;
        };
        float mx = -INFINITY;
#pragma unroll 1
        for (int i = 0; i < 8; ++i) {
          const f32x16 sc = i ? MFMA32(lds128(kb + i * KVT + e * 1024), q, zero) : merged(zero);
#pragma unroll
          for (int u = 0; u < 16; u += 2) mx = max3(mx, sc[u], sc[u + 1]);
        }
        const auto sw = __builtin_amdgcn_permlane32_swap(asu(mx), asu(mx), false, false);
        mx = __builtin_fmaxf(asf(sw[0]), asf(sw[1]));
        f32x16 negm, o;
#pragma unroll
        for (int u = 0; u < 16; ++u) negm[u] = -mx;
        {
          f32x16 sc = merged(negm);
#pragma unroll
          for (int u = 0; u < 16; ++u) sc[u] = EXP2(sc[u]);
          const v4u p0 = pack8(sc, 0), p1 = pack8(sc, 1);
          o = MFMA32(lds128(vbe), p0 & am0, zero);
          o = MFMA32(lds128(vbe + VB), p1 & am1, o);
          o = MFMA32(lds128(vbe + 8 * KVT), p0 & ~am0, o);
          o = MFMA32(lds128(vbe + 8 * KVT + VB), p1 & ~am1, o);
        }
#pragma unroll 1
        for (int i = 1; i < 8; ++i) {
          f32x16 sc = MFMA32(lds128(kb + i * KVT + e * 1024), q, negm);
#pragma unroll
          for (int u = 0; u < 16; ++u) sc[u] = EXP2(sc[u]);
          o = MFMA32(lds128(vbe + i * KVT), pack8(sc, 0), o);
          o = MFMA32(lds128(vbe + i * KVT + VB), pack8(sc, 1), o);
        }
        o_store(o, e);
      }
      wait_vm<0>();
      if (lane == 0) __hip_atomic_store(flag(FL_O0, T, j), tag, RLX_AGENT);
    }
    STAMP_AT(6);
    wait_vm<0>();
    if (lane == 0) __hip_atomic_store(flag(FL_O1, T, j), tag, RLX_AGENT);
    STAMP_AT(7);
    res_loads();
    wait_vm<0>();
    res_sum();
    if (blockIdx.x == 0 && wave == 7) {
#pragma unroll 1
      for (int i = 0; i < 4; ++i) poll_flags(flag(FL_KV, 4 * (4 * i + g), lane & 15), tag);
      if (lane == 0) __hip_atomic_store((GAS u64*)(ws + WS_EPOCH), tag, RLX_AGENT);
    }
  }
  {
    int t2 = threadIdx.x; PIN(t2);
    const int row = t2 >> 3, ch = t2 & 7;
    unsigned a = lds0 + L_WG + (((row >> 5) * 8 + ch) * 66 + (row & 31)) * 16, oo = (64 * rt + row) * 1024 + (64 * ct + ch * 8) * 2;
    PIN(a); PIN(oo);
    bar();
    f32x4 pl[4], ph[4];
#pragma unroll
    for (int w = 0; w < 4; ++w) { pl[w] = __builtin_bit_cast(f32x4, lds128(a + w * PART)); ph[w] = __builtin_bit_cast(f32x4, lds128(a + w * PART + 512)); }
    SBAR();
#pragma unroll
    for (int w = 0; w < 4; ++w) { lo += pl[w]; hi += ph[w]; }
    const v4u o = {pk2(lo[0], lo[1]), pk2(lo[2], lo[3]), pk2(hi[0], hi[1]), pk2(hi[2], hi[3])};
    __builtin_amdgcn_raw_buffer_store_b128(o, __builtin_amdgcn_make_buffer_rsrc(out, 0, SEQ * DM * 2, 0x00020000), oo, 0, 16);
  }
  STAMP_AT(8);
}

extern "C" void kernel_launch(void* const* d_in, const int* in_sizes, int n_in, void* d_out, int out_size, void* d_ws, size_t ws_size,
                              hipStream_t stream) {
  (void)in_sizes; (void)n_in; (void)out_size; (void)ws_size;
  (void)hipFuncSetAttribute((const void*)fused_block, hipFuncAttributeMaxDynamicSharedMemorySize, LDS_BYTES);
  fused_block<<<256, 512, LDS_BYTES, stream>>>(d_in[0], d_in[1], d_in[2], d_in[4], d_in[5], d_in[6], d_in[7], d_out, (char*)d_ws);
}
```

```cpp
#include <hip/hip_runtime.h>
#include <hip/hip_bf16.h>
#include <stdint.h>
#include <type_traits>

typedef unsigned v4u __attribute__((ext_vector_type(4)));
typedef unsigned v2u __attribute__((ext_vector_type(2)));
typedef float f32x2 __attribute__((ext_vector_type(2)));
typedef float f32x4 __attribute__((ext_vector_type(4)));
typedef float f32x16 __attribute__((ext_vector_type(16)));
typedef short bf16x8 __attribute__((ext_vector_type(8)));
typedef short s16x4 __attribute__((ext_vector_type(4)));
typedef __bf16 bf16x2_t __attribute__((ext_vector_type(2)));
typedef unsigned long long u64;
#define GAS __attribute__((address_space(1)))
#define LAS __attribute__((address_space(3)))
#define DI __device__ __forceinline__
#define RLX_AGENT __ATOMIC_RELAXED, __HIP_MEMORY_SCOPE_AGENT

constexpr int SEQ = 2048, DM = 512, FF = 1792;
constexpr unsigned WS_EPOCH = 0;
constexpr unsigned WS_STATS = 32768;
constexpr unsigned WS_FLAG = 65536;
constexpr unsigned WS_KV = WS_FLAG + 5 * 1024 * 128;
constexpr int AFT = 145;
constexpr unsigned WS_AF = WS_KV + 16 * 64 * 4096;
constexpr unsigned WS_END = WS_AF + 64 * AFT * 1024;
constexpr unsigned WS_STAMP = WS_END;
enum { FL_KV = 0, FL_H0 = 1, FL_H1 = 2, FL_O0 = 3, FL_O1 = 4 };
constexpr int L_WA = 0;
constexpr int L_WG = 86016;
constexpr int VB = 1040;
constexpr int KVT = 2048 + 2 * VB;
constexpr int L_KV = 32768;
constexpr int RD = 8, NR = 12;
constexpr int PART = 16 * 66 * 16;
constexpr int L_CS = L_WG + 4 * PART;
constexpr int L_RS = L_CS + 3584;
constexpr int L_CNT = L_RS + 512;
constexpr int LDS_BYTES = L_CNT + 64;
constexpr int xbuf(int s) { return ((s + 1) % 3) * 16384; }
constexpr int qbuf(int s) { return 49152 + (s % 3) * 12288; }
constexpr int w1buf(int s) { return L_WG + (s % 3) * PART + 8192; }
static_assert(L_KV + 8 * KVT >= 65536 && xbuf(7) == 32768 && qbuf(2) + 12288 <= L_WG && L_KV + 12 * KVT <= L_WG && PART >= RD * 2048 && LDS_BYTES <= 163840, "lds map");
#define AWIN 6
constexpr int AW = AWIN;
constexpr unsigned SPIN_CAP = 1u << 23;

#define MFMA32(a, b, c) __builtin_amdgcn_mfma_f32_32x32x16_bf16(__builtin_bit_cast(bf16x8, a), __builtin_bit_cast(bf16x8, b), c, 0, 0, 0)
#define MFMA16(a, b, c) __builtin_amdgcn_mfma_f32_16x16x32_bf16(__builtin_bit_cast(bf16x8, a), __builtin_bit_cast(bf16x8, b), c, 0, 0, 0)
#define SBAR() __builtin_amdgcn_sched_barrier(0)

#define STAMP_AT(i) do { } while (0)

DI float asf(unsigned u) { return __builtin_bit_cast(float, u); }
DI unsigned asu(float f) { return __builtin_bit_cast(unsigned, f); }
DI unsigned pk2(float lo, float hi) { return __builtin_bit_cast(unsigned, __builtin_convertvector((f32x2){lo, hi}, bf16x2_t)); }
DI float bflo(unsigned u) { return __builtin_bit_cast(float, u << 16); }
DI float bfhi(unsigned u) { return __builtin_bit_cast(float, u & 0xffff0000u); }
DI v4u make_srd(const void* p, unsigned bytes) { const u64 a = (u64)p; return (v4u){(unsigned)a, (unsigned)(a >> 32) & 0xffffu, bytes, 0x00020000u}; }
DI void dma16(v4u srd, unsigned voff, unsigned soff, unsigned lds) {
  asm volatile("s_mov_b32 m0, %3\n\ts_nop 0\n\tbuffer_load_dwordx4 %0, %1, %2 offen lds" :: "v"(voff), "s"(srd), "s"(soff), "s"(lds) : "memory");
}
DI void dma16_sc1(v4u srd, unsigned voff, unsigned soff, unsigned lds) {
  asm volatile("s_mov_b32 m0, %3\n\ts_nop 0\n\tbuffer_load_dwordx4 %0, %1, %2 offen sc1 lds" :: "v"(voff), "s"(srd), "s"(soff), "s"(lds) : "memory");
}
DI void dma16_a(v4u srd, unsigned voff, unsigned soff, unsigned lds) { dma16(srd, voff, soff, lds); }
DI void dma16_b(v4u srd, unsigned voff1, unsigned soff) {
  asm volatile("buffer_load_dwordx4 %0, %1, %2 offen offset:1024 lds" :: "v"(voff1), "s"(srd), "s"(soff) : "memory");
}
DI v4u ld16_sc1_async(v4u srd, unsigned voff, unsigned soff) {
  v4u r; asm volatile("buffer_load_dwordx4 %0, %1, %2, %3 offen sc1" : "=v"(r) : "v"(voff), "s"(srd), "s"(soff) : "memory"); return r;
}
DI v4u ld16_async(v4u srd, unsigned voff, unsigned soff) {
  v4u r; asm volatile("buffer_load_dwordx4 %0, %1, %2, %3 offen" : "=v"(r) : "v"(voff), "s"(srd), "s"(soff) : "memory"); return r;
}
DI unsigned ld2_async(v4u srd, unsigned voff, unsigned soff) {
  unsigned r; asm volatile("buffer_load_ushort %0, %1, %2, %3 offen" : "=v"(r) : "v"(voff), "s"(srd), "s"(soff) : "memory"); return r;
}
DI void st16_sc1(v4u v, v4u srd, unsigned voff, unsigned soff) {
  asm volatile("buffer_store_dwordx4 %0, %1, %2, %3 offen sc1\n\ts_nop 1" :: "v"(v), "v"(voff), "s"(srd), "s"(soff) : "memory");
}
template <int N> DI void wait_vm1(v4u& a) { asm volatile("s_waitcnt vmcnt(%1)" : "+v"(a) : "i"(N) : "memory"); }
template <int N> DI void wait_vm2(v4u& a, v4u& b) { asm volatile("s_waitcnt vmcnt(%2)" : "+v"(a), "+v"(b) : "i"(N) : "memory"); }
template <int N> DI void wait_vm3(v4u& a, v4u& b, v4u& c) { asm volatile("s_waitcnt vmcnt(%3)" : "+v"(a), "+v"(b), "+v"(c) : "i"(N) : "memory"); }
DI void pin_s(u64& v) { asm volatile("" : "+s"(v)); }
template <class V> DI void pin(V& v) { asm volatile("" : "+v"(v)); }
template <int N> DI void wait_vm() { asm volatile("s_waitcnt vmcnt(%0)" :: "i"(N) : "memory"); }
DI void bar() { asm volatile("s_waitcnt lgkmcnt(0)\n\ts_barrier" ::: "memory"); }
DI v4u lds128(unsigned a) { return *(const LAS v4u*)(size_t)a; }
DI void sts128(unsigned a, v4u v) { *(LAS v4u*)(size_t)a = v; }
DI v2u ldtr(unsigned a) { return __builtin_bit_cast(v2u, __builtin_amdgcn_ds_read_tr16_b64_v4i16((LAS s16x4*)(size_t)a)); }
DI v4u ldtr2(unsigned a, int second) { const v2u lo = ldtr(a), hi = ldtr(a + second); return (v4u){lo.x, lo.y, hi.x, hi.y}; }
template <int CTRL> DI float dpp(float v) { return __builtin_bit_cast(float, __builtin_amdgcn_update_dpp(0, __builtin_bit_cast(int, v), CTRL, 0xf, 0xf, true)); }
DI float rdl(float v, int l) { return __builtin_bit_cast(float, __builtin_amdgcn_readlane(__builtin_bit_cast(int, v), l)); }
DI v4u pack8(const f32x16& a, int s) { return (v4u){pk2(a[8 * s], a[8 * s + 1]), pk2(a[8 * s + 2], a[8 * s + 3]), pk2(a[8 * s + 4], a[8 * s + 5]), pk2(a[8 * s + 6], a[8 * s + 7])}; }
DI void poll_flags(const GAS u64* p, u64 tag) {
#pragma unroll 1
  for (unsigned spin = 0; spin < SPIN_CAP; ++spin) {
    const u64 v = __hip_atomic_load(p, RLX_AGENT);
    if (__all(v == tag)) break;
    __builtin_amdgcn_s_sleep(1);
  }
  asm volatile("" ::: "memory");
}
constexpr u64 upper_mask(int reg) {
  u64 m = 0;
  for (int l = 0; l < 64; ++l) { const int row = (reg & 3) + 8 * (reg >> 2) + 4 * (l >> 5); if (row > (l & 31)) m |= 1ull << l; }
  return m;
}
template <int REG> DI bool upper() { return __builtin_amdgcn_inverse_ballot_w64(upper_mask(REG)); }
template <int I, int N, class Fn> DI void static_for(Fn&& f) { if constexpr (I < N) { f(std::integral_constant<int, I>{}); static_for<I + 1, N>(f); } }
DI float max3(float a, float b, float c) { return __builtin_fmaxf(__builtin_fmaxf(a, b), c); }

constexpr int H1_CK = 16 - AW, H1_LD = H1_CK - 3, O_CK = 28 - AW, O_LD = O_CK - 3;
constexpr int g2_issued(int t) { return ((t >= 1 && t - 1 + NR + RD < 36) ? 2 : 0) + ((t + AW < 28) ? 2 : 0) + (t == H1_LD) + (t == O_LD) + (t == O_CK ? 10 : 0) + (t == 26); }
constexpr int g2_range(int a, int b) { int n = 0; for (int t = a; t < b; ++t) n += g2_issued(t); return n; }
#define GELU_PK(P) (P == 1)
#define PRIO_G2 2
#define PRIO_ATT 3
#define PRIO_G1 2
#define ROT r
#define CODEWARM 38000
#define MIDWARM 1
#define KVPUB 2
#define EXP2(v) __builtin_amdgcn_exp2f(v)
#define PIN(v) pin(v)
extern "C" __device__ float mul_unpacked(float, float) __asm("llvm.amdgcn.fmul.legacy");
#define MUL1(a, b) mul_unpacked(a, b)
#define FMA1(a, b, c) __builtin_amdgcn_div_fmasf(a, b, c, false)
DI v2u ld8_sc1_async(v4u srd, unsigned voff) {
  v2u r; asm volatile("buffer_load_dwordx2 %0, %1, %2, 0 offen sc1" : "=v"(r) : "v"(voff), "s"(srd) : "memory"); return r;
}
template <int N> DI void wait_vmf(v2u& a) { asm volatile("s_waitcnt vmcnt(%1)" : "+v"(a) : "i"(N) : "memory"); }
DI void arrive(unsigned cnt, int lane) {
  asm volatile("s_waitcnt lgkmcnt(0)" ::: "memory");
  if (lane == 0) __hip_atomic_fetch_add((LAS unsigned*)(size_t)cnt, 1u, __ATOMIC_RELAXED, __HIP_MEMORY_SCOPE_WORKGROUP);
}
DI void arrive_nowait(unsigned cnt) {
  asm volatile("s_mov_b64 exec, 1\n\tds_add_u32 %0, %1\n\ts_mov_b64 exec, -1" :: "v"(cnt), "v"(1u) : "memory");
}
DI unsigned peek(unsigned cnt) { return __hip_atomic_load((LAS unsigned*)(size_t)cnt, __ATOMIC_RELAXED, __HIP_MEMORY_SCOPE_WORKGROUP); }
DI void await(unsigned cnt, unsigned n) {
#pragma unroll 1
  for (unsigned spin = 0; spin < SPIN_CAP * 32; ++spin) {
    if (__builtin_amdgcn_readfirstlane(peek(cnt)) >= n) break;
    __builtin_amdgcn_s_sleep(1);
  }
  asm volatile("" ::: "memory");
}

__global__ __launch_bounds__(512, 2) void fused_block(const void* __restrict__ x, const void* __restrict__ wqkv, const void* __restrict__ wo,
                                                      const void* __restrict__ w1, const void* __restrict__ b1, const void* __restrict__ w2,
                                                      const void* __restrict__ b2, void* __restrict__ out, char* __restrict__ ws) {
  constexpr unsigned lds0 = 0;
  const int wave = __builtin_amdgcn_readfirstlane(threadIdx.x >> 6);
  const int m = wave & 3;
  const bool isG = wave < 4;
  const int xcd = blockIdx.x & 7, idx = blockIdx.x >> 3;
  const int r = (xcd >> 2) * 8 + (idx >> 2), j = (xcd & 3) * 4 + (idx & 3);
  const int rt = (xcd >> 1) * 8 + (idx >> 2), ct = (xcd & 1) * 4 + (idx & 3);
  int lane = threadIdx.x & 63, r32, h, g, q4, p4;
  STAMP_AT(0);
  asm volatile("" :: "s"(x), "s"(wqkv), "s"(wo), "s"(w1), "s"(b1), "s"(w2), "s"(b2), "s"(out), "s"(ws));

  const v4u sx = make_srd(x, SEQ * DM * 2), sws = make_srd(ws, WS_END);
  const __amdgpu_buffer_rsrc_t rws = __builtin_amdgcn_make_buffer_rsrc(ws, 0, WS_END, 0x00020000);
  const unsigned one2 = 0x3f803f80u;
  const int T = 4 * r + m;
  const int rot = ROT;
  auto flag_off = [&](int kind, int tile, int jj) { return WS_FLAG + ((kind * 64 + tile) * 16 + jj) * 128; };
  auto flag = [&](int kind, int tile, int jj) { return (GAS u64*)(ws + flag_off(kind, tile, jj)); };
  u64 tag;
  unsigned taglo, tag32;
  auto get_tag = [&]() {
    const v2u tg = *(const LAS v2u*)(size_t)(lds0 + L_CNT + 32);
    taglo = __builtin_amdgcn_readfirstlane(tg[0]); tag32 = __builtin_amdgcn_readfirstlane(tg[1]);
    tag = ((u64)tag32 << 32) | taglo;
  };
  v4u xres, bres, xaug, st;
  float mean, var, rstd, sd;
  v4u sel;
  unsigned xo[4], swz;
  auto lane_setup = [&]() {
    lane = threadIdx.x & 63; PIN(lane);
    r32 = lane & 31; h = lane >> 5; g = lane >> 4; q4 = (lane >> 2) & 3; p4 = lane & 3;
    const unsigned selv = ((lane & 15) == (g & 1)) ? one2 : 0u;
    sel = (v4u){selv, selv, selv, selv};
#pragma unroll
    for (int p = 0; p < 4; ++p) xo[p] = (32 * m + r32) * 128 + (((2 * ((p + m) & 3) + h) ^ ((r32 >> 1) & 7)) * 16);
    swz = ((((lane >> 2) & 1) ^ (g & 1)) * 64) + p4 * 16;
  };

  auto stat_load = [&]() { st = ld16_sc1_async(sws, WS_STATS + (128 * r + 32 * m + r32) * 16, 0); };
  auto res_loads = [&]() {
    int t2 = threadIdx.x; PIN(t2);
    const int row = 64 * rt + (t2 >> 3), col = 64 * ct + (t2 & 7) * 8;
    xres = ld16_async(sx, row * 1024 + col * 2, 0);
    bres = ld16_async(make_srd(b2, DM * 2), col * 2, 0);
  };
  f32x4 lo, hi;
  auto res_sum = [&]() {
    PIN(xres); PIN(bres);
    lo = (f32x4){bflo(xres[0]) + bflo(bres[0]), bfhi(xres[0]) + bfhi(bres[0]), bflo(xres[1]) + bflo(bres[1]), bfhi(xres[1]) + bfhi(bres[1])};
    hi = (f32x4){bflo(xres[2]) + bflo(bres[2]), bfhi(xres[2]) + bfhi(bres[2]), bflo(xres[3]) + bflo(bres[3]), bfhi(xres[3]) + bfhi(bres[3])};
    PIN(lo); PIN(hi);
  };
  auto row_stats = [&]() {
    if (!__all(st[1] == tag32 && st[3] == tag32)) {
#pragma unroll 1
      for (unsigned spin = 0; spin < SPIN_CAP; ++spin) {
        st = ld16_sc1_async(sws, WS_STATS + (128 * r + 32 * m + r32) * 16, 0);
        wait_vm1<0>(st);
        if (__all(st[1] == tag32 && st[3] == tag32)) break;
      }
    }
    mean = asf(st[0]); var = asf(st[2]);
    PIN(mean); PIN(var);
  };
  auto make_xaug = [&]() {
    PIN(mean); PIN(var);
    rstd = __builtin_amdgcn_rsqf(var); sd = var * rstd;
    const unsigned uh = pk2(-mean, sd);
    const unsigned ul = pk2(-mean - bflo(uh), FMA1(bfhi(uh), -1.f, sd));
    xaug = (v4u){(uh & 0xffffu) | (uh << 16), (ul & 0xffffu) | (uh & 0xffff0000u), ul >> 16, 0u};
    if (h) xaug = (v4u){0u, 0u, 0u, 0u};
    PIN(xaug);
  };
  auto cswr = [&](int tile, f32x4 c) {
    if (lane < 16) { LAS float* p = (LAS float*)(size_t)(lds0 + L_CS + (tile * 32 + lane) * 16 + m * 4); p[0] = c[0]; p[64] = c[1]; }
  };
  auto csrd = [&](int tile) { return __builtin_bit_cast(f32x4, lds128(lds0 + L_CS + (tile * 32 + r32) * 16)); };
  auto waug = [&](f32x4 c4, unsigned b16) {
    const float c = ((c4[0] + c4[1]) + c4[2]) + c4[3];
    const unsigned uh = pk2(c, 0.f);
    const unsigned ul = pk2(c - bflo(uh), 0.f);
    return (v4u){uh | (ul << 16), uh | (b16 << 16), b16, 0u};
  };

  const v4u sw1 = make_srd(w1, DM * FF * 2);
  const int hcol0 = 448 * (j >> 2) + 128 * (j & 3);
  const bool heavy = (j & 3) != 3;
  unsigned bias0, bias1;
  auto bias_load = [&](int P) {
    bias0 = ld2_async(make_srd(b1, FF * 2), r32 * 2, (hcol0 + 64 * P) * 2);
    bias1 = ld2_async(make_srd(b1, FF * 2), r32 * 2, (hcol0 + 64 * P + 32) * 2);
  };
  auto w1dma = [&](int P, int c, unsigned dst, int half) {
    const unsigned vw1 = (lane >> 3) * (FF * 2) + swz, vw1b = vw1 + 8 * (FF * 2) - 1024;
    const unsigned so = (64 * ((c + rot) & 7) + 16 * m) * (FF * 2) + (hcol0 + 64 * P) * 2;
    if (half == 0) dma16_a(sw1, vw1, so, dst + m * 2048); else dma16_b(sw1, vw1b, so);
  };
  f32x16 hac[2];
  unsigned rk[4][2];
  auto hsetup = [&](int P) {
#pragma unroll
    for (int p = 0; p < 4; ++p)
#pragma unroll
      for (int c = 0; c < 2; ++c) {
        rk[p][c] = lds0 + (P ? L_WA : w1buf(0)) + ((p + m) & 3) * 2048 + h * 1024 + q4 * 128 + ((c ^ (q4 >> 1)) * 64) + (g & 1) * 32 + p4 * 8;
        PIN(rk[p][c]);
      }
  };
  auto hmm = [&](auto pc, const v4u (&xf)[32], auto&& hook) {
    constexpr int P = pc.value;
    constexpr int D = 4;
    const unsigned cnt = lds0 + L_CNT + 4;
    f32x4 cs[2] = {}, c0, c1;
    v4u wf[D][2], au0, au1;
    unsigned seen = 0;
    hac[0] = (f32x16){}; hac[1] = (f32x16){};
    auto rd = [&](int ks, int t) { wf[ks % D][t] = ldtr2(rk[ks & 3][t] + ((ks >> 2) ^ 4) * 8192, 512); };
#pragma unroll
    for (int ks = 0; ks < D; ++ks) { rd(ks, 0); rd(ks, 1); }
    SBAR();
    static_for<0, 32>([&](auto kc) {
      constexpr int ks = kc.value;
      hook(kc);
      if constexpr (ks == 12) arrive_nowait(cnt + 16);
      if constexpr (ks == 29) seen = peek(cnt);
#pragma unroll
      for (int t = 0; t < 2; ++t) {
        hac[t] = MFMA32(wf[ks % D][t], xf[ks ^ 16], hac[t]);
        if ((ks & 3) == 0 && ks < 28) { cs[t] = MFMA16(sel, wf[ks % D][t], cs[t]); PIN(cs[t]); }
        if (ks == 26) { cs[t] = MFMA16(sel, wf[28 % D][t], cs[t]); PIN(cs[t]); }
        SBAR();
        if constexpr (ks + D < 32) rd(ks + D, t);
        if constexpr (ks == 31) { if (t == 0) { au0 = waug(c0, bias0); PIN(au0); } else { au1 = waug(c1, bias1); PIN(au1); } }
        SBAR();
      }
      if constexpr (ks == 30) {
        PIN(hac[0]); PIN(hac[1]);
        if (__builtin_amdgcn_readfirstlane(seen) < 4) await(cnt, 4);
        asm volatile("" ::: "memory");
        if constexpr (ks == 30) { c0 = csrd(2 * P); c1 = csrd(2 * P + 1); }
      }
      if constexpr (ks == 27) { cswr(2 * P, cs[0]); cswr(2 * P + 1, cs[1]); arrive_nowait(cnt); }
      SBAR();
    });
    hac[0] = MFMA32(au0, xaug, hac[0]);
    hac[1] = MFMA32(au1, xaug, hac[1]);
    SBAR();
  };
  auto hgelu = [&](auto pc, auto&& mid) {
    constexpr int P = pc.value;
    constexpr bool PK = GELU_PK(P);
    const float C0 = 2.3022081986f, C1 = C0 * 0.044715f;
    const float ga = -C1 * rstd * rstd * rstd, gb = -C0 * rstd;
    auto mul = [&](f32x2 a, f32x2 b) { return PK ? a * b : (f32x2){MUL1(a[0], b[0]), MUL1(a[1], b[1])}; };
    auto fma = [&](f32x2 a, float b, float c) {
      return PK ? __builtin_elementwise_fma(a, (f32x2){b, b}, (f32x2){c, c}) : (f32x2){FMA1(a[0], b, c), FMA1(a[1], b, c)}; };
#pragma unroll
    for (int t = 0; t < 2; ++t) {
      f32x2 v[8], z[8];
#pragma unroll
      for (int e = 0; e < 8; ++e) { v[e] = (f32x2){hac[t][2 * e], hac[t][2 * e + 1]}; z[e] = mul(v[e], v[e]); }
      SBAR();
#pragma unroll
      for (int e = 0; e < 8; ++e) z[e] = fma(z[e], ga, gb);
      SBAR();
#pragma unroll
      for (int e = 0; e < 8; ++e) z[e] = mul(z[e], v[e]);
      SBAR();
#pragma unroll
      for (int e = 0; e < 8; ++e) z[e] = (f32x2){EXP2(z[e][0]), EXP2(z[e][1])};
      SBAR();
#pragma unroll
      for (int e = 0; e < 8; ++e) z[e] = fma(z[e], sd, sd);
      SBAR();
#pragma unroll
      for (int e = 0; e < 8; ++e) z[e] = (f32x2){__builtin_amdgcn_rcpf(z[e][0]), __builtin_amdgcn_rcpf(z[e][1])};
      SBAR();
#pragma unroll
      for (int e = 0; e < 8; ++e) z[e] = mul(z[e], v[e]);
      SBAR();
#pragma unroll
      for (int s = 0; s < 2; ++s) {
        const v4u f = {pk2(z[4 * s][0], z[4 * s][1]), pk2(z[4 * s + 1][0], z[4 * s + 1][1]), pk2(z[4 * s + 2][0], z[4 * s + 2][1]), pk2(z[4 * s + 3][0], z[4 * s + 3][1])};
        st16_sc1(f, sws, lane * 16, WS_AF + (T * AFT + (hcol0 >> 4) + 4 * P + 2 * t + s) * 1024);
      }
      if (t == 0) mid();
      SBAR();
    }
  };

  if (isG) {
    lane_setup();
    const v4u sw2 = make_srd(w2, FF * DM * 2), swo = make_srd(wo, DM * DM * 2);
    const unsigned vw2 = (lane >> 3) * 1024 + swz, vw2b = vw2 + 7 * 1024;
    const int ro = rt + ct, ro3 = ro % 3;
    auto kmap = [&](int kk) {
      if (kk < 16) return 8 * (((kk >> 2) + ro) & 3) + (kk & 3);
      const int p = ((kk - 16) >> 2) + ro3;
      return 8 * (p >= 3 ? p - 3 : p) + 4 + (kk & 3);
    };
    auto omap = [&](int i) { return 2 * ((i + ro) & 3) + (i >> 2); };
    auto wdmah = [&](int kk, int half) {
      const unsigned so = (kk < 28 ? 448 * m + 16 * kmap(kk) : 128 * m + 16 * omap(kk - 28)) * 1024 + 128 * ct;
      if (half == 0) dma16_a(kk < 28 ? sw2 : swo, vw2, so, lds0 + L_WG + m * PART + (kk % RD) * 2048); else dma16_b(kk < 28 ? sw2 : swo, vw2b, so);
    };
    auto wdma = [&](int kk) { wdmah(kk, 0); wdmah(kk, 1); };
    {
      const v4u swq = make_srd(wqkv, DM * 3 * DM * 2);
      const unsigned vx0 = (lane >> 3) * 1024 + (((lane & 7) ^ g) * 16), vx1 = (7 + (lane >> 3)) * 1024 + (((lane & 7) ^ (4 + g)) * 16);
      const unsigned vwq = (lane >> 2) * (3 * DM * 2) + p4 * 16;
      auto piece = [&](int s, int i) {
        const int kc = (s + rot) & 7;
        if (i < 4) {
          const unsigned so = (128 * r + 32 * m + 16 * (i >> 1)) * 1024 + kc * 128;
          if (i & 1) dma16_b(sx, vx1, so); else dma16_a(sx, vx0, so, lds0 + xbuf(s) + m * 4096 + (i >> 1) * 2048);
        } else if (i < 7) dma16(swq, vwq, (64 * kc + 16 * m) * (3 * DM * 2) + (512 * (i - 4) + 32 * j) * 2, lds0 + qbuf(s) + ((i - 4) * 4 + m) * 1024);
        else w1dma(0, s, lds0 + w1buf(s), i - 7);
      };
#pragma unroll
      for (int i = 0; i < 18; ++i) { piece(i / 9, i % 9); if (i == 8) __builtin_amdgcn_s_barrier(); }
      __builtin_amdgcn_s_setprio(PRIO_G1);
      hsetup(0);
      f32x4 cs[2] = {};
      v4u xf[4], wf[4][2];
      hac[0] = (f32x16){}; hac[1] = (f32x16){};
      static_for<0, 8>([&](auto sc) {
        constexpr int s = sc.value;
        auto fill = [&](int i) {
          if constexpr (s < 6) piece(s + 2, i);
          if constexpr (s == 6) {
            if (i == 0) { stat_load(); bias_load(0); }
            else if (i < 5) { if (heavy) w1dma(1, (i - 1) >> 1, lds0 + L_WA + ((i - 1) >> 1) * 8192, (i - 1) & 1); }
            else wdmah((i - 5) >> 1, (i - 5) & 1);
          }
          if constexpr (s == 7) {
            if (heavy && i < 4) w1dma(1, 2 + (i >> 1), lds0 + L_WA + (2 + (i >> 1)) * 8192, i & 1);
            if (heavy && i >= 4) { w1dma(1, i, lds0 + L_WA + i * 8192, 0); w1dma(1, i, 0, 1); }
          }
          SBAR();
        };
        auto rd = [&](int p) {
          xf[p] = lds128(xbuf(s) + xo[p]);
          wf[p][0] = ldtr2(rk[p][0] + (s % 3) * PART, 512); wf[p][1] = ldtr2(rk[p][1] + (s % 3) * PART, 512);
          SBAR();
        };
        if constexpr (s < 7) wait_vm<9>(); else { if (heavy) wait_vm<11>(); else wait_vm<7>(); }
        bar();
        STAMP_AT(10 + s);
        rd(0);
        if constexpr (s < 7) fill(0);
        rd(1);
        if constexpr (s < 7) fill(1);
        if constexpr (s == 2) get_tag();
#pragma unroll
        for (int p = 0; p < 4; ++p) {
#pragma unroll
          for (int t = 0; t < 2; ++t) {
            hac[t] = MFMA32(wf[p][t], xf[p], hac[t]);
            if (p == 0) { cs[t] = MFMA16(sel, wf[p][t], cs[t]); PIN(cs[t]); }
            SBAR();
            if (p < 2 && t == 0) rd(p + 2);
            if (s == 7) fill(2 * p + t); else if (2 * p + t < 7) fill(2 + 2 * p + t);
          }
          if (s == 7 && p == 1) {
            cswr(0, cs[0]); cswr(1, cs[1]);
            bar();
            STAMP_AT(1);
          }
        }
        if constexpr (s == 7) {
          const f32x4 c0 = csrd(0), c1 = csrd(1);
          if (heavy) wait_vm<20>(); else wait_vm<4>();
          PIN(st); PIN(bias0); PIN(bias1);
          row_stats(); make_xaug();
          hac[0] = MFMA32(waug(c0, bias0), xaug, hac[0]);
          hac[1] = MFMA32(waug(c1, bias1), xaug, hac[1]);
          SBAR();
        }
      });
    }
    STAMP_AT(18);
    if (heavy) { wait_vm<0>(); arrive_nowait(lds0 + L_CNT + 16); }
    hgelu(std::integral_constant<int, 0>{}, [&]() {});
    __builtin_amdgcn_s_setprio(0);
    STAMP_AT(19);
    unsigned rb2[2];
#pragma unroll
    for (int c = 0; c < 2; ++c) rb2[c] = lds0 + L_WG + m * PART + h * 512 + q4 * 128 + ((c ^ (q4 >> 1)) * 64) + (g & 1) * 32 + p4 * 8;
    v4u wq[36][2];
    auto wrd = [&](int kk) {
#pragma unroll
      for (int c = 0; c < 2; ++c) wq[kk][c] = ldtr2(rb2[c] + (kk % RD) * 2048, 1024);
    };
    auto wgot = [&](int kk) { PIN(wq[kk][0]); PIN(wq[kk][1]); };
    const int ftile = 2 * rt + ((lane >> 2) & 1);
    const int T0 = 4 * r - 8 + 2 * m;
    const bool live = T0 >= 0;
    auto kv_fetch = [&]() {
      const unsigned src = WS_KV + (j * 64 + (live ? T0 : 0)) * 4096, vk = live ? lane * 16 : h * 512, vv = live ? lane * 16 : 0x40000000u;
#pragma unroll
      for (int i = 0; i < 8; ++i)
        dma16_sc1(sws, (i & 2) ? vv : vk, src + (live ? i : (i & 3)) * 1024, lds0 + L_KV + (2 * m + (i >> 2)) * KVT + (i & 3) * 1024 + ((i & 3) == 3 ? VB - 1024 : 0));
      const unsigned on = (lane < 2 || live) ? one2 : 0u;
      if (lane < 6) sts128(lds0 + L_KV + (lane < 2 ? 8 + m : 2 * m + ((lane - 2) >> 1)) * KVT + 2048 + (lane & 1) * VB + 1024, (v4u){on, on, on, on});
    };
    v4u af[28][2], of[8][2];
    v2u fl;
    const unsigned a0 = WS_AF + (2 * rt * AFT + 28 * m) * 1024, o0 = WS_AF + (2 * rt * AFT + 112 + 8 * m) * 1024;
    fl = ld8_sc1_async(sws, flag_off(FL_KV, live ? T0 + (lane & 1) : 0, j));
    for (int kk = 2; kk < 6; ++kk) wdma(kk);
    wait_vmf<8>(fl);
    if (lane == 0) __hip_atomic_store(flag(FL_H0, T, j), tag, RLX_AGENT);
    STAMP_AT(2);
    if (!__all(fl[0] == taglo && fl[1] == tag32)) poll_flags(flag(FL_KV, live ? T0 + (lane & 1) : 0, j), tag);
    if (heavy) await(lds0 + L_CNT + 20, 4);
    kv_fetch();
    wdma(6); wdma(7);
    static_for<0, 3>([&](auto ic) {
      constexpr int i = ic.value;
      wait_vm<(i == 0) ? 17 : 8>();
      for (int kk = 4 * i; kk < 4 * i + 4; ++kk) wrd(kk);
      for (int kk = 4 * i; kk < 4 * i + 4; ++kk) wgot(kk);
      if constexpr (i == 0) { wait_vm<4>(); arrive_nowait(lds0 + L_CNT); }
      if constexpr (i == 2) fl = ld8_sc1_async(sws, flag_off(FL_H0, ftile, 4 * m + p4));
      for (int kk = 4 * i; kk < 4 * i + 4; ++kk) wdma(kk + RD);
    });
    STAMP_AT(3);
    wait_vmf<8>(fl);
    if (!__all(fl[0] == taglo && fl[1] == tag32)) poll_flags(flag(FL_H0, ftile, 4 * m + p4), tag);
    STAMP_AT(4);
#pragma unroll
    for (int kk = 0; kk < AW; ++kk) {
      af[kk][0] = ld16_sc1_async(sws, lane * 16, a0 + kmap(kk) * 1024);
      af[kk][1] = ld16_sc1_async(sws, lane * 16, a0 + (AFT + kmap(kk)) * 1024);
    }
    __builtin_amdgcn_s_setprio(PRIO_G2);
    f32x16 c2[2][2] = {};
    auto mm2 = [&](int kk, const v4u (&a)[2], auto&& fill) {
      SBAR();
#pragma unroll
      for (int i = 0; i < 4; ++i) { c2[i >> 1][i & 1] = MFMA32(wq[kk][i & 1], a[i >> 1], c2[i >> 1][i & 1]); SBAR(); fill(i); SBAR(); }
    };
    static_for<0, 28>([&](auto kc) {
      constexpr int kk = kc.value;
      wait_vm2<(kk < AW) ? 2 * (AW - 1 - kk) + g2_range(0, kk) : g2_range(kk - AW + 1, kk)>(af[kk][0], af[kk][1]);
      if constexpr (kk == H1_CK) {
        STAMP_AT(20);
        wait_vmf<g2_range(H1_LD, H1_CK) - 1>(fl);
        if (!__all(fl[0] == taglo && fl[1] == tag32)) poll_flags(flag(FL_H1, ftile, 4 * m + p4), tag);
        STAMP_AT(21);
      }
      if constexpr (kk == H1_LD) fl = ld8_sc1_async(sws, flag_off(FL_H1, ftile, 4 * m + p4));
      if constexpr (kk == O_LD) fl = ld8_sc1_async(sws, flag_off(FL_O0, ftile, 4 * m + p4));
      if constexpr (kk == 26) fl = ld8_sc1_async(sws, flag_off(FL_O1, ftile, 4 * m + p4));
      constexpr bool dw = kk >= 1 && kk - 1 + NR + RD < 36;
      if constexpr (dw) wgot(kk - 1 + NR);
      mm2(kk, af[kk], [&](int i) {
        if constexpr (dw) { if (i < 2) wdmah(kk - 1 + NR + RD, i); }
        if constexpr (kk + NR < 28) { if (i == 1) wrd(kk + NR); }
        if constexpr (kk == 27) { if (i == 0) wrd(28); }
        if constexpr (kk + AW < 28) { if (i >= 2) af[kk + AW][i - 2] = ld16_sc1_async(sws, lane * 16, a0 + (AFT * (i - 2) + kmap(kk + AW)) * 1024); }
      });
      if constexpr (kk == O_CK) {
        wait_vmf<g2_range(O_LD, O_CK) - 1>(fl);
        if (!__all(fl[0] == taglo && fl[1] == tag32)) poll_flags(flag(FL_O0, ftile, 4 * m + p4), tag);
        res_loads();
#pragma unroll
        for (int i = 0; i < 4; ++i) {
          of[i][0] = ld16_sc1_async(sws, lane * 16, o0 + omap(i) * 1024);
          of[i][1] = ld16_sc1_async(sws, lane * 16, o0 + (AFT + omap(i)) * 1024);
        }
      }
    });
    static_for<0, 4>([&](auto kc) {
      constexpr int kk = kc.value;
      if (kk < 3) wrd(28 + kk + 1);
      if constexpr (kk == 1) {
        res_sum();
        STAMP_AT(5);
        wait_vmf<0>(fl);
        if (!__all(fl[0] == taglo && fl[1] == tag32)) poll_flags(flag(FL_O1, ftile, 4 * m + p4), tag);
        STAMP_AT(6);
#pragma unroll
        for (int i = 0; i < 8; ++i) of[4 + (i & 3)][i >> 2] = ld16_sc1_async(sws, lane * 16, o0 + (AFT * (i >> 2) + omap(4 + (i & 3))) * 1024);
      }
      wait_vm2<(kk == 0) ? 7 : 14 - 2 * kk>(of[kk][0], of[kk][1]);
      mm2(28 + kk, of[kk], [&](int i) { PIN(c2[i >> 1][i & 1]); if (kk == 3) wrd(32 + i); });
    });
    for (int kk = 32; kk < 36; ++kk) wgot(kk);
    unsigned pa = lds0 + L_WG + m * PART + lane * 16;
    PIN(pa);
    auto put = [&](int qn, int qd) {
      const f32x16& c = c2[qn >> 1][qn & 1];
      sts128(pa + (qn * 4 + qd) * 66 * 16, __builtin_bit_cast(v4u, (f32x4){c[4 * qd], c[4 * qd + 1], c[4 * qd + 2], c[4 * qd + 3]}));
    };
    static_for<0, 4>([&](auto qc) {
      constexpr int qn = qc.value;
      if constexpr ((qn & 1) == 0) { wait_vm2<qn ? 0 : 4>(of[4][qn >> 1], of[5][qn >> 1]); PIN(of[6][qn >> 1]); PIN(of[7][qn >> 1]); }
      SBAR();
#pragma unroll
      for (int i = 0; i < 4; ++i) {
        c2[qn >> 1][qn & 1] = MFMA32(wq[32 + i][qn & 1], of[4 + i][qn >> 1], c2[qn >> 1][qn & 1]);
        SBAR();
        if (qn > 0 && i >= 2) { put(qn - 1, 2 * i - 4); put(qn - 1, 2 * i - 3); }
        SBAR();
      }
    });
#pragma unroll
    for (int qd = 0; qd < 4; ++qd) put(3, qd);
    STAMP_AT(7);
  } else {
    __builtin_amdgcn_s_setprio(1);
    lane_setup();
    v4u xr[2];
    xr[0] = ld16_async(sx, lane * 16, (128 * r + 8 * j + 2 * m) * 1024); xr[1] = ld16_async(sx, lane * 16, (128 * r + 8 * j + 2 * m + 1) * 1024);
    unsigned warm;
    {
      u64 pc; asm volatile("s_getpc_b64 %0" : "=s"(pc));
      warm = ld2_async(make_srd((const void*)(pc & ~127ull), CODEWARM), lane < 3 ? lane * 128 : 0x40000000u, (idx * 4 + m) * 384);
    }
    __builtin_amdgcn_s_barrier();
    v2u ep = ld8_sc1_async(sws, m == 0 ? WS_EPOCH : 0x40000000u);
    float sval;
    const int wi = blockIdx.x * 4 + m;
    const u64 wbase = wi < 448 ? (u64)w1 + wi * 4096 : wi < 896 ? (u64)w2 + (wi - 448) * 4096 : (u64)wo + (wi - 896) * 4096;
    auto make_stats = [&]() {
      wait_vm2<1>(xr[0], xr[1]);
      PIN(warm);
      float piv[2], a[4];
#pragma unroll
      for (int u = 0; u < 2; ++u) {
        piv[u] = rdl(bflo(xr[u][0]), 0);
        float s1 = 0.f, s2 = 0.f;
#pragma unroll
        for (int i = 0; i < 8; ++i) { const float d = ((i & 1) ? bfhi(xr[u][i >> 1]) : bflo(xr[u][i >> 1])) - piv[u]; s1 += d; s2 = __builtin_fmaf(d, d, s2); }
        a[2 * u] = s1; a[2 * u + 1] = s2;
      }
#pragma unroll
      for (int i = 0; i < 4; ++i) a[i] += dpp<0xB1>(a[i]);
#pragma unroll
      for (int i = 0; i < 4; ++i) a[i] += dpp<0x4E>(a[i]);
      float t = p4 == 0 ? a[0] : p4 == 1 ? a[1] : p4 == 2 ? a[2] : a[3];
      t += dpp<0x114>(t); t += dpp<0x118>(t);
      const auto s16 = __builtin_amdgcn_permlane16_swap(asu(t), asu(t), false, false);
      t = asf(s16[0]) + asf(s16[1]);
      const auto s32 = __builtin_amdgcn_permlane32_swap(asu(t), asu(t), false, false);
      t = (asf(s32[0]) + asf(s32[1])) * (1.f / DM);
      const float e1 = dpp<0xA0>(t);
      sval = (p4 & 1) ? __builtin_fmaxf(__builtin_fmaf(-e1, e1, t), 0.f) + 1e-5f : t + (p4 ? piv[1] : piv[0]);
      PIN(sval);
    };
    const unsigned acnt = lds0 + L_CNT;
    const unsigned vpos = 2048 + (g & 1) * 512 + h * 256 + (lane & 15) * 16;
    v4u qf[2], am0, am1;
    u64 um[16];
    auto masks = [&]() {
      const int dq = r32 - 4 * h;
#pragma unroll
      for (int u = 0; u < 16; ++u) { um[u] = __builtin_amdgcn_ballot_w64((u & 3) + 8 * (u >> 2) > dq); pin_s(um[u]); }
#pragma unroll
      for (int d = 0; d < 4; ++d) {
        am0[d] = __builtin_amdgcn_inverse_ballot_w64(um[2 * d]) ? 0xffffffffu : __builtin_amdgcn_inverse_ballot_w64(um[2 * d + 1]) ? 0xffff0000u : 0u;
        am1[d] = __builtin_amdgcn_inverse_ballot_w64(um[8 + 2 * d]) ? 0xffffffffu : __builtin_amdgcn_inverse_ballot_w64(um[9 + 2 * d]) ? 0xffff0000u : 0u;
      }
      PIN(am0); PIN(am1);
    };
    {
      v4u xf[32];
      f32x16 acc[3] = {};
      f32x4 cs[3] = {};
      v4u wf[4][3];
      unsigned wb[4];
#pragma unroll
      for (int p = 0; p < 4; ++p) { wb[p] = qbuf(0) + ((p + m) & 3) * 1024 + (8 * h + q4) * 64 + (g & 1) * 32 + p4 * 8; PIN(wb[p]); }
      auto rd = [&](int ks, int t) {
        if (t == 0) xf[ks] = lds128(xbuf(ks >> 2) + xo[ks & 3]);
        wf[ks & 3][t] = ldtr2(wb[ks & 3] + ((ks >> 2) % 3) * 12288 + t * 4096, 256);
      };
      auto mm = [&](int ks, int t) {
        acc[t] = t == 2 ? MFMA32(xf[ks], wf[ks & 3][t], acc[t]) : MFMA32(wf[ks & 3][t], xf[ks], acc[t]);
        if ((ks & 3) == 0) { cs[t] = MFMA16(sel, wf[ks & 3][t], cs[t]); PIN(cs[t]); }
        SBAR();
      };
      make_stats();
      bar();
      STAMP_AT(10);
#pragma unroll
      for (int t = 0; t < 6; ++t) rd(t / 3, t % 3);
      SBAR();
      static_for<0, 8>([&](auto sc) {
        constexpr int s = sc.value;
#pragma unroll
        for (int p = 0; p < 2; ++p) {
#pragma unroll
          for (int t = 0; t < 3; ++t) { mm(4 * s + p, t); rd(4 * s + p + 2, t); SBAR(); }
          if (s == 7 && p == 0) { cswr(4, cs[0]); cswr(5, cs[1]); cswr(6, cs[2]); SBAR(); }
        }
        if constexpr (s == 0) {
          wait_vmf<0>(ep);
          if (m == 0 && lane < 10)
            *(LAS unsigned*)(size_t)(lds0 + L_CNT + lane * 4) = lane == 8 ? ep[0] * 0x9E3779B1u + 0x7F4A7C15u : lane == 9 ? ep[1] * 0x85EBCA77u + 0x165667B1u : 0u;
        }
        if constexpr (s == 1) {
          get_tag();
          if ((lane >> 2) == 3) {
            const v2u gr = {asu(sval), tag32};
            __builtin_amdgcn_raw_buffer_store_b64(gr, rws, WS_STATS + (128 * r + 8 * j + 2 * m) * 16 + p4 * 8, 0, 16);
          }
        }
        if constexpr (s == MIDWARM) {
          warm = ld2_async(make_srd((const void*)wbase, 4096), lane * 128, 0);
        }
        if constexpr (s == 5) stat_load();
        if constexpr (s == 6) {
          wait_vm1<0>(st);
          PIN(warm);

          row_stats();
        }
        mm(4 * s + 2, 0); mm(4 * s + 2, 1);
        bar();
        if constexpr (s < 7) {
          STAMP_AT(11 + s);
          mm(4 * s + 2, 2);
          rd(4 * s + 4, 0); rd(4 * s + 4, 1); rd(4 * s + 4, 2); SBAR();
#pragma unroll
          for (int t = 0; t < 3; ++t) { mm(4 * s + 3, t); rd(4 * s + 5, t); SBAR(); }
        }
      });
      STAMP_AT(1);
      __builtin_amdgcn_s_setprio(3);
      const f32x4 c5 = csrd(5), c6 = csrd(6), c4 = csrd(4);
      SBAR();
      mm(30, 2); mm(31, 1);
      make_xaug();
      SBAR();
      mm(31, 2); mm(31, 0);
      if (h == 0) *(LAS float*)(size_t)(lds0 + L_RS + (m * 32 + r32) * 4) = rstd;
      acc[1] = MFMA32(waug(c5, 0u), xaug, acc[1]);
      acc[2] = MFMA32(xaug, waug(c6, 0u), acc[2]);
      acc[0] = MFMA32(waug(c4, 0u), xaug, acc[0]);
      SBAR();
      f32x4 rs[4];
#pragma unroll
      for (int qd = 0; qd < 4; ++qd) rs[qd] = __builtin_bit_cast(f32x4, lds128(lds0 + L_RS + (m * 32 + 8 * qd + 4 * h) * 4));
      const unsigned kvo = WS_KV + (j * 64 + T) * 4096;
#pragma unroll
      for (int e = 0; e < 16; ++e) acc[1][e] = MUL1(acc[1][e], rstd);
#pragma unroll
      for (int e = 0; e < 2; ++e) {
        const v4u f = pack8(acc[1], e);
        st16_sc1(f, sws, lane * 16, kvo + e * 1024);
        sts128(lds0 + L_KV + (8 + m) * KVT + e * 1024 + lane * 16, f);
      }
#pragma unroll
      for (int qd = 0; qd < 4; ++qd)
#pragma unroll
        for (int e = 0; e < 4; ++e) acc[2][4 * qd + e] = MUL1(acc[2][4 * qd + e], rs[qd][e]);
#pragma unroll
      for (int s = 0; s < 2; ++s) {
        const v4u f = pack8(acc[2], s);
        st16_sc1(f, sws, vpos, kvo + s * 1024);
        sts128(lds0 + L_KV + (8 + m) * KVT + s * VB + vpos, f);
      }
      SBAR();
      const float qs = rstd * (0.25f * 1.4426950408889634f);
#pragma unroll
      for (int e = 0; e < 16; ++e) acc[0][e] = MUL1(acc[0][e], qs);
      qf[0] = pack8(acc[0], 0); qf[1] = pack8(acc[0], 1);
      PIN(qf[0]); PIN(qf[1]);
      arrive_nowait(acnt);
      STAMP_AT(2);
      if (heavy) {
        hsetup(1);
        await(acnt + 16, 4);
        STAMP_AT(3);
        hmm(std::integral_constant<int, 1>{}, xf, [&](auto kc) {
          constexpr int ks = kc.value;
          if constexpr (ks == KVPUB) {
            wait_vm<0>();
            if (lane == 0) __hip_atomic_store(flag(FL_KV, T, j), tag, RLX_AGENT);
          }
          if constexpr (ks == KVPUB + 2) bias_load(1);
          if constexpr (ks == 31) { wait_vm<0>(); PIN(bias0); PIN(bias1); }
        });
        STAMP_AT(18);
        hgelu(std::integral_constant<int, 1>{}, [&]() {});
        STAMP_AT(4);
      } else {
        wait_vm<0>();
        if (lane == 0) __hip_atomic_store(flag(FL_KV, T, j), tag, RLX_AGENT);
      }
    }
    masks();
    STAMP_AT(19);
    const unsigned kb = lds0 + L_KV + m * KVT + lane * 16;
    unsigned vb[2];
#pragma unroll
    for (int e = 0; e < 2; ++e) { vb[e] = lds0 + L_KV + m * KVT + 2048 + ((g & 1) ? 1024 : e * 512 + h * 256 + (lane & 15) * 16); PIN(vb[e]); }
    bool pend = true;
    if (__builtin_amdgcn_readfirstlane(peek(acnt)) < 8) {
      wait_vm<0>();
      if (lane == 0) __hip_atomic_store(flag(FL_H1, T, j), tag, RLX_AGENT);
      pend = false;
      await(acnt, 8);
    }
    asm volatile("" ::: "memory");
    __builtin_amdgcn_s_setprio(PRIO_ATT);
    STAMP_AT(5);
    const f32x16 zero = {};
    auto o_store = [&](f32x16& o, int e) {
      const float rl = __builtin_amdgcn_rcpf(o[8]);
#pragma unroll
      for (int u = 0; u < 8; ++u) o[u] = MUL1(o[u], rl);
      st16_sc1(pack8(o, 0), sws, lane * 16, WS_AF + (T * AFT + 112 + 2 * j + e) * 1024);
    };
    int redo = 0;
    {
      f32x16 o[2], sa, sb, su;
      v4u kn, ku, va, vc, vn0, vn1, vu0, vu1, p0, p1, pu;
      auto kaddr = [&](int n) { return kb + ((n & 7) + 1) * KVT + (n >> 3) * 1024; };
      auto vaddr = [&](int n) { return vb[n >> 3] + ((n & 7) + 1) * KVT; };
      kn = lds128(kaddr(0)); vn0 = lds128(vaddr(0)); vn1 = lds128(vaddr(0) + VB);
      sa = MFMA32(kn, qf[0], zero);
      kn = lds128(kaddr(1));
      SBAR();
      auto step = [&](auto nc, f32x16& sc, f32x16& sn) {
        constexpr int n = nc.value, e = n >> 3, i = n & 7;
        if constexpr (n < 15) sn = MFMA32(kn, qf[(n + 1) >> 3], zero);
        if constexpr (n < 14) kn = lds128(kaddr(n + 2));
        if constexpr (i == 5) ku = lds128(kb + e * 1024);
        v4u v0 = vn0, v1 = vn1;
        if constexpr (n < 15) { vn0 = lds128(vaddr(n + 1)); vn1 = lds128(vaddr(n + 1) + VB); }
        if constexpr (i == 6) { vu0 = lds128(vb[e]); vu1 = lds128(vb[e] + VB); }
        SBAR();
        if constexpr (i == 7) {
#pragma unroll
          for (int u = 0; u < 16; ++u) sc[u] = __builtin_amdgcn_inverse_ballot_w64(um[u]) ? su[u] : sc[u];
          PIN(sc); SBAR();
        }
#pragma unroll
        for (int u = 0; u < 6; ++u) sc[u] = EXP2(sc[u]);
        PIN(sc); SBAR();
        if constexpr (n == 8) { o[0] = MFMA32(vc, p1, o[0]); SBAR(); }
        else if constexpr (n > 0) { o[e] = MFMA32(vc, p1, o[e]); SBAR(); }
        sc[6] = EXP2(sc[6]); sc[7] = EXP2(sc[7]);
        p0 = pack8(sc, 0);
        sc[8] = EXP2(sc[8]); sc[9] = EXP2(sc[9]);
        if constexpr (i == 7) { va = p0 & am0; p0 &= ~am0; PIN(va); }
        PIN(sc); PIN(p0); SBAR();
        if constexpr (n == 8) { o[0] = MFMA32(vu1, pu, o[0]); SBAR(); }
        o[e] = MFMA32(v0, p0, i == 0 ? zero : o[e]);
        SBAR();
        if constexpr (i == 6) { su = MFMA32(ku, qf[e], zero); SBAR(); }
#pragma unroll
        for (int u = 10; u < 13; ++u) sc[u] = EXP2(sc[u]);
        if constexpr (i == 7) { PIN(sc); SBAR(); o[e] = MFMA32(vu0, va, o[e]); SBAR(); }
#pragma unroll
        for (int u = 13; u < 16; ++u) sc[u] = EXP2(sc[u]);
        p1 = pack8(sc, 1); vc = v1;
        if constexpr (i == 7) { pu = p1 & am1; p1 &= ~am1; PIN(pu); }
        PIN(p1); SBAR();
        if constexpr (n == 8) {
          if (__all(o[0][8] >= 0x1p-30f && o[0][8] <= 0x1p30f)) o_store(o[0], 0); else redo = 1;
          SBAR();
        }
        if constexpr (n == 10) pend = !redo;
        if constexpr (i == 3 || i == 5 || i == 7) {
          if (pend && (i == 7 || (__builtin_amdgcn_s_getreg(7 | (3 << 11)) & 15) == 0)) {
            wait_vm<0>();
            if (lane == 0) __hip_atomic_store(flag(FL_H1 + e, T, j), tag, RLX_AGENT);
            pend = false;
          }
        }
      };
      static_for<0, 8>([&](auto hc) {
        step(std::integral_constant<int, 2 * hc.value>{}, sa, sb);
        step(std::integral_constant<int, 2 * hc.value + 1>{}, sb, sa);
      });
      o[1] = MFMA32(vc, p1, o[1]);
      o[1] = MFMA32(vu1, pu, o[1]);
      if (__all(o[1][8] >= 0x1p-30f && o[1][8] <= 0x1p30f)) o_store(o[1], 1); else redo |= 2;
    }
    if (redo) {
#pragma unroll 1
      for (int e = 0; e < 2; ++e) {
        if (!((redo >> e) & 1)) continue;
        const v4u q = e ? qf[1] : qf[0];
        const unsigned vbe = e ? vb[1] : vb[0];
        auto merged = [&](const f32x16& c) {
          f32x16 sl = MFMA32(lds128(kb + 8 * KVT + e * 1024), q, c);
          const f32x16 su = MFMA32(lds128(kb + e * 1024), q, c);
          static_for<0, 16>([&](auto u) { sl[u.value] = upper<u.value>() ? su[u.value] : sl[u.value]; });
          return sl;
        };
        float mx = -INFINITY;
#pragma unroll 1
        for (int i = 0; i < 8; ++i) {
          const f32x16 sc = i ? MFMA32(lds128(kb + i * KVT + e * 1024), q, zero) : merged(zero);
#pragma unroll
          for (int u = 0; u < 16; u += 2) mx = max3(mx, sc[u], sc[u + 1]);
        }
        const auto sw = __builtin_amdgcn_permlane32_swap(asu(mx), asu(mx), false, false);
        mx = __builtin_fmaxf(asf(sw[0]), asf(sw[1]));
        f32x16 negm, o;
#pragma unroll
        for (int u = 0; u < 16; ++u) negm[u] = -mx;
        {
          f32x16 sc = merged(negm);
#pragma unroll
          for (int u = 0; u < 16; ++u) sc[u] = EXP2(sc[u]);
          const v4u p0 = pack8(sc, 0), p1 = pack8(sc, 1);
          o = MFMA32(lds128(vbe), p0 & am0, zero);
          o = MFMA32(lds128(vbe + VB), p1 & am1, o);
          o = MFMA32(lds128(vbe + 8 * KVT), p0 & ~am0, o);
          o = MFMA32(lds128(vbe + 8 * KVT + VB), p1 & ~am1, o);
        }
#pragma unroll 1
        for (int i = 1; i < 8; ++i) {
          f32x16 sc = MFMA32(lds128(kb + i * KVT + e * 1024), q, negm);
#pragma unroll
          for (int u = 0; u < 16; ++u) sc[u] = EXP2(sc[u]);
          o = MFMA32(lds128(vbe + i * KVT), pack8(sc, 0), o);
          o = MFMA32(lds128(vbe + i * KVT + VB), pack8(sc, 1), o);
        }
        o_store(o, e);
      }
      wait_vm<0>();
      if (lane == 0) __hip_atomic_store(flag(FL_O0, T, j), tag, RLX_AGENT);
    }
    STAMP_AT(6);
    wait_vm<0>();
    if (lane == 0) __hip_atomic_store(flag(FL_O1, T, j), tag, RLX_AGENT);
    STAMP_AT(7);
    res_loads();
    wait_vm<0>();
    res_sum();
    if (blockIdx.x == 0 && wave == 7) {
#pragma unroll 1
      for (int i = 0; i < 4; ++i) poll_flags(flag(FL_KV, 4 * (4 * i + g), lane & 15), tag);
      if (lane == 0) __hip_atomic_store((GAS u64*)(ws + WS_EPOCH), tag, RLX_AGENT);
    }
  }
  {
    int t2 = threadIdx.x; PIN(t2);
    const int row = t2 >> 3, ch = t2 & 7;
    unsigned a = lds0 + L_WG + (((row >> 5) * 8 + ch) * 66 + (row & 31)) * 16, oo = (64 * rt + row) * 1024 + (64 * ct + ch * 8) * 2;
    PIN(a); PIN(oo);
    bar();
    f32x4 pl[4], ph[4];
#pragma unroll
    for (int w = 0; w < 4; ++w) { pl[w] = __builtin_bit_cast(f32x4, lds128(a + w * PART)); ph[w] = __builtin_bit_cast(f32x4, lds128(a + w * PART + 512)); }
    SBAR();
#pragma unroll
    for (int w = 0; w < 4; ++w) { lo += pl[w]; hi += ph[w]; }
    const v4u o = {pk2(lo[0], lo[1]), pk2(lo[2], lo[3]), pk2(hi[0], hi[1]), pk2(hi[2], hi[3])};
    __builtin_amdgcn_raw_buffer_store_b128(o, __builtin_amdgcn_make_buffer_rsrc(out, 0, SEQ * DM * 2, 0x00020000), oo, 0, 16);
  }
  STAMP_AT(8);
}

extern "C" void kernel_launch(void* const* d_in, const int* in_sizes, int n_in, void* d_out, int out_size, void* d_ws, size_t ws_size,
                              hipStream_t stream) {
  (void)in_sizes; (void)n_in; (void)out_size; (void)ws_size;
  (void)hipFuncSetAttribute((const void*)fused_block, hipFuncAttributeMaxDynamicSharedMemorySize, LDS_BYTES);
  fused_block<<<256, 512, LDS_BYTES, stream>>>(d_in[0], d_in[1], d_in[2], d_in[4], d_in[5], d_in[6], d_in[7], d_out, (char*)d_ws);
}
```

```cpp
#include <hip/hip_runtime.h>
#include <hip/hip_bf16.h>
#include <stdint.h>
#include <type_traits>

typedef unsigned v4u __attribute__((ext_vector_type(4)));
typedef unsigned v2u __attribute__((ext_vector_type(2)));
typedef float f32x2 __attribute__((ext_vector_type(2)));
typedef float f32x4 __attribute__((ext_vector_type(4)));
typedef float f32x16 __attribute__((ext_vector_type(16)));
typedef short bf16x8 __attribute__((ext_vector_type(8)));
typedef short s16x4 __attribute__((ext_vector_type(4)));
typedef __bf16 bf16x2_t __attribute__((ext_vector_type(2)));
typedef unsigned long long u64;
#define GAS __attribute__((address_space(1)))
#define LAS __attribute__((address_space(3)))
#define DI __device__ __forceinline__
#define RLX_AGENT __ATOMIC_RELAXED, __HIP_MEMORY_SCOPE_AGENT

constexpr int SEQ = 2048, DM = 512, FF = 1792;
constexpr unsigned WS_EPOCH = 0;
constexpr unsigned WS_STATS = 32768;
constexpr unsigned WS_FLAG = 65536;
constexpr unsigned WS_KV = WS_FLAG + 5 * 1024 * 128;
constexpr int AFT = 145;
constexpr unsigned WS_AF = WS_KV + 16 * 64 * 4096;
constexpr unsigned WS_END = WS_AF + 64 * AFT * 1024;
constexpr unsigned WS_STAMP = WS_END;
enum { FL_KV = 0, FL_H0 = 1, FL_H1 = 2, FL_O0 = 3, FL_O1 = 4 };
constexpr int L_WA = 0;
constexpr int L_WG = 86016;
constexpr int VB = 1040;
constexpr int KVT = 2048 + 2 * VB;
constexpr int L_KV = 32768;
constexpr int RD = 8, NR = 12;
constexpr int PART = 16 * 66 * 16;
constexpr int L_CS = L_WG + 4 * PART;
constexpr int L_RS = L_CS + 3584;
constexpr int L_CNT = L_RS + 512;
constexpr int LDS_BYTES = L_CNT + 64;
constexpr int xbuf(int s) { return ((s + 1) % 3) * 16384; }
constexpr int qbuf(int s) { return 49152 + (s % 3) * 12288; }
constexpr int w1buf(int s) { return L_WG + (s % 3) * PART + 8192; }
static_assert(L_KV + 8 * KVT >= 65536 && xbuf(7) == 32768 && qbuf(2) + 12288 <= L_WG && L_KV + 12 * KVT <= L_WG && PART >= RD * 2048 && LDS_BYTES <= 163840, "lds map");
#define AWIN 6
constexpr int AW = AWIN;
constexpr unsigned SPIN_CAP = 1u << 23;

#define MFMA32(a, b, c) __builtin_amdgcn_mfma_f32_32x32x16_bf16(__builtin_bit_cast(bf16x8, a), __builtin_bit_cast(bf16x8, b), c, 0, 0, 0)
#define MFMA16(a, b, c) __builtin_amdgcn_mfma_f32_16x16x32_bf16(__builtin_bit_cast(bf16x8, a), __builtin_bit_cast(bf16x8, b), c, 0, 0, 0)
#define SBAR() __builtin_amdgcn_sched_barrier(0)

#define STAMP_AT(i) do { } while (0)

DI float asf(unsigned u) { return __builtin_bit_cast(float, u); }
DI unsigned asu(float f) { return __builtin_bit_cast(unsigned, f); }
DI unsigned pk2(float lo, float hi) { return __builtin_bit_cast(unsigned, __builtin_convertvector((f32x2){lo, hi}, bf16x2_t)); }
DI float bflo(unsigned u) { return __builtin_bit_cast(float, u << 16); }
DI float bfhi(unsigned u) { return __builtin_bit_cast(float, u & 0xffff0000u); }
DI v4u make_srd(const void* p, unsigned bytes) { const u64 a = (u64)p; return (v4u){(unsigned)a, (unsigned)(a >> 32) & 0xffffu, bytes, 0x00020000u}; }
DI void dma16(v4u srd, unsigned voff, unsigned soff, unsigned lds) {
  asm volatile("s_mov_b32 m0, %3\n\ts_nop 0\n\tbuffer_load_dwordx4 %0, %1, %2 offen lds" :: "v"(voff), "s"(srd), "s"(soff), "s"(lds) : "memory");
}
DI void dma16_sc1(v4u srd, unsigned voff, unsigned soff, unsigned lds) {
  asm volatile("s_mov_b32 m0, %3\n\ts_nop 0\n\tbuffer_load_dwordx4 %0, %1, %2 offen sc1 lds" :: "v"(voff), "s"(srd), "s"(soff), "s"(lds) : "memory");
}
DI void dma16_a(v4u srd, unsigned voff, unsigned soff, unsigned lds) { dma16(srd, voff, soff, lds); }
DI void dma16_b(v4u srd, unsigned voff1, unsigned soff) {
  asm volatile("buffer_load_dwordx4 %0, %1, %2 offen offset:1024 lds" :: "v"(voff1), "s"(srd), "s"(soff) : "memory");
}
DI v4u ld16_sc1_async(v4u srd, unsigned voff, unsigned soff) {
  v4u r; asm volatile("buffer_load_dwordx4 %0, %1, %2, %3 offen sc1" : "=v"(r) : "v"(voff), "s"(srd), "s"(soff) : "memory"); return r;
}
DI v4u ld16_async(v4u srd, unsigned voff, unsigned soff) {
  v4u r; asm volatile("buffer_load_dwordx4 %0, %1, %2, %3 offen" : "=v"(r) : "v"(voff), "s"(srd), "s"(soff) : "memory"); return r;
}
DI unsigned ld2_async(v4u srd, unsigned voff, unsigned soff) {
  unsigned r; asm volatile("buffer_load_ushort %0, %1, %2, %3 offen" : "=v"(r) : "v"(voff), "s"(srd), "s"(soff) : "memory"); return r;
}
DI void st16_sc1(v4u v, v4u srd, unsigned voff, unsigned soff) {
  asm volatile("buffer_store_dwordx4 %0, %1, %2, %3 offen sc1\n\ts_nop 1" :: "v"(v), "v"(voff), "s"(srd), "s"(soff) : "memory");
}
template <int N> DI void wait_vm1(v4u& a) { asm volatile("s_waitcnt vmcnt(%1)" : "+v"(a) : "i"(N) : "memory"); }
template <int N> DI void wait_vm2(v4u& a, v4u& b) { asm volatile("s_waitcnt vmcnt(%2)" : "+v"(a), "+v"(b) : "i"(N) : "memory"); }
template <int N> DI void wait_vm3(v4u& a, v4u& b, v4u& c) { asm volatile("s_waitcnt vmcnt(%3)" : "+v"(a), "+v"(b), "+v"(c) : "i"(N) : "memory"); }
DI void pin_s(u64& v) { asm volatile("" : "+s"(v)); }
template <class V> DI void pin(V& v) { asm volatile("" : "+v"(v)); }
template <int N> DI void wait_vm() { asm volatile("s_waitcnt vmcnt(%0)" :: "i"(N) : "memory"); }
DI void bar() { asm volatile("s_waitcnt lgkmcnt(0)\n\ts_barrier" ::: "memory"); }
DI v4u lds128(unsigned a) { return *(const LAS v4u*)(size_t)a; }
DI void sts128(unsigned a, v4u v) { *(LAS v4u*)(size_t)a = v; }
DI v2u ldtr(unsigned a) { return __builtin_bit_cast(v2u, __builtin_amdgcn_ds_read_tr16_b64_v4i16((LAS s16x4*)(size_t)a)); }
DI v4u ldtr2(unsigned a, int second) { const v2u lo = ldtr(a), hi = ldtr(a + second); return (v4u){lo.x, lo.y, hi.x, hi.y}; }
template <int CTRL> DI float dpp(float v) { return __builtin_bit_cast(float, __builtin_amdgcn_update_dpp(0, __builtin_bit_cast(int, v), CTRL, 0xf, 0xf, true)); }
DI float rdl(float v, int l) { return __builtin_bit_cast(float, __builtin_amdgcn_readlane(__builtin_bit_cast(int, v), l)); }
DI v4u pack8(const f32x16& a, int s) { return (v4u){pk2(a[8 * s], a[8 * s + 1]), pk2(a[8 * s + 2], a[8 * s + 3]), pk2(a[8 * s + 4], a[8 * s + 5]), pk2(a[8 * s + 6], a[8 * s + 7])}; }
DI void poll_flags(const GAS u64* p, u64 tag) {
#pragma unroll 1
  for (unsigned spin = 0; spin < SPIN_CAP; ++spin) {
    const u64 v = __hip_atomic_load(p, RLX_AGENT);
    if (__all(v == tag)) break;
    __builtin_amdgcn_s_sleep(1);
  }
  asm volatile("" ::: "memory");
}
constexpr u64 upper_mask(int reg) {
  u64 m = 0;
  for (int l = 0; l < 64; ++l) { const int row = (reg & 3) + 8 * (reg >> 2) + 4 * (l >> 5); if (row > (l & 31)) m |= 1ull << l; }
  return m;
}
template <int REG> DI bool upper() { return __builtin_amdgcn_inverse_ballot_w64(upper_mask(REG)); }
template <int I, int N, class Fn> DI void static_for(Fn&& f) { if constexpr (I < N) { f(std::integral_constant<int, I>{}); static_for<I + 1, N>(f); } }
DI float max3(float a, float b, float c) { return __builtin_fmaxf(__builtin_fmaxf(a, b), c); }

constexpr int H1_CK = 16 - AW, H1_LD = H1_CK - 3, O_CK = 28 - AW, O_LD = O_CK - 3;
constexpr int g2_issued(int t) { return ((t >= 1 && t - 1 + NR + RD < 36) ? 2 : 0) + ((t + AW < 28) ? 2 : 0) + (t == H1_LD) + (t == O_LD) + (t == O_CK ? 10 : 0) + (t == 26); }
constexpr int g2_range(int a, int b) { int n = 0; for (int t = a; t < b; ++t) n += g2_issued(t); return n; }
#define GELU_PK(P) (P == 1)
#define PRIO_G2 2
#define PRIO_ATT 3
#define PRIO_G1 2
#define ROT r
#define CODEWARM 38000
#define MIDWARM 1
#define KVPUB 2
#define EXP2(v) __builtin_amdgcn_exp2f(v)
#define PIN(v) pin(v)
extern "C" __device__ float mul_unpacked(float, float) __asm("llvm.amdgcn.fmul.legacy");
#define MUL1(a, b) mul_unpacked(a, b)
#define FMA1(a, b, c) __builtin_amdgcn_div_fmasf(a, b, c, false)
DI v2u ld8_sc1_async(v4u srd, unsigned voff) {
  v2u r; asm volatile("buffer_load_dwordx2 %0, %1, %2, 0 offen sc1" : "=v"(r) : "v"(voff), "s"(srd) : "memory"); return r;
}
template <int N> DI void wait_vmf(v2u& a) { asm volatile("s_waitcnt vmcnt(%1)" : "+v"(a) : "i"(N) : "memory"); }
DI void arrive(unsigned cnt, int lane) {
  asm volatile("s_waitcnt lgkmcnt(0)" ::: "memory");
  if (lane == 0) __hip_atomic_fetch_add((LAS unsigned*)(size_t)cnt, 1u, __ATOMIC_RELAXED, __HIP_MEMORY_SCOPE_WORKGROUP);
}
DI void arrive_nowait(unsigned cnt) {
  asm volatile("s_mov_b64 exec, 1\n\tds_add_u32 %0, %1\n\ts_mov_b64 exec, -1" :: "v"(cnt), "v"(1u) : "memory");
}
DI unsigned peek(unsigned cnt) { return __hip_atomic_load((LAS unsigned*)(size_t)cnt, __ATOMIC_RELAXED, __HIP_MEMORY_SCOPE_WORKGROUP); }
DI void await(unsigned cnt, unsigned n) {
#pragma unroll 1
  for (unsigned spin = 0; spin < SPIN_CAP * 32; ++spin) {
    if (__builtin_amdgcn_readfirstlane(peek(cnt)) >= n) break;
    __builtin_amdgcn_s_sleep(1);
  }
  asm volatile("" ::: "memory");
}

__global__ __launch_bounds__(512, 2) void fused_block(const void* __restrict__ x, const void* __restrict__ wqkv, const void* __restrict__ wo,
                                                      const void* __restrict__ w1, const void* __restrict__ b1, const void* __restrict__ w2,
                                                      const void* __restrict__ b2, void* __restrict__ out, char* __restrict__ ws) {
  constexpr unsigned lds0 = 0;
  const int wave = __builtin_amdgcn_readfirstlane(threadIdx.x >> 6);
  const int m = wave & 3;
  const bool isG = wave < 4;
  const int xcd = blockIdx.x & 7, idx = blockIdx.x >> 3;
  const int r = (xcd >> 2) * 8 + (idx >> 2), j = (xcd & 3) * 4 + (idx & 3);
  const int rt = (xcd >> 1) * 8 + (idx >> 2), ct = (xcd & 1) * 4 + (idx & 3);
  int lane = threadIdx.x & 63, r32, h, g, q4, p4;
  STAMP_AT(0);
  asm volatile("" :: "s"(x), "s"(wqkv), "s"(wo), "s"(w1), "s"(b1), "s"(w2), "s"(b2), "s"(out), "s"(ws));

  const v4u sx = make_srd(x, SEQ * DM * 2), sws = make_srd(ws, WS_END);
  const __amdgpu_buffer_rsrc_t rws = __builtin_amdgcn_make_buffer_rsrc(ws, 0, WS_END, 0x00020000);
  const unsigned one2 = 0x3f803f80u;
  const int T = 4 * r + m;
  const int rot = ROT;
  auto flag_off = [&](int kind, int tile, int jj) { return WS_FLAG + ((kind * 64 + tile) * 16 + jj) * 128; };
  auto flag = [&](int kind, int tile, int jj) { return (GAS u64*)(ws + flag_off(kind, tile, jj)); };
  u64 tag;
  unsigned taglo, tag32;
  auto get_tag = [&]() {
    const v2u tg = *(const LAS v2u*)(size_t)(lds0 + L_CNT + 32);
    taglo = __builtin_amdgcn_readfirstlane(tg[0]); tag32 = __builtin_amdgcn_readfirstlane(tg[1]);
    tag = ((u64)tag32 << 32) | taglo;
  };
  v4u xres, bres, xaug, st;
  float mean, var, rstd, sd;
  v4u sel;
  unsigned xo[4], swz;
  auto lane_setup = [&]() {
    lane = threadIdx.x & 63; PIN(lane);
    r32 = lane & 31; h = lane >> 5; g = lane >> 4; q4 = (lane >> 2) & 3; p4 = lane & 3;
    const unsigned selv = ((lane & 15) == (g & 1)) ? one2 : 0u;
    sel = (v4u){selv, selv, selv, selv};
#pragma unroll
    for (int p = 0; p < 4; ++p) xo[p] = (32 * m + r32) * 128 + (((2 * ((p + m) & 3) + h) ^ ((r32 >> 1) & 7)) * 16);
    swz = ((((lane >> 2) & 1) ^ (g & 1)) * 64) + p4 * 16;
  };

  auto stat_load = [&]() { st = ld16_sc1_async(sws, WS_STATS + (128 * r + 32 * m + r32) * 16, 0); };
  auto res_loads = [&]() {
    int t2 = threadIdx.x; PIN(t2);
    const int row = 64 * rt + (t2 >> 3), col = 64 * ct + (t2 & 7) * 8;
    xres = ld16_async(sx, row * 1024 + col * 2, 0);
    bres = ld16_async(make_srd(b2, DM * 2), col * 2, 0);
  };
  f32x4 lo, hi;
  auto res_sum = [&]() {
    PIN(xres); PIN(bres);
    lo = (f32x4){bflo(xres[0]) + bflo(bres[0]), bfhi(xres[0]) + bfhi(bres[0]), bflo(xres[1]) + bflo(bres[1]), bfhi(xres[1]) + bfhi(bres[1])};
    hi = (f32x4){bflo(xres[2]) + bflo(bres[2]), bfhi(xres[2]) + bfhi(bres[2]), bflo(xres[3]) + bflo(bres[3]), bfhi(xres[3]) + bfhi(bres[3])};
    PIN(lo); PIN(hi);
  };
  auto row_stats = [&]() {
    if (!__all(st[1] == tag32 && st[3] == tag32)) {
#pragma unroll 1
      for (unsigned spin = 0; spin < SPIN_CAP; ++spin) {
        st = ld16_sc1_async(sws, WS_STATS + (128 * r + 32 * m + r32) * 16, 0);
        wait_vm1<0>(st);
        if (__all(st[1] == tag32 && st[3] == tag32)) break;
      }
    }
    mean = asf(st[0]); var = asf(st[2]);
    PIN(mean); PIN(var);
  };
  auto make_xaug = [&]() {
    PIN(mean); PIN(var);
    rstd = __builtin_amdgcn_rsqf(var); sd = var * rstd;
    const unsigned uh = pk2(-mean, sd);
    const unsigned ul = pk2(-mean - bflo(uh), FMA1(bfhi(uh), -1.f, sd));
    xaug = (v4u){(uh & 0xffffu) | (uh << 16), (ul & 0xffffu) | (uh & 0xffff0000u), ul >> 16, 0u};
    if (h) xaug = (v4u){0u, 0u, 0u, 0u};
    PIN(xaug);
  };
  auto cswr = [&](int tile, f32x4 c) {
    if (lane < 16) { LAS float* p = (LAS float*)(size_t)(lds0 + L_CS + (tile * 32 + lane) * 16 + m * 4); p[0] = c[0]; p[64] = c[1]; }
  };
  auto csrd = [&](int tile) { return __builtin_bit_cast(f32x4, lds128(lds0 + L_CS + (tile * 32 + r32) * 16)); };
  auto waug = [&](f32x4 c4, unsigned b16) {
    const float c = ((c4[0] + c4[1]) + c4[2]) + c4[3];
    const unsigned uh = pk2(c, 0.f);
    const unsigned ul = pk2(c - bflo(uh), 0.f);
    return (v4u){uh | (ul << 16), uh | (b16 << 16), b16, 0u};
  };

  const v4u sw1 = make_srd(w1, DM * FF * 2);
  const int hcol0 = 448 * (j >> 2) + 128 * (j & 3);
  const bool heavy = (j & 3) != 3;
  unsigned bias0, bias1;
  auto bias_load = [&](int P) {
    bias0 = ld2_async(make_srd(b1, FF * 2), r32 * 2, (hcol0 + 64 * P) * 2);
    bias1 = ld2_async(make_srd(b1, FF * 2), r32 * 2, (hcol0 + 64 * P + 32) * 2);
  };
  auto w1dma = [&](int P, int c, unsigned dst, int half) {
    const unsigned vw1 = (lane >> 3) * (FF * 2) + swz, vw1b = vw1 + 8 * (FF * 2) - 1024;
    const unsigned so = (64 * ((c + rot) & 7) + 16 * m) * (FF * 2) + (hcol0 + 64 * P) * 2;
    if (half == 0) dma16_a(sw1, vw1, so, dst + m * 2048); else dma16_b(sw1, vw1b, so);
  };
  f32x16 hac[2];
  unsigned rk[4][2];
  auto hsetup = [&](int P) {
#pragma unroll
    for (int p = 0; p < 4; ++p)
#pragma unroll
      for (int c = 0; c < 2; ++c) {
        rk[p][c] = lds0 + (P ? L_WA : w1buf(0)) + ((p + m) & 3) * 2048 + h * 1024 + q4 * 128 + ((c ^ (q4 >> 1)) * 64) + (g & 1) * 32 + p4 * 8;
        PIN(rk[p][c]);
      }
  };
  auto hmm = [&](auto pc, const v4u (&xf)[32], auto&& hook) {
    constexpr int P = pc.value;
    constexpr int D = 4;
    const unsigned cnt = lds0 + L_CNT + 4;
    f32x4 cs[2] = {}, c0, c1;
    v4u wf[D][2], au0, au1;
    unsigned seen = 0;
    hac[0] = (f32x16){}; hac[1] = (f32x16){};
    auto rd = [&](int ks, int t) { wf[ks % D][t] = ldtr2(rk[ks & 3][t] + ((ks >> 2) ^ 4) * 8192, 512); };
#pragma unroll
    for (int ks = 0; ks < D; ++ks) { rd(ks, 0); rd(ks, 1); }
    SBAR();
    static_for<0, 32>([&](auto kc) {
      constexpr int ks = kc.value;
      hook(kc);
      if constexpr (ks == 12) arrive_nowait(cnt + 16);
      if constexpr (ks == 29) seen = peek(cnt);
#pragma unroll
      for (int t = 0; t < 2; ++t) {
        hac[t] = MFMA32(wf[ks % D][t], xf[ks ^ 16], hac[t]);
        if ((ks & 3) == 0 && ks < 28) { cs[t] = MFMA16(sel, wf[ks % D][t], cs[t]); PIN(cs[t]); }
        if (ks == 26) { cs[t] = MFMA16(sel, wf[28 % D][t], cs[t]); PIN(cs[t]); }
        SBAR();
        if constexpr (ks + D < 32) rd(ks + D, t);
        if constexpr (ks == 31) { if (t == 0) { au0 = waug(c0, bias0); PIN(au0); } else { au1 = waug(c1, bias1); PIN(au1); } }
        SBAR();
      }
      if constexpr (ks == 30) {
        PIN(hac[0]); PIN(hac[1]);
        if (__builtin_amdgcn_readfirstlane(seen) < 4) await(cnt, 4);
        asm volatile("" ::: "memory");
        if constexpr (ks == 30) { c0 = csrd(2 * P); c1 = csrd(2 * P + 1); }
      }
      if constexpr (ks == 27) { cswr(2 * P, cs[0]); cswr(2 * P + 1, cs[1]); arrive_nowait(cnt); }
      SBAR();
    });
    hac[0] = MFMA32(au0, xaug, hac[0]);
    hac[1] = MFMA32(au1, xaug, hac[1]);
    SBAR();
  };
  auto hgelu = [&](auto pc, auto&& mid) {
    constexpr int P = pc.value;
    constexpr bool PK = GELU_PK(P);
    const float C0 = 2.3022081986f, C1 = C0 * 0.044715f;
    const float ga = -C1 * rstd * rstd * rstd, gb = -C0 * rstd;
    auto mul = [&](f32x2 a, f32x2 b) { return PK ? a * b : (f32x2){MUL1(a[0], b[0]), MUL1(a[1], b[1])}; };
    auto fma = [&](f32x2 a, float b, float c) {
      return PK ? __builtin_elementwise_fma(a, (f32x2){b, b}, (f32x2){c, c}) : (f32x2){FMA1(a[0], b, c), FMA1(a[1], b, c)}; };
#pragma unroll
    for (int t = 0; t < 2; ++t) {
      f32x2 v[8], z[8];
#pragma unroll
      for (int e = 0; e < 8; ++e) { v[e] = (f32x2){hac[t][2 * e], hac[t][2 * e + 1]}; z[e] = mul(v[e], v[e]); }
      SBAR();
#pragma unroll
      for (int e = 0; e < 8; ++e) z[e] = fma(z[e], ga, gb);
      SBAR();
#pragma unroll
      for (int e = 0; e < 8; ++e) z[e] = mul(z[e], v[e]);
      SBAR();
#pragma unroll
      for (int e = 0; e < 8; ++e) z[e] = (f32x2){EXP2(z[e][0]), EXP2(z[e][1])};
      SBAR();
#pragma unroll
      for (int e = 0; e < 8; ++e) z[e] = fma(z[e], sd, sd);
      SBAR();
#pragma unroll
      for (int e = 0; e < 8; ++e) z[e] = (f32x2){__builtin_amdgcn_rcpf(z[e][0]), __builtin_amdgcn_rcpf(z[e][1])};
      SBAR();
#pragma unroll
      for (int e = 0; e < 8; ++e) z[e] = mul(z[e], v[e]);
      SBAR();
#pragma unroll
      for (int s = 0; s < 2; ++s) {
        const v4u f = {pk2(z[4 * s][0], z[4 * s][1]), pk2(z[4 * s + 1][0], z[4 * s + 1][1]), pk2(z[4 * s + 2][0], z[4 * s + 2][1]), pk2(z[4 * s + 3][0], z[4 * s + 3][1])};
        st16_sc1(f, sws, lane * 16, WS_AF + (T * AFT + (hcol0 >> 4) + 4 * P + 2 * t + s) * 1024);
      }
      if (t == 0) mid();
      SBAR();
    }
  };

  if (isG) {
    lane_setup();
    const v4u sw2 = make_srd(w2, FF * DM * 2), swo = make_srd(wo, DM * DM * 2);
    const unsigned vw2 = (lane >> 3) * 1024 + swz, vw2b = vw2 + 7 * 1024;
    const int ro3 = rt % 3;
    auto kmap = [&](int kk) {
      if (kk < 16) return 8 * (((kk >> 2) + rt) & 3) + (kk & 3);
      const int p = ((kk - 16) >> 2) + ro3;
      return 8 * (p >= 3 ? p - 3 : p) + 4 + (kk & 3);
    };
    auto omap = [&](int i) { return 2 * ((i + rt) & 3) + (i >> 2); };
    auto wdmah = [&](int kk, int half) {
      const unsigned so = (kk < 28 ? 448 * m + 16 * kmap(kk) : 128 * m + 16 * omap(kk - 28)) * 1024 + 128 * ct;
      if (half == 0) dma16_a(kk < 28 ? sw2 : swo, vw2, so, lds0 + L_WG + m * PART + (kk % RD) * 2048); else dma16_b(kk < 28 ? sw2 : swo, vw2b, so);
    };
    auto wdma = [&](int kk) { wdmah(kk, 0); wdmah(kk, 1); };
    {
      const v4u swq = make_srd(wqkv, DM * 3 * DM * 2);
      const unsigned vx0 = (lane >> 3) * 1024 + (((lane & 7) ^ g) * 16), vx1 = (7 + (lane >> 3)) * 1024 + (((lane & 7) ^ (4 + g)) * 16);
      const unsigned vwq = (lane >> 2) * (3 * DM * 2) + p4 * 16;
      auto piece = [&](int s, int i) {
        const int kc = (s + rot) & 7;
        if (i < 4) {
          const unsigned so = (128 * r + 32 * m + 16 * (i >> 1)) * 1024 + kc * 128;
          if (i & 1) dma16_b(sx, vx1, so); else dma16_a(sx, vx0, so, lds0 + xbuf(s) + m * 4096 + (i >> 1) * 2048);
        } else if (i < 7) dma16(swq, vwq, (64 * kc + 16 * m) * (3 * DM * 2) + (512 * (i - 4) + 32 * j) * 2, lds0 + qbuf(s) + ((i - 4) * 4 + m) * 1024);
        else w1dma(0, s, lds0 + w1buf(s), i - 7);
      };
#pragma unroll
      for (int i = 0; i < 18; ++i) { piece(i / 9, i % 9); if (i == 8) __builtin_amdgcn_s_barrier(); }
      __builtin_amdgcn_s_setprio(PRIO_G1);
      hsetup(0);
      f32x4 cs[2] = {};
      v4u xf[4], wf[4][2];
      hac[0] = (f32x16){}; hac[1] = (f32x16){};
      static_for<0, 8>([&](auto sc) {
        constexpr int s = sc.value;
        auto fill = [&](int i) {
          if constexpr (s < 6) piece(s + 2, i);
          if constexpr (s == 6) {
            if (i == 0) { stat_load(); bias_load(0); }
            else if (i < 5) { if (heavy) w1dma(1, (i - 1) >> 1, lds0 + L_WA + ((i - 1) >> 1) * 8192, (i - 1) & 1); }
            else wdmah((i - 5) >> 1, (i - 5) & 1);
          }
          if constexpr (s == 7) {
            if (heavy && i < 4) w1dma(1, 2 + (i >> 1), lds0 + L_WA + (2 + (i >> 1)) * 8192, i & 1);
            if (heavy && i >= 4) { w1dma(1, i, lds0 + L_WA + i * 8192, 0); w1dma(1, i, 0, 1); }
          }
          SBAR();
        };
        auto rd = [&](int p) {
          xf[p] = lds128(xbuf(s) + xo[p]);
          wf[p][0] = ldtr2(rk[p][0] + (s % 3) * PART, 512); wf[p][1] = ldtr2(rk[p][1] + (s % 3) * PART, 512);
          SBAR();
        };
        if constexpr (s < 7) wait_vm<9>(); else { if (heavy) wait_vm<11>(); else wait_vm<7>(); }
        bar();
        STAMP_AT(10 + s);
        rd(0);
        if constexpr (s < 7) fill(0);
        rd(1);
        if constexpr (s < 7) fill(1);
        if constexpr (s == 2) get_tag();
#pragma unroll
        for (int p = 0; p < 4; ++p) {
#pragma unroll
          for (int t = 0; t < 2; ++t) {
            hac[t] = MFMA32(wf[p][t], xf[p], hac[t]);
            if (p == 0) { cs[t] = MFMA16(sel, wf[p][t], cs[t]); PIN(cs[t]); }
            SBAR();
            if (p < 2 && t == 0) rd(p + 2);
            if (s == 7) fill(2 * p + t); else if (2 * p + t < 7) fill(2 + 2 * p + t);
          }
          if (s == 7 && p == 1) {
            cswr(0, cs[0]); cswr(1, cs[1]);
            bar();
            STAMP_AT(1);
          }
        }
        if constexpr (s == 7) {
          const f32x4 c0 = csrd(0), c1 = csrd(1);
          if (heavy) wait_vm<20>(); else wait_vm<4>();
          PIN(st); PIN(bias0); PIN(bias1);
          row_stats(); make_xaug();
          hac[0] = MFMA32(waug(c0, bias0), xaug, hac[0]);
          hac[1] = MFMA32(waug(c1, bias1), xaug, hac[1]);
          SBAR();
        }
      });
    }
    STAMP_AT(18);
    if (heavy) { wait_vm<0>(); arrive_nowait(lds0 + L_CNT + 16); }
    hgelu(std::integral_constant<int, 0>{}, [&]() {});
    __builtin_amdgcn_s_setprio(0);
    STAMP_AT(19);
    unsigned rb2[2];
#pragma unroll
    for (int c = 0; c < 2; ++c) rb2[c] = lds0 + L_WG + m * PART + h * 512 + q4 * 128 + ((c ^ (q4 >> 1)) * 64) + (g & 1) * 32 + p4 * 8;
    v4u wq[36][2];
    auto wrd = [&](int kk) {
#pragma unroll
      for (int c = 0; c < 2; ++c) wq[kk][c] = ldtr2(rb2[c] + (kk % RD) * 2048, 1024);
    };
    auto wgot = [&](int kk) { PIN(wq[kk][0]); PIN(wq[kk][1]); };
    const int ftile = 2 * rt + ((lane >> 2) & 1);
    const int T0 = 4 * r - 8 + 2 * m;
    const bool live = T0 >= 0;
    auto kv_fetch = [&]() {
      const unsigned src = WS_KV + (j * 64 + (live ? T0 : 0)) * 4096, vk = live ? lane * 16 : h * 512, vv = live ? lane * 16 : 0x40000000u;
#pragma unroll
      for (int i = 0; i < 8; ++i)
        dma16_sc1(sws, (i & 2) ? vv : vk, src + (live ? i : (i & 3)) * 1024, lds0 + L_KV + (2 * m + (i >> 2)) * KVT + (i & 3) * 1024 + ((i & 3) == 3 ? VB - 1024 : 0));
      const unsigned on = (lane < 2 || live) ? one2 : 0u;
      if (lane < 6) sts128(lds0 + L_KV + (lane < 2 ? 8 + m : 2 * m + ((lane - 2) >> 1)) * KVT + 2048 + (lane & 1) * VB + 1024, (v4u){on, on, on, on});
    };
    v4u af[28][2], of[8][2];
    v2u fl;
    const unsigned a0 = WS_AF + (2 * rt * AFT + 28 * m) * 1024, o0 = WS_AF + (2 * rt * AFT + 112 + 8 * m) * 1024;
    fl = ld8_sc1_async(sws, flag_off(FL_KV, live ? T0 + (lane & 1) : 0, j));
    for (int kk = 2; kk < 6; ++kk) wdma(kk);
    wait_vmf<8>(fl);
    if (lane == 0) __hip_atomic_store(flag(FL_H0, T, j), tag, RLX_AGENT);
    STAMP_AT(2);
    if (!__all(fl[0] == taglo && fl[1] == tag32)) poll_flags(flag(FL_KV, live ? T0 + (lane & 1) : 0, j), tag);
    if (heavy) await(lds0 + L_CNT + 20, 4);
    kv_fetch();
    wdma(6); wdma(7);
    static_for<0, 3>([&](auto ic) {
      constexpr int i = ic.value;
      wait_vm<(i == 0) ? 17 : 8>();
      for (int kk = 4 * i; kk < 4 * i + 4; ++kk) wrd(kk);
      for (int kk = 4 * i; kk < 4 * i + 4; ++kk) wgot(kk);
      if constexpr (i == 0) { wait_vm<4>(); arrive_nowait(lds0 + L_CNT); }
      if constexpr (i == 2) fl = ld8_sc1_async(sws, flag_off(FL_H0, ftile, 4 * m + p4));
      for (int kk = 4 * i; kk < 4 * i + 4; ++kk) wdma(kk + RD);
    });
    STAMP_AT(3);
    wait_vmf<8>(fl);
    if (!__all(fl[0] == taglo && fl[1] == tag32)) poll_flags(flag(FL_H0, ftile, 4 * m + p4), tag);
    STAMP_AT(4);
#pragma unroll
    for (int kk = 0; kk < AW; ++kk) {
      af[kk][0] = ld16_sc1_async(sws, lane * 16, a0 + kmap(kk) * 1024);
      af[kk][1] = ld16_sc1_async(sws, lane * 16, a0 + (AFT + kmap(kk)) * 1024);
    }
    __builtin_amdgcn_s_setprio(PRIO_G2);
    f32x16 c2[2][2] = {};
    auto mm2 = [&](int kk, const v4u (&a)[2], auto&& fill) {
      SBAR();
#pragma unroll
      for (int i = 0; i < 4; ++i) { c2[i >> 1][i & 1] = MFMA32(wq[kk][i & 1], a[i >> 1], c2[i >> 1][i & 1]); SBAR(); fill(i); SBAR(); }
    };
    static_for<0, 28>([&](auto kc) {
      constexpr int kk = kc.value;
      wait_vm2<(kk < AW) ? 2 * (AW - 1 - kk) + g2_range(0, kk) : g2_range(kk - AW + 1, kk)>(af[kk][0], af[kk][1]);
      if constexpr (kk == H1_CK) {
        STAMP_AT(20);
        wait_vmf<g2_range(H1_LD, H1_CK) - 1>(fl);
        if (!__all(fl[0] == taglo && fl[1] == tag32)) poll_flags(flag(FL_H1, ftile, 4 * m + p4), tag);
        STAMP_AT(21);
      }
      if constexpr (kk == H1_LD) fl = ld8_sc1_async(sws, flag_off(FL_H1, ftile, 4 * m + p4));
      if constexpr (kk == O_LD) fl = ld8_sc1_async(sws, flag_off(FL_O0, ftile, 4 * m + p4));
      if constexpr (kk == 26) fl = ld8_sc1_async(sws, flag_off(FL_O1, ftile, 4 * m + p4));
      constexpr bool dw = kk >= 1 && kk - 1 + NR + RD < 36;
      if constexpr (dw) wgot(kk - 1 + NR);
      mm2(kk, af[kk], [&](int i) {
        if constexpr (dw) { if (i < 2) wdmah(kk - 1 + NR + RD, i); }
        if constexpr (kk + NR < 28) { if (i == 1) wrd(kk + NR); }
        if constexpr (kk == 27) { if (i == 0) wrd(28); }
        if constexpr (kk + AW < 28) { if (i >= 2) af[kk + AW][i - 2] = ld16_sc1_async(sws, lane * 16, a0 + (AFT * (i - 2) + kmap(kk + AW)) * 1024); }
      });
      if constexpr (kk == O_CK) {
        wait_vmf<g2_range(O_LD, O_CK) - 1>(fl);
        if (!__all(fl[0] == taglo && fl[1] == tag32)) poll_flags(flag(FL_O0, ftile, 4 * m + p4), tag);
        res_loads();
#pragma unroll
        for (int i = 0; i < 4; ++i) {
          of[i][0] = ld16_sc1_async(sws, lane * 16, o0 + omap(i) * 1024);
          of[i][1] = ld16_sc1_async(sws, lane * 16, o0 + (AFT + omap(i)) * 1024);
        }
      }
    });
    static_for<0, 4>([&](auto kc) {
      constexpr int kk = kc.value;
      if (kk < 3) wrd(28 + kk + 1);
      if constexpr (kk == 1) {
        res_sum();
        STAMP_AT(5);
        wait_vmf<0>(fl);
        if (!__all(fl[0] == taglo && fl[1] == tag32)) poll_flags(flag(FL_O1, ftile, 4 * m + p4), tag);
        STAMP_AT(6);
#pragma unroll
        for (int i = 0; i < 8; ++i) of[4 + (i & 3)][i >> 2] = ld16_sc1_async(sws, lane * 16, o0 + (AFT * (i >> 2) + omap(4 + (i & 3))) * 1024);
      }
      wait_vm2<(kk == 0) ? 7 : 14 - 2 * kk>(of[kk][0], of[kk][1]);
      mm2(28 + kk, of[kk], [&](int i) { PIN(c2[i >> 1][i & 1]); if (kk == 3) wrd(32 + i); });
    });
    for (int kk = 32; kk < 36; ++kk) wgot(kk);
    unsigned pa = lds0 + L_WG + m * PART + lane * 16;
    PIN(pa);
    auto put = [&](int qn, int qd) {
      const f32x16& c = c2[qn >> 1][qn & 1];
      sts128(pa + (qn * 4 + qd) * 66 * 16, __builtin_bit_cast(v4u, (f32x4){c[4 * qd], c[4 * qd + 1], c[4 * qd + 2], c[4 * qd + 3]}));
    };
    static_for<0, 4>([&](auto qc) {
      constexpr int qn = qc.value;
      if constexpr ((qn & 1) == 0) { wait_vm2<qn ? 0 : 4>(of[4][qn >> 1], of[5][qn >> 1]); PIN(of[6][qn >> 1]); PIN(of[7][qn >> 1]); }
      SBAR();
#pragma unroll
      for (int i = 0; i < 4; ++i) {
        c2[qn >> 1][qn & 1] = MFMA32(wq[32 + i][qn & 1], of[4 + i][qn >> 1], c2[qn >> 1][qn & 1]);
        SBAR();
        if (qn > 0 && i >= 2) { put(qn - 1, 2 * i - 4); put(qn - 1, 2 * i - 3); }
        SBAR();
      }
    });
#pragma unroll
    for (int qd = 0; qd < 4; ++qd) put(3, qd);
    STAMP_AT(7);
  } else {
    __builtin_amdgcn_s_setprio(1);
    lane_setup();
    v4u xr[2];
    xr[0] = ld16_async(sx, lane * 16, (128 * r + 8 * j + 2 * m) * 1024); xr[1] = ld16_async(sx, lane * 16, (128 * r + 8 * j + 2 * m + 1) * 1024);
    unsigned warm;
    {
      u64 pc; asm volatile("s_getpc_b64 %0" : "=s"(pc));
      warm = ld2_async(make_srd((const void*)(pc & ~127ull), CODEWARM), lane < 3 ? lane * 128 : 0x40000000u, (idx * 4 + m) * 384);
    }
    __builtin_amdgcn_s_barrier();
    v2u ep = ld8_sc1_async(sws, m == 0 ? WS_EPOCH : 0x40000000u);
    float sval;
    const int wi = blockIdx.x * 4 + m;
    const u64 wbase = wi < 448 ? (u64)w1 + wi * 4096 : wi < 896 ? (u64)w2 + (wi - 448) * 4096 : (u64)wo + (wi - 896) * 4096;
    auto make_stats = [&]() {
      wait_vm2<1>(xr[0], xr[1]);
      PIN(warm);
      float piv[2], a[4];
#pragma unroll
      for (int u = 0; u < 2; ++u) {
        piv[u] = rdl(bflo(xr[u][0]), 0);
        float s1 = 0.f, s2 = 0.f;
#pragma unroll
        for (int i = 0; i < 8; ++i) { const float d = ((i & 1) ? bfhi(xr[u][i >> 1]) : bflo(xr[u][i >> 1])) - piv[u]; s1 += d; s2 = __builtin_fmaf(d, d, s2); }
        a[2 * u] = s1; a[2 * u + 1] = s2;
      }
#pragma unroll
      for (int i = 0; i < 4; ++i) a[i] += dpp<0xB1>(a[i]);
#pragma unroll
      for (int i = 0; i < 4; ++i) a[i] += dpp<0x4E>(a[i]);
      float t = p4 == 0 ? a[0] : p4 == 1 ? a[1] : p4 == 2 ? a[2] : a[3];
      t += dpp<0x114>(t); t += dpp<0x118>(t);
      const auto s16 = __builtin_amdgcn_permlane16_swap(asu(t), asu(t), false, false);
      t = asf(s16[0]) + asf(s16[1]);
      const auto s32 = __builtin_amdgcn_permlane32_swap(asu(t), asu(t), false, false);
      t = (asf(s32[0]) + asf(s32[1])) * (1.f / DM);
      const float e1 = dpp<0xA0>(t);
      sval = (p4 & 1) ? __builtin_fmaxf(__builtin_fmaf(-e1, e1, t), 0.f) + 1e-5f : t + (p4 ? piv[1] : piv[0]);
      PIN(sval);
    };
    const unsigned acnt = lds0 + L_CNT;
    const unsigned vpos = 2048 + (g & 1) * 512 + h * 256 + (lane & 15) * 16;
    v4u qf[2], am0, am1;
    u64 um[16];
    auto masks = [&]() {
      const int dq = r32 - 4 * h;
#pragma unroll
      for (int u = 0; u < 16; ++u) { um[u] = __builtin_amdgcn_ballot_w64((u & 3) + 8 * (u >> 2) > dq); pin_s(um[u]); }
#pragma unroll
      for (int d = 0; d < 4; ++d) {
        am0[d] = __builtin_amdgcn_inverse_ballot_w64(um[2 * d]) ? 0xffffffffu : __builtin_amdgcn_inverse_ballot_w64(um[2 * d + 1]) ? 0xffff0000u : 0u;
        am1[d] = __builtin_amdgcn_inverse_ballot_w64(um[8 + 2 * d]) ? 0xffffffffu : __builtin_amdgcn_inverse_ballot_w64(um[9 + 2 * d]) ? 0xffff0000u : 0u;
      }
      PIN(am0); PIN(am1);
    };
    {
      v4u xf[32];
      f32x16 acc[3] = {};
      f32x4 cs[3] = {};
      v4u wf[4][3];
      unsigned wb[4];
#pragma unroll
      for (int p = 0; p < 4; ++p) { wb[p] = qbuf(0) + ((p + m) & 3) * 1024 + (8 * h + q4) * 64 + (g & 1) * 32 + p4 * 8; PIN(wb[p]); }
      auto rd = [&](int ks, int t) {
        if (t == 0) xf[ks] = lds128(xbuf(ks >> 2) + xo[ks & 3]);
        wf[ks & 3][t] = ldtr2(wb[ks & 3] + ((ks >> 2) % 3) * 12288 + t * 4096, 256);
      };
      auto mm = [&](int ks, int t) {
        acc[t] = t == 2 ? MFMA32(xf[ks], wf[ks & 3][t], acc[t]) : MFMA32(wf[ks & 3][t], xf[ks], acc[t]);
        if ((ks & 3) == 0) { cs[t] = MFMA16(sel, wf[ks & 3][t], cs[t]); PIN(cs[t]); }
        SBAR();
      };
      make_stats();
      bar();
      STAMP_AT(10);
#pragma unroll
      for (int t = 0; t < 6; ++t) rd(t / 3, t % 3);
      SBAR();
      static_for<0, 8>([&](auto sc) {
        constexpr int s = sc.value;
#pragma unroll
        for (int p = 0; p < 2; ++p) {
#pragma unroll
          for (int t = 0; t < 3; ++t) { mm(4 * s + p, t); rd(4 * s + p + 2, t); SBAR(); }
          if (s == 7 && p == 0) { cswr(4, cs[0]); cswr(5, cs[1]); cswr(6, cs[2]); SBAR(); }
        }
        if constexpr (s == 0) {
          wait_vmf<0>(ep);
          if (m == 0 && lane < 10)
            *(LAS unsigned*)(size_t)(lds0 + L_CNT + lane * 4) = lane == 8 ? ep[0] * 0x9E3779B1u + 0x7F4A7C15u : lane == 9 ? ep[1] * 0x85EBCA77u + 0x165667B1u : 0u;
        }
        if constexpr (s == 1) {
          get_tag();
          if ((lane >> 2) == 3) {
            const v2u gr = {asu(sval), tag32};
            __builtin_amdgcn_raw_buffer_store_b64(gr, rws, WS_STATS + (128 * r + 8 * j + 2 * m) * 16 + p4 * 8, 0, 16);
          }
        }
        if constexpr (s == MIDWARM) {
          warm = ld2_async(make_srd((const void*)wbase, 4096), lane * 128, 0);
        }
        if constexpr (s == 5) stat_load();
        if constexpr (s == 6) {
          wait_vm1<0>(st);
          PIN(warm);

          row_stats();
        }
        mm(4 * s + 2, 0); mm(4 * s + 2, 1);
        bar();
        if constexpr (s < 7) {
          STAMP_AT(11 + s);
          mm(4 * s + 2, 2);
          rd(4 * s + 4, 0); rd(4 * s + 4, 1); rd(4 * s + 4, 2); SBAR();
#pragma unroll
          for (int t = 0; t < 3; ++t) { mm(4 * s + 3, t); rd(4 * s + 5, t); SBAR(); }
        }
      });
      STAMP_AT(1);
      __builtin_amdgcn_s_setprio(3);
      const f32x4 c5 = csrd(5), c6 = csrd(6), c4 = csrd(4);
      SBAR();
      mm(30, 2); mm(31, 1);
      make_xaug();
      SBAR();
      mm(31, 2); mm(31, 0);
      if (h == 0) *(LAS float*)(size_t)(lds0 + L_RS + (m * 32 + r32) * 4) = rstd;
      acc[1] = MFMA32(waug(c5, 0u), xaug, acc[1]);
      acc[2] = MFMA32(xaug, waug(c6, 0u), acc[2]);
      acc[0] = MFMA32(waug(c4, 0u), xaug, acc[0]);
      SBAR();
      f32x4 rs[4];
#pragma unroll
      for (int qd = 0; qd < 4; ++qd) rs[qd] = __builtin_bit_cast(f32x4, lds128(lds0 + L_RS + (m * 32 + 8 * qd + 4 * h) * 4));
      const unsigned kvo = WS_KV + (j * 64 + T) * 4096;
#pragma unroll
      for (int e = 0; e < 16; ++e) acc[1][e] = MUL1(acc[1][e], rstd);
#pragma unroll
      for (int e = 0; e < 2; ++e) {
        const v4u f = pack8(acc[1], e);
        st16_sc1(f, sws, lane * 16, kvo + e * 1024);
        sts128(lds0 + L_KV + (8 + m) * KVT + e * 1024 + lane * 16, f);
      }
#pragma unroll
      for (int qd = 0; qd < 4; ++qd)
#pragma unroll
        for (int e = 0; e < 4; ++e) acc[2][4 * qd + e] = MUL1(acc[2][4 * qd + e], rs[qd][e]);
#pragma unroll
      for (int s = 0; s < 2; ++s) {
        const v4u f = pack8(acc[2], s);
        st16_sc1(f, sws, vpos, kvo + s * 1024);
        sts128(lds0 + L_KV + (8 + m) * KVT + s * VB + vpos, f);
      }
      SBAR();
      const float qs = rstd * (0.25f * 1.4426950408889634f);
#pragma unroll
      for (int e = 0; e < 16; ++e) acc[0][e] = MUL1(acc[0][e], qs);
      qf[0] = pack8(acc[0], 0); qf[1] = pack8(acc[0], 1);
      PIN(qf[0]); PIN(qf[1]);
      arrive_nowait(acnt);
      STAMP_AT(2);
      if (heavy) {
        hsetup(1);
        await(acnt + 16, 4);
        __builtin_amdgcn_s_setprio(1);
        STAMP_AT(3);
        hmm(std::integral_constant<int, 1>{}, xf, [&](auto kc) {
          constexpr int ks = kc.value;
          if constexpr (ks == KVPUB) {
            wait_vm<0>();
            if (lane == 0) __hip_atomic_store(flag(FL_KV, T, j), tag, RLX_AGENT);
          }
          if constexpr (ks == KVPUB + 2) bias_load(1);
          if constexpr (ks == 31) { wait_vm<0>(); PIN(bias0); PIN(bias1); }
        });
        STAMP_AT(18);
        __builtin_amdgcn_s_setprio(3);
        hgelu(std::integral_constant<int, 1>{}, [&]() {});
        STAMP_AT(4);
      } else {
        wait_vm<0>();
        if (lane == 0) __hip_atomic_store(flag(FL_KV, T, j), tag, RLX_AGENT);
      }
    }
    masks();
    STAMP_AT(19);
    const unsigned kb = lds0 + L_KV + m * KVT + lane * 16;
    unsigned vb[2];
#pragma unroll
    for (int e = 0; e < 2; ++e) { vb[e] = lds0 + L_KV + m * KVT + 2048 + ((g & 1) ? 1024 : e * 512 + h * 256 + (lane & 15) * 16); PIN(vb[e]); }
    bool pend = true;
    if (__builtin_amdgcn_readfirstlane(peek(acnt)) < 8) {
      wait_vm<0>();
      if (lane == 0) __hip_atomic_store(flag(FL_H1, T, j), tag, RLX_AGENT);
      pend = false;
      await(acnt, 8);
    }
    asm volatile("" ::: "memory");
    __builtin_amdgcn_s_setprio(PRIO_ATT);
    STAMP_AT(5);
    const f32x16 zero = {};
    auto o_store = [&](f32x16& o, int e) {
      const float rl = __builtin_amdgcn_rcpf(o[8]);
#pragma unroll
      for (int u = 0; u < 8; ++u) o[u] = MUL1(o[u], rl);
      st16_sc1(pack8(o, 0), sws, lane * 16, WS_AF + (T * AFT + 112 + 2 * j + e) * 1024);
    };
    int redo = 0;
    {
      f32x16 o[2], sa, sb, su;
      v4u kn, ku, va, vc, vn0, vn1, vu0, vu1, p0, p1, pu;
      auto kaddr = [&](int n) { return kb + ((n & 7) + 1) * KVT + (n >> 3) * 1024; };
      auto vaddr = [&](int n) { return vb[n >> 3] + ((n & 7) + 1) * KVT; };
      kn = lds128(kaddr(0)); vn0 = lds128(vaddr(0)); vn1 = lds128(vaddr(0) + VB);
      sa = MFMA32(kn, qf[0], zero);
      kn = lds128(kaddr(1));
      SBAR();
      auto step = [&](auto nc, f32x16& sc, f32x16& sn) {
        constexpr int n = nc.value, e = n >> 3, i = n & 7;
        if constexpr (n < 15) sn = MFMA32(kn, qf[(n + 1) >> 3], zero);
        if constexpr (n < 14) kn = lds128(kaddr(n + 2));
        if constexpr (i == 5) ku = lds128(kb + e * 1024);
        v4u v0 = vn0, v1 = vn1;
        if constexpr (n < 15) { vn0 = lds128(vaddr(n + 1)); vn1 = lds128(vaddr(n + 1) + VB); }
        if constexpr (i == 6) { vu0 = lds128(vb[e]); vu1 = lds128(vb[e] + VB); }
        SBAR();
        if constexpr (i == 7) {
#pragma unroll
          for (int u = 0; u < 16; ++u) sc[u] = __builtin_amdgcn_inverse_ballot_w64(um[u]) ? su[u] : sc[u];
          PIN(sc); SBAR();
        }
#pragma unroll
        for (int u = 0; u < 6; ++u) sc[u] = EXP2(sc[u]);
        PIN(sc); SBAR();
        if constexpr (n == 8) { o[0] = MFMA32(vc, p1, o[0]); SBAR(); }
        else if constexpr (n > 0) { o[e] = MFMA32(vc, p1, o[e]); SBAR(); }
        sc[6] = EXP2(sc[6]); sc[7] = EXP2(sc[7]);
        p0 = pack8(sc, 0);
        sc[8] = EXP2(sc[8]); sc[9] = EXP2(sc[9]);
        if constexpr (i == 7) { va = p0 & am0; p0 &= ~am0; PIN(va); }
        PIN(sc); PIN(p0); SBAR();
        if constexpr (n == 8) { o[0] = MFMA32(vu1, pu, o[0]); SBAR(); }
        o[e] = MFMA32(v0, p0, i == 0 ? zero : o[e]);
        SBAR();
        if constexpr (i == 6) { su = MFMA32(ku, qf[e], zero); SBAR(); }
#pragma unroll
        for (int u = 10; u < 13; ++u) sc[u] = EXP2(sc[u]);
        if constexpr (i == 7) { PIN(sc); SBAR(); o[e] = MFMA32(vu0, va, o[e]); SBAR(); }
#pragma unroll
        for (int u = 13; u < 16; ++u) sc[u] = EXP2(sc[u]);
        p1 = pack8(sc, 1); vc = v1;
        if constexpr (i == 7) { pu = p1 & am1; p1 &= ~am1; PIN(pu); }
        PIN(p1); SBAR();
        if constexpr (n == 8) {
          if (__all(o[0][8] >= 0x1p-30f && o[0][8] <= 0x1p30f)) o_store(o[0], 0); else redo = 1;
          SBAR();
        }
        if constexpr (n == 10) pend = !redo;
        if constexpr (i == 3 || i == 5 || i == 7) {
          if (pend && (i == 7 || (__builtin_amdgcn_s_getreg(7 | (3 << 11)) & 15) == 0)) {
            wait_vm<0>();
            if (lane == 0) __hip_atomic_store(flag(FL_H1 + e, T, j), tag, RLX_AGENT);
            pend = false;
          }
        }
      };
      static_for<0, 8>([&](auto hc) {
        step(std::integral_constant<int, 2 * hc.value>{}, sa, sb);
        step(std::integral_constant<int, 2 * hc.value + 1>{}, sb, sa);
      });
      o[1] = MFMA32(vc, p1, o[1]);
      o[1] = MFMA32(vu1, pu, o[1]);
      if (__all(o[1][8] >= 0x1p-30f && o[1][8] <= 0x1p30f)) o_store(o[1], 1); else redo |= 2;
    }
    if (redo) {
#pragma unroll 1
      for (int e = 0; e < 2; ++e) {
        if (!((redo >> e) & 1)) continue;
        const v4u q = e ? qf[1] : qf[0];
        const unsigned vbe = e ? vb[1] : vb[0];
        auto merged = [&](const f32x16& c) {
          f32x16 sl = MFMA32(lds128(kb + 8 * KVT + e * 1024), q, c);
          const f32x16 su = MFMA32(lds128(kb + e * 1024), q, c);
          static_for<0, 16>([&](auto u) { sl[u.value] = upper<u.value>() ? su[u.value] : sl[u.value]; });
          return sl;
        };
        float mx = -INFINITY;
#pragma unroll 1
        for (int i = 0; i < 8; ++i) {
          const f32x16 sc = i ? MFMA32(lds128(kb + i * KVT + e * 1024), q, zero) : merged(zero);
#pragma unroll
          for (int u = 0; u < 16; u += 2) mx = max3(mx, sc[u], sc[u + 1]);
        }
        const auto sw = __builtin_amdgcn_permlane32_swap(asu(mx), asu(mx), false, false);
        mx = __builtin_fmaxf(asf(sw[0]), asf(sw[1]));
        f32x16 negm, o;
#pragma unroll
        for (int u = 0; u < 16; ++u) negm[u] = -mx;
        {
          f32x16 sc = merged(negm);
#pragma unroll
          for (int u = 0; u < 16; ++u) sc[u] = EXP2(sc[u]);
          const v4u p0 = pack8(sc, 0), p1 = pack8(sc, 1);
          o = MFMA32(lds128(vbe), p0 & am0, zero);
          o = MFMA32(lds128(vbe + VB), p1 & am1, o);
          o = MFMA32(lds128(vbe + 8 * KVT), p0 & ~am0, o);
          o = MFMA32(lds128(vbe + 8 * KVT + VB), p1 & ~am1, o);
        }
#pragma unroll 1
        for (int i = 1; i < 8; ++i) {
          f32x16 sc = MFMA32(lds128(kb + i * KVT + e * 1024), q, negm);
#pragma unroll
          for (int u = 0; u < 16; ++u) sc[u] = EXP2(sc[u]);
          o = MFMA32(lds128(vbe + i * KVT), pack8(sc, 0), o);
          o = MFMA32(lds128(vbe + i * KVT + VB), pack8(sc, 1), o);
        }
        o_store(o, e);
      }
      wait_vm<0>();
      if (lane == 0) __hip_atomic_store(flag(FL_O0, T, j), tag, RLX_AGENT);
    }
    STAMP_AT(6);
    wait_vm<0>();
    if (lane == 0) __hip_atomic_store(flag(FL_O1, T, j), tag, RLX_AGENT);
    STAMP_AT(7);
    res_loads();
    wait_vm<0>();
    res_sum();
    if (blockIdx.x == 0 && wave == 7) {
#pragma unroll 1
      for (int i = 0; i < 4; ++i) poll_flags(flag(FL_KV, 4 * (4 * i + g), lane & 15), tag);
      if (lane == 0) __hip_atomic_store((GAS u64*)(ws + WS_EPOCH), tag, RLX_AGENT);
    }
  }
  {
    int t2 = threadIdx.x; PIN(t2);
    const int row = t2 >> 3, ch = t2 & 7;
    unsigned a = lds0 + L_WG + (((row >> 5) * 8 + ch) * 66 + (row & 31)) * 16, oo = (64 * rt + row) * 1024 + (64 * ct + ch * 8) * 2;
    PIN(a); PIN(oo);
    bar();
    f32x4 pl[4], ph[4];
#pragma unroll
    for (int w = 0; w < 4; ++w) { pl[w] = __builtin_bit_cast(f32x4, lds128(a + w * PART)); ph[w] = __builtin_bit_cast(f32x4, lds128(a + w * PART + 512)); }
    SBAR();
#pragma unroll
    for (int w = 0; w < 4; ++w) { lo += pl[w]; hi += ph[w]; }
    const v4u o = {pk2(lo[0], lo[1]), pk2(lo[2], lo[3]), pk2(hi[0], hi[1]), pk2(hi[2], hi[3])};
    __builtin_amdgcn_raw_buffer_store_b128(o, __builtin_amdgcn_make_buffer_rsrc(out, 0, SEQ * DM * 2, 0x00020000), oo, 0, 16);
  }
  STAMP_AT(8);
}

extern "C" void kernel_launch(void* const* d_in, const int* in_sizes, int n_in, void* d_out, int out_size, void* d_ws, size_t ws_size,
                              hipStream_t stream) {
  (void)in_sizes; (void)n_in; (void)out_size; (void)ws_size;
  (void)hipFuncSetAttribute((const void*)fused_block, hipFuncAttributeMaxDynamicSharedMemorySize, LDS_BYTES);
  fused_block<<<256, 512, LDS_BYTES, stream>>>(d_in[0], d_in[1], d_in[2], d_in[4], d_in[5], d_in[6], d_in[7], d_out, (char*)d_ws);
}
```

```cpp
#define KVS 1
#include <hip/hip_runtime.h>
#include <hip/hip_bf16.h>
#include <stdint.h>
#include <type_traits>

typedef unsigned v4u __attribute__((ext_vector_type(4)));
typedef unsigned v2u __attribute__((ext_vector_type(2)));
typedef float f32x2 __attribute__((ext_vector_type(2)));
typedef float f32x4 __attribute__((ext_vector_type(4)));
typedef float f32x16 __attribute__((ext_vector_type(16)));
typedef short bf16x8 __attribute__((ext_vector_type(8)));
typedef short s16x4 __attribute__((ext_vector_type(4)));
typedef __bf16 bf16x2_t __attribute__((ext_vector_type(2)));
typedef unsigned long long u64;
#define GAS __attribute__((address_space(1)))
#define LAS __attribute__((address_space(3)))
#define DI __device__ __forceinline__
#define RLX_AGENT __ATOMIC_RELAXED, __HIP_MEMORY_SCOPE_AGENT

constexpr int SEQ = 2048, DM = 512, FF = 1792;
constexpr unsigned WS_EPOCH = 0;
constexpr unsigned WS_STATS = 32768;
constexpr unsigned WS_FLAG = 65536;
constexpr unsigned WS_KV = WS_FLAG + 5 * 1024 * 128;
constexpr int AFT = 145;
constexpr unsigned WS_AF = WS_KV + 16 * 64 * 4096;
constexpr unsigned WS_END = WS_AF + 64 * AFT * 1024;
constexpr unsigned WS_STAMP = WS_END;
enum { FL_KV = 0, FL_H0 = 1, FL_H1 = 2, FL_O0 = 3, FL_O1 = 4 };
constexpr int L_WA = 0;
constexpr int L_WG = 86016;
constexpr int VB = 1040;
constexpr int KVT = 2048 + 2 * VB;
constexpr int L_KV = 32768;
constexpr int RD = 8, NR = 12;
constexpr int PART = 16 * 66 * 16;
constexpr int L_CS = L_WG + 4 * PART;
constexpr int L_RS = L_CS + 3584;
constexpr int L_CNT = L_RS + 512;
constexpr int LDS_BYTES = L_CNT + 64;
constexpr int xbuf(int s) { return ((s + 1) % 3) * 16384; }
constexpr int qbuf(int s) { return 49152 + (s % 3) * 12288; }
constexpr int w1buf(int s) { return L_WG + (s % 3) * PART + 8192; }
static_assert(L_KV + 8 * KVT >= 65536 && xbuf(7) == 32768 && qbuf(2) + 12288 <= L_WG && L_KV + 12 * KVT <= L_WG && PART >= RD * 2048 && LDS_BYTES <= 163840, "lds map");
#define AWIN 6
constexpr int AW = AWIN;
constexpr unsigned SPIN_CAP = 1u << 23;

#define MFMA32(a, b, c) __builtin_amdgcn_mfma_f32_32x32x16_bf16(__builtin_bit_cast(bf16x8, a), __builtin_bit_cast(bf16x8, b), c, 0, 0, 0)
#define MFMA16(a, b, c) __builtin_amdgcn_mfma_f32_16x16x32_bf16(__builtin_bit_cast(bf16x8, a), __builtin_bit_cast(bf16x8, b), c, 0, 0, 0)
#define SBAR() __builtin_amdgcn_sched_barrier(0)

#define STAMP_AT(i) do { } while (0)

DI float asf(unsigned u) { return __builtin_bit_cast(float, u); }
DI unsigned asu(float f) { return __builtin_bit_cast(unsigned, f); }
DI unsigned pk2(float lo, float hi) { return __builtin_bit_cast(unsigned, __builtin_convertvector((f32x2){lo, hi}, bf16x2_t)); }
DI float bflo(unsigned u) { return __builtin_bit_cast(float, u << 16); }
DI float bfhi(unsigned u) { return __builtin_bit_cast(float, u & 0xffff0000u); }
DI v4u make_srd(const void* p, unsigned bytes) { const u64 a = (u64)p; return (v4u){(unsigned)a, (unsigned)(a >> 32) & 0xffffu, bytes, 0x00020000u}; }
DI void dma16(v4u srd, unsigned voff, unsigned soff, unsigned lds) {
  asm volatile("s_mov_b32 m0, %3\n\ts_nop 0\n\tbuffer_load_dwordx4 %0, %1, %2 offen lds" :: "v"(voff), "s"(srd), "s"(soff), "s"(lds) : "memory");
}
DI void dma16_sc1(v4u srd, unsigned voff, unsigned soff, unsigned lds) {
  asm volatile("s_mov_b32 m0, %3\n\ts_nop 0\n\tbuffer_load_dwordx4 %0, %1, %2 offen sc1 lds" :: "v"(voff), "s"(srd), "s"(soff), "s"(lds) : "memory");
}
DI void dma16_a(v4u srd, unsigned voff, unsigned soff, unsigned lds) { dma16(srd, voff, soff, lds); }
DI void dma16_b(v4u srd, unsigned voff1, unsigned soff) {
  asm volatile("buffer_load_dwordx4 %0, %1, %2 offen offset:1024 lds" :: "v"(voff1), "s"(srd), "s"(soff) : "memory");
}
DI v4u ld16_sc1_async(v4u srd, unsigned voff, unsigned soff) {
  v4u r; asm volatile("buffer_load_dwordx4 %0, %1, %2, %3 offen sc1" : "=v"(r) : "v"(voff), "s"(srd), "s"(soff) : "memory"); return r;
}
DI v4u ld16_async(v4u srd, unsigned voff, unsigned soff) {
  v4u r; asm volatile("buffer_load_dwordx4 %0, %1, %2, %3 offen" : "=v"(r) : "v"(voff), "s"(srd), "s"(soff) : "memory"); return r;
}
DI unsigned ld2_async(v4u srd, unsigned voff, unsigned soff) {
  unsigned r; asm volatile("buffer_load_ushort %0, %1, %2, %3 offen" : "=v"(r) : "v"(voff), "s"(srd), "s"(soff) : "memory"); return r;
}
DI void st16_sc1(v4u v, v4u srd, unsigned voff, unsigned soff) {
  asm volatile("buffer_store_dwordx4 %0, %1, %2, %3 offen sc1\n\ts_nop 1" :: "v"(v), "v"(voff), "s"(srd), "s"(soff) : "memory");
}
template <int N> DI void wait_vm1(v4u& a) { asm volatile("s_waitcnt vmcnt(%1)" : "+v"(a) : "i"(N) : "memory"); }
template <int N> DI void wait_vm2(v4u& a, v4u& b) { asm volatile("s_waitcnt vmcnt(%2)" : "+v"(a), "+v"(b) : "i"(N) : "memory"); }
template <int N> DI void wait_vm3(v4u& a, v4u& b, v4u& c) { asm volatile("s_waitcnt vmcnt(%3)" : "+v"(a), "+v"(b), "+v"(c) : "i"(N) : "memory"); }
DI void pin_s(u64& v) { asm volatile("" : "+s"(v)); }
template <class V> DI void pin(V& v) { asm volatile("" : "+v"(v)); }
template <int N> DI void wait_vm() { asm volatile("s_waitcnt vmcnt(%0)" :: "i"(N) : "memory"); }
DI void bar() { asm volatile("s_waitcnt lgkmcnt(0)\n\ts_barrier" ::: "memory"); }
DI v4u lds128(unsigned a) { return *(const LAS v4u*)(size_t)a; }
DI void sts128(unsigned a, v4u v) { *(LAS v4u*)(size_t)a = v; }
DI v2u ldtr(unsigned a) { return __builtin_bit_cast(v2u, __builtin_amdgcn_ds_read_tr16_b64_v4i16((LAS s16x4*)(size_t)a)); }
DI v4u ldtr2(unsigned a, int second) { const v2u lo = ldtr(a), hi = ldtr(a + second); return (v4u){lo.x, lo.y, hi.x, hi.y}; }
template <int CTRL> DI float dpp(float v) { return __builtin_bit_cast(float, __builtin_amdgcn_update_dpp(0, __builtin_bit_cast(int, v), CTRL, 0xf, 0xf, true)); }
DI float rdl(float v, int l) { return __builtin_bit_cast(float, __builtin_amdgcn_readlane(__builtin_bit_cast(int, v), l)); }
DI v4u pack8(const f32x16& a, int s) { return (v4u){pk2(a[8 * s], a[8 * s + 1]), pk2(a[8 * s + 2], a[8 * s + 3]), pk2(a[8 * s + 4], a[8 * s + 5]), pk2(a[8 * s + 6], a[8 * s + 7])}; }
DI void poll_flags(const GAS u64* p, u64 tag) {
#pragma unroll 1
  for (unsigned spin = 0; spin < SPIN_CAP; ++spin) {
    const u64 v = __hip_atomic_load(p, RLX_AGENT);
    if (__all(v == tag)) break;
    __builtin_amdgcn_s_sleep(1);
  }
  asm volatile("" ::: "memory");
}
constexpr u64 upper_mask(int reg) {
  u64 m = 0;
  for (int l = 0; l < 64; ++l) { const int row = (reg & 3) + 8 * (reg >> 2) + 4 * (l >> 5); if (row > (l & 31)) m |= 1ull << l; }
  return m;
}
template <int REG> DI bool upper() { return __builtin_amdgcn_inverse_ballot_w64(upper_mask(REG)); }
template <int I, int N, class Fn> DI void static_for(Fn&& f) { if constexpr (I < N) { f(std::integral_constant<int, I>{}); static_for<I + 1, N>(f); } }
DI float max3(float a, float b, float c) { return __builtin_fmaxf(__builtin_fmaxf(a, b), c); }

constexpr int H1_CK = 16 - AW, H1_LD = H1_CK - 3, O_CK = 28 - AW, O_LD = O_CK - 3;
constexpr int g2_issued(int t) { return ((t >= 1 && t - 1 + NR + RD < 36) ? 2 : 0) + ((t + AW < 28) ? 2 : 0) + (t == H1_LD) + (t == O_LD) + (t == O_CK ? 10 : 0) + (t == 26); }
constexpr int g2_range(int a, int b) { int n = 0; for (int t = a; t < b; ++t) n += g2_issued(t); return n; }
#define GELU_PK(P) (P == 1)
#define PRIO_G2 2
#define PRIO_ATT 3
#define PRIO_G1 2
#define ROT r
#define CODEWARM 38000
#define MIDWARM 1
constexpr int HLD = 9;
#define KVPUB 2
#define EXP2(v) __builtin_amdgcn_exp2f(v)
#define PIN(v) pin(v)
extern "C" __device__ float mul_unpacked(float, float) __asm("llvm.amdgcn.fmul.legacy");
#define MUL1(a, b) mul_unpacked(a, b)
#define FMA1(a, b, c) __builtin_amdgcn_div_fmasf(a, b, c, false)
DI v2u ld8_sc1_async(v4u srd, unsigned voff) {
  v2u r; asm volatile("buffer_load_dwordx2 %0, %1, %2, 0 offen sc1" : "=v"(r) : "v"(voff), "s"(srd) : "memory"); return r;
}
template <int N> DI void wait_vmf(v2u& a) { asm volatile("s_waitcnt vmcnt(%1)" : "+v"(a) : "i"(N) : "memory"); }
DI void arrive(unsigned cnt, int lane) {
  asm volatile("s_waitcnt lgkmcnt(0)" ::: "memory");
  if (lane == 0) __hip_atomic_fetch_add((LAS unsigned*)(size_t)cnt, 1u, __ATOMIC_RELAXED, __HIP_MEMORY_SCOPE_WORKGROUP);
}
DI void arrive_nowait(unsigned cnt) {
  asm volatile("s_mov_b64 exec, 1\n\tds_add_u32 %0, %1\n\ts_mov_b64 exec, -1" :: "v"(cnt), "v"(1u) : "memory");
}
DI unsigned peek(unsigned cnt) { return __hip_atomic_load((LAS unsigned*)(size_t)cnt, __ATOMIC_RELAXED, __HIP_MEMORY_SCOPE_WORKGROUP); }
DI void await(unsigned cnt, unsigned n) {
#pragma unroll 1
  for (unsigned spin = 0; spin < SPIN_CAP * 32; ++spin) {
    if (__builtin_amdgcn_readfirstlane(peek(cnt)) >= n) break;
    __builtin_amdgcn_s_sleep(1);
  }
  asm volatile("" ::: "memory");
}

__global__ __launch_bounds__(512, 2) void fused_block(const void* __restrict__ x, const void* __restrict__ wqkv, const void* __restrict__ wo,
                                                      const void* __restrict__ w1, const void* __restrict__ b1, const void* __restrict__ w2,
                                                      const void* __restrict__ b2, void* __restrict__ out, char* __restrict__ ws) {
  constexpr unsigned lds0 = 0;
  const int wave = __builtin_amdgcn_readfirstlane(threadIdx.x >> 6);
  const int m = wave & 3;
  const bool isG = wave < 4;
  const int xcd = blockIdx.x & 7, idx = blockIdx.x >> 3;
  const int r = (xcd >> 2) * 8 + (idx >> 2), j = (xcd & 3) * 4 + (idx & 3);
  const int rt = (xcd >> 1) * 8 + (idx >> 2), ct = (xcd & 1) * 4 + (idx & 3);
  int lane = threadIdx.x & 63, r32, h, g, q4, p4;
  STAMP_AT(0);
  asm volatile("" :: "s"(x), "s"(wqkv), "s"(wo), "s"(w1), "s"(b1), "s"(w2), "s"(b2), "s"(out), "s"(ws));

  const v4u sx = make_srd(x, SEQ * DM * 2), sws = make_srd(ws, WS_END);
  const __amdgpu_buffer_rsrc_t rws = __builtin_amdgcn_make_buffer_rsrc(ws, 0, WS_END, 0x00020000);
  const unsigned one2 = 0x3f803f80u;
  const int T = 4 * r + m;
  const int rot = ROT;
  auto flag_off = [&](int kind, int tile, int jj) { return WS_FLAG + ((kind * 64 + tile) * 16 + jj) * 128; };
  auto flag = [&](int kind, int tile, int jj) { return (GAS u64*)(ws + flag_off(kind, tile, jj)); };
  u64 tag;
  unsigned taglo, tag32;
  auto get_tag = [&]() {
    const v2u tg = *(const LAS v2u*)(size_t)(lds0 + L_CNT + 32);
    taglo = __builtin_amdgcn_readfirstlane(tg[0]); tag32 = __builtin_amdgcn_readfirstlane(tg[1]);
    tag = ((u64)tag32 << 32) | taglo;
  };
  v4u xres, bres, xaug, st;
  float mean, var, rstd, sd;
  v4u sel;
  unsigned xo[4], swz;
  auto lane_setup = [&]() {
    lane = threadIdx.x & 63; PIN(lane);
    r32 = lane & 31; h = lane >> 5; g = lane >> 4; q4 = (lane >> 2) & 3; p4 = lane & 3;
    const unsigned selv = ((lane & 15) == (g & 1)) ? one2 : 0u;
    sel = (v4u){selv, selv, selv, selv};
#pragma unroll
    for (int p = 0; p < 4; ++p) xo[p] = (32 * m + r32) * 128 + (((2 * ((p + m) & 3) + h) ^ ((r32 >> 1) & 7)) * 16);
    swz = ((((lane >> 2) & 1) ^ (g & 1)) * 64) + p4 * 16;
  };

  auto stat_load = [&]() { st = ld16_sc1_async(sws, WS_STATS + (128 * r + 32 * m + r32) * 16, 0); };
  auto res_loads = [&]() {
    int t2 = threadIdx.x; PIN(t2);
    const int row = 64 * rt + (t2 >> 3), col = 64 * ct + (t2 & 7) * 8;
    xres = ld16_async(sx, row * 1024 + col * 2, 0);
    bres = ld16_async(make_srd(b2, DM * 2), col * 2, 0);
  };
  f32x4 lo, hi;
  auto res_sum = [&]() {
    PIN(xres); PIN(bres);
    lo = (f32x4){bflo(xres[0]) + bflo(bres[0]), bfhi(xres[0]) + bfhi(bres[0]), bflo(xres[1]) + bflo(bres[1]), bfhi(xres[1]) + bfhi(bres[1])};
    hi = (f32x4){bflo(xres[2]) + bflo(bres[2]), bfhi(xres[2]) + bfhi(bres[2]), bflo(xres[3]) + bflo(bres[3]), bfhi(xres[3]) + bfhi(bres[3])};
    PIN(lo); PIN(hi);
  };
  auto row_stats = [&]() {
    if (!__all(st[1] == tag32 && st[3] == tag32)) {
#pragma unroll 1
      for (unsigned spin = 0; spin < SPIN_CAP; ++spin) {
        st = ld16_sc1_async(sws, WS_STATS + (128 * r + 32 * m + r32) * 16, 0);
        wait_vm1<0>(st);
        if (__all(st[1] == tag32 && st[3] == tag32)) break;
      }
    }
    mean = asf(st[0]); var = asf(st[2]);
    PIN(mean); PIN(var);
  };
  auto make_xaug = [&]() {
    PIN(mean); PIN(var);
    rstd = __builtin_amdgcn_rsqf(var); sd = var * rstd;
    const unsigned uh = pk2(-mean, sd);
    const unsigned ul = pk2(-mean - bflo(uh), FMA1(bfhi(uh), -1.f, sd));
    xaug = (v4u){(uh & 0xffffu) | (uh << 16), (ul & 0xffffu) | (uh & 0xffff0000u), ul >> 16, 0u};
    if (h) xaug = (v4u){0u, 0u, 0u, 0u};
    PIN(xaug);
  };
  auto cswr = [&](int tile, f32x4 c) {
    if (lane < 16) { LAS float* p = (LAS float*)(size_t)(lds0 + L_CS + (tile * 32 + lane) * 16 + m * 4); p[0] = c[0]; p[64] = c[1]; }
  };
  auto csrd = [&](int tile) { return __builtin_bit_cast(f32x4, lds128(lds0 + L_CS + (tile * 32 + r32) * 16)); };
  auto waug = [&](f32x4 c4, unsigned b16) {
    const float c = ((c4[0] + c4[1]) + c4[2]) + c4[3];
    const unsigned uh = pk2(c, 0.f);
    const unsigned ul = pk2(c - bflo(uh), 0.f);
    return (v4u){uh | (ul << 16), uh | (b16 << 16), b16, 0u};
  };

  const v4u sw1 = make_srd(w1, DM * FF * 2);
  const int hcol0 = 448 * (j >> 2) + 128 * (j & 3);
  const bool heavy = (j & 3) != 3;
  unsigned bias0, bias1;
  auto bias_load = [&](int P) {
    bias0 = ld2_async(make_srd(b1, FF * 2), r32 * 2, (hcol0 + 64 * P) * 2);
    bias1 = ld2_async(make_srd(b1, FF * 2), r32 * 2, (hcol0 + 64 * P + 32) * 2);
  };
  auto w1dma = [&](int P, int c, unsigned dst, int half) {
    const unsigned vw1 = (lane >> 3) * (FF * 2) + swz, vw1b = vw1 + 8 * (FF * 2) - 1024;
    const unsigned so = (64 * ((c + rot) & 7) + 16 * m) * (FF * 2) + (hcol0 + 64 * P) * 2;
    if (half == 0) dma16_a(sw1, vw1, so, dst + m * 2048); else dma16_b(sw1, vw1b, so);
  };
  f32x16 hac[2];
  unsigned rk[4][2];
  auto hsetup = [&](int P) {
#pragma unroll
    for (int p = 0; p < 4; ++p)
#pragma unroll
      for (int c = 0; c < 2; ++c) {
        rk[p][c] = lds0 + (P ? L_WA : w1buf(0)) + ((p + m) & 3) * 2048 + h * 1024 + q4 * 128 + ((c ^ (q4 >> 1)) * 64) + (g & 1) * 32 + p4 * 8;
        PIN(rk[p][c]);
      }
  };
  auto hmm = [&](auto pc, const v4u (&xf)[32], auto&& hook) {
    constexpr int P = pc.value;
    constexpr int D = 4;
    const unsigned cnt = lds0 + L_CNT + 4;
    f32x4 cs[2] = {}, c0, c1;
    v4u wf[D][2], au0, au1;
    unsigned seen = 0;
    hac[0] = (f32x16){}; hac[1] = (f32x16){};
    auto rd = [&](int ks, int t) { wf[ks % D][t] = ldtr2(rk[ks & 3][t] + ((ks >> 2) ^ 4) * 8192, 512); };
#pragma unroll
    for (int ks = 0; ks < D; ++ks) { rd(ks, 0); rd(ks, 1); }
    SBAR();
    static_for<0, 32>([&](auto kc) {
      constexpr int ks = kc.value;
      hook(kc);
      if constexpr (ks == 12) arrive_nowait(cnt + 16);
      if constexpr (ks == 29) seen = peek(cnt);
#pragma unroll
      for (int t = 0; t < 2; ++t) {
        hac[t] = MFMA32(wf[ks % D][t], xf[ks ^ 16], hac[t]);
        if ((ks & 3) == 0 && ks < 28) { cs[t] = MFMA16(sel, wf[ks % D][t], cs[t]); PIN(cs[t]); }
        if (ks == 26) { cs[t] = MFMA16(sel, wf[28 % D][t], cs[t]); PIN(cs[t]); }
        SBAR();
        if constexpr (ks + D < 32) rd(ks + D, t);
        if constexpr (ks == 31) { if (t == 0) { au0 = waug(c0, bias0); PIN(au0); } else { au1 = waug(c1, bias1); PIN(au1); } }
        SBAR();
      }
      if constexpr (ks == 30) {
        PIN(hac[0]); PIN(hac[1]);
        if (__builtin_amdgcn_readfirstlane(seen) < 4) await(cnt, 4);
        asm volatile("" ::: "memory");
        if constexpr (ks == 30) { c0 = csrd(2 * P); c1 = csrd(2 * P + 1); }
      }
      if constexpr (ks == 27) { cswr(2 * P, cs[0]); cswr(2 * P + 1, cs[1]); arrive_nowait(cnt); }
      SBAR();
    });
    hac[0] = MFMA32(au0, xaug, hac[0]);
    hac[1] = MFMA32(au1, xaug, hac[1]);
    SBAR();
  };
  auto hgelu = [&](auto pc, auto&& mid) {
    constexpr int P = pc.value;
    constexpr bool PK = GELU_PK(P);
    const float C0 = 2.3022081986f, C1 = C0 * 0.044715f;
    const float ga = -C1 * rstd * rstd * rstd, gb = -C0 * rstd;
    auto mul = [&](f32x2 a, f32x2 b) { return PK ? a * b : (f32x2){MUL1(a[0], b[0]), MUL1(a[1], b[1])}; };
    auto fma = [&](f32x2 a, float b, float c) {
      return PK ? __builtin_elementwise_fma(a, (f32x2){b, b}, (f32x2){c, c}) : (f32x2){FMA1(a[0], b, c), FMA1(a[1], b, c)}; };
#pragma unroll
    for (int t = 0; t < 2; ++t) {
      f32x2 v[8], z[8];
#pragma unroll
      for (int e = 0; e < 8; ++e) { v[e] = (f32x2){hac[t][2 * e], hac[t][2 * e + 1]}; z[e] = mul(v[e], v[e]); }
      SBAR();
#pragma unroll
      for (int e = 0; e < 8; ++e) z[e] = fma(z[e], ga, gb);
      SBAR();
#pragma unroll
      for (int e = 0; e < 8; ++e) z[e] = mul(z[e], v[e]);
      SBAR();
#pragma unroll
      for (int e = 0; e < 8; ++e) z[e] = (f32x2){EXP2(z[e][0]), EXP2(z[e][1])};
      SBAR();
#pragma unroll
      for (int e = 0; e < 8; ++e) z[e] = fma(z[e], sd, sd);
      SBAR();
#pragma unroll
      for (int e = 0; e < 8; ++e) z[e] = (f32x2){__builtin_amdgcn_rcpf(z[e][0]), __builtin_amdgcn_rcpf(z[e][1])};
      SBAR();
#pragma unroll
      for (int e = 0; e < 8; ++e) z[e] = mul(z[e], v[e]);
      SBAR();
#pragma unroll
      for (int s = 0; s < 2; ++s) {
        const v4u f = {pk2(z[4 * s][0], z[4 * s][1]), pk2(z[4 * s + 1][0], z[4 * s + 1][1]), pk2(z[4 * s + 2][0], z[4 * s + 2][1]), pk2(z[4 * s + 3][0], z[4 * s + 3][1])};
        st16_sc1(f, sws, lane * 16, WS_AF + (T * AFT + (hcol0 >> 4) + 4 * P + 2 * t + s) * 1024);
      }
      if (t == 0) mid();
      SBAR();
    }
  };

  if (isG) {
    lane_setup();
    const v4u sw2 = make_srd(w2, FF * DM * 2), swo = make_srd(wo, DM * DM * 2);
    const unsigned vw2 = (lane >> 3) * 1024 + swz, vw2b = vw2 + 7 * 1024;
    const int ro3 = rt % 3;
    auto kmap = [&](int kk) {
      if (kk < 16) return 8 * (((kk >> 2) + rt) & 3) + (kk & 3);
      const int p = ((kk - 16) >> 2) + ro3;
      return 8 * (p >= 3 ? p - 3 : p) + 4 + (kk & 3);
    };
    auto omap = [&](int i) { return 2 * ((i + rt) & 3) + (i >> 2); };
    auto wdmah = [&](int kk, int half) {
      const unsigned so = (kk < 28 ? 448 * m + 16 * kmap(kk) : 128 * m + 16 * omap(kk - 28)) * 1024 + 128 * ct;
      if (half == 0) dma16_a(kk < 28 ? sw2 : swo, vw2, so, lds0 + L_WG + m * PART + (kk % RD) * 2048); else dma16_b(kk < 28 ? sw2 : swo, vw2b, so);
    };
    auto wdma = [&](int kk) { wdmah(kk, 0); wdmah(kk, 1); };
    {
      const v4u swq = make_srd(wqkv, DM * 3 * DM * 2);
      const unsigned vx0 = (lane >> 3) * 1024 + (((lane & 7) ^ g) * 16), vx1 = (7 + (lane >> 3)) * 1024 + (((lane & 7) ^ (4 + g)) * 16);
      const unsigned vwq = (lane >> 2) * (3 * DM * 2) + p4 * 16;
      auto piece = [&](int s, int i) {
        const int kc = (s + rot) & 7;
        if (i < 4) {
          const unsigned so = (128 * r + 32 * m + 16 * (i >> 1)) * 1024 + kc * 128;
          if (i & 1) dma16_b(sx, vx1, so); else dma16_a(sx, vx0, so, lds0 + xbuf(s) + m * 4096 + (i >> 1) * 2048);
        } else if (i < 7) dma16(swq, vwq, (64 * kc + 16 * m) * (3 * DM * 2) + (512 * (i - 4) + 32 * j) * 2, lds0 + qbuf(s) + ((i - 4) * 4 + m) * 1024);
        else w1dma(0, s, lds0 + w1buf(s), i - 7);
      };
#pragma unroll
      for (int i = 0; i < 18; ++i) { piece(i / 9, i % 9); if (i == 8) __builtin_amdgcn_s_barrier(); }
      __builtin_amdgcn_s_setprio(PRIO_G1);
      hsetup(0);
      f32x4 cs[2] = {};
      v4u xf[4], wf[4][2];
      hac[0] = (f32x16){}; hac[1] = (f32x16){};
      static_for<0, 8>([&](auto sc) {
        constexpr int s = sc.value;
        auto fill = [&](int i) {
          if constexpr (s < 6) piece(s + 2, i);
          if constexpr (s == 6) {
            if (i == 0) { stat_load(); bias_load(0); }
            else if (i < 5) { if (heavy) w1dma(1, (i - 1) >> 1, lds0 + L_WA + ((i - 1) >> 1) * 8192, (i - 1) & 1); }
            else wdmah((i - 5) >> 1, (i - 5) & 1);
          }
          if constexpr (s == 7) {
            if (heavy && i < 4) w1dma(1, 2 + (i >> 1), lds0 + L_WA + (2 + (i >> 1)) * 8192, i & 1);
            if (heavy && i >= 4) { w1dma(1, i, lds0 + L_WA + i * 8192, 0); w1dma(1, i, 0, 1); }
          }
          SBAR();
        };
        auto rd = [&](int p) {
          xf[p] = lds128(xbuf(s) + xo[p]);
          wf[p][0] = ldtr2(rk[p][0] + (s % 3) * PART, 512); wf[p][1] = ldtr2(rk[p][1] + (s % 3) * PART, 512);
          SBAR();
        };
        if constexpr (s < 7) wait_vm<9>(); else { if (heavy) wait_vm<11>(); else wait_vm<7>(); }
        bar();
        STAMP_AT(10 + s);
        rd(0);
        if constexpr (s < 7) fill(0);
        rd(1);
        if constexpr (s < 7) fill(1);
        if constexpr (s == 2) get_tag();
#pragma unroll
        for (int p = 0; p < 4; ++p) {
#pragma unroll
          for (int t = 0; t < 2; ++t) {
            hac[t] = MFMA32(wf[p][t], xf[p], hac[t]);
            if (p == 0) { cs[t] = MFMA16(sel, wf[p][t], cs[t]); PIN(cs[t]); }
            SBAR();
            if (p < 2 && t == 0) rd(p + 2);
            if (s == 7) fill(2 * p + t); else if (2 * p + t < 7) fill(2 + 2 * p + t);
          }
          if (s == 7 && p == 1) {
            cswr(0, cs[0]); cswr(1, cs[1]);
            bar();
            STAMP_AT(1);
          }
        }
        if constexpr (s == 7) {
          const f32x4 c0 = csrd(0), c1 = csrd(1);
          if (heavy) wait_vm<20>(); else wait_vm<4>();
          PIN(st); PIN(bias0); PIN(bias1);
          row_stats(); make_xaug();
          hac[0] = MFMA32(waug(c0, bias0), xaug, hac[0]);
          hac[1] = MFMA32(waug(c1, bias1), xaug, hac[1]);
          SBAR();
        }
      });
    }
    STAMP_AT(18);
    if (heavy) { wait_vm<0>(); arrive_nowait(lds0 + L_CNT + 16); }
    hgelu(std::integral_constant<int, 0>{}, [&]() {});
    __builtin_amdgcn_s_setprio(0);
    STAMP_AT(19);
    unsigned rb2[2];
#pragma unroll
    for (int c = 0; c < 2; ++c) rb2[c] = lds0 + L_WG + m * PART + h * 512 + q4 * 128 + ((c ^ (q4 >> 1)) * 64) + (g & 1) * 32 + p4 * 8;
    v4u wq[36][2];
    auto wrd = [&](int kk) {
#pragma unroll
      for (int c = 0; c < 2; ++c) wq[kk][c] = ldtr2(rb2[c] + (kk % RD) * 2048, 1024);
    };
    auto wgot = [&](int kk) { PIN(wq[kk][0]); PIN(wq[kk][1]); };
    const int ftile = 2 * rt + ((lane >> 2) & 1);
    const int T0 = 4 * r - 8 + 2 * m;
    const bool live = T0 >= 0;
    auto kv_fetch = [&]() {
      const unsigned src = WS_KV + (j * 64 + (live ? T0 : 0)) * 4096, vk = live ? lane * 16 : h * 512, vv = live ? lane * 16 : 0x40000000u;
#pragma unroll
      for (int i = 0; i < 8; ++i)
        dma16_sc1(sws, (i & 2) ? vv : vk, src + (live ? i : (i & 3)) * 1024, lds0 + L_KV + (2 * m + (i >> 2)) * KVT + (i & 3) * 1024 + ((i & 3) == 3 ? VB - 1024 : 0));
      const unsigned on = (lane < 2 || live) ? one2 : 0u;
      if (lane < 6) sts128(lds0 + L_KV + (lane < 2 ? 8 + m : 2 * m + ((lane - 2) >> 1)) * KVT + 2048 + (lane & 1) * VB + 1024, (v4u){on, on, on, on});
    };
    v4u af[28][2], of[8][2];
    v2u fl;
    const unsigned a0 = WS_AF + (2 * rt * AFT + 28 * m) * 1024, o0 = WS_AF + (2 * rt * AFT + 112 + 8 * m) * 1024;
    fl = ld8_sc1_async(sws, flag_off(FL_KV, live ? T0 + (lane & 1) : 0, j));
    for (int kk = 2; kk < 6; ++kk) wdma(kk);
    wait_vmf<8>(fl);
    if (lane == 0) __hip_atomic_store(flag(FL_H0, T, j), tag, RLX_AGENT);
    STAMP_AT(2);
    if (!__all(fl[0] == taglo && fl[1] == tag32)) poll_flags(flag(FL_KV, live ? T0 + (lane & 1) : 0, j), tag);
    if (heavy) await(lds0 + L_CNT + 20, 4);
    kv_fetch();
    wdma(6); wdma(7);
    static_for<0, NR + 1>([&](auto kc) {
      constexpr int k = kc.value;
      constexpr int refills = k >= 2 ? 2 * (k - 1) : 0;
      if constexpr (k < NR) {
        wait_vm<(k < 6 ? 2 * (5 - (k < 2 ? 1 : k)) + 13 : 2 * (7 - k)) + refills + (k > HLD ? 1 : 0)>();
        wrd(k);
      }
      if constexpr (k == KVS) { wait_vm<4 + refills>(); arrive_nowait(lds0 + L_CNT); }
      if constexpr (k == HLD) fl = ld8_sc1_async(sws, flag_off(FL_H0, ftile, 4 * m + p4));
      if constexpr (k >= 1) { wgot(k - 1); wdma(k - 1 + RD); }
    });
    STAMP_AT(3);
    wait_vmf<2 * (NR + 1 - HLD)>(fl);
    if (!__all(fl[0] == taglo && fl[1] == tag32)) poll_flags(flag(FL_H0, ftile, 4 * m + p4), tag);
    STAMP_AT(4);
#pragma unroll
    for (int kk = 0; kk < AW; ++kk) {
      af[kk][0] = ld16_sc1_async(sws, lane * 16, a0 + kmap(kk) * 1024);
      af[kk][1] = ld16_sc1_async(sws, lane * 16, a0 + (AFT + kmap(kk)) * 1024);
    }
    __builtin_amdgcn_s_setprio(PRIO_G2);
    f32x16 c2[2][2] = {};
    auto mm2 = [&](int kk, const v4u (&a)[2], auto&& fill) {
      SBAR();
#pragma unroll
      for (int i = 0; i < 4; ++i) { c2[i >> 1][i & 1] = MFMA32(wq[kk][i & 1], a[i >> 1], c2[i >> 1][i & 1]); SBAR(); fill(i); SBAR(); }
    };
    static_for<0, 28>([&](auto kc) {
      constexpr int kk = kc.value;
      wait_vm2<(kk < AW) ? 2 * (AW - 1 - kk) + g2_range(0, kk) : g2_range(kk - AW + 1, kk)>(af[kk][0], af[kk][1]);
      if constexpr (kk == H1_CK) {
        STAMP_AT(20);
        wait_vmf<g2_range(H1_LD, H1_CK) - 1>(fl);
        if (!__all(fl[0] == taglo && fl[1] == tag32)) poll_flags(flag(FL_H1, ftile, 4 * m + p4), tag);
        STAMP_AT(21);
      }
      if constexpr (kk == H1_LD) fl = ld8_sc1_async(sws, flag_off(FL_H1, ftile, 4 * m + p4));
      if constexpr (kk == O_LD) fl = ld8_sc1_async(sws, flag_off(FL_O0, ftile, 4 * m + p4));
      if constexpr (kk == 26) fl = ld8_sc1_async(sws, flag_off(FL_O1, ftile, 4 * m + p4));
      constexpr bool dw = kk >= 1 && kk - 1 + NR + RD < 36;
      if constexpr (dw) wgot(kk - 1 + NR);
      mm2(kk, af[kk], [&](int i) {
        if constexpr (dw) { if (i < 2) wdmah(kk - 1 + NR + RD, i); }
        if constexpr (kk + NR < 28) { if (i == 1) wrd(kk + NR); }
        if constexpr (kk == 27) { if (i == 0) wrd(28); }
        if constexpr (kk + AW < 28) { if (i >= 2) af[kk + AW][i - 2] = ld16_sc1_async(sws, lane * 16, a0 + (AFT * (i - 2) + kmap(kk + AW)) * 1024); }
      });
      if constexpr (kk == O_CK) {
        wait_vmf<g2_range(O_LD, O_CK) - 1>(fl);
        if (!__all(fl[0] == taglo && fl[1] == tag32)) poll_flags(flag(FL_O0, ftile, 4 * m + p4), tag);
        res_loads();
#pragma unroll
        for (int i = 0; i < 4; ++i) {
          of[i][0] = ld16_sc1_async(sws, lane * 16, o0 + omap(i) * 1024);
          of[i][1] = ld16_sc1_async(sws, lane * 16, o0 + (AFT + omap(i)) * 1024);
        }
      }
    });
    static_for<0, 4>([&](auto kc) {
      constexpr int kk = kc.value;
      if (kk < 3) wrd(28 + kk + 1);
      if constexpr (kk == 1) {
        res_sum();
        STAMP_AT(5);
        wait_vmf<0>(fl);
        if (!__all(fl[0] == taglo && fl[1] == tag32)) poll_flags(flag(FL_O1, ftile, 4 * m + p4), tag);
        STAMP_AT(6);
#pragma unroll
        for (int i = 0; i < 8; ++i) of[4 + (i & 3)][i >> 2] = ld16_sc1_async(sws, lane * 16, o0 + (AFT * (i >> 2) + omap(4 + (i & 3))) * 1024);
      }
      wait_vm2<(kk == 0) ? 7 : 14 - 2 * kk>(of[kk][0], of[kk][1]);
      mm2(28 + kk, of[kk], [&](int i) { PIN(c2[i >> 1][i & 1]); if (kk == 3) wrd(32 + i); });
    });
    for (int kk = 32; kk < 36; ++kk) wgot(kk);
    unsigned pa = lds0 + L_WG + m * PART + lane * 16;
    PIN(pa);
    auto put = [&](int qn, int qd) {
      const f32x16& c = c2[qn >> 1][qn & 1];
      sts128(pa + (qn * 4 + qd) * 66 * 16, __builtin_bit_cast(v4u, (f32x4){c[4 * qd], c[4 * qd + 1], c[4 * qd + 2], c[4 * qd + 3]}));
    };
    static_for<0, 4>([&](auto qc) {
      constexpr int qn = qc.value;
      if constexpr ((qn & 1) == 0) { wait_vm2<qn ? 0 : 4>(of[4][qn >> 1], of[5][qn >> 1]); PIN(of[6][qn >> 1]); PIN(of[7][qn >> 1]); }
      SBAR();
#pragma unroll
      for (int i = 0; i < 4; ++i) {
        c2[qn >> 1][qn & 1] = MFMA32(wq[32 + i][qn & 1], of[4 + i][qn >> 1], c2[qn >> 1][qn & 1]);
        SBAR();
        if (qn > 0 && i >= 2) { put(qn - 1, 2 * i - 4); put(qn - 1, 2 * i - 3); }
        SBAR();
      }
    });
#pragma unroll
    for (int qd = 0; qd < 4; ++qd) put(3, qd);
    STAMP_AT(7);
  } else {
    __builtin_amdgcn_s_setprio(1);
    lane_setup();
    v4u xr[2];
    xr[0] = ld16_async(sx, lane * 16, (128 * r + 8 * j + 2 * m) * 1024); xr[1] = ld16_async(sx, lane * 16, (128 * r + 8 * j + 2 * m + 1) * 1024);
    unsigned warm;
    {
      u64 pc; asm volatile("s_getpc_b64 %0" : "=s"(pc));
      warm = ld2_async(make_srd((const void*)(pc & ~127ull), CODEWARM), lane < 3 ? lane * 128 : 0x40000000u, (idx * 4 + m) * 384);
    }
    __builtin_amdgcn_s_barrier();
    v2u ep = ld8_sc1_async(sws, m == 0 ? WS_EPOCH : 0x40000000u);
    float sval;
    const int wi = blockIdx.x * 4 + m;
    const u64 wbase = wi < 448 ? (u64)w1 + wi * 4096 : wi < 896 ? (u64)w2 + (wi - 448) * 4096 : (u64)wo + (wi - 896) * 4096;
    auto make_stats = [&]() {
      wait_vm2<1>(xr[0], xr[1]);
      PIN(warm);
      float piv[2], a[4];
#pragma unroll
      for (int u = 0; u < 2; ++u) {
        piv[u] = rdl(bflo(xr[u][0]), 0);
        float s1 = 0.f, s2 = 0.f;
#pragma unroll
        for (int i = 0; i < 8; ++i) { const float d = ((i & 1) ? bfhi(xr[u][i >> 1]) : bflo(xr[u][i >> 1])) - piv[u]; s1 += d; s2 = __builtin_fmaf(d, d, s2); }
        a[2 * u] = s1; a[2 * u + 1] = s2;
      }
#pragma unroll
      for (int i = 0; i < 4; ++i) a[i] += dpp<0xB1>(a[i]);
#pragma unroll
      for (int i = 0; i < 4; ++i) a[i] += dpp<0x4E>(a[i]);
      float t = p4 == 0 ? a[0] : p4 == 1 ? a[1] : p4 == 2 ? a[2] : a[3];
      t += dpp<0x114>(t); t += dpp<0x118>(t);
      const auto s16 = __builtin_amdgcn_permlane16_swap(asu(t), asu(t), false, false);
      t = asf(s16[0]) + asf(s16[1]);
      const auto s32 = __builtin_amdgcn_permlane32_swap(asu(t), asu(t), false, false);
      t = (asf(s32[0]) + asf(s32[1])) * (1.f / DM);
      const float e1 = dpp<0xA0>(t);
      sval = (p4 & 1) ? __builtin_fmaxf(__builtin_fmaf(-e1, e1, t), 0.f) + 1e-5f : t + (p4 ? piv[1] : piv[0]);
      PIN(sval);
    };
    const unsigned acnt = lds0 + L_CNT;
    const unsigned vpos = 2048 + (g & 1) * 512 + h * 256 + (lane & 15) * 16;
    v4u qf[2], am0, am1;
    u64 um[16];
    auto masks = [&]() {
      const int dq = r32 - 4 * h;
#pragma unroll
      for (int u = 0; u < 16; ++u) { um[u] = __builtin_amdgcn_ballot_w64((u & 3) + 8 * (u >> 2) > dq); pin_s(um[u]); }
#pragma unroll
      for (int d = 0; d < 4; ++d) {
        am0[d] = __builtin_amdgcn_inverse_ballot_w64(um[2 * d]) ? 0xffffffffu : __builtin_amdgcn_inverse_ballot_w64(um[2 * d + 1]) ? 0xffff0000u : 0u;
        am1[d] = __builtin_amdgcn_inverse_ballot_w64(um[8 + 2 * d]) ? 0xffffffffu : __builtin_amdgcn_inverse_ballot_w64(um[9 + 2 * d]) ? 0xffff0000u : 0u;
      }
      PIN(am0); PIN(am1);
    };
    {
      v4u xf[32];
      f32x16 acc[3] = {};
      f32x4 cs[3] = {};
      v4u wf[4][3];
      unsigned wb[4];
#pragma unroll
      for (int p = 0; p < 4; ++p) { wb[p] = qbuf(0) + ((p + m) & 3) * 1024 + (8 * h + q4) * 64 + (g & 1) * 32 + p4 * 8; PIN(wb[p]); }
      auto rd = [&](int ks, int t) {
        if (t == 0) xf[ks] = lds128(xbuf(ks >> 2) + xo[ks & 3]);
        wf[ks & 3][t] = ldtr2(wb[ks & 3] + ((ks >> 2) % 3) * 12288 + t * 4096, 256);
      };
      auto mm = [&](int ks, int t) {
        acc[t] = t == 2 ? MFMA32(xf[ks], wf[ks & 3][t], acc[t]) : MFMA32(wf[ks & 3][t], xf[ks], acc[t]);
        if ((ks & 3) == 0) { cs[t] = MFMA16(sel, wf[ks & 3][t], cs[t]); PIN(cs[t]); }
        SBAR();
      };
      make_stats();
      bar();
      STAMP_AT(10);
#pragma unroll
      for (int t = 0; t < 6; ++t) rd(t / 3, t % 3);
      SBAR();
      static_for<0, 8>([&](auto sc) {
        constexpr int s = sc.value;
#pragma unroll
        for (int p = 0; p < 2; ++p) {
#pragma unroll
          for (int t = 0; t < 3; ++t) { mm(4 * s + p, t); rd(4 * s + p + 2, t); SBAR(); }
          if (s == 7 && p == 0) { cswr(4, cs[0]); cswr(5, cs[1]); cswr(6, cs[2]); SBAR(); }
        }
        if constexpr (s == 0) {
          wait_vmf<0>(ep);
          if (m == 0 && lane < 10)
            *(LAS unsigned*)(size_t)(lds0 + L_CNT + lane * 4) = lane == 8 ? ep[0] * 0x9E3779B1u + 0x7F4A7C15u : lane == 9 ? ep[1] * 0x85EBCA77u + 0x165667B1u : 0u;
        }
        if constexpr (s == 1) {
          get_tag();
          if ((lane >> 2) == 3) {
            const v2u gr = {asu(sval), tag32};
            __builtin_amdgcn_raw_buffer_store_b64(gr, rws, WS_STATS + (128 * r + 8 * j + 2 * m) * 16 + p4 * 8, 0, 16);
          }
        }
        if constexpr (s == MIDWARM) {
          warm = ld2_async(make_srd((const void*)wbase, 4096), lane * 128, 0);
        }
        if constexpr (s == 5) stat_load();
        if constexpr (s == 6) {
          wait_vm1<0>(st);
          PIN(warm);

          row_stats();
        }
        mm(4 * s + 2, 0); mm(4 * s + 2, 1);
        bar();
        if constexpr (s < 7) {
          STAMP_AT(11 + s);
          mm(4 * s + 2, 2);
          rd(4 * s + 4, 0); rd(4 * s + 4, 1); rd(4 * s + 4, 2); SBAR();
#pragma unroll
          for (int t = 0; t < 3; ++t) { mm(4 * s + 3, t); rd(4 * s + 5, t); SBAR(); }
        }
      });
      STAMP_AT(1);
      __builtin_amdgcn_s_setprio(3);
      const f32x4 c5 = csrd(5), c6 = csrd(6), c4 = csrd(4);
      SBAR();
      mm(30, 2); mm(31, 1);
      make_xaug();
      SBAR();
      mm(31, 2); mm(31, 0);
      if (h == 0) *(LAS float*)(size_t)(lds0 + L_RS + (m * 32 + r32) * 4) = rstd;
      acc[1] = MFMA32(waug(c5, 0u), xaug, acc[1]);
      acc[2] = MFMA32(xaug, waug(c6, 0u), acc[2]);
      acc[0] = MFMA32(waug(c4, 0u), xaug, acc[0]);
      SBAR();
      f32x4 rs[4];
#pragma unroll
      for (int qd = 0; qd < 4; ++qd) rs[qd] = __builtin_bit_cast(f32x4, lds128(lds0 + L_RS + (m * 32 + 8 * qd + 4 * h) * 4));
      const unsigned kvo = WS_KV + (j * 64 + T) * 4096;
#pragma unroll
      for (int e = 0; e < 16; ++e) acc[1][e] = MUL1(acc[1][e], rstd);
#pragma unroll
      for (int e = 0; e < 2; ++e) {
        const v4u f = pack8(acc[1], e);
        st16_sc1(f, sws, lane * 16, kvo + e * 1024);
        sts128(lds0 + L_KV + (8 + m) * KVT + e * 1024 + lane * 16, f);
      }
#pragma unroll
      for (int qd = 0; qd < 4; ++qd)
#pragma unroll
        for (int e = 0; e < 4; ++e) acc[2][4 * qd + e] = MUL1(acc[2][4 * qd + e], rs[qd][e]);
#pragma unroll
      for (int s = 0; s < 2; ++s) {
        const v4u f = pack8(acc[2], s);
        st16_sc1(f, sws, vpos, kvo + s * 1024);
        sts128(lds0 + L_KV + (8 + m) * KVT + s * VB + vpos, f);
      }
      SBAR();
      const float qs = rstd * (0.25f * 1.4426950408889634f);
#pragma unroll
      for (int e = 0; e < 16; ++e) acc[0][e] = MUL1(acc[0][e], qs);
      qf[0] = pack8(acc[0], 0); qf[1] = pack8(acc[0], 1);
      PIN(qf[0]); PIN(qf[1]);
      arrive_nowait(acnt);
      STAMP_AT(2);
      if (heavy) {
        hsetup(1);
        await(acnt + 16, 4);
        __builtin_amdgcn_s_setprio(1);
        STAMP_AT(3);
        hmm(std::integral_constant<int, 1>{}, xf, [&](auto kc) {
          constexpr int ks = kc.value;
          if constexpr (ks == KVPUB) {
            wait_vm<0>();
            if (lane == 0) __hip_atomic_store(flag(FL_KV, T, j), tag, RLX_AGENT);
          }
          if constexpr (ks == KVPUB + 2) bias_load(1);
          if constexpr (ks == 31) { wait_vm<0>(); PIN(bias0); PIN(bias1); }
        });
        STAMP_AT(18);
        __builtin_amdgcn_s_setprio(3);
        hgelu(std::integral_constant<int, 1>{}, [&]() {});
        STAMP_AT(4);
      } else {
        wait_vm<0>();
        if (lane == 0) __hip_atomic_store(flag(FL_KV, T, j), tag, RLX_AGENT);
      }
    }
    masks();
    STAMP_AT(19);
    const unsigned kb = lds0 + L_KV + m * KVT + lane * 16;
    unsigned vb[2];
#pragma unroll
    for (int e = 0; e < 2; ++e) { vb[e] = lds0 + L_KV + m * KVT + 2048 + ((g & 1) ? 1024 : e * 512 + h * 256 + (lane & 15) * 16); PIN(vb[e]); }
    bool pend = true;
    if (__builtin_amdgcn_readfirstlane(peek(acnt)) < 8) {
      wait_vm<0>();
      if (lane == 0) __hip_atomic_store(flag(FL_H1, T, j), tag, RLX_AGENT);
      pend = false;
      await(acnt, 8);
    }
    asm volatile("" ::: "memory");
    __builtin_amdgcn_s_setprio(PRIO_ATT);
    STAMP_AT(5);
    const f32x16 zero = {};
    auto o_store = [&](f32x16& o, int e) {
      const float rl = __builtin_amdgcn_rcpf(o[8]);
#pragma unroll
      for (int u = 0; u < 8; ++u) o[u] = MUL1(o[u], rl);
      st16_sc1(pack8(o, 0), sws, lane * 16, WS_AF + (T * AFT + 112 + 2 * j + e) * 1024);
    };
    int redo = 0;
    {
      f32x16 o[2], sa, sb, su;
      v4u kn, ku, va, vc, vn0, vn1, vu0, vu1, p0, p1, pu;
      auto kaddr = [&](int n) { return kb + ((n & 7) + 1) * KVT + (n >> 3) * 1024; };
      auto vaddr = [&](int n) { return vb[n >> 3] + ((n & 7) + 1) * KVT; };
      kn = lds128(kaddr(0)); vn0 = lds128(vaddr(0)); vn1 = lds128(vaddr(0) + VB);
      sa = MFMA32(kn, qf[0], zero);
      kn = lds128(kaddr(1));
      SBAR();
      auto step = [&](auto nc, f32x16& sc, f32x16& sn) {
        constexpr int n = nc.value, e = n >> 3, i = n & 7;
        if constexpr (n < 15) sn = MFMA32(kn, qf[(n + 1) >> 3], zero);
        if constexpr (n < 14) kn = lds128(kaddr(n + 2));
        if constexpr (i == 5) ku = lds128(kb + e * 1024);
        v4u v0 = vn0, v1 = vn1;
        if constexpr (n < 15) { vn0 = lds128(vaddr(n + 1)); vn1 = lds128(vaddr(n + 1) + VB); }
        if constexpr (i == 6) { vu0 = lds128(vb[e]); vu1 = lds128(vb[e] + VB); }
        SBAR();
        if constexpr (i == 7) {
#pragma unroll
          for (int u = 0; u < 16; ++u) sc[u] = __builtin_amdgcn_inverse_ballot_w64(um[u]) ? su[u] : sc[u];
          PIN(sc); SBAR();
        }
#pragma unroll
        for (int u = 0; u < 6; ++u) sc[u] = EXP2(sc[u]);
        PIN(sc); SBAR();
        if constexpr (n == 8) { o[0] = MFMA32(vc, p1, o[0]); SBAR(); }
        else if constexpr (n > 0) { o[e] = MFMA32(vc, p1, o[e]); SBAR(); }
        sc[6] = EXP2(sc[6]); sc[7] = EXP2(sc[7]);
        p0 = pack8(sc, 0);
        sc[8] = EXP2(sc[8]); sc[9] = EXP2(sc[9]);
        if constexpr (i == 7) { va = p0 & am0; p0 &= ~am0; PIN(va); }
        PIN(sc); PIN(p0); SBAR();
        if constexpr (n == 8) { o[0] = MFMA32(vu1, pu, o[0]); SBAR(); }
        o[e] = MFMA32(v0, p0, i == 0 ? zero : o[e]);
        SBAR();
        if constexpr (i == 6) { su = MFMA32(ku, qf[e], zero); SBAR(); }
#pragma unroll
        for (int u = 10; u < 13; ++u) sc[u] = EXP2(sc[u]);
        if constexpr (i == 7) { PIN(sc); SBAR(); o[e] = MFMA32(vu0, va, o[e]); SBAR(); }
#pragma unroll
        for (int u = 13; u < 16; ++u) sc[u] = EXP2(sc[u]);
        p1 = pack8(sc, 1); vc = v1;
        if constexpr (i == 7) { pu = p1 & am1; p1 &= ~am1; PIN(pu); }
        PIN(p1); SBAR();
        if constexpr (n == 8) {
          if (__all(o[0][8] >= 0x1p-30f && o[0][8] <= 0x1p30f)) o_store(o[0], 0); else redo = 1;
          SBAR();
        }
        if constexpr (n == 10) pend = !redo;
        if constexpr (i == 3 || i == 5 || i == 7) {
          if (pend && (i == 7 || (__builtin_amdgcn_s_getreg(7 | (3 << 11)) & 15) == 0)) {
            wait_vm<0>();
            if (lane == 0) __hip_atomic_store(flag(FL_H1 + e, T, j), tag, RLX_AGENT);
            pend = false;
          }
        }
      };
      static_for<0, 8>([&](auto hc) {
        step(std::integral_constant<int, 2 * hc.value>{}, sa, sb);
        step(std::integral_constant<int, 2 * hc.value + 1>{}, sb, sa);
      });
      o[1] = MFMA32(vc, p1, o[1]);
      o[1] = MFMA32(vu1, pu, o[1]);
      if (__all(o[1][8] >= 0x1p-30f && o[1][8] <= 0x1p30f)) o_store(o[1], 1); else redo |= 2;
    }
    if (redo) {
#pragma unroll 1
      for (int e = 0; e < 2; ++e) {
        if (!((redo >> e) & 1)) continue;
        const v4u q = e ? qf[1] : qf[0];
        const unsigned vbe = e ? vb[1] : vb[0];
        auto merged = [&](const f32x16& c) {
          f32x16 sl = MFMA32(lds128(kb + 8 * KVT + e * 1024), q, c);
          const f32x16 su = MFMA32(lds128(kb + e * 1024), q, c);
          static_for<0, 16>([&](auto u) { sl[u.value] = upper<u.value>() ? su[u.value] : sl[u.value]; });
          return sl;
        };
        float mx = -INFINITY;
#pragma unroll 1
        for (int i = 0; i < 8; ++i) {
          const f32x16 sc = i ? MFMA32(lds128(kb + i * KVT + e * 1024), q, zero) : merged(zero);
#pragma unroll
          for (int u = 0; u < 16; u += 2) mx = max3(mx, sc[u], sc[u + 1]);
        }
        const auto sw = __builtin_amdgcn_permlane32_swap(asu(mx), asu(mx), false, false);
        mx = __builtin_fmaxf(asf(sw[0]), asf(sw[1]));
        f32x16 negm, o;
#pragma unroll
        for (int u = 0; u < 16; ++u) negm[u] = -mx;
        {
          f32x16 sc = merged(negm);
#pragma unroll
          for (int u = 0; u < 16; ++u) sc[u] = EXP2(sc[u]);
          const v4u p0 = pack8(sc, 0), p1 = pack8(sc, 1);
          o = MFMA32(lds128(vbe), p0 & am0, zero);
          o = MFMA32(lds128(vbe + VB), p1 & am1, o);
          o = MFMA32(lds128(vbe + 8 * KVT), p0 & ~am0, o);
          o = MFMA32(lds128(vbe + 8 * KVT + VB), p1 & ~am1, o);
        }
#pragma unroll 1
        for (int i = 1; i < 8; ++i) {
          f32x16 sc = MFMA32(lds128(kb + i * KVT + e * 1024), q, negm);
#pragma unroll
          for (int u = 0; u < 16; ++u) sc[u] = EXP2(sc[u]);
          o = MFMA32(lds128(vbe + i * KVT), pack8(sc, 0), o);
          o = MFMA32(lds128(vbe + i * KVT + VB), pack8(sc, 1), o);
        }
        o_store(o, e);
      }
      wait_vm<0>();
      if (lane == 0) __hip_atomic_store(flag(FL_O0, T, j), tag, RLX_AGENT);
    }
    STAMP_AT(6);
    wait_vm<0>();
    if (lane == 0) __hip_atomic_store(flag(FL_O1, T, j), tag, RLX_AGENT);
    STAMP_AT(7);
    res_loads();
    wait_vm<0>();
    res_sum();
    if (blockIdx.x == 0 && wave == 7) {
#pragma unroll 1
      for (int i = 0; i < 4; ++i) poll_flags(flag(FL_KV, 4 * (4 * i + g), lane & 15), tag);
      if (lane == 0) __hip_atomic_store((GAS u64*)(ws + WS_EPOCH), tag, RLX_AGENT);
    }
  }
  {
    int t2 = threadIdx.x; PIN(t2);
    const int row = t2 >> 3, ch = t2 & 7;
    unsigned a = lds0 + L_WG + (((row >> 5) * 8 + ch) * 66 + (row & 31)) * 16, oo = (64 * rt + row) * 1024 + (64 * ct + ch * 8) * 2;
    PIN(a); PIN(oo);
    bar();
    f32x4 pl[4], ph[4];
#pragma unroll
    for (int w = 0; w < 4; ++w) { pl[w] = __builtin_bit_cast(f32x4, lds128(a + w * PART)); ph[w] = __builtin_bit_cast(f32x4, lds128(a + w * PART + 512)); }
    SBAR();
#pragma unroll
    for (int w = 0; w < 4; ++w) { lo += pl[w]; hi += ph[w]; }
    const v4u o = {pk2(lo[0], lo[1]), pk2(lo[2], lo[3]), pk2(hi[0], hi[1]), pk2(hi[2], hi[3])};
    __builtin_amdgcn_raw_buffer_store_b128(o, __builtin_amdgcn_make_buffer_rsrc(out, 0, SEQ * DM * 2, 0x00020000), oo, 0, 16);
  }
  STAMP_AT(8);
}

extern "C" void kernel_launch(void* const* d_in, const int* in_sizes, int n_in, void* d_out, int out_size, void* d_ws, size_t ws_size,
                              hipStream_t stream) {
  (void)in_sizes; (void)n_in; (void)out_size; (void)ws_size;
  (void)hipFuncSetAttribute((const void*)fused_block, hipFuncAttributeMaxDynamicSharedMemorySize, LDS_BYTES);
  fused_block<<<256, 512, LDS_BYTES, stream>>>(d_in[0], d_in[1], d_in[2], d_in[4], d_in[5], d_in[6], d_in[7], d_out, (char*)d_ws);
}
```

```cpp
#include <hip/hip_runtime.h>
#include <hip/hip_bf16.h>
#include <stdint.h>
#include <type_traits>

typedef unsigned v4u __attribute__((ext_vector_type(4)));
typedef unsigned v2u __attribute__((ext_vector_type(2)));
typedef float f32x2 __attribute__((ext_vector_type(2)));
typedef float f32x4 __attribute__((ext_vector_type(4)));
typedef float f32x16 __attribute__((ext_vector_type(16)));
typedef short bf16x8 __attribute__((ext_vector_type(8)));
typedef short s16x4 __attribute__((ext_vector_type(4)));
typedef __bf16 bf16x2_t __attribute__((ext_vector_type(2)));
typedef unsigned long long u64;
#define GAS __attribute__((address_space(1)))
#define LAS __attribute__((address_space(3)))
#define DI __device__ __forceinline__
#define RLX_AGENT __ATOMIC_RELAXED, __HIP_MEMORY_SCOPE_AGENT

constexpr int SEQ = 2048, DM = 512, FF = 1792;
constexpr unsigned WS_EPOCH = 0;
constexpr unsigned WS_STATS = 32768;
constexpr unsigned WS_FLAG = 65536;
constexpr unsigned WS_KV = WS_FLAG + 5 * 1024 * 128;
constexpr int AFT = 145;
constexpr unsigned WS_AF = WS_KV + 16 * 64 * 4096;
constexpr unsigned WS_END = WS_AF + 64 * AFT * 1024;
constexpr unsigned WS_STAMP = WS_END;
enum { FL_KV = 0, FL_H0 = 1, FL_H1 = 2, FL_O0 = 3, FL_O1 = 4 };
constexpr int L_WA = 0;
constexpr int L_WG = 86016;
constexpr int VB = 1040;
constexpr int KVT = 2048 + 2 * VB;
constexpr int L_KV = 32768;
constexpr int RD = 8, NR = 12;
constexpr int PART = 16 * 66 * 16;
constexpr int L_CS = L_WG + 4 * PART;
constexpr int L_RS = L_CS + 3584;
constexpr int L_CNT = L_RS + 512;
constexpr int LDS_BYTES = L_CNT + 64;
constexpr int xbuf(int s) { return ((s + 1) % 3) * 16384; }
constexpr int qbuf(int s) { return 49152 + (s % 3) * 12288; }
constexpr int w1buf(int s) { return L_WG + (s % 3) * PART + 8192; }
static_assert(L_KV + 8 * KVT >= 65536 && xbuf(7) == 32768 && qbuf(2) + 12288 <= L_WG && L_KV + 12 * KVT <= L_WG && PART >= RD * 2048 && LDS_BYTES <= 163840, "lds map");
#define AWIN 6
constexpr int AW = AWIN;
constexpr unsigned SPIN_CAP = 1u << 23;

#define MFMA32(a, b, c) __builtin_amdgcn_mfma_f32_32x32x16_bf16(__builtin_bit_cast(bf16x8, a), __builtin_bit_cast(bf16x8, b), c, 0, 0, 0)
#define MFMA16(a, b, c) __builtin_amdgcn_mfma_f32_16x16x32_bf16(__builtin_bit_cast(bf16x8, a), __builtin_bit_cast(bf16x8, b), c, 0, 0, 0)
#define SBAR() __builtin_amdgcn_sched_barrier(0)

#define STAMP_AT(i) do { } while (0)

DI float asf(unsigned u) { return __builtin_bit_cast(float, u); }
DI unsigned asu(float f) { return __builtin_bit_cast(unsigned, f); }
DI unsigned pk2(float lo, float hi) { return __builtin_bit_cast(unsigned, __builtin_convertvector((f32x2){lo, hi}, bf16x2_t)); }
DI float bflo(unsigned u) { return __builtin_bit_cast(float, u << 16); }
DI float bfhi(unsigned u) { return __builtin_bit_cast(float, u & 0xffff0000u); }
DI v4u make_srd(const void* p, unsigned bytes) { const u64 a = (u64)p; return (v4u){(unsigned)a, (unsigned)(a >> 32) & 0xffffu, bytes, 0x00020000u}; }
DI void dma16(v4u srd, unsigned voff, unsigned soff, unsigned lds) {
  asm volatile("s_mov_b32 m0, %3\n\ts_nop 0\n\tbuffer_load_dwordx4 %0, %1, %2 offen lds" :: "v"(voff), "s"(srd), "s"(soff), "s"(lds) : "memory");
}
DI void dma16_sc1(v4u srd, unsigned voff, unsigned soff, unsigned lds) {
  asm volatile("s_mov_b32 m0, %3\n\ts_nop 0\n\tbuffer_load_dwordx4 %0, %1, %2 offen sc1 lds" :: "v"(voff), "s"(srd), "s"(soff), "s"(lds) : "memory");
}
DI void dma16_a(v4u srd, unsigned voff, unsigned soff, unsigned lds) { dma16(srd, voff, soff, lds); }
DI void dma16_b(v4u srd, unsigned voff1, unsigned soff) {
  asm volatile("buffer_load_dwordx4 %0, %1, %2 offen offset:1024 lds" :: "v"(voff1), "s"(srd), "s"(soff) : "memory");
}
DI v4u ld16_sc1_async(v4u srd, unsigned voff, unsigned soff) {
  v4u r; asm volatile("buffer_load_dwordx4 %0, %1, %2, %3 offen sc1" : "=v"(r) : "v"(voff), "s"(srd), "s"(soff) : "memory"); return r;
}
DI v4u ld16_async(v4u srd, unsigned voff, unsigned soff) {
  v4u r; asm volatile("buffer_load_dwordx4 %0, %1, %2, %3 offen" : "=v"(r) : "v"(voff), "s"(srd), "s"(soff) : "memory"); return r;
}
DI unsigned ld2_async(v4u srd, unsigned voff, unsigned soff) {
  unsigned r; asm volatile("buffer_load_ushort %0, %1, %2, %3 offen" : "=v"(r) : "v"(voff), "s"(srd), "s"(soff) : "memory"); return r;
}
DI void st16_sc1(v4u v, v4u srd, unsigned voff, unsigned soff) {
  asm volatile("buffer_store_dwordx4 %0, %1, %2, %3 offen sc1\n\ts_nop 1" :: "v"(v), "v"(voff), "s"(srd), "s"(soff) : "memory");
}
template <int N> DI void wait_vm1(v4u& a) { asm volatile("s_waitcnt vmcnt(%1)" : "+v"(a) : "i"(N) : "memory"); }
template <int N> DI void wait_vm2(v4u& a, v4u& b) { asm volatile("s_waitcnt vmcnt(%2)" : "+v"(a), "+v"(b) : "i"(N) : "memory"); }
template <int N> DI void wait_vm3(v4u& a, v4u& b, v4u& c) { asm volatile("s_waitcnt vmcnt(%3)" : "+v"(a), "+v"(b), "+v"(c) : "i"(N) : "memory"); }
DI void pin_s(u64& v) { asm volatile("" : "+s"(v)); }
template <class V> DI void pin(V& v) { asm volatile("" : "+v"(v)); }
template <int N> DI void wait_vm() { asm volatile("s_waitcnt vmcnt(%0)" :: "i"(N) : "memory"); }
DI void bar() { asm volatile("s_waitcnt lgkmcnt(0)\n\ts_barrier" ::: "memory"); }
DI v4u lds128(unsigned a) { return *(const LAS v4u*)(size_t)a; }
DI void sts128(unsigned a, v4u v) { *(LAS v4u*)(size_t)a = v; }
DI v2u ldtr(unsigned a) { return __builtin_bit_cast(v2u, __builtin_amdgcn_ds_read_tr16_b64_v4i16((LAS s16x4*)(size_t)a)); }
DI v4u ldtr2(unsigned a, int second) { const v2u lo = ldtr(a), hi = ldtr(a + second); return (v4u){lo.x, lo.y, hi.x, hi.y}; }
template <int CTRL> DI float dpp(float v) { return __builtin_bit_cast(float, __builtin_amdgcn_update_dpp(0, __builtin_bit_cast(int, v), CTRL, 0xf, 0xf, true)); }
DI float rdl(float v, int l) { return __builtin_bit_cast(float, __builtin_amdgcn_readlane(__builtin_bit_cast(int, v), l)); }
DI v4u pack8(const f32x16& a, int s) { return (v4u){pk2(a[8 * s], a[8 * s + 1]), pk2(a[8 * s + 2], a[8 * s + 3]), pk2(a[8 * s + 4], a[8 * s + 5]), pk2(a[8 * s + 6], a[8 * s + 7])}; }
DI void poll_flags(const GAS u64* p, u64 tag) {
#pragma unroll 1
  for (unsigned spin = 0; spin < SPIN_CAP; ++spin) {
    const u64 v = __hip_atomic_load(p, RLX_AGENT);
    if (__all(v == tag)) break;
    __builtin_amdgcn_s_sleep(1);
  }
  asm volatile("" ::: "memory");
}
constexpr u64 upper_mask(int reg) {
  u64 m = 0;
  for (int l = 0; l < 64; ++l) { const int row = (reg & 3) + 8 * (reg >> 2) + 4 * (l >> 5); if (row > (l & 31)) m |= 1ull << l; }
  return m;
}
template <int REG> DI bool upper() { return __builtin_amdgcn_inverse_ballot_w64(upper_mask(REG)); }
template <int I, int N, class Fn> DI void static_for(Fn&& f) { if constexpr (I < N) { f(std::integral_constant<int, I>{}); static_for<I + 1, N>(f); } }
DI float max3(float a, float b, float c) { return __builtin_fmaxf(__builtin_fmaxf(a, b), c); }

#define FLAHEAD 4
constexpr int FLA = FLAHEAD, H1_CK = 16 - AW, O0_CK = 28 - AW, O1_CK = 32 - AW, RES_LD = 12;
constexpr bool fl_load(int t) { return t == H1_CK - FLA || t == O0_CK - FLA || t == O1_CK - FLA; }
constexpr int g2_issued(int t) { return ((t >= 1 && t - 1 + NR + RD < 36) ? 2 : 0) + ((t + AW < 36) ? 2 : 0) + fl_load(t) + (t == RES_LD ? 2 : 0); }
constexpr int g2_range(int a, int b) { int n = 0; for (int t = a; t < b; ++t) n += g2_issued(t); return n; }
#define GELU_PK(P) (P == 1)
#define PRIO_G2 2
#define PRIO_ATT 3
#define PRIO_G1 2
#define ROT r
#define CODEWARM 38000
#define MIDWARM 1
#define KVS 1
constexpr int HLD = 9;
#define KVPUB 2
#define EXP2(v) __builtin_amdgcn_exp2f(v)
#define PIN(v) pin(v)
extern "C" __device__ float mul_unpacked(float, float) __asm("llvm.amdgcn.fmul.legacy");
#define MUL1(a, b) mul_unpacked(a, b)
#define FMA1(a, b, c) __builtin_amdgcn_div_fmasf(a, b, c, false)
DI v2u ld8_sc1_async(v4u srd, unsigned voff) {
  v2u r; asm volatile("buffer_load_dwordx2 %0, %1, %2, 0 offen sc1" : "=v"(r) : "v"(voff), "s"(srd) : "memory"); return r;
}
template <int N> DI void wait_vmf(v2u& a) { asm volatile("s_waitcnt vmcnt(%1)" : "+v"(a) : "i"(N) : "memory"); }
DI void arrive(unsigned cnt, int lane) {
  asm volatile("s_waitcnt lgkmcnt(0)" ::: "memory");
  if (lane == 0) __hip_atomic_fetch_add((LAS unsigned*)(size_t)cnt, 1u, __ATOMIC_RELAXED, __HIP_MEMORY_SCOPE_WORKGROUP);
}
DI void arrive_nowait(unsigned cnt) {
  asm volatile("s_mov_b64 exec, 1\n\tds_add_u32 %0, %1\n\ts_mov_b64 exec, -1" :: "v"(cnt), "v"(1u) : "memory");
}
DI unsigned peek(unsigned cnt) { return __hip_atomic_load((LAS unsigned*)(size_t)cnt, __ATOMIC_RELAXED, __HIP_MEMORY_SCOPE_WORKGROUP); }
DI void await(unsigned cnt, unsigned n) {
#pragma unroll 1
  for (unsigned spin = 0; spin < SPIN_CAP * 32; ++spin) {
    if (__builtin_amdgcn_readfirstlane(peek(cnt)) >= n) break;
    __builtin_amdgcn_s_sleep(1);
  }
  asm volatile("" ::: "memory");
}

__global__ __launch_bounds__(512, 2) void fused_block(const void* __restrict__ x, const void* __restrict__ wqkv, const void* __restrict__ wo,
                                                      const void* __restrict__ w1, const void* __restrict__ b1, const void* __restrict__ w2,
                                                      const void* __restrict__ b2, void* __restrict__ out, char* __restrict__ ws) {
  constexpr unsigned lds0 = 0;
  const int wave = __builtin_amdgcn_readfirstlane(threadIdx.x >> 6);
  const int m = wave & 3;
  const bool isG = wave < 4;
  const int xcd = blockIdx.x & 7, idx = blockIdx.x >> 3;
  const int r = (xcd >> 2) * 8 + (idx >> 2), j = (xcd & 3) * 4 + (idx & 3);
  const int rt = (xcd >> 1) * 8 + (idx >> 2), ct = (xcd & 1) * 4 + (idx & 3);
  int lane = threadIdx.x & 63, r32, h, g, q4, p4;
  STAMP_AT(0);
  asm volatile("" :: "s"(x), "s"(wqkv), "s"(wo), "s"(w1), "s"(b1), "s"(w2), "s"(b2), "s"(out), "s"(ws));

  const v4u sx = make_srd(x, SEQ * DM * 2), sws = make_srd(ws, WS_END);
  const __amdgpu_buffer_rsrc_t rws = __builtin_amdgcn_make_buffer_rsrc(ws, 0, WS_END, 0x00020000);
  const unsigned one2 = 0x3f803f80u;
  const int T = 4 * r + m;
  const int rot = ROT;
  auto flag_off = [&](int kind, int tile, int jj) { return WS_FLAG + ((kind * 64 + tile) * 16 + jj) * 128; };
  auto flag = [&](int kind, int tile, int jj) { return (GAS u64*)(ws + flag_off(kind, tile, jj)); };
  u64 tag;
  unsigned taglo, tag32;
  auto get_tag = [&]() {
    const v2u tg = *(const LAS v2u*)(size_t)(lds0 + L_CNT + 32);
    taglo = __builtin_amdgcn_readfirstlane(tg[0]); tag32 = __builtin_amdgcn_readfirstlane(tg[1]);
    tag = ((u64)tag32 << 32) | taglo;
  };
  v4u xres, bres, xaug, st;
  float mean, var, rstd, sd;
  v4u sel;
  unsigned xo[4], swz;
  auto lane_setup = [&]() {
    lane = threadIdx.x & 63; PIN(lane);
    r32 = lane & 31; h = lane >> 5; g = lane >> 4; q4 = (lane >> 2) & 3; p4 = lane & 3;
    const unsigned selv = ((lane & 15) == (g & 1)) ? one2 : 0u;
    sel = (v4u){selv, selv, selv, selv};
#pragma unroll
    for (int p = 0; p < 4; ++p) xo[p] = (32 * m + r32) * 128 + (((2 * ((p + m) & 3) + h) ^ ((r32 >> 1) & 7)) * 16);
    swz = ((((lane >> 2) & 1) ^ (g & 1)) * 64) + p4 * 16;
  };

  auto stat_load = [&]() { st = ld16_sc1_async(sws, WS_STATS + (128 * r + 32 * m + r32) * 16, 0); };
  auto res_loads = [&]() {
    int t2 = threadIdx.x; PIN(t2);
    const int row = 64 * rt + (t2 >> 3), col = 64 * ct + (t2 & 7) * 8;
    xres = ld16_async(sx, row * 1024 + col * 2, 0);
    bres = ld16_async(make_srd(b2, DM * 2), col * 2, 0);
  };
  f32x4 lo, hi;
  auto res_sum = [&]() {
    PIN(xres); PIN(bres);
    lo = (f32x4){bflo(xres[0]) + bflo(bres[0]), bfhi(xres[0]) + bfhi(bres[0]), bflo(xres[1]) + bflo(bres[1]), bfhi(xres[1]) + bfhi(bres[1])};
    hi = (f32x4){bflo(xres[2]) + bflo(bres[2]), bfhi(xres[2]) + bfhi(bres[2]), bflo(xres[3]) + bflo(bres[3]), bfhi(xres[3]) + bfhi(bres[3])};
    PIN(lo); PIN(hi);
  };
  auto row_stats = [&]() {
    if (!__all(st[1] == tag32 && st[3] == tag32)) {
#pragma unroll 1
      for (unsigned spin = 0; spin < SPIN_CAP; ++spin) {
        st = ld16_sc1_async(sws, WS_STATS + (128 * r + 32 * m + r32) * 16, 0);
        wait_vm1<0>(st);
        if (__all(st[1] == tag32 && st[3] == tag32)) break;
      }
    }
    mean = asf(st[0]); var = asf(st[2]);
    PIN(mean); PIN(var);
  };
  auto make_xaug = [&]() {
    PIN(mean); PIN(var);
    rstd = __builtin_amdgcn_rsqf(var); sd = var * rstd;
    const unsigned uh = pk2(-mean, sd);
    const unsigned ul = pk2(-mean - bflo(uh), FMA1(bfhi(uh), -1.f, sd));
    xaug = (v4u){(uh & 0xffffu) | (uh << 16), (ul & 0xffffu) | (uh & 0xffff0000u), ul >> 16, 0u};
    if (h) xaug = (v4u){0u, 0u, 0u, 0u};
    PIN(xaug);
  };
  auto cswr = [&](int tile, f32x4 c) {
    if (lane < 16) { LAS float* p = (LAS float*)(size_t)(lds0 + L_CS + (tile * 32 + lane) * 16 + m * 4); p[0] = c[0]; p[64] = c[1]; }
  };
  auto csrd = [&](int tile) { return __builtin_bit_cast(f32x4, lds128(lds0 + L_CS + (tile * 32 + r32) * 16)); };
  auto waug = [&](f32x4 c4, unsigned b16) {
    const float c = ((c4[0] + c4[1]) + c4[2]) + c4[3];
    const unsigned uh = pk2(c, 0.f);
    const unsigned ul = pk2(c - bflo(uh), 0.f);
    return (v4u){uh | (ul << 16), uh | (b16 << 16), b16, 0u};
  };

  const v4u sw1 = make_srd(w1, DM * FF * 2);
  const int hcol0 = 448 * (j >> 2) + 128 * (j & 3);
  const bool heavy = (j & 3) != 3;
  unsigned bias0, bias1;
  auto bias_load = [&](int P) {
    bias0 = ld2_async(make_srd(b1, FF * 2), r32 * 2, (hcol0 + 64 * P) * 2);
    bias1 = ld2_async(make_srd(b1, FF * 2), r32 * 2, (hcol0 + 64 * P + 32) * 2);
  };
  auto w1dma = [&](int P, int c, unsigned dst, int half) {
    const unsigned vw1 = (lane >> 3) * (FF * 2) + swz, vw1b = vw1 + 8 * (FF * 2) - 1024;
    const unsigned so = (64 * ((c + rot) & 7) + 16 * m) * (FF * 2) + (hcol0 + 64 * P) * 2;
    if (half == 0) dma16_a(sw1, vw1, so, dst + m * 2048); else dma16_b(sw1, vw1b, so);
  };
  f32x16 hac[2];
  unsigned rk[4][2];
  auto hsetup = [&](int P) {
#pragma unroll
    for (int p = 0; p < 4; ++p)
#pragma unroll
      for (int c = 0; c < 2; ++c) {
        rk[p][c] = lds0 + (P ? L_WA : w1buf(0)) + ((p + m) & 3) * 2048 + h * 1024 + q4 * 128 + ((c ^ (q4 >> 1)) * 64) + (g & 1) * 32 + p4 * 8;
        PIN(rk[p][c]);
      }
  };
  auto hmm = [&](auto pc, const v4u (&xf)[32], auto&& hook) {
    constexpr int P = pc.value;
    constexpr int D = 4;
    const unsigned cnt = lds0 + L_CNT + 4;
    f32x4 cs[2] = {}, c0, c1;
    v4u wf[D][2], au0, au1;
    unsigned seen = 0;
    hac[0] = (f32x16){}; hac[1] = (f32x16){};
    auto rd = [&](int ks, int t) { wf[ks % D][t] = ldtr2(rk[ks & 3][t] + ((ks >> 2) ^ 4) * 8192, 512); };
#pragma unroll
    for (int ks = 0; ks < D; ++ks) { rd(ks, 0); rd(ks, 1); }
    SBAR();
    static_for<0, 32>([&](auto kc) {
      constexpr int ks = kc.value;
      hook(kc);
      if constexpr (ks == 12) arrive_nowait(cnt + 16);
      if constexpr (ks == 29) seen = peek(cnt);
#pragma unroll
      for (int t = 0; t < 2; ++t) {
        hac[t] = MFMA32(wf[ks % D][t], xf[ks ^ 16], hac[t]);
        if ((ks & 3) == 0 && ks < 28) { cs[t] = MFMA16(sel, wf[ks % D][t], cs[t]); PIN(cs[t]); }
        if (ks == 26) { cs[t] = MFMA16(sel, wf[28 % D][t], cs[t]); PIN(cs[t]); }
        SBAR();
        if constexpr (ks + D < 32) rd(ks + D, t);
        if constexpr (ks == 31) { if (t == 0) { au0 = waug(c0, bias0); PIN(au0); } else { au1 = waug(c1, bias1); PIN(au1); } }
        SBAR();
      }
      if constexpr (ks == 30) {
        PIN(hac[0]); PIN(hac[1]);
        if (__builtin_amdgcn_readfirstlane(seen) < 4) await(cnt, 4);
        asm volatile("" ::: "memory");
        if constexpr (ks == 30) { c0 = csrd(2 * P); c1 = csrd(2 * P + 1); }
      }
      if constexpr (ks == 27) { cswr(2 * P, cs[0]); cswr(2 * P + 1, cs[1]); arrive_nowait(cnt); }
      SBAR();
    });
    hac[0] = MFMA32(au0, xaug, hac[0]);
    hac[1] = MFMA32(au1, xaug, hac[1]);
    SBAR();
  };
  auto hgelu = [&](auto pc, auto&& mid) {
    constexpr int P = pc.value;
    constexpr bool PK = GELU_PK(P);
    const float C0 = 2.3022081986f, C1 = C0 * 0.044715f;
    const float ga = -C1 * rstd * rstd * rstd, gb = -C0 * rstd;
    auto mul = [&](f32x2 a, f32x2 b) { return PK ? a * b : (f32x2){MUL1(a[0], b[0]), MUL1(a[1], b[1])}; };
    auto fma = [&](f32x2 a, float b, float c) {
      return PK ? __builtin_elementwise_fma(a, (f32x2){b, b}, (f32x2){c, c}) : (f32x2){FMA1(a[0], b, c), FMA1(a[1], b, c)}; };
#pragma unroll
    for (int t = 0; t < 2; ++t) {
      f32x2 v[8], z[8];
#pragma unroll
      for (int e = 0; e < 8; ++e) { v[e] = (f32x2){hac[t][2 * e], hac[t][2 * e + 1]}; z[e] = mul(v[e], v[e]); }
      SBAR();
#pragma unroll
      for (int e = 0; e < 8; ++e) z[e] = fma(z[e], ga, gb);
      SBAR();
#pragma unroll
      for (int e = 0; e < 8; ++e) z[e] = mul(z[e], v[e]);
      SBAR();
#pragma unroll
      for (int e = 0; e < 8; ++e) z[e] = (f32x2){EXP2(z[e][0]), EXP2(z[e][1])};
      SBAR();
#pragma unroll
      for (int e = 0; e < 8; ++e) z[e] = fma(z[e], sd, sd);
      SBAR();
#pragma unroll
      for (int e = 0; e < 8; ++e) z[e] = (f32x2){__builtin_amdgcn_rcpf(z[e][0]), __builtin_amdgcn_rcpf(z[e][1])};
      SBAR();
#pragma unroll
      for (int e = 0; e < 8; ++e) z[e] = mul(z[e], v[e]);
      SBAR();
#pragma unroll
      for (int s = 0; s < 2; ++s) {
        const v4u f = {pk2(z[4 * s][0], z[4 * s][1]), pk2(z[4 * s + 1][0], z[4 * s + 1][1]), pk2(z[4 * s + 2][0], z[4 * s + 2][1]), pk2(z[4 * s + 3][0], z[4 * s + 3][1])};
        st16_sc1(f, sws, lane * 16, WS_AF + (T * AFT + (hcol0 >> 4) + 4 * P + 2 * t + s) * 1024);
      }
      if (t == 0) mid();
      SBAR();
    }
  };

  if (isG) {
    lane_setup();
    const v4u sw2 = make_srd(w2, FF * DM * 2), swo = make_srd(wo, DM * DM * 2);
    const unsigned vw2 = (lane >> 3) * 1024 + swz, vw2b = vw2 + 7 * 1024;
    const int ro3 = rt % 3;
    auto kmap = [&](int kk) {
      if (kk < 16) return 8 * (((kk >> 2) + rt) & 3) + (kk & 3);
      const int p = ((kk - 16) >> 2) + ro3;
      return 8 * (p >= 3 ? p - 3 : p) + 4 + (kk & 3);
    };
    auto omap = [&](int i) { return 2 * ((i + rt) & 3) + (i >> 2); };
    auto wdmah = [&](int kk, int half) {
      const unsigned so = (kk < 28 ? 448 * m + 16 * kmap(kk) : 128 * m + 16 * omap(kk - 28)) * 1024 + 128 * ct;
      if (half == 0) dma16_a(kk < 28 ? sw2 : swo, vw2, so, lds0 + L_WG + m * PART + (kk % RD) * 2048); else dma16_b(kk < 28 ? sw2 : swo, vw2b, so);
    };
    auto wdma = [&](int kk) { wdmah(kk, 0); wdmah(kk, 1); };
    {
      const v4u swq = make_srd(wqkv, DM * 3 * DM * 2);
      const unsigned vx0 = (lane >> 3) * 1024 + (((lane & 7) ^ g) * 16), vx1 = (7 + (lane >> 3)) * 1024 + (((lane & 7) ^ (4 + g)) * 16);
      const unsigned vwq = (lane >> 2) * (3 * DM * 2) + p4 * 16;
      auto piece = [&](int s, int i) {
        const int kc = (s + rot) & 7;
        if (i < 4) {
          const unsigned so = (128 * r + 32 * m + 16 * (i >> 1)) * 1024 + kc * 128;
          if (i & 1) dma16_b(sx, vx1, so); else dma16_a(sx, vx0, so, lds0 + xbuf(s) + m * 4096 + (i >> 1) * 2048);
        } else if (i < 7) dma16(swq, vwq, (64 * kc + 16 * m) * (3 * DM * 2) + (512 * (i - 4) + 32 * j) * 2, lds0 + qbuf(s) + ((i - 4) * 4 + m) * 1024);
        else w1dma(0, s, lds0 + w1buf(s), i - 7);
      };
#pragma unroll
      for (int i = 0; i < 18; ++i) { piece(i / 9, i % 9); if (i == 8) __builtin_amdgcn_s_barrier(); }
      __builtin_amdgcn_s_setprio(PRIO_G1);
      hsetup(0);
      f32x4 cs[2] = {};
      v4u xf[4], wf[4][2];
      hac[0] = (f32x16){}; hac[1] = (f32x16){};
      static_for<0, 8>([&](auto sc) {
        constexpr int s = sc.value;
        auto fill = [&](int i) {
          if constexpr (s < 6) piece(s + 2, i);
          if constexpr (s == 6) {
            if (i == 0) { stat_load(); bias_load(0); }
            else if (i < 5) { if (heavy) w1dma(1, (i - 1) >> 1, lds0 + L_WA + ((i - 1) >> 1) * 8192, (i - 1) & 1); }
            else wdmah((i - 5) >> 1, (i - 5) & 1);
          }
          if constexpr (s == 7) {
            if (heavy && i < 4) w1dma(1, 2 + (i >> 1), lds0 + L_WA + (2 + (i >> 1)) * 8192, i & 1);
            if (heavy && i >= 4) { w1dma(1, i, lds0 + L_WA + i * 8192, 0); w1dma(1, i, 0, 1); }
          }
          SBAR();
        };
        auto rd = [&](int p) {
          xf[p] = lds128(xbuf(s) + xo[p]);
          wf[p][0] = ldtr2(rk[p][0] + (s % 3) * PART, 512); wf[p][1] = ldtr2(rk[p][1] + (s % 3) * PART, 512);
          SBAR();
        };
        if constexpr (s < 7) wait_vm<9>(); else { if (heavy) wait_vm<11>(); else wait_vm<7>(); }
        bar();
        STAMP_AT(10 + s);
        rd(0);
        if constexpr (s < 7) fill(0);
        rd(1);
        if constexpr (s < 7) fill(1);
        if constexpr (s == 2) get_tag();
#pragma unroll
        for (int p = 0; p < 4; ++p) {
#pragma unroll
          for (int t = 0; t < 2; ++t) {
            hac[t] = MFMA32(wf[p][t], xf[p], hac[t]);
            if (p == 0) { cs[t] = MFMA16(sel, wf[p][t], cs[t]); PIN(cs[t]); }
            SBAR();
            if (p < 2 && t == 0) rd(p + 2);
            if (s == 7) fill(2 * p + t); else if (2 * p + t < 7) fill(2 + 2 * p + t);
          }
          if (s == 7 && p == 1) {
            cswr(0, cs[0]); cswr(1, cs[1]);
            bar();
            STAMP_AT(1);
          }
        }
        if constexpr (s == 7) {
          const f32x4 c0 = csrd(0), c1 = csrd(1);
          if (heavy) wait_vm<20>(); else wait_vm<4>();
          PIN(st); PIN(bias0); PIN(bias1);
          row_stats(); make_xaug();
          hac[0] = MFMA32(waug(c0, bias0), xaug, hac[0]);
          hac[1] = MFMA32(waug(c1, bias1), xaug, hac[1]);
          SBAR();
        }
      });
    }
    STAMP_AT(18);
    if (heavy) { wait_vm<0>(); arrive_nowait(lds0 + L_CNT + 16); }
    hgelu(std::integral_constant<int, 0>{}, [&]() {});
    __builtin_amdgcn_s_setprio(0);
    STAMP_AT(19);
    unsigned rb2[2];
#pragma unroll
    for (int c = 0; c < 2; ++c) rb2[c] = lds0 + L_WG + m * PART + h * 512 + q4 * 128 + ((c ^ (q4 >> 1)) * 64) + (g & 1) * 32 + p4 * 8;
    v4u wq[36][2];
    auto wrd = [&](int kk) {
#pragma unroll
      for (int c = 0; c < 2; ++c) wq[kk][c] = ldtr2(rb2[c] + (kk % RD) * 2048, 1024);
    };
    auto wgot = [&](int kk) { PIN(wq[kk][0]); PIN(wq[kk][1]); };
    const int ftile = 2 * rt + ((lane >> 2) & 1);
    const int T0 = 4 * r - 8 + 2 * m;
    const bool live = T0 >= 0;
    auto kv_fetch = [&]() {
      const unsigned src = WS_KV + (j * 64 + (live ? T0 : 0)) * 4096, vk = live ? lane * 16 : h * 512, vv = live ? lane * 16 : 0x40000000u;
#pragma unroll
      for (int i = 0; i < 8; ++i)
        dma16_sc1(sws, (i & 2) ? vv : vk, src + (live ? i : (i & 3)) * 1024, lds0 + L_KV + (2 * m + (i >> 2)) * KVT + (i & 3) * 1024 + ((i & 3) == 3 ? VB - 1024 : 0));
      const unsigned on = (lane < 2 || live) ? one2 : 0u;
      if (lane < 6) sts128(lds0 + L_KV + (lane < 2 ? 8 + m : 2 * m + ((lane - 2) >> 1)) * KVT + 2048 + (lane & 1) * VB + 1024, (v4u){on, on, on, on});
    };
    v4u af[36][2];
    v2u fl;
    const unsigned a0 = WS_AF + (2 * rt * AFT + 28 * m) * 1024, o0 = WS_AF + (2 * rt * AFT + 112 + 8 * m) * 1024;
    fl = ld8_sc1_async(sws, flag_off(FL_KV, live ? T0 + (lane & 1) : 0, j));
    for (int kk = 2; kk < 6; ++kk) wdma(kk);
    wait_vmf<8>(fl);
    if (lane == 0) __hip_atomic_store(flag(FL_H0, T, j), tag, RLX_AGENT);
    STAMP_AT(2);
    if (!__all(fl[0] == taglo && fl[1] == tag32)) poll_flags(flag(FL_KV, live ? T0 + (lane & 1) : 0, j), tag);
    if (heavy) await(lds0 + L_CNT + 20, 4);
    kv_fetch();
    wdma(6); wdma(7);
    static_for<0, NR + 1>([&](auto kc) {
      constexpr int k = kc.value;
      constexpr int refills = k >= 2 ? 2 * (k - 1) : 0;
      if constexpr (k < NR) {
        wait_vm<(k < 6 ? 2 * (5 - (k < 2 ? 1 : k)) + 13 : 2 * (7 - k)) + refills + (k > HLD ? 1 : 0)>();
        wrd(k);
      }
      if constexpr (k == KVS) { wait_vm<4 + refills>(); arrive_nowait(lds0 + L_CNT); }
      if constexpr (k == HLD) fl = ld8_sc1_async(sws, flag_off(FL_H0, ftile, 4 * m + p4));
      if constexpr (k >= 1) { wgot(k - 1); wdma(k - 1 + RD); }
    });
    STAMP_AT(3);
    wait_vmf<2 * (NR + 1 - HLD)>(fl);
    if (!__all(fl[0] == taglo && fl[1] == tag32)) poll_flags(flag(FL_H0, ftile, 4 * m + p4), tag);
    STAMP_AT(4);
#pragma unroll
    for (int kk = 0; kk < AW; ++kk) {
      af[kk][0] = ld16_sc1_async(sws, lane * 16, a0 + kmap(kk) * 1024);
      af[kk][1] = ld16_sc1_async(sws, lane * 16, a0 + (AFT + kmap(kk)) * 1024);
    }
    __builtin_amdgcn_s_setprio(PRIO_G2);
    f32x16 c2[2][2] = {};
    auto mm2 = [&](int kk, const v4u (&a)[2], auto&& fill) {
      SBAR();
#pragma unroll
      for (int i = 0; i < 4; ++i) { c2[i >> 1][i & 1] = MFMA32(wq[kk][i & 1], a[i >> 1], c2[i >> 1][i & 1]); SBAR(); fill(i); SBAR(); }
    };
    static_assert(FLA <= 4, "one flag register");
    static_for<0, 32>([&](auto kc) {
      constexpr int kk = kc.value;
      wait_vm2<(kk < AW) ? 2 * (AW - 1 - kk) + g2_range(0, kk) : g2_range(kk - AW + 1, kk)>(af[kk][0], af[kk][1]);
      if constexpr (kk == H1_CK || kk == O0_CK || kk == O1_CK) {
        constexpr int kind = kk == H1_CK ? FL_H1 : kk == O0_CK ? FL_O0 : FL_O1;
        if constexpr (kk == O1_CK) STAMP_AT(5);
        wait_vmf<g2_range(kk - FLA, kk) - 1>(fl);
        if (!__all(fl[0] == taglo && fl[1] == tag32)) poll_flags(flag(kind, ftile, 4 * m + p4), tag);
        if constexpr (kk == O1_CK) STAMP_AT(6);
      }
      if constexpr (fl_load(kk)) fl = ld8_sc1_async(sws, flag_off(kk + FLA == H1_CK ? FL_H1 : kk + FLA == O0_CK ? FL_O0 : FL_O1, ftile, 4 * m + p4));
      if constexpr (kk == RES_LD) res_loads();
      if constexpr (kk == 20) res_sum();
      constexpr bool dw = kk >= 1 && kk - 1 + NR + RD < 36;
      if constexpr (dw) wgot(kk - 1 + NR);
      mm2(kk, af[kk], [&](int i) {
        if constexpr (dw) { if (i < 2) wdmah(kk - 1 + NR + RD, i); }
        if constexpr (kk + NR < 28) { if (i == 1) wrd(kk + NR); }
        if constexpr (kk >= 24) { if (i == 0) wrd(kk + 4); }
        if constexpr (kk + AW < 36) {
          if (i >= 2) af[kk + AW][i - 2] = ld16_sc1_async(sws, lane * 16, (kk + AW < 28 ? a0 + kmap(kk + AW) * 1024 : o0 + omap(kk + AW - 28) * 1024) + AFT * (i - 2) * 1024);
        }
        if constexpr (kk == 31) PIN(c2[i >> 1][i & 1]);
      });
    });
    for (int kk = 32; kk < 36; ++kk) wgot(kk);
    unsigned pa = lds0 + L_WG + m * PART + lane * 16;
    PIN(pa);
    auto put = [&](int qn, int qd) {
      const f32x16& c = c2[qn >> 1][qn & 1];
      sts128(pa + (qn * 4 + qd) * 66 * 16, __builtin_bit_cast(v4u, (f32x4){c[4 * qd], c[4 * qd + 1], c[4 * qd + 2], c[4 * qd + 3]}));
    };
    static_for<0, 4>([&](auto qc) {
      constexpr int qn = qc.value;
      if constexpr ((qn & 1) == 0) { wait_vm2<0>(af[32][qn >> 1], af[33][qn >> 1]); PIN(af[34][qn >> 1]); PIN(af[35][qn >> 1]); }
      SBAR();
#pragma unroll
      for (int i = 0; i < 4; ++i) {
        c2[qn >> 1][qn & 1] = MFMA32(wq[32 + i][qn & 1], af[32 + i][qn >> 1], c2[qn >> 1][qn & 1]);
        SBAR();
        if (qn > 0 && i >= 2) { put(qn - 1, 2 * i - 4); put(qn - 1, 2 * i - 3); }
        SBAR();
      }
    });
#pragma unroll
    for (int qd = 0; qd < 4; ++qd) put(3, qd);
    STAMP_AT(7);
  } else {
    __builtin_amdgcn_s_setprio(1);
    lane_setup();
    v4u xr[2];
    xr[0] = ld16_async(sx, lane * 16, (128 * r + 8 * j + 2 * m) * 1024); xr[1] = ld16_async(sx, lane * 16, (128 * r + 8 * j + 2 * m + 1) * 1024);
    unsigned warm;
    {
      u64 pc; asm volatile("s_getpc_b64 %0" : "=s"(pc));
      warm = ld2_async(make_srd((const void*)(pc & ~127ull), CODEWARM), lane < 3 ? lane * 128 : 0x40000000u, (idx * 4 + m) * 384);
    }
    __builtin_amdgcn_s_barrier();
    v2u ep = ld8_sc1_async(sws, m == 0 ? WS_EPOCH : 0x40000000u);
    float sval;
    const int wi = blockIdx.x * 4 + m;
    const u64 wbase = wi < 448 ? (u64)w1 + wi * 4096 : wi < 896 ? (u64)w2 + (wi - 448) * 4096 : (u64)wo + (wi - 896) * 4096;
    auto make_stats = [&]() {
      wait_vm2<1>(xr[0], xr[1]);
      PIN(warm);
      float piv[2], a[4];
#pragma unroll
      for (int u = 0; u < 2; ++u) {
        piv[u] = rdl(bflo(xr[u][0]), 0);
        float s1 = 0.f, s2 = 0.f;
#pragma unroll
        for (int i = 0; i < 8; ++i) { const float d = ((i & 1) ? bfhi(xr[u][i >> 1]) : bflo(xr[u][i >> 1])) - piv[u]; s1 += d; s2 = __builtin_fmaf(d, d, s2); }
        a[2 * u] = s1; a[2 * u + 1] = s2;
      }
#pragma unroll
      for (int i = 0; i < 4; ++i) a[i] += dpp<0xB1>(a[i]);
#pragma unroll
      for (int i = 0; i < 4; ++i) a[i] += dpp<0x4E>(a[i]);
      float t = p4 == 0 ? a[0] : p4 == 1 ? a[1] : p4 == 2 ? a[2] : a[3];
      t += dpp<0x114>(t); t += dpp<0x118>(t);
      const auto s16 = __builtin_amdgcn_permlane16_swap(asu(t), asu(t), false, false);
      t = asf(s16[0]) + asf(s16[1]);
      const auto s32 = __builtin_amdgcn_permlane32_swap(asu(t), asu(t), false, false);
      t = (asf(s32[0]) + asf(s32[1])) * (1.f / DM);
      const float e1 = dpp<0xA0>(t);
      sval = (p4 & 1) ? __builtin_fmaxf(__builtin_fmaf(-e1, e1, t), 0.f) + 1e-5f : t + (p4 ? piv[1] : piv[0]);
      PIN(sval);
    };
    const unsigned acnt = lds0 + L_CNT;
    const unsigned vpos = 2048 + (g & 1) * 512 + h * 256 + (lane & 15) * 16;
    v4u qf[2], am0, am1;
    u64 um[16];
    auto masks = [&]() {
      const int dq = r32 - 4 * h;
#pragma unroll
      for (int u = 0; u < 16; ++u) { um[u] = __builtin_amdgcn_ballot_w64((u & 3) + 8 * (u >> 2) > dq); pin_s(um[u]); }
#pragma unroll
      for (int d = 0; d < 4; ++d) {
        am0[d] = __builtin_amdgcn_inverse_ballot_w64(um[2 * d]) ? 0xffffffffu : __builtin_amdgcn_inverse_ballot_w64(um[2 * d + 1]) ? 0xffff0000u : 0u;
        am1[d] = __builtin_amdgcn_inverse_ballot_w64(um[8 + 2 * d]) ? 0xffffffffu : __builtin_amdgcn_inverse_ballot_w64(um[9 + 2 * d]) ? 0xffff0000u : 0u;
      }
      PIN(am0); PIN(am1);
    };
    {
      v4u xf[32];
      f32x16 acc[3] = {};
      f32x4 cs[3] = {};
      v4u wf[4][3];
      unsigned wb[4];
#pragma unroll
      for (int p = 0; p < 4; ++p) { wb[p] = qbuf(0) + ((p + m) & 3) * 1024 + (8 * h + q4) * 64 + (g & 1) * 32 + p4 * 8; PIN(wb[p]); }
      auto rd = [&](int ks, int t) {
        if (t == 0) xf[ks] = lds128(xbuf(ks >> 2) + xo[ks & 3]);
        wf[ks & 3][t] = ldtr2(wb[ks & 3] + ((ks >> 2) % 3) * 12288 + t * 4096, 256);
      };
      auto mm = [&](int ks, int t) {
        acc[t] = t == 2 ? MFMA32(xf[ks], wf[ks & 3][t], acc[t]) : MFMA32(wf[ks & 3][t], xf[ks], acc[t]);
        if ((ks & 3) == 0) { cs[t] = MFMA16(sel, wf[ks & 3][t], cs[t]); PIN(cs[t]); }
        SBAR();
      };
      make_stats();
      bar();
      STAMP_AT(10);
#pragma unroll
      for (int t = 0; t < 6; ++t) rd(t / 3, t % 3);
      SBAR();
      static_for<0, 8>([&](auto sc) {
        constexpr int s = sc.value;
#pragma unroll
        for (int p = 0; p < 2; ++p) {
#pragma unroll
          for (int t = 0; t < 3; ++t) { mm(4 * s + p, t); rd(4 * s + p + 2, t); SBAR(); }
          if (s == 7 && p == 0) { cswr(4, cs[0]); cswr(5, cs[1]); cswr(6, cs[2]); SBAR(); }
        }
        if constexpr (s == 0) {
          wait_vmf<0>(ep);
          if (m == 0 && lane < 10)
            *(LAS unsigned*)(size_t)(lds0 + L_CNT + lane * 4) = lane == 8 ? ep[0] * 0x9E3779B1u + 0x7F4A7C15u : lane == 9 ? ep[1] * 0x85EBCA77u + 0x165667B1u : 0u;
        }
        if constexpr (s == 1) {
          get_tag();
          if ((lane >> 2) == 3) {
            const v2u gr = {asu(sval), tag32};
            __builtin_amdgcn_raw_buffer_store_b64(gr, rws, WS_STATS + (128 * r + 8 * j + 2 * m) * 16 + p4 * 8, 0, 16);
          }
        }
        if constexpr (s == MIDWARM) {
          warm = ld2_async(make_srd((const void*)wbase, 4096), lane * 128, 0);
        }
        if constexpr (s == 5) stat_load();
        if constexpr (s == 6) {
          wait_vm1<0>(st);
          PIN(warm);

          row_stats();
        }
        mm(4 * s + 2, 0); mm(4 * s + 2, 1);
        bar();
        if constexpr (s < 7) {
          STAMP_AT(11 + s);
          mm(4 * s + 2, 2);
          rd(4 * s + 4, 0); rd(4 * s + 4, 1); rd(4 * s + 4, 2); SBAR();
#pragma unroll
          for (int t = 0; t < 3; ++t) { mm(4 * s + 3, t); rd(4 * s + 5, t); SBAR(); }
        }
      });
      STAMP_AT(1);
      __builtin_amdgcn_s_setprio(3);
      const f32x4 c5 = csrd(5), c6 = csrd(6), c4 = csrd(4);
      SBAR();
      mm(30, 2); mm(31, 1);
      make_xaug();
      SBAR();
      mm(31, 2); mm(31, 0);
      if (h == 0) *(LAS float*)(size_t)(lds0 + L_RS + (m * 32 + r32) * 4) = rstd;
      acc[1] = MFMA32(waug(c5, 0u), xaug, acc[1]);
      acc[2] = MFMA32(xaug, waug(c6, 0u), acc[2]);
      acc[0] = MFMA32(waug(c4, 0u), xaug, acc[0]);
      SBAR();
      f32x4 rs[4];
#pragma unroll
      for (int qd = 0; qd < 4; ++qd) rs[qd] = __builtin_bit_cast(f32x4, lds128(lds0 + L_RS + (m * 32 + 8 * qd + 4 * h) * 4));
      const unsigned kvo = WS_KV + (j * 64 + T) * 4096;
#pragma unroll
      for (int e = 0; e < 16; ++e) acc[1][e] = MUL1(acc[1][e], rstd);
#pragma unroll
      for (int e = 0; e < 2; ++e) {
        const v4u f = pack8(acc[1], e);
        st16_sc1(f, sws, lane * 16, kvo + e * 1024);
        sts128(lds0 + L_KV + (8 + m) * KVT + e * 1024 + lane * 16, f);
      }
#pragma unroll
      for (int qd = 0; qd < 4; ++qd)
#pragma unroll
        for (int e = 0; e < 4; ++e) acc[2][4 * qd + e] = MUL1(acc[2][4 * qd + e], rs[qd][e]);
#pragma unroll
      for (int s = 0; s < 2; ++s) {
        const v4u f = pack8(acc[2], s);
        st16_sc1(f, sws, vpos, kvo + s * 1024);
        sts128(lds0 + L_KV + (8 + m) * KVT + s * VB + vpos, f);
      }
      SBAR();
      const float qs = rstd * (0.25f * 1.4426950408889634f);
#pragma unroll
      for (int e = 0; e < 16; ++e) acc[0][e] = MUL1(acc[0][e], qs);
      qf[0] = pack8(acc[0], 0); qf[1] = pack8(acc[0], 1);
      PIN(qf[0]); PIN(qf[1]);
      arrive_nowait(acnt);
      STAMP_AT(2);
      if (heavy) {
        hsetup(1);
        await(acnt + 16, 4);
        __builtin_amdgcn_s_setprio(1);
        STAMP_AT(3);
        hmm(std::integral_constant<int, 1>{}, xf, [&](auto kc) {
          constexpr int ks = kc.value;
          if constexpr (ks == KVPUB) {
            wait_vm<0>();
            if (lane == 0) __hip_atomic_store(flag(FL_KV, T, j), tag, RLX_AGENT);
          }
          if constexpr (ks == KVPUB + 2) bias_load(1);
          if constexpr (ks == 31) { wait_vm<0>(); PIN(bias0); PIN(bias1); }
        });
        STAMP_AT(18);
        __builtin_amdgcn_s_setprio(3);
        hgelu(std::integral_constant<int, 1>{}, [&]() {});
        STAMP_AT(4);
      } else {
        wait_vm<0>();
        if (lane == 0) __hip_atomic_store(flag(FL_KV, T, j), tag, RLX_AGENT);
      }
    }
    masks();
    STAMP_AT(19);
    const unsigned kb = lds0 + L_KV + m * KVT + lane * 16;
    unsigned vb[2];
#pragma unroll
    for (int e = 0; e < 2; ++e) { vb[e] = lds0 + L_KV + m * KVT + 2048 + ((g & 1) ? 1024 : e * 512 + h * 256 + (lane & 15) * 16); PIN(vb[e]); }
    bool pend = true;
    if (__builtin_amdgcn_readfirstlane(peek(acnt)) < 8) {
      wait_vm<0>();
      if (lane == 0) __hip_atomic_store(flag(FL_H1, T, j), tag, RLX_AGENT);
      pend = false;
      await(acnt, 8);
    }
    asm volatile("" ::: "memory");
    __builtin_amdgcn_s_setprio(PRIO_ATT);
    STAMP_AT(5);
    const f32x16 zero = {};
    auto o_store = [&](f32x16& o, int e) {
      const float rl = __builtin_amdgcn_rcpf(o[8]);
#pragma unroll
      for (int u = 0; u < 8; ++u) o[u] = MUL1(o[u], rl);
      st16_sc1(pack8(o, 0), sws, lane * 16, WS_AF + (T * AFT + 112 + 2 * j + e) * 1024);
    };
    int redo = 0;
    {
      f32x16 o[2], sa, sb, su;
      v4u kn, ku, va, vc, vn0, vn1, vu0, vu1, p0, p1, pu;
      auto kaddr = [&](int n) { return kb + ((n & 7) + 1) * KVT + (n >> 3) * 1024; };
      auto vaddr = [&](int n) { return vb[n >> 3] + ((n & 7) + 1) * KVT; };
      kn = lds128(kaddr(0)); vn0 = lds128(vaddr(0)); vn1 = lds128(vaddr(0) + VB);
      sa = MFMA32(kn, qf[0], zero);
      kn = lds128(kaddr(1));
      SBAR();
      auto step = [&](auto nc, f32x16& sc, f32x16& sn) {
        constexpr int n = nc.value, e = n >> 3, i = n & 7;
        if constexpr (n < 15) sn = MFMA32(kn, qf[(n + 1) >> 3], zero);
        if constexpr (n < 14) kn = lds128(kaddr(n + 2));
        if constexpr (i == 5) ku = lds128(kb + e * 1024);
        v4u v0 = vn0, v1 = vn1;
        if constexpr (n < 15) { vn0 = lds128(vaddr(n + 1)); vn1 = lds128(vaddr(n + 1) + VB); }
        if constexpr (i == 6) { vu0 = lds128(vb[e]); vu1 = lds128(vb[e] + VB); }
        SBAR();
        if constexpr (i == 7) {
#pragma unroll
          for (int u = 0; u < 16; ++u) sc[u] = __builtin_amdgcn_inverse_ballot_w64(um[u]) ? su[u] : sc[u];
          PIN(sc); SBAR();
        }
#pragma unroll
        for (int u = 0; u < 6; ++u) sc[u] = EXP2(sc[u]);
        PIN(sc); SBAR();
        if constexpr (n == 8) { o[0] = MFMA32(vc, p1, o[0]); SBAR(); }
        else if constexpr (n > 0) { o[e] = MFMA32(vc, p1, o[e]); SBAR(); }
        sc[6] = EXP2(sc[6]); sc[7] = EXP2(sc[7]);
        p0 = pack8(sc, 0);
        sc[8] = EXP2(sc[8]); sc[9] = EXP2(sc[9]);
        if constexpr (i == 7) { va = p0 & am0; p0 &= ~am0; PIN(va); }
        PIN(sc); PIN(p0); SBAR();
        if constexpr (n == 8) { o[0] = MFMA32(vu1, pu, o[0]); SBAR(); }
        o[e] = MFMA32(v0, p0, i == 0 ? zero : o[e]);
        SBAR();
        if constexpr (i == 6) { su = MFMA32(ku, qf[e], zero); SBAR(); }
#pragma unroll
        for (int u = 10; u < 13; ++u) sc[u] = EXP2(sc[u]);
        if constexpr (i == 7) { PIN(sc); SBAR(); o[e] = MFMA32(vu0, va, o[e]); SBAR(); }
#pragma unroll
        for (int u = 13; u < 16; ++u) sc[u] = EXP2(sc[u]);
        p1 = pack8(sc, 1); vc = v1;
        if constexpr (i == 7) { pu = p1 & am1; p1 &= ~am1; PIN(pu); }
        PIN(p1); SBAR();
        if constexpr (n == 8) {
          if (__all(o[0][8] >= 0x1p-30f && o[0][8] <= 0x1p30f)) o_store(o[0], 0); else redo = 1;
          SBAR();
        }
        if constexpr (n == 10) pend = !redo;
        if constexpr (i == 3 || i == 5 || i == 7) {
          if (pend && (i == 7 || (__builtin_amdgcn_s_getreg(7 | (3 << 11)) & 15) == 0)) {
            wait_vm<0>();
            if (lane == 0) __hip_atomic_store(flag(FL_H1 + e, T, j), tag, RLX_AGENT);
            pend = false;
          }
        }
      };
      static_for<0, 8>([&](auto hc) {
        step(std::integral_constant<int, 2 * hc.value>{}, sa, sb);
        step(std::integral_constant<int, 2 * hc.value + 1>{}, sb, sa);
      });
      o[1] = MFMA32(vc, p1, o[1]);
      o[1] = MFMA32(vu1, pu, o[1]);
      if (__all(o[1][8] >= 0x1p-30f && o[1][8] <= 0x1p30f)) o_store(o[1], 1); else redo |= 2;
    }
    if (redo) {
#pragma unroll 1
      for (int e = 0; e < 2; ++e) {
        if (!((redo >> e) & 1)) continue;
        const v4u q = e ? qf[1] : qf[0];
        const unsigned vbe = e ? vb[1] : vb[0];
        auto merged = [&](const f32x16& c) {
          f32x16 sl = MFMA32(lds128(kb + 8 * KVT + e * 1024), q, c);
          const f32x16 su = MFMA32(lds128(kb + e * 1024), q, c);
          static_for<0, 16>([&](auto u) { sl[u.value] = upper<u.value>() ? su[u.value] : sl[u.value]; });
          return sl;
        };
        float mx = -INFINITY;
#pragma unroll 1
        for (int i = 0; i < 8; ++i) {
          const f32x16 sc = i ? MFMA32(lds128(kb + i * KVT + e * 1024), q, zero) : merged(zero);
#pragma unroll
          for (int u = 0; u < 16; u += 2) mx = max3(mx, sc[u], sc[u + 1]);
        }
        const auto sw = __builtin_amdgcn_permlane32_swap(asu(mx), asu(mx), false, false);
        mx = __builtin_fmaxf(asf(sw[0]), asf(sw[1]));
        f32x16 negm, o;
#pragma unroll
        for (int u = 0; u < 16; ++u) negm[u] = -mx;
        {
          f32x16 sc = merged(negm);
#pragma unroll
          for (int u = 0; u < 16; ++u) sc[u] = EXP2(sc[u]);
          const v4u p0 = pack8(sc, 0), p1 = pack8(sc, 1);
          o = MFMA32(lds128(vbe), p0 & am0, zero);
          o = MFMA32(lds128(vbe + VB), p1 & am1, o);
          o = MFMA32(lds128(vbe + 8 * KVT), p0 & ~am0, o);
          o = MFMA32(lds128(vbe + 8 * KVT + VB), p1 & ~am1, o);
        }
#pragma unroll 1
        for (int i = 1; i < 8; ++i) {
          f32x16 sc = MFMA32(lds128(kb + i * KVT + e * 1024), q, negm);
#pragma unroll
          for (int u = 0; u < 16; ++u) sc[u] = EXP2(sc[u]);
          o = MFMA32(lds128(vbe + i * KVT), pack8(sc, 0), o);
          o = MFMA32(lds128(vbe + i * KVT + VB), pack8(sc, 1), o);
        }
        o_store(o, e);
      }
      wait_vm<0>();
      if (lane == 0) __hip_atomic_store(flag(FL_O0, T, j), tag, RLX_AGENT);
    }
    STAMP_AT(6);
    wait_vm<0>();
    if (lane == 0) __hip_atomic_store(flag(FL_O1, T, j), tag, RLX_AGENT);
    STAMP_AT(7);
    res_loads();
    wait_vm<0>();
    res_sum();
    if (blockIdx.x == 0 && wave == 7) {
#pragma unroll 1
      for (int i = 0; i < 4; ++i) poll_flags(flag(FL_KV, 4 * (4 * i + g), lane & 15), tag);
      if (lane == 0) __hip_atomic_store((GAS u64*)(ws + WS_EPOCH), tag, RLX_AGENT);
    }
  }
  {
    int t2 = threadIdx.x; PIN(t2);
    const int row = t2 >> 3, ch = t2 & 7;
    unsigned a = lds0 + L_WG + (((row >> 5) * 8 + ch) * 66 + (row & 31)) * 16, oo = (64 * rt + row) * 1024 + (64 * ct + ch * 8) * 2;
    PIN(a); PIN(oo);
    bar();
    f32x4 pl[4], ph[4];
#pragma unroll
    for (int w = 0; w < 4; ++w) { pl[w] = __builtin_bit_cast(f32x4, lds128(a + w * PART)); ph[w] = __builtin_bit_cast(f32x4, lds128(a + w * PART + 512)); }
    SBAR();
#pragma unroll
    for (int w = 0; w < 4; ++w) { lo += pl[w]; hi += ph[w]; }
    const v4u o = {pk2(lo[0], lo[1]), pk2(lo[2], lo[3]), pk2(hi[0], hi[1]), pk2(hi[2], hi[3])};
    __builtin_amdgcn_raw_buffer_store_b128(o, __builtin_amdgcn_make_buffer_rsrc(out, 0, SEQ * DM * 2, 0x00020000), oo, 0, 16);
  }
  STAMP_AT(8);
}

extern "C" void kernel_launch(void* const* d_in, const int* in_sizes, int n_in, void* d_out, int out_size, void* d_ws, size_t ws_size,
                              hipStream_t stream) {
  (void)in_sizes; (void)n_in; (void)out_size; (void)ws_size;
  (void)hipFuncSetAttribute((const void*)fused_block, hipFuncAttributeMaxDynamicSharedMemorySize, LDS_BYTES);
  fused_block<<<256, 512, LDS_BYTES, stream>>>(d_in[0], d_in[1], d_in[2], d_in[4], d_in[5], d_in[6], d_in[7], d_out, (char*)d_ws);
}
```

```cpp
#define HCKV 6
#include <hip/hip_runtime.h>
#include <hip/hip_bf16.h>
#include <stdint.h>
#include <type_traits>

typedef unsigned v4u __attribute__((ext_vector_type(4)));
typedef unsigned v2u __attribute__((ext_vector_type(2)));
typedef float f32x2 __attribute__((ext_vector_type(2)));
typedef float f32x4 __attribute__((ext_vector_type(4)));
typedef float f32x16 __attribute__((ext_vector_type(16)));
typedef short bf16x8 __attribute__((ext_vector_type(8)));
typedef short s16x4 __attribute__((ext_vector_type(4)));
typedef __bf16 bf16x2_t __attribute__((ext_vector_type(2)));
typedef unsigned long long u64;
#define GAS __attribute__((address_space(1)))
#define LAS __attribute__((address_space(3)))
#define DI __device__ __forceinline__
#define RLX_AGENT __ATOMIC_RELAXED, __HIP_MEMORY_SCOPE_AGENT

constexpr int SEQ = 2048, DM = 512, FF = 1792;
constexpr unsigned WS_EPOCH = 0;
constexpr unsigned WS_STATS = 32768;
constexpr unsigned WS_FLAG = 65536;
constexpr unsigned WS_KV = WS_FLAG + 5 * 1024 * 128;
constexpr int AFT = 145;
constexpr unsigned WS_AF = WS_KV + 16 * 64 * 4096;
constexpr unsigned WS_END = WS_AF + 64 * AFT * 1024;
constexpr unsigned WS_STAMP = WS_END;
enum { FL_KV = 0, FL_H0 = 1, FL_H1 = 2, FL_O0 = 3, FL_O1 = 4 };
constexpr int L_WA = 0;
constexpr int L_WG = 86016;
constexpr int VB = 1040;
constexpr int KVT = 2048 + 2 * VB;
constexpr int L_KV = 32768;
constexpr int RD = 8, NR = 12;
constexpr int PART = 16 * 66 * 16;
constexpr int L_CS = L_WG + 4 * PART;
constexpr int L_RS = L_CS + 3584;
constexpr int L_CNT = L_RS + 512;
constexpr int LDS_BYTES = L_CNT + 64;
constexpr int xbuf(int s) { return ((s + 1) % 3) * 16384; }
constexpr int qbuf(int s) { return 49152 + (s % 3) * 12288; }
constexpr int w1buf(int s) { return L_WG + (s % 3) * PART + 8192; }
static_assert(L_KV + 8 * KVT >= 65536 && xbuf(7) == 32768 && qbuf(2) + 12288 <= L_WG && L_KV + 12 * KVT <= L_WG && PART >= RD * 2048 && LDS_BYTES <= 163840, "lds map");
#define AWIN 6
constexpr int AW = AWIN;
constexpr unsigned SPIN_CAP = 1u << 23;

#define MFMA32(a, b, c) __builtin_amdgcn_mfma_f32_32x32x16_bf16(__builtin_bit_cast(bf16x8, a), __builtin_bit_cast(bf16x8, b), c, 0, 0, 0)
#define MFMA16(a, b, c) __builtin_amdgcn_mfma_f32_16x16x32_bf16(__builtin_bit_cast(bf16x8, a), __builtin_bit_cast(bf16x8, b), c, 0, 0, 0)
#define SBAR() __builtin_amdgcn_sched_barrier(0)

#define STAMP_AT(i) do { } while (0)

DI float asf(unsigned u) { return __builtin_bit_cast(float, u); }
DI unsigned asu(float f) { return __builtin_bit_cast(unsigned, f); }
DI unsigned pk2(float lo, float hi) { return __builtin_bit_cast(unsigned, __builtin_convertvector((f32x2){lo, hi}, bf16x2_t)); }
DI float bflo(unsigned u) { return __builtin_bit_cast(float, u << 16); }
DI float bfhi(unsigned u) { return __builtin_bit_cast(float, u & 0xffff0000u); }
DI v4u make_srd(const void* p, unsigned bytes) { const u64 a = (u64)p; return (v4u){(unsigned)a, (unsigned)(a >> 32) & 0xffffu, bytes, 0x00020000u}; }
DI void dma16(v4u srd, unsigned voff, unsigned soff, unsigned lds) {
  asm volatile("s_mov_b32 m0, %3\n\ts_nop 0\n\tbuffer_load_dwordx4 %0, %1, %2 offen lds" :: "v"(voff), "s"(srd), "s"(soff), "s"(lds) : "memory");
}
DI void dma16_sc1(v4u srd, unsigned voff, unsigned soff, unsigned lds) {
  asm volatile("s_mov_b32 m0, %3\n\ts_nop 0\n\tbuffer_load_dwordx4 %0, %1, %2 offen sc1 lds" :: "v"(voff), "s"(srd), "s"(soff), "s"(lds) : "memory");
}
DI void dma16_a(v4u srd, unsigned voff, unsigned soff, unsigned lds) { dma16(srd, voff, soff, lds); }
DI void dma16_b(v4u srd, unsigned voff1, unsigned soff) {
  asm volatile("buffer_load_dwordx4 %0, %1, %2 offen offset:1024 lds" :: "v"(voff1), "s"(srd), "s"(soff) : "memory");
}
DI v4u ld16_sc1_async(v4u srd, unsigned voff, unsigned soff) {
  v4u r; asm volatile("buffer_load_dwordx4 %0, %1, %2, %3 offen sc1" : "=v"(r) : "v"(voff), "s"(srd), "s"(soff) : "memory"); return r;
}
DI v4u ld16_async(v4u srd, unsigned voff, unsigned soff) {
  v4u r; asm volatile("buffer_load_dwordx4 %0, %1, %2, %3 offen" : "=v"(r) : "v"(voff), "s"(srd), "s"(soff) : "memory"); return r;
}
DI unsigned ld2_async(v4u srd, unsigned voff, unsigned soff) {
  unsigned r; asm volatile("buffer_load_ushort %0, %1, %2, %3 offen" : "=v"(r) : "v"(voff), "s"(srd), "s"(soff) : "memory"); return r;
}
DI void st16_sc1(v4u v, v4u srd, unsigned voff, unsigned soff) {
  asm volatile("buffer_store_dwordx4 %0, %1, %2, %3 offen sc1\n\ts_nop 1" :: "v"(v), "v"(voff), "s"(srd), "s"(soff) : "memory");
}
template <int N> DI void wait_vm1(v4u& a) { asm volatile("s_waitcnt vmcnt(%1)" : "+v"(a) : "i"(N) : "memory"); }
template <int N> DI void wait_vm2(v4u& a, v4u& b) { asm volatile("s_waitcnt vmcnt(%2)" : "+v"(a), "+v"(b) : "i"(N) : "memory"); }
template <int N> DI void wait_vm3(v4u& a, v4u& b, v4u& c) { asm volatile("s_waitcnt vmcnt(%3)" : "+v"(a), "+v"(b), "+v"(c) : "i"(N) : "memory"); }
DI void pin_s(u64& v) { asm volatile("" : "+s"(v)); }
template <class V> DI void pin(V& v) { asm volatile("" : "+v"(v)); }
template <int N> DI void wait_vm() { asm volatile("s_waitcnt vmcnt(%0)" :: "i"(N) : "memory"); }
DI void bar() { asm volatile("s_waitcnt lgkmcnt(0)\n\ts_barrier" ::: "memory"); }
DI v4u lds128(unsigned a) { return *(const LAS v4u*)(size_t)a; }
DI void sts128(unsigned a, v4u v) { *(LAS v4u*)(size_t)a = v; }
DI v2u ldtr(unsigned a) { return __builtin_bit_cast(v2u, __builtin_amdgcn_ds_read_tr16_b64_v4i16((LAS s16x4*)(size_t)a)); }
DI v4u ldtr2(unsigned a, int second) { const v2u lo = ldtr(a), hi = ldtr(a + second); return (v4u){lo.x, lo.y, hi.x, hi.y}; }
template <int CTRL> DI float dpp(float v) { return __builtin_bit_cast(float, __builtin_amdgcn_update_dpp(0, __builtin_bit_cast(int, v), CTRL, 0xf, 0xf, true)); }
DI float rdl(float v, int l) { return __builtin_bit_cast(float, __builtin_amdgcn_readlane(__builtin_bit_cast(int, v), l)); }
DI v4u pack8(const f32x16& a, int s) { return (v4u){pk2(a[8 * s], a[8 * s + 1]), pk2(a[8 * s + 2], a[8 * s + 3]), pk2(a[8 * s + 4], a[8 * s + 5]), pk2(a[8 * s + 6], a[8 * s + 7])}; }
DI void poll_flags(const GAS u64* p, u64 tag) {
#pragma unroll 1
  for (unsigned spin = 0; spin < SPIN_CAP; ++spin) {
    const u64 v = __hip_atomic_load(p, RLX_AGENT);
    if (__all(v == tag)) break;
    __builtin_amdgcn_s_sleep(1);
  }
  asm volatile("" ::: "memory");
}
constexpr u64 upper_mask(int reg) {
  u64 m = 0;
  for (int l = 0; l < 64; ++l) { const int row = (reg & 3) + 8 * (reg >> 2) + 4 * (l >> 5); if (row > (l & 31)) m |= 1ull << l; }
  return m;
}
template <int REG> DI bool upper() { return __builtin_amdgcn_inverse_ballot_w64(upper_mask(REG)); }
template <int I, int N, class Fn> DI void static_for(Fn&& f) { if constexpr (I < N) { f(std::integral_constant<int, I>{}); static_for<I + 1, N>(f); } }
DI float max3(float a, float b, float c) { return __builtin_fmaxf(__builtin_fmaxf(a, b), c); }

#define FLAHEAD 4
constexpr int FLA = FLAHEAD, H1_CK = 16 - AW, O0_CK = 28 - AW, O1_CK = 32 - AW, RES_LD = 12;
constexpr bool fl_load(int t) { return t == H1_CK - FLA || t == O0_CK - FLA || t == O1_CK - FLA; }
constexpr int g2_issued(int t) { return ((t >= 1 && t - 1 + NR + RD < 36) ? 2 : 0) + ((t + AW < 36) ? 2 : 0) + fl_load(t) + (t == RES_LD ? 2 : 0); }
constexpr int g2_range(int a, int b) { int n = 0; for (int t = a; t < b; ++t) n += g2_issued(t); return n; }
#define GELU_PK(P) (P == 1)
#define PRIO_G2 2
#define PRIO_ATT 3
#define PRIO_G1 2
#define ROT r
#define CODEWARM 38000
#define MIDWARM 1
#define KVS 1
constexpr int HCK = HCKV, HLD = HCK - 3;
constexpr int pre_ops(int k) { return (k == HLD) + (k >= HCK && k < HCK + 3 ? 4 : 0) + (k >= 1 ? 2 : 0); }
constexpr int pre_range(int a, int b) { int n = 0; for (int k = a; k < b; ++k) n += pre_ops(k); return n; }
constexpr int first_wait(int kk) {
  const int a = (kk & 1 ? 0 : 2) + 2 + pre_range(HCK + kk / 2 + 1, NR + 1), w = pre_range(kk + NR - RD + 2, NR + 1);
  return (a < w ? a : w) + g2_range(0, kk);
}
#define KVPUB 2
#define EXP2(v) __builtin_amdgcn_exp2f(v)
#define PIN(v) pin(v)
extern "C" __device__ float mul_unpacked(float, float) __asm("llvm.amdgcn.fmul.legacy");
#define MUL1(a, b) mul_unpacked(a, b)
#define FMA1(a, b, c) __builtin_amdgcn_div_fmasf(a, b, c, false)
DI v2u ld8_sc1_async(v4u srd, unsigned voff) {
  v2u r; asm volatile("buffer_load_dwordx2 %0, %1, %2, 0 offen sc1" : "=v"(r) : "v"(voff), "s"(srd) : "memory"); return r;
}
template <int N> DI void wait_vmf(v2u& a) { asm volatile("s_waitcnt vmcnt(%1)" : "+v"(a) : "i"(N) : "memory"); }
DI void arrive(unsigned cnt, int lane) {
  asm volatile("s_waitcnt lgkmcnt(0)" ::: "memory");
  if (lane == 0) __hip_atomic_fetch_add((LAS unsigned*)(size_t)cnt, 1u, __ATOMIC_RELAXED, __HIP_MEMORY_SCOPE_WORKGROUP);
}
DI void arrive_nowait(unsigned cnt) {
  asm volatile("s_mov_b64 exec, 1\n\tds_add_u32 %0, %1\n\ts_mov_b64 exec, -1" :: "v"(cnt), "v"(1u) : "memory");
}
DI unsigned peek(unsigned cnt) { return __hip_atomic_load((LAS unsigned*)(size_t)cnt, __ATOMIC_RELAXED, __HIP_MEMORY_SCOPE_WORKGROUP); }
DI void await(unsigned cnt, unsigned n) {
#pragma unroll 1
  for (unsigned spin = 0; spin < SPIN_CAP * 32; ++spin) {
    if (__builtin_amdgcn_readfirstlane(peek(cnt)) >= n) break;
    __builtin_amdgcn_s_sleep(1);
  }
  asm volatile("" ::: "memory");
}

__global__ __launch_bounds__(512, 2) void fused_block(const void* __restrict__ x, const void* __restrict__ wqkv, const void* __restrict__ wo,
                                                      const void* __restrict__ w1, const void* __restrict__ b1, const void* __restrict__ w2,
                                                      const void* __restrict__ b2, void* __restrict__ out, char* __restrict__ ws) {
  constexpr unsigned lds0 = 0;
  const int wave = __builtin_amdgcn_readfirstlane(threadIdx.x >> 6);
  const int m = wave & 3;
  const bool isG = wave < 4;
  const int xcd = blockIdx.x & 7, idx = blockIdx.x >> 3;
  const int r = (xcd >> 2) * 8 + (idx >> 2), j = (xcd & 3) * 4 + (idx & 3);
  const int rt = (xcd >> 1) * 8 + (idx >> 2), ct = (xcd & 1) * 4 + (idx & 3);
  int lane = threadIdx.x & 63, r32, h, g, q4, p4;
  STAMP_AT(0);
  asm volatile("" :: "s"(x), "s"(wqkv), "s"(wo), "s"(w1), "s"(b1), "s"(w2), "s"(b2), "s"(out), "s"(ws));

  const v4u sx = make_srd(x, SEQ * DM * 2), sws = make_srd(ws, WS_END);
  const __amdgpu_buffer_rsrc_t rws = __builtin_amdgcn_make_buffer_rsrc(ws, 0, WS_END, 0x00020000);
  const unsigned one2 = 0x3f803f80u;
  const int T = 4 * r + m;
  const int rot = ROT;
  auto flag_off = [&](int kind, int tile, int jj) { return WS_FLAG + ((kind * 64 + tile) * 16 + jj) * 128; };
  auto flag = [&](int kind, int tile, int jj) { return (GAS u64*)(ws + flag_off(kind, tile, jj)); };
  u64 tag;
  unsigned taglo, tag32;
  auto get_tag = [&]() {
    const v2u tg = *(const LAS v2u*)(size_t)(lds0 + L_CNT + 32);
    taglo = __builtin_amdgcn_readfirstlane(tg[0]); tag32 = __builtin_amdgcn_readfirstlane(tg[1]);
    tag = ((u64)tag32 << 32) | taglo;
  };
  v4u xres, bres, xaug, st;
  float mean, var, rstd, sd;
  v4u sel;
  unsigned xo[4], swz;
  auto lane_setup = [&]() {
    lane = threadIdx.x & 63; PIN(lane);
    r32 = lane & 31; h = lane >> 5; g = lane >> 4; q4 = (lane >> 2) & 3; p4 = lane & 3;
    const unsigned selv = ((lane & 15) == (g & 1)) ? one2 : 0u;
    sel = (v4u){selv, selv, selv, selv};
#pragma unroll
    for (int p = 0; p < 4; ++p) xo[p] = (32 * m + r32) * 128 + (((2 * ((p + m) & 3) + h) ^ ((r32 >> 1) & 7)) * 16);
    swz = ((((lane >> 2) & 1) ^ (g & 1)) * 64) + p4 * 16;
  };

  auto stat_load = [&]() { st = ld16_sc1_async(sws, WS_STATS + (128 * r + 32 * m + r32) * 16, 0); };
  auto res_loads = [&]() {
    int t2 = threadIdx.x; PIN(t2);
    const int row = 64 * rt + (t2 >> 3), col = 64 * ct + (t2 & 7) * 8;
    xres = ld16_async(sx, row * 1024 + col * 2, 0);
    bres = ld16_async(make_srd(b2, DM * 2), col * 2, 0);
  };
  f32x4 lo, hi;
  auto res_sum = [&]() {
    PIN(xres); PIN(bres);
    lo = (f32x4){bflo(xres[0]) + bflo(bres[0]), bfhi(xres[0]) + bfhi(bres[0]), bflo(xres[1]) + bflo(bres[1]), bfhi(xres[1]) + bfhi(bres[1])};
    hi = (f32x4){bflo(xres[2]) + bflo(bres[2]), bfhi(xres[2]) + bfhi(bres[2]), bflo(xres[3]) + bflo(bres[3]), bfhi(xres[3]) + bfhi(bres[3])};
    PIN(lo); PIN(hi);
  };
  auto row_stats = [&]() {
    if (!__all(st[1] == tag32 && st[3] == tag32)) {
#pragma unroll 1
      for (unsigned spin = 0; spin < SPIN_CAP; ++spin) {
        st = ld16_sc1_async(sws, WS_STATS + (128 * r + 32 * m + r32) * 16, 0);
        wait_vm1<0>(st);
        if (__all(st[1] == tag32 && st[3] == tag32)) break;
      }
    }
    mean = asf(st[0]); var = asf(st[2]);
    PIN(mean); PIN(var);
  };
  auto make_xaug = [&]() {
    PIN(mean); PIN(var);
    rstd = __builtin_amdgcn_rsqf(var); sd = var * rstd;
    const unsigned uh = pk2(-mean, sd);
    const unsigned ul = pk2(-mean - bflo(uh), FMA1(bfhi(uh), -1.f, sd));
    xaug = (v4u){(uh & 0xffffu) | (uh << 16), (ul & 0xffffu) | (uh & 0xffff0000u), ul >> 16, 0u};
    if (h) xaug = (v4u){0u, 0u, 0u, 0u};
    PIN(xaug);
  };
  auto cswr = [&](int tile, f32x4 c) {
    if (lane < 16) { LAS float* p = (LAS float*)(size_t)(lds0 + L_CS + (tile * 32 + lane) * 16 + m * 4); p[0] = c[0]; p[64] = c[1]; }
  };
  auto csrd = [&](int tile) { return __builtin_bit_cast(f32x4, lds128(lds0 + L_CS + (tile * 32 + r32) * 16)); };
  auto waug = [&](f32x4 c4, unsigned b16) {
    const float c = ((c4[0] + c4[1]) + c4[2]) + c4[3];
    const unsigned uh = pk2(c, 0.f);
    const unsigned ul = pk2(c - bflo(uh), 0.f);
    return (v4u){uh | (ul << 16), uh | (b16 << 16), b16, 0u};
  };

  const v4u sw1 = make_srd(w1, DM * FF * 2);
  const int hcol0 = 448 * (j >> 2) + 128 * (j & 3);
  const bool heavy = (j & 3) != 3;
  unsigned bias0, bias1;
  auto bias_load = [&](int P) {
    bias0 = ld2_async(make_srd(b1, FF * 2), r32 * 2, (hcol0 + 64 * P) * 2);
    bias1 = ld2_async(make_srd(b1, FF * 2), r32 * 2, (hcol0 + 64 * P + 32) * 2);
  };
  auto w1dma = [&](int P, int c, unsigned dst, int half) {
    const unsigned vw1 = (lane >> 3) * (FF * 2) + swz, vw1b = vw1 + 8 * (FF * 2) - 1024;
    const unsigned so = (64 * ((c + rot) & 7) + 16 * m) * (FF * 2) + (hcol0 + 64 * P) * 2;
    if (half == 0) dma16_a(sw1, vw1, so, dst + m * 2048); else dma16_b(sw1, vw1b, so);
  };
  f32x16 hac[2];
  unsigned rk[4][2];
  auto hsetup = [&](int P) {
#pragma unroll
    for (int p = 0; p < 4; ++p)
#pragma unroll
      for (int c = 0; c < 2; ++c) {
        rk[p][c] = lds0 + (P ? L_WA : w1buf(0)) + ((p + m) & 3) * 2048 + h * 1024 + q4 * 128 + ((c ^ (q4 >> 1)) * 64) + (g & 1) * 32 + p4 * 8;
        PIN(rk[p][c]);
      }
  };
  auto hmm = [&](auto pc, const v4u (&xf)[32], auto&& hook) {
    constexpr int P = pc.value;
    constexpr int D = 4;
    const unsigned cnt = lds0 + L_CNT + 4;
    f32x4 cs[2] = {}, c0, c1;
    v4u wf[D][2], au0, au1;
    unsigned seen = 0;
    hac[0] = (f32x16){}; hac[1] = (f32x16){};
    auto rd = [&](int ks, int t) { wf[ks % D][t] = ldtr2(rk[ks & 3][t] + ((ks >> 2) ^ 4) * 8192, 512); };
#pragma unroll
    for (int ks = 0; ks < D; ++ks) { rd(ks, 0); rd(ks, 1); }
    SBAR();
    static_for<0, 32>([&](auto kc) {
      constexpr int ks = kc.value;
      hook(kc);
      if constexpr (ks == 12) arrive_nowait(cnt + 16);
      if constexpr (ks == 29) seen = peek(cnt);
#pragma unroll
      for (int t = 0; t < 2; ++t) {
        hac[t] = MFMA32(wf[ks % D][t], xf[ks ^ 16], hac[t]);
        if ((ks & 3) == 0 && ks < 28) { cs[t] = MFMA16(sel, wf[ks % D][t], cs[t]); PIN(cs[t]); }
        if (ks == 26) { cs[t] = MFMA16(sel, wf[28 % D][t], cs[t]); PIN(cs[t]); }
        SBAR();
        if constexpr (ks + D < 32) rd(ks + D, t);
        if constexpr (ks == 31) { if (t == 0) { au0 = waug(c0, bias0); PIN(au0); } else { au1 = waug(c1, bias1); PIN(au1); } }
        SBAR();
      }
      if constexpr (ks == 30) {
        PIN(hac[0]); PIN(hac[1]);
        if (__builtin_amdgcn_readfirstlane(seen) < 4) await(cnt, 4);
        asm volatile("" ::: "memory");
        if constexpr (ks == 30) { c0 = csrd(2 * P); c1 = csrd(2 * P + 1); }
      }
      if constexpr (ks == 27) { cswr(2 * P, cs[0]); cswr(2 * P + 1, cs[1]); arrive_nowait(cnt); }
      SBAR();
    });
    hac[0] = MFMA32(au0, xaug, hac[0]);
    hac[1] = MFMA32(au1, xaug, hac[1]);
    SBAR();
  };
  auto hgelu = [&](auto pc, auto&& mid) {
    constexpr int P = pc.value;
    constexpr bool PK = GELU_PK(P);
    const float C0 = 2.3022081986f, C1 = C0 * 0.044715f;
    const float ga = -C1 * rstd * rstd * rstd, gb = -C0 * rstd;
    auto mul = [&](f32x2 a, f32x2 b) { return PK ? a * b : (f32x2){MUL1(a[0], b[0]), MUL1(a[1], b[1])}; };
    auto fma = [&](f32x2 a, float b, float c) {
      return PK ? __builtin_elementwise_fma(a, (f32x2){b, b}, (f32x2){c, c}) : (f32x2){FMA1(a[0], b, c), FMA1(a[1], b, c)}; };
#pragma unroll
    for (int t = 0; t < 2; ++t) {
      f32x2 v[8], z[8];
#pragma unroll
      for (int e = 0; e < 8; ++e) { v[e] = (f32x2){hac[t][2 * e], hac[t][2 * e + 1]}; z[e] = mul(v[e], v[e]); }
      SBAR();
#pragma unroll
      for (int e = 0; e < 8; ++e) z[e] = fma(z[e], ga, gb);
      SBAR();
#pragma unroll
      for (int e = 0; e < 8; ++e) z[e] = mul(z[e], v[e]);
      SBAR();
#pragma unroll
      for (int e = 0; e < 8; ++e) z[e] = (f32x2){EXP2(z[e][0]), EXP2(z[e][1])};
      SBAR();
#pragma unroll
      for (int e = 0; e < 8; ++e) z[e] = fma(z[e], sd, sd);
      SBAR();
#pragma unroll
      for (int e = 0; e < 8; ++e) z[e] = (f32x2){__builtin_amdgcn_rcpf(z[e][0]), __builtin_amdgcn_rcpf(z[e][1])};
      SBAR();
#pragma unroll
      for (int e = 0; e < 8; ++e) z[e] = mul(z[e], v[e]);
      SBAR();
#pragma unroll
      for (int s = 0; s < 2; ++s) {
        const v4u f = {pk2(z[4 * s][0], z[4 * s][1]), pk2(z[4 * s + 1][0], z[4 * s + 1][1]), pk2(z[4 * s + 2][0], z[4 * s + 2][1]), pk2(z[4 * s + 3][0], z[4 * s + 3][1])};
        st16_sc1(f, sws, lane * 16, WS_AF + (T * AFT + (hcol0 >> 4) + 4 * P + 2 * t + s) * 1024);
      }
      if (t == 0) mid();
      SBAR();
    }
  };

  if (isG) {
    lane_setup();
    const v4u sw2 = make_srd(w2, FF * DM * 2), swo = make_srd(wo, DM * DM * 2);
    const unsigned vw2 = (lane >> 3) * 1024 + swz, vw2b = vw2 + 7 * 1024;
    const int ro3 = rt % 3;
    auto kmap = [&](int kk) {
      if (kk < 16) return 8 * (((kk >> 2) + rt) & 3) + (kk & 3);
      const int p = ((kk - 16) >> 2) + ro3;
      return 8 * (p >= 3 ? p - 3 : p) + 4 + (kk & 3);
    };
    auto omap = [&](int i) { return 2 * ((i + rt) & 3) + (i >> 2); };
    auto wdmah = [&](int kk, int half) {
      const unsigned so = (kk < 28 ? 448 * m + 16 * kmap(kk) : 128 * m + 16 * omap(kk - 28)) * 1024 + 128 * ct;
      if (half == 0) dma16_a(kk < 28 ? sw2 : swo, vw2, so, lds0 + L_WG + m * PART + (kk % RD) * 2048); else dma16_b(kk < 28 ? sw2 : swo, vw2b, so);
    };
    auto wdma = [&](int kk) { wdmah(kk, 0); wdmah(kk, 1); };
    {
      const v4u swq = make_srd(wqkv, DM * 3 * DM * 2);
      const unsigned vx0 = (lane >> 3) * 1024 + (((lane & 7) ^ g) * 16), vx1 = (7 + (lane >> 3)) * 1024 + (((lane & 7) ^ (4 + g)) * 16);
      const unsigned vwq = (lane >> 2) * (3 * DM * 2) + p4 * 16;
      auto piece = [&](int s, int i) {
        const int kc = (s + rot) & 7;
        if (i < 4) {
          const unsigned so = (128 * r + 32 * m + 16 * (i >> 1)) * 1024 + kc * 128;
          if (i & 1) dma16_b(sx, vx1, so); else dma16_a(sx, vx0, so, lds0 + xbuf(s) + m * 4096 + (i >> 1) * 2048);
        } else if (i < 7) dma16(swq, vwq, (64 * kc + 16 * m) * (3 * DM * 2) + (512 * (i - 4) + 32 * j) * 2, lds0 + qbuf(s) + ((i - 4) * 4 + m) * 1024);
        else w1dma(0, s, lds0 + w1buf(s), i - 7);
      };
#pragma unroll
      for (int i = 0; i < 18; ++i) { piece(i / 9, i % 9); if (i == 8) __builtin_amdgcn_s_barrier(); }
      __builtin_amdgcn_s_setprio(PRIO_G1);
      hsetup(0);
      f32x4 cs[2] = {};
      v4u xf[4], wf[4][2];
      hac[0] = (f32x16){}; hac[1] = (f32x16){};
      static_for<0, 8>([&](auto sc) {
        constexpr int s = sc.value;
        auto fill = [&](int i) {
          if constexpr (s < 6) piece(s + 2, i);
          if constexpr (s == 6) {
            if (i == 0) { stat_load(); bias_load(0); }
            else if (i < 5) { if (heavy) w1dma(1, (i - 1) >> 1, lds0 + L_WA + ((i - 1) >> 1) * 8192, (i - 1) & 1); }
            else wdmah((i - 5) >> 1, (i - 5) & 1);
          }
          if constexpr (s == 7) {
            if (heavy && i < 4) w1dma(1, 2 + (i >> 1), lds0 + L_WA + (2 + (i >> 1)) * 8192, i & 1);
            if (heavy && i >= 4) { w1dma(1, i, lds0 + L_WA + i * 8192, 0); w1dma(1, i, 0, 1); }
          }
          SBAR();
        };
        auto rd = [&](int p) {
          xf[p] = lds128(xbuf(s) + xo[p]);
          wf[p][0] = ldtr2(rk[p][0] + (s % 3) * PART, 512); wf[p][1] = ldtr2(rk[p][1] + (s % 3) * PART, 512);
          SBAR();
        };
        if constexpr (s < 7) wait_vm<9>(); else { if (heavy) wait_vm<11>(); else wait_vm<7>(); }
        bar();
        STAMP_AT(10 + s);
        rd(0);
        if constexpr (s < 7) fill(0);
        rd(1);
        if constexpr (s < 7) fill(1);
        if constexpr (s == 2) get_tag();
#pragma unroll
        for (int p = 0; p < 4; ++p) {
#pragma unroll
          for (int t = 0; t < 2; ++t) {
            hac[t] = MFMA32(wf[p][t], xf[p], hac[t]);
            if (p == 0) { cs[t] = MFMA16(sel, wf[p][t], cs[t]); PIN(cs[t]); }
            SBAR();
            if (p < 2 && t == 0) rd(p + 2);
            if (s == 7) fill(2 * p + t); else if (2 * p + t < 7) fill(2 + 2 * p + t);
          }
          if (s == 7 && p == 1) {
            cswr(0, cs[0]); cswr(1, cs[1]);
            bar();
            STAMP_AT(1);
          }
        }
        if constexpr (s == 7) {
          const f32x4 c0 = csrd(0), c1 = csrd(1);
          if (heavy) wait_vm<20>(); else wait_vm<4>();
          PIN(st); PIN(bias0); PIN(bias1);
          row_stats(); make_xaug();
          hac[0] = MFMA32(waug(c0, bias0), xaug, hac[0]);
          hac[1] = MFMA32(waug(c1, bias1), xaug, hac[1]);
          SBAR();
        }
      });
    }
    STAMP_AT(18);
    if (heavy) { wait_vm<0>(); arrive_nowait(lds0 + L_CNT + 16); }
    hgelu(std::integral_constant<int, 0>{}, [&]() {});
    __builtin_amdgcn_s_setprio(0);
    STAMP_AT(19);
    unsigned rb2[2];
#pragma unroll
    for (int c = 0; c < 2; ++c) rb2[c] = lds0 + L_WG + m * PART + h * 512 + q4 * 128 + ((c ^ (q4 >> 1)) * 64) + (g & 1) * 32 + p4 * 8;
    v4u wq[36][2];
    auto wrd = [&](int kk) {
#pragma unroll
      for (int c = 0; c < 2; ++c) wq[kk][c] = ldtr2(rb2[c] + (kk % RD) * 2048, 1024);
    };
    auto wgot = [&](int kk) { PIN(wq[kk][0]); PIN(wq[kk][1]); };
    const int ftile = 2 * rt + ((lane >> 2) & 1);
    const int T0 = 4 * r - 8 + 2 * m;
    const bool live = T0 >= 0;
    auto kv_fetch = [&]() {
      const unsigned src = WS_KV + (j * 64 + (live ? T0 : 0)) * 4096, vk = live ? lane * 16 : h * 512, vv = live ? lane * 16 : 0x40000000u;
#pragma unroll
      for (int i = 0; i < 8; ++i)
        dma16_sc1(sws, (i & 2) ? vv : vk, src + (live ? i : (i & 3)) * 1024, lds0 + L_KV + (2 * m + (i >> 2)) * KVT + (i & 3) * 1024 + ((i & 3) == 3 ? VB - 1024 : 0));
      const unsigned on = (lane < 2 || live) ? one2 : 0u;
      if (lane < 6) sts128(lds0 + L_KV + (lane < 2 ? 8 + m : 2 * m + ((lane - 2) >> 1)) * KVT + 2048 + (lane & 1) * VB + 1024, (v4u){on, on, on, on});
    };
    v4u af[36][2];
    v2u fl;
    const unsigned a0 = WS_AF + (2 * rt * AFT + 28 * m) * 1024, o0 = WS_AF + (2 * rt * AFT + 112 + 8 * m) * 1024;
    fl = ld8_sc1_async(sws, flag_off(FL_KV, live ? T0 + (lane & 1) : 0, j));
    for (int kk = 2; kk < 6; ++kk) wdma(kk);
    wait_vmf<8>(fl);
    if (lane == 0) __hip_atomic_store(flag(FL_H0, T, j), tag, RLX_AGENT);
    STAMP_AT(2);
    if (!__all(fl[0] == taglo && fl[1] == tag32)) poll_flags(flag(FL_KV, live ? T0 + (lane & 1) : 0, j), tag);
    if (heavy) await(lds0 + L_CNT + 20, 4);
    kv_fetch();
    wdma(6); wdma(7);
    static_assert(AW == 6 && HCK + 2 <= NR && KVS < HLD, "lead-in");
    static_for<0, NR + 1>([&](auto kc) {
      constexpr int k = kc.value;
      if constexpr (k < NR) {
        wait_vm<(k < 8) ? (k < 6 ? 2 * (5 - (k < 2 ? 1 : k)) + 13 : 2 * (7 - k)) + pre_range(0, k) : pre_range(k - 6, k)>();
        wrd(k);
      }
      if constexpr (k == KVS) { wait_vm<4 + pre_range(0, k)>(); arrive_nowait(lds0 + L_CNT); }
      if constexpr (k == HLD) fl = ld8_sc1_async(sws, flag_off(FL_H0, ftile, 4 * m + p4));
      if constexpr (k == HCK) {
        STAMP_AT(3);
        wait_vmf<pre_range(HLD, HCK) - 1>(fl);
        if (!__all(fl[0] == taglo && fl[1] == tag32)) poll_flags(flag(FL_H0, ftile, 4 * m + p4), tag);
        STAMP_AT(4);
      }
      if constexpr (k >= HCK && k < HCK + 3) {
#pragma unroll
        for (int i = 0; i < 4; ++i) af[2 * (k - HCK) + (i >> 1)][i & 1] = ld16_sc1_async(sws, lane * 16, a0 + (AFT * (i & 1) + kmap(2 * (k - HCK) + (i >> 1))) * 1024);
      }
      if constexpr (k >= 1) { wgot(k - 1); wdma(k - 1 + RD); }
    });
    __builtin_amdgcn_s_setprio(PRIO_G2);
    f32x16 c2[2][2] = {};
    auto mm2 = [&](int kk, const v4u (&a)[2], auto&& fill) {
      SBAR();
#pragma unroll
      for (int i = 0; i < 4; ++i) { c2[i >> 1][i & 1] = MFMA32(wq[kk][i & 1], a[i >> 1], c2[i >> 1][i & 1]); SBAR(); fill(i); SBAR(); }
    };
    static_assert(FLA <= 4, "one flag register");
    static_for<0, 32>([&](auto kc) {
      constexpr int kk = kc.value;
      wait_vm2<(kk < AW) ? first_wait(kk) : g2_range(kk - AW + 1, kk)>(af[kk][0], af[kk][1]);
      if constexpr (kk == H1_CK || kk == O0_CK || kk == O1_CK) {
        constexpr int kind = kk == H1_CK ? FL_H1 : kk == O0_CK ? FL_O0 : FL_O1;
        if constexpr (kk == O1_CK) STAMP_AT(5);
        wait_vmf<g2_range(kk - FLA, kk) - 1>(fl);
        if (!__all(fl[0] == taglo && fl[1] == tag32)) poll_flags(flag(kind, ftile, 4 * m + p4), tag);
        if constexpr (kk == O1_CK) STAMP_AT(6);
      }
      if constexpr (fl_load(kk)) fl = ld8_sc1_async(sws, flag_off(kk + FLA == H1_CK ? FL_H1 : kk + FLA == O0_CK ? FL_O0 : FL_O1, ftile, 4 * m + p4));
      if constexpr (kk == RES_LD) res_loads();
      if constexpr (kk == 20) res_sum();
      constexpr bool dw = kk >= 1 && kk - 1 + NR + RD < 36;
      if constexpr (dw) wgot(kk - 1 + NR);
      mm2(kk, af[kk], [&](int i) {
        if constexpr (dw) { if (i < 2) wdmah(kk - 1 + NR + RD, i); }
        if constexpr (kk + NR < 28) { if (i == 1) wrd(kk + NR); }
        if constexpr (kk >= 24) { if (i == 0) wrd(kk + 4); }
        if constexpr (kk + AW < 36) {
          if (i >= 2) af[kk + AW][i - 2] = ld16_sc1_async(sws, lane * 16, (kk + AW < 28 ? a0 + kmap(kk + AW) * 1024 : o0 + omap(kk + AW - 28) * 1024) + AFT * (i - 2) * 1024);
        }
        if constexpr (kk == 31) PIN(c2[i >> 1][i & 1]);
      });
    });
    for (int kk = 32; kk < 36; ++kk) wgot(kk);
    unsigned pa = lds0 + L_WG + m * PART + lane * 16;
    PIN(pa);
    auto put = [&](int qn, int qd) {
      const f32x16& c = c2[qn >> 1][qn & 1];
      sts128(pa + (qn * 4 + qd) * 66 * 16, __builtin_bit_cast(v4u, (f32x4){c[4 * qd], c[4 * qd + 1], c[4 * qd + 2], c[4 * qd + 3]}));
    };
    static_for<0, 4>([&](auto qc) {
      constexpr int qn = qc.value;
      if constexpr ((qn & 1) == 0) { wait_vm2<0>(af[32][qn >> 1], af[33][qn >> 1]); PIN(af[34][qn >> 1]); PIN(af[35][qn >> 1]); }
      SBAR();
#pragma unroll
      for (int i = 0; i < 4; ++i) {
        c2[qn >> 1][qn & 1] = MFMA32(wq[32 + i][qn & 1], af[32 + i][qn >> 1], c2[qn >> 1][qn & 1]);
        SBAR();
        if (qn > 0 && i >= 2) { put(qn - 1, 2 * i - 4); put(qn - 1, 2 * i - 3); }
        SBAR();
      }
    });
#pragma unroll
    for (int qd = 0; qd < 4; ++qd) put(3, qd);
    STAMP_AT(7);
  } else {
    __builtin_amdgcn_s_setprio(1);
    lane_setup();
    v4u xr[2];
    xr[0] = ld16_async(sx, lane * 16, (128 * r + 8 * j + 2 * m) * 1024); xr[1] = ld16_async(sx, lane * 16, (128 * r + 8 * j + 2 * m + 1) * 1024);
    unsigned warm;
    {
      u64 pc; asm volatile("s_getpc_b64 %0" : "=s"(pc));
      warm = ld2_async(make_srd((const void*)(pc & ~127ull), CODEWARM), lane < 3 ? lane * 128 : 0x40000000u, (idx * 4 + m) * 384);
    }
    __builtin_amdgcn_s_barrier();
    v2u ep = ld8_sc1_async(sws, m == 0 ? WS_EPOCH : 0x40000000u);
    float sval;
    const int wi = blockIdx.x * 4 + m;
    const u64 wbase = wi < 448 ? (u64)w1 + wi * 4096 : wi < 896 ? (u64)w2 + (wi - 448) * 4096 : (u64)wo + (wi - 896) * 4096;
    auto make_stats = [&]() {
      wait_vm2<1>(xr[0], xr[1]);
      PIN(warm);
      float piv[2], a[4];
#pragma unroll
      for (int u = 0; u < 2; ++u) {
        piv[u] = rdl(bflo(xr[u][0]), 0);
        float s1 = 0.f, s2 = 0.f;
#pragma unroll
        for (int i = 0; i < 8; ++i) { const float d = ((i & 1) ? bfhi(xr[u][i >> 1]) : bflo(xr[u][i >> 1])) - piv[u]; s1 += d; s2 = __builtin_fmaf(d, d, s2); }
        a[2 * u] = s1; a[2 * u + 1] = s2;
      }
#pragma unroll
      for (int i = 0; i < 4; ++i) a[i] += dpp<0xB1>(a[i]);
#pragma unroll
      for (int i = 0; i < 4; ++i) a[i] += dpp<0x4E>(a[i]);
      float t = p4 == 0 ? a[0] : p4 == 1 ? a[1] : p4 == 2 ? a[2] : a[3];
      t += dpp<0x114>(t); t += dpp<0x118>(t);
      const auto s16 = __builtin_amdgcn_permlane16_swap(asu(t), asu(t), false, false);
      t = asf(s16[0]) + asf(s16[1]);
      const auto s32 = __builtin_amdgcn_permlane32_swap(asu(t), asu(t), false, false);
      t = (asf(s32[0]) + asf(s32[1])) * (1.f / DM);
      const float e1 = dpp<0xA0>(t);
      sval = (p4 & 1) ? __builtin_fmaxf(__builtin_fmaf(-e1, e1, t), 0.f) + 1e-5f : t + (p4 ? piv[1] : piv[0]);
      PIN(sval);
    };
    const unsigned acnt = lds0 + L_CNT;
    const unsigned vpos = 2048 + (g & 1) * 512 + h * 256 + (lane & 15) * 16;
    v4u qf[2], am0, am1;
    u64 um[16];
    auto masks = [&]() {
      const int dq = r32 - 4 * h;
#pragma unroll
      for (int u = 0; u < 16; ++u) { um[u] = __builtin_amdgcn_ballot_w64((u & 3) + 8 * (u >> 2) > dq); pin_s(um[u]); }
#pragma unroll
      for (int d = 0; d < 4; ++d) {
        am0[d] = __builtin_amdgcn_inverse_ballot_w64(um[2 * d]) ? 0xffffffffu : __builtin_amdgcn_inverse_ballot_w64(um[2 * d + 1]) ? 0xffff0000u : 0u;
        am1[d] = __builtin_amdgcn_inverse_ballot_w64(um[8 + 2 * d]) ? 0xffffffffu : __builtin_amdgcn_inverse_ballot_w64(um[9 + 2 * d]) ? 0xffff0000u : 0u;
      }
      PIN(am0); PIN(am1);
    };
    {
      v4u xf[32];
      f32x16 acc[3] = {};
      f32x4 cs[3] = {};
      v4u wf[4][3];
      unsigned wb[4];
#pragma unroll
      for (int p = 0; p < 4; ++p) { wb[p] = qbuf(0) + ((p + m) & 3) * 1024 + (8 * h + q4) * 64 + (g & 1) * 32 + p4 * 8; PIN(wb[p]); }
      auto rd = [&](int ks, int t) {
        if (t == 0) xf[ks] = lds128(xbuf(ks >> 2) + xo[ks & 3]);
        wf[ks & 3][t] = ldtr2(wb[ks & 3] + ((ks >> 2) % 3) * 12288 + t * 4096, 256);
      };
      auto mm = [&](int ks, int t) {
        acc[t] = t == 2 ? MFMA32(xf[ks], wf[ks & 3][t], acc[t]) : MFMA32(wf[ks & 3][t], xf[ks], acc[t]);
        if ((ks & 3) == 0) { cs[t] = MFMA16(sel, wf[ks & 3][t], cs[t]); PIN(cs[t]); }
        SBAR();
      };
      make_stats();
      bar();
      STAMP_AT(10);
#pragma unroll
      for (int t = 0; t < 6; ++t) rd(t / 3, t % 3);
      SBAR();
      static_for<0, 8>([&](auto sc) {
        constexpr int s = sc.value;
#pragma unroll
        for (int p = 0; p < 2; ++p) {
#pragma unroll
          for (int t = 0; t < 3; ++t) { mm(4 * s + p, t); rd(4 * s + p + 2, t); SBAR(); }
          if (s == 7 && p == 0) { cswr(4, cs[0]); cswr(5, cs[1]); cswr(6, cs[2]); SBAR(); }
        }
        if constexpr (s == 0) {
          wait_vmf<0>(ep);
          if (m == 0 && lane < 10)
            *(LAS unsigned*)(size_t)(lds0 + L_CNT + lane * 4) = lane == 8 ? ep[0] * 0x9E3779B1u + 0x7F4A7C15u : lane == 9 ? ep[1] * 0x85EBCA77u + 0x165667B1u : 0u;
        }
        if constexpr (s == 1) {
          get_tag();
          if ((lane >> 2) == 3) {
            const v2u gr = {asu(sval), tag32};
            __builtin_amdgcn_raw_buffer_store_b64(gr, rws, WS_STATS + (128 * r + 8 * j + 2 * m) * 16 + p4 * 8, 0, 16);
          }
        }
        if constexpr (s == MIDWARM) {
          warm = ld2_async(make_srd((const void*)wbase, 4096), lane * 128, 0);
        }
        if constexpr (s == 5) stat_load();
        if constexpr (s == 6) {
          wait_vm1<0>(st);
          PIN(warm);

          row_stats();
        }
        mm(4 * s + 2, 0); mm(4 * s + 2, 1);
        bar();
        if constexpr (s < 7) {
          STAMP_AT(11 + s);
          mm(4 * s + 2, 2);
          rd(4 * s + 4, 0); rd(4 * s + 4, 1); rd(4 * s + 4, 2); SBAR();
#pragma unroll
          for (int t = 0; t < 3; ++t) { mm(4 * s + 3, t); rd(4 * s + 5, t); SBAR(); }
        }
      });
      STAMP_AT(1);
      __builtin_amdgcn_s_setprio(3);
      const f32x4 c5 = csrd(5), c6 = csrd(6), c4 = csrd(4);
      SBAR();
      mm(30, 2); mm(31, 1);
      make_xaug();
      SBAR();
      mm(31, 2); mm(31, 0);
      if (h == 0) *(LAS float*)(size_t)(lds0 + L_RS + (m * 32 + r32) * 4) = rstd;
      acc[1] = MFMA32(waug(c5, 0u), xaug, acc[1]);
      acc[2] = MFMA32(xaug, waug(c6, 0u), acc[2]);
      acc[0] = MFMA32(waug(c4, 0u), xaug, acc[0]);
      SBAR();
      f32x4 rs[4];
#pragma unroll
      for (int qd = 0; qd < 4; ++qd) rs[qd] = __builtin_bit_cast(f32x4, lds128(lds0 + L_RS + (m * 32 + 8 * qd + 4 * h) * 4));
      const unsigned kvo = WS_KV + (j * 64 + T) * 4096;
#pragma unroll
      for (int e = 0; e < 16; ++e) acc[1][e] = MUL1(acc[1][e], rstd);
#pragma unroll
      for (int e = 0; e < 2; ++e) {
        const v4u f = pack8(acc[1], e);
        st16_sc1(f, sws, lane * 16, kvo + e * 1024);
        sts128(lds0 + L_KV + (8 + m) * KVT + e * 1024 + lane * 16, f);
      }
#pragma unroll
      for (int qd = 0; qd < 4; ++qd)
#pragma unroll
        for (int e = 0; e < 4; ++e) acc[2][4 * qd + e] = MUL1(acc[2][4 * qd + e], rs[qd][e]);
#pragma unroll
      for (int s = 0; s < 2; ++s) {
        const v4u f = pack8(acc[2], s);
        st16_sc1(f, sws, vpos, kvo + s * 1024);
        sts128(lds0 + L_KV + (8 + m) * KVT + s * VB + vpos, f);
      }
      SBAR();
      const float qs = rstd * (0.25f * 1.4426950408889634f);
#pragma unroll
      for (int e = 0; e < 16; ++e) acc[0][e] = MUL1(acc[0][e], qs);
      qf[0] = pack8(acc[0], 0); qf[1] = pack8(acc[0], 1);
      PIN(qf[0]); PIN(qf[1]);
      arrive_nowait(acnt);
      STAMP_AT(2);
      if (heavy) {
        hsetup(1);
        await(acnt + 16, 4);
        __builtin_amdgcn_s_setprio(1);
        STAMP_AT(3);
        hmm(std::integral_constant<int, 1>{}, xf, [&](auto kc) {
          constexpr int ks = kc.value;
          if constexpr (ks == KVPUB) {
            wait_vm<0>();
            if (lane == 0) __hip_atomic_store(flag(FL_KV, T, j), tag, RLX_AGENT);
          }
          if constexpr (ks == KVPUB + 2) bias_load(1);
          if constexpr (ks == 31) { wait_vm<0>(); PIN(bias0); PIN(bias1); }
        });
        STAMP_AT(18);
        __builtin_amdgcn_s_setprio(3);
        hgelu(std::integral_constant<int, 1>{}, [&]() {});
        STAMP_AT(4);
      } else {
        wait_vm<0>();
        if (lane == 0) __hip_atomic_store(flag(FL_KV, T, j), tag, RLX_AGENT);
      }
    }
    masks();
    STAMP_AT(19);
    const unsigned kb = lds0 + L_KV + m * KVT + lane * 16;
    unsigned vb[2];
#pragma unroll
    for (int e = 0; e < 2; ++e) { vb[e] = lds0 + L_KV + m * KVT + 2048 + ((g & 1) ? 1024 : e * 512 + h * 256 + (lane & 15) * 16); PIN(vb[e]); }
    bool pend = true;
    if (__builtin_amdgcn_readfirstlane(peek(acnt)) < 8) {
      wait_vm<0>();
      if (lane == 0) __hip_atomic_store(flag(FL_H1, T, j), tag, RLX_AGENT);
      pend = false;
      await(acnt, 8);
    }
    asm volatile("" ::: "memory");
    __builtin_amdgcn_s_setprio(PRIO_ATT);
    STAMP_AT(5);
    const f32x16 zero = {};
    auto o_store = [&](f32x16& o, int e) {
      const float rl = __builtin_amdgcn_rcpf(o[8]);
#pragma unroll
      for (int u = 0; u < 8; ++u) o[u] = MUL1(o[u], rl);
      st16_sc1(pack8(o, 0), sws, lane * 16, WS_AF + (T * AFT + 112 + 2 * j + e) * 1024);
    };
    int redo = 0;
    {
      f32x16 o[2], sa, sb, su;
      v4u kn, ku, va, vc, vn0, vn1, vu0, vu1, p0, p1, pu;
      auto kaddr = [&](int n) { return kb + ((n & 7) + 1) * KVT + (n >> 3) * 1024; };
      auto vaddr = [&](int n) { return vb[n >> 3] + ((n & 7) + 1) * KVT; };
      kn = lds128(kaddr(0)); vn0 = lds128(vaddr(0)); vn1 = lds128(vaddr(0) + VB);
      sa = MFMA32(kn, qf[0], zero);
      kn = lds128(kaddr(1));
      SBAR();
      auto step = [&](auto nc, f32x16& sc, f32x16& sn) {
        constexpr int n = nc.value, e = n >> 3, i = n & 7;
        if constexpr (n < 15) sn = MFMA32(kn, qf[(n + 1) >> 3], zero);
        if constexpr (n < 14) kn = lds128(kaddr(n + 2));
        if constexpr (i == 5) ku = lds128(kb + e * 1024);
        v4u v0 = vn0, v1 = vn1;
        if constexpr (n < 15) { vn0 = lds128(vaddr(n + 1)); vn1 = lds128(vaddr(n + 1) + VB); }
        if constexpr (i == 6) { vu0 = lds128(vb[e]); vu1 = lds128(vb[e] + VB); }
        SBAR();
        if constexpr (i == 7) {
#pragma unroll
          for (int u = 0; u < 16; ++u) sc[u] = __builtin_amdgcn_inverse_ballot_w64(um[u]) ? su[u] : sc[u];
          PIN(sc); SBAR();
        }
#pragma unroll
        for (int u = 0; u < 6; ++u) sc[u] = EXP2(sc[u]);
        PIN(sc); SBAR();
        if constexpr (n == 8) { o[0] = MFMA32(vc, p1, o[0]); SBAR(); }
        else if constexpr (n > 0) { o[e] = MFMA32(vc, p1, o[e]); SBAR(); }
        sc[6] = EXP2(sc[6]); sc[7] = EXP2(sc[7]);
        p0 = pack8(sc, 0);
        sc[8] = EXP2(sc[8]); sc[9] = EXP2(sc[9]);
        if constexpr (i == 7) { va = p0 & am0; p0 &= ~am0; PIN(va); }
        PIN(sc); PIN(p0); SBAR();
        if constexpr (n == 8) { o[0] = MFMA32(vu1, pu, o[0]); SBAR(); }
        o[e] = MFMA32(v0, p0, i == 0 ? zero : o[e]);
        SBAR();
        if constexpr (i == 6) { su = MFMA32(ku, qf[e], zero); SBAR(); }
#pragma unroll
        for (int u = 10; u < 13; ++u) sc[u] = EXP2(sc[u]);
        if constexpr (i == 7) { PIN(sc); SBAR(); o[e] = MFMA32(vu0, va, o[e]); SBAR(); }
#pragma unroll
        for (int u = 13; u < 16; ++u) sc[u] = EXP2(sc[u]);
        p1 = pack8(sc, 1); vc = v1;
        if constexpr (i == 7) { pu = p1 & am1; p1 &= ~am1; PIN(pu); }
        PIN(p1); SBAR();
        if constexpr (n == 8) {
          if (__all(o[0][8] >= 0x1p-30f && o[0][8] <= 0x1p30f)) o_store(o[0], 0); else redo = 1;
          SBAR();
        }
        if constexpr (n == 10) pend = !redo;
        if constexpr (i == 3 || i == 5 || i == 7) {
          if (pend && (i == 7 || (__builtin_amdgcn_s_getreg(7 | (3 << 11)) & 15) == 0)) {
            wait_vm<0>();
            if (lane == 0) __hip_atomic_store(flag(FL_H1 + e, T, j), tag, RLX_AGENT);
            pend = false;
          }
        }
      };
      static_for<0, 8>([&](auto hc) {
        step(std::integral_constant<int, 2 * hc.value>{}, sa, sb);
        step(std::integral_constant<int, 2 * hc.value + 1>{}, sb, sa);
      });
      o[1] = MFMA32(vc, p1, o[1]);
      o[1] = MFMA32(vu1, pu, o[1]);
      if (__all(o[1][8] >= 0x1p-30f && o[1][8] <= 0x1p30f)) o_store(o[1], 1); else redo |= 2;
    }
    if (redo) {
#pragma unroll 1
      for (int e = 0; e < 2; ++e) {
        if (!((redo >> e) & 1)) continue;
        const v4u q = e ? qf[1] : qf[0];
        const unsigned vbe = e ? vb[1] : vb[0];
        auto merged = [&](const f32x16& c) {
          f32x16 sl = MFMA32(lds128(kb + 8 * KVT + e * 1024), q, c);
          const f32x16 su = MFMA32(lds128(kb + e * 1024), q, c);
          static_for<0, 16>([&](auto u) { sl[u.value] = upper<u.value>() ? su[u.value] : sl[u.value]; });
          return sl;
        };
        float mx = -INFINITY;
#pragma unroll 1
        for (int i = 0; i < 8; ++i) {
          const f32x16 sc = i ? MFMA32(lds128(kb + i * KVT + e * 1024), q, zero) : merged(zero);
#pragma unroll
          for (int u = 0; u < 16; u += 2) mx = max3(mx, sc[u], sc[u + 1]);
        }
        const auto sw = __builtin_amdgcn_permlane32_swap(asu(mx), asu(mx), false, false);
        mx = __builtin_fmaxf(asf(sw[0]), asf(sw[1]));
        f32x16 negm, o;
#pragma unroll
        for (int u = 0; u < 16; ++u) negm[u] = -mx;
        {
          f32x16 sc = merged(negm);
#pragma unroll
          for (int u = 0; u < 16; ++u) sc[u] = EXP2(sc[u]);
          const v4u p0 = pack8(sc, 0), p1 = pack8(sc, 1);
          o = MFMA32(lds128(vbe), p0 & am0, zero);
          o = MFMA32(lds128(vbe + VB), p1 & am1, o);
          o = MFMA32(lds128(vbe + 8 * KVT), p0 & ~am0, o);
          o = MFMA32(lds128(vbe + 8 * KVT + VB), p1 & ~am1, o);
        }
#pragma unroll 1
        for (int i = 1; i < 8; ++i) {
          f32x16 sc = MFMA32(lds128(kb + i * KVT + e * 1024), q, negm);
#pragma unroll
          for (int u = 0; u < 16; ++u) sc[u] = EXP2(sc[u]);
          o = MFMA32(lds128(vbe + i * KVT), pack8(sc, 0), o);
          o = MFMA32(lds128(vbe + i * KVT + VB), pack8(sc, 1), o);
        }
        o_store(o, e);
      }
      wait_vm<0>();
      if (lane == 0) __hip_atomic_store(flag(FL_O0, T, j), tag, RLX_AGENT);
    }
    STAMP_AT(6);
    wait_vm<0>();
    if (lane == 0) __hip_atomic_store(flag(FL_O1, T, j), tag, RLX_AGENT);
    STAMP_AT(7);
    res_loads();
    wait_vm<0>();
    res_sum();
    if (blockIdx.x == 0 && wave == 7) {
#pragma unroll 1
      for (int i = 0; i < 4; ++i) poll_flags(flag(FL_KV, 4 * (4 * i + g), lane & 15), tag);
      if (lane == 0) __hip_atomic_store((GAS u64*)(ws + WS_EPOCH), tag, RLX_AGENT);
    }
  }
  {
    int t2 = threadIdx.x; PIN(t2);
    const int row = t2 >> 3, ch = t2 & 7;
    unsigned a = lds0 + L_WG + (((row >> 5) * 8 + ch) * 66 + (row & 31)) * 16, oo = (64 * rt + row) * 1024 + (64 * ct + ch * 8) * 2;
    PIN(a); PIN(oo);
    bar();
    f32x4 pl[4], ph[4];
#pragma unroll
    for (int w = 0; w < 4; ++w) { pl[w] = __builtin_bit_cast(f32x4, lds128(a + w * PART)); ph[w] = __builtin_bit_cast(f32x4, lds128(a + w * PART + 512)); }
    SBAR();
#pragma unroll
    for (int w = 0; w < 4; ++w) { lo += pl[w]; hi += ph[w]; }
    const v4u o = {pk2(lo[0], lo[1]), pk2(lo[2], lo[3]), pk2(hi[0], hi[1]), pk2(hi[2], hi[3])};
    __builtin_amdgcn_raw_buffer_store_b128(o, __builtin_amdgcn_make_buffer_rsrc(out, 0, SEQ * DM * 2, 0x00020000), oo, 0, 16);
  }
  STAMP_AT(8);
}

extern "C" void kernel_launch(void* const* d_in, const int* in_sizes, int n_in, void* d_out, int out_size, void* d_ws, size_t ws_size,
                              hipStream_t stream) {
  (void)in_sizes; (void)n_in; (void)out_size; (void)ws_size;
  (void)hipFuncSetAttribute((const void*)fused_block, hipFuncAttributeMaxDynamicSharedMemorySize, LDS_BYTES);
  fused_block<<<256, 512, LDS_BYTES, stream>>>(d_in[0], d_in[1], d_in[2], d_in[4], d_in[5], d_in[6], d_in[7], d_out, (char*)d_ws);
}
```

```cpp
#include <hip/hip_runtime.h>
#include <hip/hip_bf16.h>
#include <stdint.h>
#include <type_traits>

typedef unsigned v4u __attribute__((ext_vector_type(4)));
typedef unsigned v2u __attribute__((ext_vector_type(2)));
typedef float f32x2 __attribute__((ext_vector_type(2)));
typedef float f32x4 __attribute__((ext_vector_type(4)));
typedef float f32x16 __attribute__((ext_vector_type(16)));
typedef short bf16x8 __attribute__((ext_vector_type(8)));
typedef short s16x4 __attribute__((ext_vector_type(4)));
typedef __bf16 bf16x2_t __attribute__((ext_vector_type(2)));
typedef unsigned long long u64;
#define GAS __attribute__((address_space(1)))
#define LAS __attribute__((address_space(3)))
#define DI __device__ __forceinline__
#define RLX_AGENT __ATOMIC_RELAXED, __HIP_MEMORY_SCOPE_AGENT

constexpr int SEQ = 2048, DM = 512, FF = 1792;
constexpr unsigned WS_EPOCH = 0;
constexpr unsigned WS_STATS = 32768;
constexpr unsigned WS_FLAG = 65536;
constexpr unsigned WS_KV = WS_FLAG + 5 * 1024 * 128;
constexpr int AFT = 145;
constexpr unsigned WS_AF = WS_KV + 16 * 64 * 4096;
constexpr unsigned WS_END = WS_AF + 64 * AFT * 1024;
enum { FL_KV = 0, FL_H0 = 1, FL_H1 = 2, FL_O0 = 3, FL_O1 = 4 };
constexpr int L_WA = 0;
constexpr int L_WG = 86016;
constexpr int VB = 1040;
constexpr int KVT = 2048 + 2 * VB;
constexpr int L_KV = 32768;
constexpr int RD = 8, NR = 12;
constexpr int PART = 16 * 66 * 16;
constexpr int L_CS = L_WG + 4 * PART;
constexpr int L_RS = L_CS + 3584;
constexpr int L_CNT = L_RS + 512;
constexpr int LDS_BYTES = L_CNT + 64;
constexpr int xbuf(int s) { return ((s + 1) % 3) * 16384; }
constexpr int qbuf(int s) { return 49152 + (s % 3) * 12288; }
constexpr int w1buf(int s) { return L_WG + (s % 3) * PART + 8192; }
static_assert(L_KV + 8 * KVT >= 65536 && xbuf(7) == 32768 && qbuf(2) + 12288 <= L_WG && L_KV + 12 * KVT <= L_WG && PART >= RD * 2048 && LDS_BYTES <= 163840, "lds map");
constexpr int AW = 6;
constexpr unsigned SPIN_CAP = 1u << 23;

#define MFMA32(a, b, c) __builtin_amdgcn_mfma_f32_32x32x16_bf16(__builtin_bit_cast(bf16x8, a), __builtin_bit_cast(bf16x8, b), c, 0, 0, 0)
#define MFMA16(a, b, c) __builtin_amdgcn_mfma_f32_16x16x32_bf16(__builtin_bit_cast(bf16x8, a), __builtin_bit_cast(bf16x8, b), c, 0, 0, 0)
#define SBAR() __builtin_amdgcn_sched_barrier(0)

DI float asf(unsigned u) { return __builtin_bit_cast(float, u); }
DI unsigned asu(float f) { return __builtin_bit_cast(unsigned, f); }
DI unsigned pk2(float lo, float hi) { return __builtin_bit_cast(unsigned, __builtin_convertvector((f32x2){lo, hi}, bf16x2_t)); }
DI float bflo(unsigned u) { return __builtin_bit_cast(float, u << 16); }
DI float bfhi(unsigned u) { return __builtin_bit_cast(float, u & 0xffff0000u); }
DI v4u make_srd(const void* p, unsigned bytes) { const u64 a = (u64)p; return (v4u){(unsigned)a, (unsigned)(a >> 32) & 0xffffu, bytes, 0x00020000u}; }
DI void dma16(v4u srd, unsigned voff, unsigned soff, unsigned lds) {
  asm volatile("s_mov_b32 m0, %3\n\ts_nop 0\n\tbuffer_load_dwordx4 %0, %1, %2 offen lds" :: "v"(voff), "s"(srd), "s"(soff), "s"(lds) : "memory");
}
DI void dma16_sc1(v4u srd, unsigned voff, unsigned soff, unsigned lds) {
  asm volatile("s_mov_b32 m0, %3\n\ts_nop 0\n\tbuffer_load_dwordx4 %0, %1, %2 offen sc1 lds" :: "v"(voff), "s"(srd), "s"(soff), "s"(lds) : "memory");
}
DI void dma16_a(v4u srd, unsigned voff, unsigned soff, unsigned lds) { dma16(srd, voff, soff, lds); }
DI void dma16_b(v4u srd, unsigned voff1, unsigned soff) {
  asm volatile("buffer_load_dwordx4 %0, %1, %2 offen offset:1024 lds" :: "v"(voff1), "s"(srd), "s"(soff) : "memory");
}
DI v4u ld16_sc1_async(v4u srd, unsigned voff, unsigned soff) {
  v4u r; asm volatile("buffer_load_dwordx4 %0, %1, %2, %3 offen sc1" : "=v"(r) : "v"(voff), "s"(srd), "s"(soff) : "memory"); return r;
}
DI v4u ld16_async(v4u srd, unsigned voff, unsigned soff) {
  v4u r; asm volatile("buffer_load_dwordx4 %0, %1, %2, %3 offen" : "=v"(r) : "v"(voff), "s"(srd), "s"(soff) : "memory"); return r;
}
DI unsigned ld2_async(v4u srd, unsigned voff, unsigned soff) {
  unsigned r; asm volatile("buffer_load_ushort %0, %1, %2, %3 offen" : "=v"(r) : "v"(voff), "s"(srd), "s"(soff) : "memory"); return r;
}
DI void st16_sc1(v4u v, v4u srd, unsigned voff, unsigned soff) {
  asm volatile("buffer_store_dwordx4 %0, %1, %2, %3 offen sc1\n\ts_nop 1" :: "v"(v), "v"(voff), "s"(srd), "s"(soff) : "memory");
}
template <int N> DI void wait_vm1(v4u& a) { asm volatile("s_waitcnt vmcnt(%1)" : "+v"(a) : "i"(N) : "memory"); }
template <int N> DI void wait_vm2(v4u& a, v4u& b) { asm volatile("s_waitcnt vmcnt(%2)" : "+v"(a), "+v"(b) : "i"(N) : "memory"); }
DI void pin_s(u64& v) { asm volatile("" : "+s"(v)); }
template <class V> DI void pin(V& v) { asm volatile("" : "+v"(v)); }
template <int N> DI void wait_vm() { asm volatile("s_waitcnt vmcnt(%0)" :: "i"(N) : "memory"); }
DI void bar() { asm volatile("s_waitcnt lgkmcnt(0)\n\ts_barrier" ::: "memory"); }
DI v4u lds128(unsigned a) { return *(const LAS v4u*)(size_t)a; }
DI void sts128(unsigned a, v4u v) { *(LAS v4u*)(size_t)a = v; }
DI v2u ldtr(unsigned a) { return __builtin_bit_cast(v2u, __builtin_amdgcn_ds_read_tr16_b64_v4i16((LAS s16x4*)(size_t)a)); }
DI v4u ldtr2(unsigned a, int second) { const v2u lo = ldtr(a), hi = ldtr(a + second); return (v4u){lo.x, lo.y, hi.x, hi.y}; }
template <int CTRL> DI float dpp(float v) { return __builtin_bit_cast(float, __builtin_amdgcn_update_dpp(0, __builtin_bit_cast(int, v), CTRL, 0xf, 0xf, true)); }
DI float rdl(float v, int l) { return __builtin_bit_cast(float, __builtin_amdgcn_readlane(__builtin_bit_cast(int, v), l)); }
DI v4u pack8(const f32x16& a, int s) { return (v4u){pk2(a[8 * s], a[8 * s + 1]), pk2(a[8 * s + 2], a[8 * s + 3]), pk2(a[8 * s + 4], a[8 * s + 5]), pk2(a[8 * s + 6], a[8 * s + 7])}; }
DI void poll_flags(const GAS u64* p, u64 tag) {
#pragma unroll 1
  for (unsigned spin = 0; spin < SPIN_CAP; ++spin) {
    const u64 v = __hip_atomic_load(p, RLX_AGENT);
    if (__all(v == tag)) break;
    __builtin_amdgcn_s_sleep(1);
  }
  asm volatile("" ::: "memory");
}
constexpr u64 upper_mask(int reg) {
  u64 m = 0;
  for (int l = 0; l < 64; ++l) { const int row = (reg & 3) + 8 * (reg >> 2) + 4 * (l >> 5); if (row > (l & 31)) m |= 1ull << l; }
  return m;
}
template <int REG> DI bool upper() { return __builtin_amdgcn_inverse_ballot_w64(upper_mask(REG)); }
template <int I, int N, class Fn> DI void static_for(Fn&& f) { if constexpr (I < N) { f(std::integral_constant<int, I>{}); static_for<I + 1, N>(f); } }
DI float max3(float a, float b, float c) { return __builtin_fmaxf(__builtin_fmaxf(a, b), c); }

constexpr int FLA = 4, H1_CK = 16 - AW, O0_CK = 28 - AW, O1_CK = 32 - AW, RES_LD = 12;
constexpr bool fl_load(int t) { return t == H1_CK - FLA || t == O0_CK - FLA || t == O1_CK - FLA; }
constexpr int g2_issued(int t) { return ((t >= 1 && t - 1 + NR + RD < 36) ? 2 : 0) + ((t + AW < 36) ? 2 : 0) + fl_load(t) + (t == RES_LD ? 2 : 0); }
constexpr int g2_range(int a, int b) { int n = 0; for (int t = a; t < b; ++t) n += g2_issued(t); return n; }
constexpr unsigned CODE_BYTES = 32000;
constexpr int KVPUB = 2, KVS = 1, HLD = 9;
#define EXP2(v) __builtin_amdgcn_exp2f(v)
#define PIN(v) pin(v)
extern "C" __device__ float mul_unpacked(float, float) __asm("llvm.amdgcn.fmul.legacy");
#define MUL1(a, b) mul_unpacked(a, b)
#define FMA1(a, b, c) __builtin_amdgcn_div_fmasf(a, b, c, false)
DI v2u ld8_sc1_async(v4u srd, unsigned voff) {
  v2u r; asm volatile("buffer_load_dwordx2 %0, %1, %2, 0 offen sc1" : "=v"(r) : "v"(voff), "s"(srd) : "memory"); return r;
}
template <int N> DI void wait_vmf(v2u& a) { asm volatile("s_waitcnt vmcnt(%1)" : "+v"(a) : "i"(N) : "memory"); }
DI void arrive_nowait(unsigned cnt) {
  asm volatile("s_mov_b64 exec, 1\n\tds_add_u32 %0, %1\n\ts_mov_b64 exec, -1" :: "v"(cnt), "v"(1u) : "memory");
}
DI unsigned peek(unsigned cnt) { return __hip_atomic_load((LAS unsigned*)(size_t)cnt, __ATOMIC_RELAXED, __HIP_MEMORY_SCOPE_WORKGROUP); }
DI void await(unsigned cnt, unsigned n) {
#pragma unroll 1
  for (unsigned spin = 0; spin < SPIN_CAP * 32; ++spin) {
    if (__builtin_amdgcn_readfirstlane(peek(cnt)) >= n) break;
    __builtin_amdgcn_s_sleep(1);
  }
  asm volatile("" ::: "memory");
}

__global__ __launch_bounds__(512, 2) void fused_block(const void* __restrict__ x, const void* __restrict__ wqkv, const void* __restrict__ wo,
                                                      const void* __restrict__ w1, const void* __restrict__ b1, const void* __restrict__ w2,
                                                      const void* __restrict__ b2, void* __restrict__ out, char* __restrict__ ws) {
  constexpr unsigned lds0 = 0;
  u64 pc;
  asm volatile("s_getpc_b64 %0" : "=s"(pc));
  const int wave = __builtin_amdgcn_readfirstlane(threadIdx.x >> 6);
  const int m = wave & 3;
  const bool isG = wave < 4;
  const int xcd = blockIdx.x & 7, idx = blockIdx.x >> 3;
  const int r = (xcd >> 2) * 8 + (idx >> 2), j = (xcd & 3) * 4 + (idx & 3);
  const int rt = (xcd >> 1) * 8 + (idx >> 2), ct = (xcd & 1) * 4 + (idx & 3);
  int lane = threadIdx.x & 63, r32, h, g, q4, p4;
  asm volatile("" :: "s"(x), "s"(wqkv), "s"(wo), "s"(w1), "s"(b1), "s"(w2), "s"(b2), "s"(out), "s"(ws));

  const v4u sx = make_srd(x, SEQ * DM * 2), sws = make_srd(ws, WS_END);
  const __amdgpu_buffer_rsrc_t rws = __builtin_amdgcn_make_buffer_rsrc(ws, 0, WS_END, 0x00020000);
  const unsigned one2 = 0x3f803f80u;
  const int T = 4 * r + m;
  auto flag_off = [&](int kind, int tile, int jj) { return WS_FLAG + ((kind * 64 + tile) * 16 + jj) * 128; };
  auto flag = [&](int kind, int tile, int jj) { return (GAS u64*)(ws + flag_off(kind, tile, jj)); };
  u64 tag;
  unsigned taglo, tag32;
  auto get_tag = [&]() {
    const v2u tg = *(const LAS v2u*)(size_t)(lds0 + L_CNT + 32);
    taglo = __builtin_amdgcn_readfirstlane(tg[0]); tag32 = __builtin_amdgcn_readfirstlane(tg[1]);
    tag = ((u64)tag32 << 32) | taglo;
  };
  v4u xres, bres, xaug, st;
  float mean, var, rstd, sd;
  v4u sel;
  unsigned xo[4], swz;
  auto lane_setup = [&]() {
    lane = threadIdx.x & 63; PIN(lane);
    r32 = lane & 31; h = lane >> 5; g = lane >> 4; q4 = (lane >> 2) & 3; p4 = lane & 3;
    const unsigned selv = ((lane & 15) == (g & 1)) ? one2 : 0u;
    sel = (v4u){selv, selv, selv, selv};
#pragma unroll
    for (int p = 0; p < 4; ++p) xo[p] = (32 * m + r32) * 128 + (((2 * ((p + m) & 3) + h) ^ ((r32 >> 1) & 7)) * 16);
    swz = ((((lane >> 2) & 1) ^ (g & 1)) * 64) + p4 * 16;
  };

  auto stat_load = [&]() { st = ld16_sc1_async(sws, WS_STATS + (128 * r + 32 * m + r32) * 16, 0); };
  auto res_loads = [&]() {
    int t2 = threadIdx.x; PIN(t2);
    const int row = 64 * rt + (t2 >> 3), col = 64 * ct + (t2 & 7) * 8;
    xres = ld16_async(sx, row * 1024 + col * 2, 0);
    bres = ld16_async(make_srd(b2, DM * 2), col * 2, 0);
  };
  f32x4 lo, hi;
  auto res_sum = [&]() {
    PIN(xres); PIN(bres);
    lo = (f32x4){bflo(xres[0]) + bflo(bres[0]), bfhi(xres[0]) + bfhi(bres[0]), bflo(xres[1]) + bflo(bres[1]), bfhi(xres[1]) + bfhi(bres[1])};
    hi = (f32x4){bflo(xres[2]) + bflo(bres[2]), bfhi(xres[2]) + bfhi(bres[2]), bflo(xres[3]) + bflo(bres[3]), bfhi(xres[3]) + bfhi(bres[3])};
    PIN(lo); PIN(hi);
  };
  auto row_stats = [&]() {
    if (!__all(st[1] == tag32 && st[3] == tag32)) {
#pragma unroll 1
      for (unsigned spin = 0; spin < SPIN_CAP; ++spin) {
        st = ld16_sc1_async(sws, WS_STATS + (128 * r + 32 * m + r32) * 16, 0);
        wait_vm1<0>(st);
        if (__all(st[1] == tag32 && st[3] == tag32)) break;
      }
    }
    mean = asf(st[0]); var = asf(st[2]);
    PIN(mean); PIN(var);
  };
  auto make_xaug = [&]() {
    PIN(mean); PIN(var);
    rstd = __builtin_amdgcn_rsqf(var); sd = var * rstd;
    const unsigned uh = pk2(-mean, sd);
    const unsigned ul = pk2(-mean - bflo(uh), FMA1(bfhi(uh), -1.f, sd));
    xaug = (v4u){(uh & 0xffffu) | (uh << 16), (ul & 0xffffu) | (uh & 0xffff0000u), ul >> 16, 0u};
    if (h) xaug = (v4u){0u, 0u, 0u, 0u};
    PIN(xaug);
  };
  auto cswr = [&](int tile, f32x4 c) {
    if (lane < 16) { LAS float* p = (LAS float*)(size_t)(lds0 + L_CS + (tile * 32 + lane) * 16 + m * 4); p[0] = c[0]; p[64] = c[1]; }
  };
  auto csrd = [&](int tile) { return __builtin_bit_cast(f32x4, lds128(lds0 + L_CS + (tile * 32 + r32) * 16)); };
  auto waug = [&](f32x4 c4, unsigned b16) {
    const float c = ((c4[0] + c4[1]) + c4[2]) + c4[3];
    const unsigned uh = pk2(c, 0.f);
    const unsigned ul = pk2(c - bflo(uh), 0.f);
    return (v4u){uh | (ul << 16), uh | (b16 << 16), b16, 0u};
  };

  const v4u sw1 = make_srd(w1, DM * FF * 2);
  const int hcol0 = 448 * (j >> 2) + 128 * (j & 3);
  const bool heavy = (j & 3) != 3;
  unsigned bias0, bias1;
  auto bias_load = [&](int P) {
    bias0 = ld2_async(make_srd(b1, FF * 2), r32 * 2, (hcol0 + 64 * P) * 2);
    bias1 = ld2_async(make_srd(b1, FF * 2), r32 * 2, (hcol0 + 64 * P + 32) * 2);
  };
  auto w1dma = [&](int P, int c, unsigned dst, int half) {
    const unsigned vw1 = (lane >> 3) * (FF * 2) + swz, vw1b = vw1 + 8 * (FF * 2) - 1024;
    const unsigned so = (64 * ((c + r) & 7) + 16 * m) * (FF * 2) + (hcol0 + 64 * P) * 2;
    if (half == 0) dma16_a(sw1, vw1, so, dst + m * 2048); else dma16_b(sw1, vw1b, so);
  };
  f32x16 hac[2];
  unsigned rk[4][2];
  auto hsetup = [&](int P) {
#pragma unroll
    for (int p = 0; p < 4; ++p)
#pragma unroll
      for (int c = 0; c < 2; ++c) {
        rk[p][c] = lds0 + (P ? L_WA : w1buf(0)) + ((p + m) & 3) * 2048 + h * 1024 + q4 * 128 + ((c ^ (q4 >> 1)) * 64) + (g & 1) * 32 + p4 * 8;
        PIN(rk[p][c]);
      }
  };
  auto hmm = [&](auto pc, const v4u (&xf)[32], auto&& hook) {
    constexpr int P = pc.value;
    constexpr int D = 4;
    const unsigned cnt = lds0 + L_CNT + 4;
    f32x4 cs[2] = {}, c0, c1;
    v4u wf[D][2], au0, au1;
    unsigned seen = 0;
    hac[0] = (f32x16){}; hac[1] = (f32x16){};
    auto rd = [&](int ks, int t) { wf[ks % D][t] = ldtr2(rk[ks & 3][t] + ((ks >> 2) ^ 4) * 8192, 512); };
#pragma unroll
    for (int ks = 0; ks < D; ++ks) { rd(ks, 0); rd(ks, 1); }
    SBAR();
    static_for<0, 32>([&](auto kc) {
      constexpr int ks = kc.value;
      hook(kc);
      if constexpr (ks == 12) arrive_nowait(cnt + 16);
      if constexpr (ks == 29) seen = peek(cnt);
#pragma unroll
      for (int t = 0; t < 2; ++t) {
        hac[t] = MFMA32(wf[ks % D][t], xf[ks ^ 16], hac[t]);
        if ((ks & 3) == 0 && ks < 28) { cs[t] = MFMA16(sel, wf[ks % D][t], cs[t]); PIN(cs[t]); }
        if (ks == 26) { cs[t] = MFMA16(sel, wf[28 % D][t], cs[t]); PIN(cs[t]); }
        SBAR();
        if constexpr (ks + D < 32) rd(ks + D, t);
        if constexpr (ks == 31) { if (t == 0) { au0 = waug(c0, bias0); PIN(au0); } else { au1 = waug(c1, bias1); PIN(au1); } }
        SBAR();
      }
      if constexpr (ks == 30) {
        PIN(hac[0]); PIN(hac[1]);
        if (__builtin_amdgcn_readfirstlane(seen) < 4) await(cnt, 4);
        asm volatile("" ::: "memory");
        if constexpr (ks == 30) { c0 = csrd(2 * P); c1 = csrd(2 * P + 1); }
      }
      if constexpr (ks == 27) { cswr(2 * P, cs[0]); cswr(2 * P + 1, cs[1]); arrive_nowait(cnt); }
      SBAR();
    });
    hac[0] = MFMA32(au0, xaug, hac[0]);
    hac[1] = MFMA32(au1, xaug, hac[1]);
    SBAR();
  };
  auto hgelu = [&](auto pc, auto&& mid) {
    constexpr int P = pc.value;
    constexpr bool PK = P == 1;
    const float C0 = 2.3022081986f, C1 = C0 * 0.044715f;
    const float ga = -C1 * rstd * rstd * rstd, gb = -C0 * rstd;
    auto mul = [&](f32x2 a, f32x2 b) { return PK ? a * b : (f32x2){MUL1(a[0], b[0]), MUL1(a[1], b[1])}; };
    auto fma = [&](f32x2 a, float b, float c) {
      return PK ? __builtin_elementwise_fma(a, (f32x2){b, b}, (f32x2){c, c}) : (f32x2){FMA1(a[0], b, c), FMA1(a[1], b, c)}; };
#pragma unroll
    for (int t = 0; t < 2; ++t) {
      f32x2 v[8], z[8];
#pragma unroll
      for (int e = 0; e < 8; ++e) { v[e] = (f32x2){hac[t][2 * e], hac[t][2 * e + 1]}; z[e] = mul(v[e], v[e]); }
      SBAR();
#pragma unroll
      for (int e = 0; e < 8; ++e) z[e] = fma(z[e], ga, gb);
      SBAR();
#pragma unroll
      for (int e = 0; e < 8; ++e) z[e] = mul(z[e], v[e]);
      SBAR();
#pragma unroll
      for (int e = 0; e < 8; ++e) z[e] = (f32x2){EXP2(z[e][0]), EXP2(z[e][1])};
      SBAR();
#pragma unroll
      for (int e = 0; e < 8; ++e) z[e] = fma(z[e], sd, sd);
      SBAR();
#pragma unroll
      for (int e = 0; e < 8; ++e) z[e] = (f32x2){__builtin_amdgcn_rcpf(z[e][0]), __builtin_amdgcn_rcpf(z[e][1])};
      SBAR();
#pragma unroll
      for (int e = 0; e < 8; ++e) z[e] = mul(z[e], v[e]);
      SBAR();
#pragma unroll
      for (int s = 0; s < 2; ++s) {
        const v4u f = {pk2(z[4 * s][0], z[4 * s][1]), pk2(z[4 * s + 1][0], z[4 * s + 1][1]), pk2(z[4 * s + 2][0], z[4 * s + 2][1]), pk2(z[4 * s + 3][0], z[4 * s + 3][1])};
        st16_sc1(f, sws, lane * 16, WS_AF + (T * AFT + (hcol0 >> 4) + 4 * P + 2 * t + s) * 1024);
      }
      if (t == 0) mid();
      SBAR();
    }
  };

  if (isG) {
    lane_setup();
    const v4u sw2 = make_srd(w2, FF * DM * 2), swo = make_srd(wo, DM * DM * 2);
    const unsigned vw2 = (lane >> 3) * 1024 + swz, vw2b = vw2 + 7 * 1024;
    const int ro3 = rt % 3;
    auto kmap = [&](int kk) {
      if (kk < 16) return 8 * (((kk >> 2) + rt) & 3) + (kk & 3);
      const int p = ((kk - 16) >> 2) + ro3;
      return 8 * (p >= 3 ? p - 3 : p) + 4 + (kk & 3);
    };
    auto omap = [&](int i) { return 2 * ((i + rt) & 3) + (i >> 2); };
    auto wdmah = [&](int kk, int half) {
      const unsigned so = (kk < 28 ? 448 * m + 16 * kmap(kk) : 128 * m + 16 * omap(kk - 28)) * 1024 + 128 * ct;
      if (half == 0) dma16_a(kk < 28 ? sw2 : swo, vw2, so, lds0 + L_WG + m * PART + (kk % RD) * 2048); else dma16_b(kk < 28 ? sw2 : swo, vw2b, so);
    };
    auto wdma = [&](int kk) { wdmah(kk, 0); wdmah(kk, 1); };
    {
      const v4u swq = make_srd(wqkv, DM * 3 * DM * 2);
      const unsigned vx0 = (lane >> 3) * 1024 + (((lane & 7) ^ g) * 16), vx1 = (7 + (lane >> 3)) * 1024 + (((lane & 7) ^ (4 + g)) * 16);
      const unsigned vwq = (lane >> 2) * (3 * DM * 2) + p4 * 16;
      auto piece = [&](int s, int i) {
        const int kc = (s + r) & 7;
        if (i < 4) {
          const unsigned so = (128 * r + 32 * m + 16 * (i >> 1)) * 1024 + kc * 128;
          if (i & 1) dma16_b(sx, vx1, so); else dma16_a(sx, vx0, so, lds0 + xbuf(s) + m * 4096 + (i >> 1) * 2048);
        } else if (i < 7) dma16(swq, vwq, (64 * kc + 16 * m) * (3 * DM * 2) + (512 * (i - 4) + 32 * j) * 2, lds0 + qbuf(s) + ((i - 4) * 4 + m) * 1024);
        else w1dma(0, s, lds0 + w1buf(s), i - 7);
      };
#pragma unroll
      for (int i = 0; i < 18; ++i) { piece(i / 9, i % 9); if (i == 8) __builtin_amdgcn_s_barrier(); }
      __builtin_amdgcn_s_setprio(2);
      hsetup(0);
      f32x4 cs[2] = {};
      v4u xf[4], wf[4][2];
      hac[0] = (f32x16){}; hac[1] = (f32x16){};
      static_for<0, 8>([&](auto sc) {
        constexpr int s = sc.value;
        auto fill = [&](int i) {
          if constexpr (s < 6) piece(s + 2, i);
          if constexpr (s == 6) {
            if (i == 0) { stat_load(); bias_load(0); }
            else if (i < 5) { if (heavy) w1dma(1, (i - 1) >> 1, lds0 + L_WA + ((i - 1) >> 1) * 8192, (i - 1) & 1); }
            else wdmah((i - 5) >> 1, (i - 5) & 1);
          }
          if constexpr (s == 7) {
            if (heavy && i < 4) w1dma(1, 2 + (i >> 1), lds0 + L_WA + (2 + (i >> 1)) * 8192, i & 1);
            if (heavy && i >= 4) { w1dma(1, i, lds0 + L_WA + i * 8192, 0); w1dma(1, i, 0, 1); }
          }
          SBAR();
        };
        auto rd = [&](int p) {
          xf[p] = lds128(xbuf(s) + xo[p]);
          wf[p][0] = ldtr2(rk[p][0] + (s % 3) * PART, 512); wf[p][1] = ldtr2(rk[p][1] + (s % 3) * PART, 512);
          SBAR();
        };
        if constexpr (s < 7) wait_vm<9>(); else { if (heavy) wait_vm<11>(); else wait_vm<7>(); }
        bar();
        rd(0);
        if constexpr (s < 7) fill(0);
        rd(1);
        if constexpr (s < 7) fill(1);
        if constexpr (s == 2) get_tag();
#pragma unroll
        for (int p = 0; p < 4; ++p) {
#pragma unroll
          for (int t = 0; t < 2; ++t) {
            hac[t] = MFMA32(wf[p][t], xf[p], hac[t]);
            if (p == 0) { cs[t] = MFMA16(sel, wf[p][t], cs[t]); PIN(cs[t]); }
            SBAR();
            if (p < 2 && t == 0) rd(p + 2);
            if (s == 7) fill(2 * p + t); else if (2 * p + t < 7) fill(2 + 2 * p + t);
          }
          if (s == 7 && p == 1) {
            cswr(0, cs[0]); cswr(1, cs[1]);
            bar();
          }
        }
        if constexpr (s == 7) {
          const f32x4 c0 = csrd(0), c1 = csrd(1);
          if (heavy) wait_vm<20>(); else wait_vm<4>();
          PIN(st); PIN(bias0); PIN(bias1);
          row_stats(); make_xaug();
          hac[0] = MFMA32(waug(c0, bias0), xaug, hac[0]);
          hac[1] = MFMA32(waug(c1, bias1), xaug, hac[1]);
          SBAR();
        }
      });
    }
    if (heavy) { wait_vm<0>(); arrive_nowait(lds0 + L_CNT + 16); }
    hgelu(std::integral_constant<int, 0>{}, [&]() {});
    __builtin_amdgcn_s_setprio(0);
    unsigned rb2[2];
#pragma unroll
    for (int c = 0; c < 2; ++c) rb2[c] = lds0 + L_WG + m * PART + h * 512 + q4 * 128 + ((c ^ (q4 >> 1)) * 64) + (g & 1) * 32 + p4 * 8;
    v4u wq[36][2];
    auto wrd = [&](int kk) {
#pragma unroll
      for (int c = 0; c < 2; ++c) wq[kk][c] = ldtr2(rb2[c] + (kk % RD) * 2048, 1024);
    };
    auto wgot = [&](int kk) { PIN(wq[kk][0]); PIN(wq[kk][1]); };
    const int ftile = 2 * rt + ((lane >> 2) & 1);
    const int T0 = 4 * r - 8 + 2 * m;
    const bool live = T0 >= 0;
    auto kv_fetch = [&]() {
      const unsigned src = WS_KV + (j * 64 + (live ? T0 : 0)) * 4096, vk = live ? lane * 16 : h * 512, vv = live ? lane * 16 : 0x40000000u;
#pragma unroll
      for (int i = 0; i < 8; ++i)
        dma16_sc1(sws, (i & 2) ? vv : vk, src + (live ? i : (i & 3)) * 1024, lds0 + L_KV + (2 * m + (i >> 2)) * KVT + (i & 3) * 1024 + ((i & 3) == 3 ? VB - 1024 : 0));
      const unsigned on = (lane < 2 || live) ? one2 : 0u;
      if (lane < 6) sts128(lds0 + L_KV + (lane < 2 ? 8 + m : 2 * m + ((lane - 2) >> 1)) * KVT + 2048 + (lane & 1) * VB + 1024, (v4u){on, on, on, on});
    };
    v4u af[36][2];
    v2u fl;
    const unsigned a0 = WS_AF + (2 * rt * AFT + 28 * m) * 1024, o0 = WS_AF + (2 * rt * AFT + 112 + 8 * m) * 1024;
    fl = ld8_sc1_async(sws, flag_off(FL_KV, live ? T0 + (lane & 1) : 0, j));
    for (int kk = 2; kk < 6; ++kk) wdma(kk);
    wait_vmf<8>(fl);
    if (lane == 0) __hip_atomic_store(flag(FL_H0, T, j), tag, RLX_AGENT);
    if (!__all(fl[0] == taglo && fl[1] == tag32)) poll_flags(flag(FL_KV, live ? T0 + (lane & 1) : 0, j), tag);
    if (heavy) await(lds0 + L_CNT + 20, 4);
    kv_fetch();
    wdma(6); wdma(7);
    static_for<0, NR + 1>([&](auto kc) {
      constexpr int k = kc.value;
      constexpr int refills = k >= 2 ? 2 * (k - 1) : 0;
      if constexpr (k < NR) {
        wait_vm<(k < 6 ? 2 * (5 - (k < 2 ? 1 : k)) + 13 : 2 * (7 - k)) + refills + (k > HLD ? 1 : 0)>();
        wrd(k);
      }
      if constexpr (k == KVS) { wait_vm<4 + refills>(); arrive_nowait(lds0 + L_CNT); }
      if constexpr (k == HLD) fl = ld8_sc1_async(sws, flag_off(FL_H0, ftile, 4 * m + p4));
      if constexpr (k >= 1) { wgot(k - 1); wdma(k - 1 + RD); }
    });
    wait_vmf<2 * (NR + 1 - HLD)>(fl);
    if (!__all(fl[0] == taglo && fl[1] == tag32)) poll_flags(flag(FL_H0, ftile, 4 * m + p4), tag);
#pragma unroll
    for (int kk = 0; kk < AW; ++kk) {
      af[kk][0] = ld16_sc1_async(sws, lane * 16, a0 + kmap(kk) * 1024);
      af[kk][1] = ld16_sc1_async(sws, lane * 16, a0 + (AFT + kmap(kk)) * 1024);
    }
    __builtin_amdgcn_s_setprio(2);
    f32x16 c2[2][2] = {};
    auto mm2 = [&](int kk, const v4u (&a)[2], auto&& fill) {
      SBAR();
#pragma unroll
      for (int i = 0; i < 4; ++i) { c2[i >> 1][i & 1] = MFMA32(wq[kk][i & 1], a[i >> 1], c2[i >> 1][i & 1]); SBAR(); fill(i); SBAR(); }
    };
    static_assert(FLA <= 4, "one flag register");
    static_for<0, 32>([&](auto kc) {
      constexpr int kk = kc.value;
      wait_vm2<(kk < AW) ? 2 * (AW - 1 - kk) + g2_range(0, kk) : g2_range(kk - AW + 1, kk)>(af[kk][0], af[kk][1]);
      if constexpr (kk == H1_CK || kk == O0_CK || kk == O1_CK) {
        constexpr int kind = kk == H1_CK ? FL_H1 : kk == O0_CK ? FL_O0 : FL_O1;
        wait_vmf<g2_range(kk - FLA, kk) - 1>(fl);
        if (!__all(fl[0] == taglo && fl[1] == tag32)) poll_flags(flag(kind, ftile, 4 * m + p4), tag);
      }
      if constexpr (fl_load(kk)) fl = ld8_sc1_async(sws, flag_off(kk + FLA == H1_CK ? FL_H1 : kk + FLA == O0_CK ? FL_O0 : FL_O1, ftile, 4 * m + p4));
      if constexpr (kk == RES_LD) res_loads();
      if constexpr (kk == 20) res_sum();
      constexpr bool dw = kk >= 1 && kk - 1 + NR + RD < 36;
      if constexpr (dw) wgot(kk - 1 + NR);
      mm2(kk, af[kk], [&](int i) {
        if constexpr (dw) { if (i < 2) wdmah(kk - 1 + NR + RD, i); }
        if constexpr (kk + NR < 28) { if (i == 1) wrd(kk + NR); }
        if constexpr (kk >= 24) { if (i == 0) wrd(kk + 4); }
        if constexpr (kk + AW < 36) {
          if (i >= 2) af[kk + AW][i - 2] = ld16_sc1_async(sws, lane * 16, (kk + AW < 28 ? a0 + kmap(kk + AW) * 1024 : o0 + omap(kk + AW - 28) * 1024) + AFT * (i - 2) * 1024);
        }
        if constexpr (kk == 31) PIN(c2[i >> 1][i & 1]);
      });
    });
    for (int kk = 32; kk < 36; ++kk) wgot(kk);
    unsigned pa = lds0 + L_WG + m * PART + lane * 16;
    PIN(pa);
    auto put = [&](int qn, int qd) {
      const f32x16& c = c2[qn >> 1][qn & 1];
      sts128(pa + (qn * 4 + qd) * 66 * 16, __builtin_bit_cast(v4u, (f32x4){c[4 * qd], c[4 * qd + 1], c[4 * qd + 2], c[4 * qd + 3]}));
    };
    static_for<0, 4>([&](auto qc) {
      constexpr int qn = qc.value;
      if constexpr ((qn & 1) == 0) { wait_vm2<0>(af[32][qn >> 1], af[33][qn >> 1]); PIN(af[34][qn >> 1]); PIN(af[35][qn >> 1]); }
      SBAR();
#pragma unroll
      for (int i = 0; i < 4; ++i) {
        c2[qn >> 1][qn & 1] = MFMA32(wq[32 + i][qn & 1], af[32 + i][qn >> 1], c2[qn >> 1][qn & 1]);
        SBAR();
        if (qn > 0 && i >= 2) { put(qn - 1, 2 * i - 4); put(qn - 1, 2 * i - 3); }
        SBAR();
      }
    });
#pragma unroll
    for (int qd = 0; qd < 4; ++qd) put(3, qd);
  } else {
    __builtin_amdgcn_s_setprio(1);
    lane_setup();
    v4u xr[2];
    xr[0] = ld16_async(sx, lane * 16, (128 * r + 8 * j + 2 * m) * 1024); xr[1] = ld16_async(sx, lane * 16, (128 * r + 8 * j + 2 * m + 1) * 1024);
    unsigned warm;
    {
      warm = ld2_async(make_srd((const void*)(pc & ~127ull), CODE_BYTES), lane < 2 ? lane * 128 : 0x40000000u, (idx * 4 + m) * 256);
    }
    __builtin_amdgcn_s_barrier();
    v2u ep = ld8_sc1_async(sws, m == 0 ? WS_EPOCH : 0x40000000u);
    float sval;
    const int wi = blockIdx.x * 4 + m;
    const u64 wbase = wi < 448 ? (u64)w1 + wi * 4096 : wi < 896 ? (u64)w2 + (wi - 448) * 4096 : (u64)wo + (wi - 896) * 4096;
    auto make_stats = [&]() {
      wait_vm2<1>(xr[0], xr[1]);
      PIN(warm);
      float piv[2], a[4];
#pragma unroll
      for (int u = 0; u < 2; ++u) {
        piv[u] = rdl(bflo(xr[u][0]), 0);
        float s1 = 0.f, s2 = 0.f;
#pragma unroll
        for (int i = 0; i < 8; ++i) { const float d = ((i & 1) ? bfhi(xr[u][i >> 1]) : bflo(xr[u][i >> 1])) - piv[u]; s1 += d; s2 = __builtin_fmaf(d, d, s2); }
        a[2 * u] = s1; a[2 * u + 1] = s2;
      }
#pragma unroll
      for (int i = 0; i < 4; ++i) a[i] += dpp<0xB1>(a[i]);
#pragma unroll
      for (int i = 0; i < 4; ++i) a[i] += dpp<0x4E>(a[i]);
      float t = p4 == 0 ? a[0] : p4 == 1 ? a[1] : p4 == 2 ? a[2] : a[3];
      t += dpp<0x114>(t); t += dpp<0x118>(t);
      const auto s16 = __builtin_amdgcn_permlane16_swap(asu(t), asu(t), false, false);
      t = asf(s16[0]) + asf(s16[1]);
      const auto s32 = __builtin_amdgcn_permlane32_swap(asu(t), asu(t), false, false);
      t = (asf(s32[0]) + asf(s32[1])) * (1.f / DM);
      const float e1 = dpp<0xA0>(t);
      sval = (p4 & 1) ? __builtin_fmaxf(__builtin_fmaf(-e1, e1, t), 0.f) + 1e-5f : t + (p4 ? piv[1] : piv[0]);
      PIN(sval);
    };
    const unsigned acnt = lds0 + L_CNT;
    const unsigned vpos = 2048 + (g & 1) * 512 + h * 256 + (lane & 15) * 16;
    v4u qf[2], am0, am1;
    u64 um[16];
    auto masks = [&]() {
      const int dq = r32 - 4 * h;
#pragma unroll
      for (int u = 0; u < 16; ++u) { um[u] = __builtin_amdgcn_ballot_w64((u & 3) + 8 * (u >> 2) > dq); pin_s(um[u]); }
#pragma unroll
      for (int d = 0; d < 4; ++d) {
        am0[d] = __builtin_amdgcn_inverse_ballot_w64(um[2 * d]) ? 0xffffffffu : __builtin_amdgcn_inverse_ballot_w64(um[2 * d + 1]) ? 0xffff0000u : 0u;
        am1[d] = __builtin_amdgcn_inverse_ballot_w64(um[8 + 2 * d]) ? 0xffffffffu : __builtin_amdgcn_inverse_ballot_w64(um[9 + 2 * d]) ? 0xffff0000u : 0u;
      }
      PIN(am0); PIN(am1);
    };
    {
      v4u xf[32];
      f32x16 acc[3] = {};
      f32x4 cs[3] = {};
      v4u wf[4][3];
      unsigned wb[4];
#pragma unroll
      for (int p = 0; p < 4; ++p) { wb[p] = qbuf(0) + ((p + m) & 3) * 1024 + (8 * h + q4) * 64 + (g & 1) * 32 + p4 * 8; PIN(wb[p]); }
      auto rd = [&](int ks, int t) {
        if (t == 0) xf[ks] = lds128(xbuf(ks >> 2) + xo[ks & 3]);
        wf[ks & 3][t] = ldtr2(wb[ks & 3] + ((ks >> 2) % 3) * 12288 + t * 4096, 256);
      };
      auto mm = [&](int ks, int t) {
        acc[t] = t == 2 ? MFMA32(xf[ks], wf[ks & 3][t], acc[t]) : MFMA32(wf[ks & 3][t], xf[ks], acc[t]);
        if ((ks & 3) == 0) { cs[t] = MFMA16(sel, wf[ks & 3][t], cs[t]); PIN(cs[t]); }
        SBAR();
      };
      make_stats();
      bar();
#pragma unroll
      for (int t = 0; t < 6; ++t) rd(t / 3, t % 3);
      SBAR();
      static_for<0, 8>([&](auto sc) {
        constexpr int s = sc.value;
#pragma unroll
        for (int p = 0; p < 2; ++p) {
#pragma unroll
          for (int t = 0; t < 3; ++t) { mm(4 * s + p, t); rd(4 * s + p + 2, t); SBAR(); }
          if (s == 7 && p == 0) { cswr(4, cs[0]); cswr(5, cs[1]); cswr(6, cs[2]); SBAR(); }
        }
        if constexpr (s == 0) {
          wait_vmf<0>(ep);
          if (m == 0 && lane < 10)
            *(LAS unsigned*)(size_t)(lds0 + L_CNT + lane * 4) = lane == 8 ? ep[0] * 0x9E3779B1u + 0x7F4A7C15u : lane == 9 ? ep[1] * 0x85EBCA77u + 0x165667B1u : 0u;
        }
        if constexpr (s == 1) {
          get_tag();
          if ((lane >> 2) == 3) {
            const v2u gr = {asu(sval), tag32};
            __builtin_amdgcn_raw_buffer_store_b64(gr, rws, WS_STATS + (128 * r + 8 * j + 2 * m) * 16 + p4 * 8, 0, 16);
          }
        }
        if constexpr (s == 1) {
          warm = ld2_async(make_srd((const void*)wbase, 4096), lane * 128, 0);
        }
        if constexpr (s == 5) stat_load();
        if constexpr (s == 6) {
          wait_vm1<0>(st);
          PIN(warm);
          row_stats();
        }
        mm(4 * s + 2, 0); mm(4 * s + 2, 1);
        bar();
        if constexpr (s < 7) {
          mm(4 * s + 2, 2);
          rd(4 * s + 4, 0); rd(4 * s + 4, 1); rd(4 * s + 4, 2); SBAR();
#pragma unroll
          for (int t = 0; t < 3; ++t) { mm(4 * s + 3, t); rd(4 * s + 5, t); SBAR(); }
        }
      });
      __builtin_amdgcn_s_setprio(3);
      const f32x4 c5 = csrd(5), c6 = csrd(6), c4 = csrd(4);
      SBAR();
      mm(30, 2); mm(31, 1);
      make_xaug();
      SBAR();
      mm(31, 2); mm(31, 0);
      if (h == 0) *(LAS float*)(size_t)(lds0 + L_RS + (m * 32 + r32) * 4) = rstd;
      acc[1] = MFMA32(waug(c5, 0u), xaug, acc[1]);
      acc[2] = MFMA32(xaug, waug(c6, 0u), acc[2]);
      acc[0] = MFMA32(waug(c4, 0u), xaug, acc[0]);
      SBAR();
      asm volatile("" ::: "memory");
      f32x4 rs[4];
#pragma unroll
      for (int qd = 0; qd < 4; ++qd) rs[qd] = __builtin_bit_cast(f32x4, lds128(lds0 + L_RS + (m * 32 + 8 * qd + 4 * h) * 4));
      const unsigned kvo = WS_KV + (j * 64 + T) * 4096;
#pragma unroll
      for (int e = 0; e < 16; ++e) acc[1][e] = MUL1(acc[1][e], rstd);
#pragma unroll
      for (int e = 0; e < 2; ++e) {
        const v4u f = pack8(acc[1], e);
        st16_sc1(f, sws, lane * 16, kvo + e * 1024);
        sts128(lds0 + L_KV + (8 + m) * KVT + e * 1024 + lane * 16, f);
      }
#pragma unroll
      for (int qd = 0; qd < 4; ++qd)
#pragma unroll
        for (int e = 0; e < 4; ++e) acc[2][4 * qd + e] = MUL1(acc[2][4 * qd + e], rs[qd][e]);
#pragma unroll
      for (int s = 0; s < 2; ++s) {
        const v4u f = pack8(acc[2], s);
        st16_sc1(f, sws, vpos, kvo + s * 1024);
        sts128(lds0 + L_KV + (8 + m) * KVT + s * VB + vpos, f);
      }
      SBAR();
      const float qs = rstd * (0.25f * 1.4426950408889634f);
#pragma unroll
      for (int e = 0; e < 16; ++e) acc[0][e] = MUL1(acc[0][e], qs);
      qf[0] = pack8(acc[0], 0); qf[1] = pack8(acc[0], 1);
      PIN(qf[0]); PIN(qf[1]);
      arrive_nowait(acnt);
      if (heavy) {
        hsetup(1);
        await(acnt + 16, 4);
        __builtin_amdgcn_s_setprio(1);
        hmm(std::integral_constant<int, 1>{}, xf, [&](auto kc) {
          constexpr int ks = kc.value;
          if constexpr (ks == KVPUB) {
            wait_vm<0>();
            if (lane == 0) __hip_atomic_store(flag(FL_KV, T, j), tag, RLX_AGENT);
          }
          if constexpr (ks == KVPUB + 2) bias_load(1);
          if constexpr (ks == 31) { wait_vm<0>(); PIN(bias0); PIN(bias1); }
        });
        __builtin_amdgcn_s_setprio(3);
        hgelu(std::integral_constant<int, 1>{}, [&]() {});
      } else {
        wait_vm<0>();
        if (lane == 0) __hip_atomic_store(flag(FL_KV, T, j), tag, RLX_AGENT);
      }
    }
    masks();
    const unsigned kb = lds0 + L_KV + m * KVT + lane * 16;
    unsigned vb[2];
#pragma unroll
    for (int e = 0; e < 2; ++e) { vb[e] = lds0 + L_KV + m * KVT + 2048 + ((g & 1) ? 1024 : e * 512 + h * 256 + (lane & 15) * 16); PIN(vb[e]); }
    bool pend = true;
    if (__builtin_amdgcn_readfirstlane(peek(acnt)) < 8) {
      wait_vm<0>();
      if (lane == 0) __hip_atomic_store(flag(FL_H1, T, j), tag, RLX_AGENT);
      pend = false;
      await(acnt, 8);
    }
    asm volatile("" ::: "memory");
    __builtin_amdgcn_s_setprio(3);
    const f32x16 zero = {};
    auto o_store = [&](f32x16& o, int e) {
      const float rl = __builtin_amdgcn_rcpf(o[8]);
#pragma unroll
      for (int u = 0; u < 8; ++u) o[u] = MUL1(o[u], rl);
      st16_sc1(pack8(o, 0), sws, lane * 16, WS_AF + (T * AFT + 112 + 2 * j + e) * 1024);
    };
    int redo = 0;
    {
      f32x16 o[2], sa, sb, su;
      v4u kn, ku, va, vc, vn0, vn1, vu0, vu1, p0, p1, pu;
      auto kaddr = [&](int n) { return kb + ((n & 7) + 1) * KVT + (n >> 3) * 1024; };
      auto vaddr = [&](int n) { return vb[n >> 3] + ((n & 7) + 1) * KVT; };
      kn = lds128(kaddr(0)); vn0 = lds128(vaddr(0)); vn1 = lds128(vaddr(0) + VB);
      sa = MFMA32(kn, qf[0], zero);
      kn = lds128(kaddr(1));
      SBAR();
      auto step = [&](auto nc, f32x16& sc, f32x16& sn) {
        constexpr int n = nc.value, e = n >> 3, i = n & 7;
        if constexpr (n < 15) sn = MFMA32(kn, qf[(n + 1) >> 3], zero);
        if constexpr (n < 14) kn = lds128(kaddr(n + 2));
        if constexpr (i == 5) ku = lds128(kb + e * 1024);
        v4u v0 = vn0, v1 = vn1;
        if constexpr (n < 15) { vn0 = lds128(vaddr(n + 1)); vn1 = lds128(vaddr(n + 1) + VB); }
        if constexpr (i == 6) { vu0 = lds128(vb[e]); vu1 = lds128(vb[e] + VB); }
        SBAR();
        if constexpr (i == 7) {
#pragma unroll
          for (int u = 0; u < 16; ++u) sc[u] = __builtin_amdgcn_inverse_ballot_w64(um[u]) ? su[u] : sc[u];
          PIN(sc); SBAR();
        }
#pragma unroll
        for (int u = 0; u < 6; ++u) sc[u] = EXP2(sc[u]);
        PIN(sc); SBAR();
        if constexpr (n == 8) { o[0] = MFMA32(vc, p1, o[0]); SBAR(); }
        else if constexpr (n > 0) { o[e] = MFMA32(vc, p1, o[e]); SBAR(); }
        sc[6] = EXP2(sc[6]); sc[7] = EXP2(sc[7]);
        p0 = pack8(sc, 0);
        sc[8] = EXP2(sc[8]); sc[9] = EXP2(sc[9]);
        if constexpr (i == 7) { va = p0 & am0; p0 &= ~am0; PIN(va); }
        PIN(sc); PIN(p0); SBAR();
        if constexpr (n == 8) { o[0] = MFMA32(vu1, pu, o[0]); SBAR(); }
        o[e] = MFMA32(v0, p0, i == 0 ? zero : o[e]);
        SBAR();
        if constexpr (i == 6) { su = MFMA32(ku, qf[e], zero); SBAR(); }
#pragma unroll
        for (int u = 10; u < 13; ++u) sc[u] = EXP2(sc[u]);
        if constexpr (i == 7) { PIN(sc); SBAR(); o[e] = MFMA32(vu0, va, o[e]); SBAR(); }
#pragma unroll
        for (int u = 13; u < 16; ++u) sc[u] = EXP2(sc[u]);
        p1 = pack8(sc, 1); vc = v1;
        if constexpr (i == 7) { pu = p1 & am1; p1 &= ~am1; PIN(pu); }
        PIN(p1); SBAR();
        if constexpr (n == 8) {
          if (__all(o[0][8] >= 0x1p-30f && o[0][8] <= 0x1p30f)) o_store(o[0], 0); else redo = 1;
          SBAR();
        }
        if constexpr (n == 10) pend = !redo;
        if constexpr (i == 3 || i == 5 || i == 7) {
          if (pend && (i == 7 || (__builtin_amdgcn_s_getreg(7 | (3 << 11)) & 15) == 0)) {
            wait_vm<0>();
            if (lane == 0) __hip_atomic_store(flag(FL_H1 + e, T, j), tag, RLX_AGENT);
            pend = false;
          }
        }
      };
      static_for<0, 8>([&](auto hc) {
        step(std::integral_constant<int, 2 * hc.value>{}, sa, sb);
        step(std::integral_constant<int, 2 * hc.value + 1>{}, sb, sa);
      });
      o[1] = MFMA32(vc, p1, o[1]);
      o[1] = MFMA32(vu1, pu, o[1]);
      if (__all(o[1][8] >= 0x1p-30f && o[1][8] <= 0x1p30f)) o_store(o[1], 1); else redo |= 2;
    }
    if (redo) {
#pragma unroll 1
      for (int e = 0; e < 2; ++e) {
        if (!((redo >> e) & 1)) continue;
        const v4u q = e ? qf[1] : qf[0];
        const unsigned vbe = e ? vb[1] : vb[0];
        auto merged = [&](const f32x16& c) {
          f32x16 sl = MFMA32(lds128(kb + 8 * KVT + e * 1024), q, c);
          const f32x16 su = MFMA32(lds128(kb + e * 1024), q, c);
          static_for<0, 16>([&](auto u) { sl[u.value] = upper<u.value>() ? su[u.value] : sl[u.value]; });
          return sl;
        };
        float mx = -INFINITY;
#pragma unroll 1
        for (int i = 0; i < 8; ++i) {
          const f32x16 sc = i ? MFMA32(lds128(kb + i * KVT + e * 1024), q, zero) : merged(zero);
#pragma unroll
          for (int u = 0; u < 16; u += 2) mx = max3(mx, sc[u], sc[u + 1]);
        }
        const auto sw = __builtin_amdgcn_permlane32_swap(asu(mx), asu(mx), false, false);
        mx = __builtin_fmaxf(asf(sw[0]), asf(sw[1]));
        f32x16 negm, o;
#pragma unroll
        for (int u = 0; u < 16; ++u) negm[u] = -mx;
        {
          f32x16 sc = merged(negm);
#pragma unroll
          for (int u = 0; u < 16; ++u) sc[u] = EXP2(sc[u]);
          const v4u p0 = pack8(sc, 0), p1 = pack8(sc, 1);
          o = MFMA32(lds128(vbe), p0 & am0, zero);
          o = MFMA32(lds128(vbe + VB), p1 & am1, o);
          o = MFMA32(lds128(vbe + 8 * KVT), p0 & ~am0, o);
          o = MFMA32(lds128(vbe + 8 * KVT + VB), p1 & ~am1, o);
        }
#pragma unroll 1
        for (int i = 1; i < 8; ++i) {
          f32x16 sc = MFMA32(lds128(kb + i * KVT + e * 1024), q, negm);
#pragma unroll
          for (int u = 0; u < 16; ++u) sc[u] = EXP2(sc[u]);
          o = MFMA32(lds128(vbe + i * KVT), pack8(sc, 0), o);
          o = MFMA32(lds128(vbe + i * KVT + VB), pack8(sc, 1), o);
        }
        o_store(o, e);
      }
      wait_vm<0>();
      if (lane == 0) __hip_atomic_store(flag(FL_O0, T, j), tag, RLX_AGENT);
    }
    wait_vm<0>();
    if (lane == 0) __hip_atomic_store(flag(FL_O1, T, j), tag, RLX_AGENT);
    res_loads();
    wait_vm<0>();
    res_sum();
    if (blockIdx.x == 0 && wave == 7) {
#pragma unroll 1
      for (int i = 0; i < 4; ++i) poll_flags(flag(FL_KV, 4 * (4 * i + g), lane & 15), tag);
      if (lane == 0) __hip_atomic_store((GAS u64*)(ws + WS_EPOCH), tag, RLX_AGENT);
    }
  }
  {
    int t2 = threadIdx.x; PIN(t2);
    const int row = t2 >> 3, ch = t2 & 7;
    unsigned a = lds0 + L_WG + (((row >> 5) * 8 + ch) * 66 + (row & 31)) * 16, oo = (64 * rt + row) * 1024 + (64 * ct + ch * 8) * 2;
    PIN(a); PIN(oo);
    bar();
    f32x4 pl[4], ph[4];
#pragma unroll
    for (int w = 0; w < 4; ++w) { pl[w] = __builtin_bit_cast(f32x4, lds128(a + w * PART)); ph[w] = __builtin_bit_cast(f32x4, lds128(a + w * PART + 512)); }
    SBAR();
#pragma unroll
    for (int w = 0; w < 4; ++w) { lo += pl[w]; hi += ph[w]; }
    const v4u o = {pk2(lo[0], lo[1]), pk2(lo[2], lo[3]), pk2(hi[0], hi[1]), pk2(hi[2], hi[3])};
    __builtin_amdgcn_raw_buffer_store_b128(o, __builtin_amdgcn_make_buffer_rsrc(out, 0, SEQ * DM * 2, 0x00020000), oo, 0, 16);
  }
}

extern "C" void kernel_launch(void* const* d_in, const int* in_sizes, int n_in, void* d_out, int out_size, void* d_ws, size_t ws_size,
                              hipStream_t stream) {
  (void)in_sizes; (void)n_in; (void)out_size; (void)ws_size;
  (void)hipFuncSetAttribute((const void*)fused_block, hipFuncAttributeMaxDynamicSharedMemorySize, LDS_BYTES);
  fused_block<<<256, 512, LDS_BYTES, stream>>>(d_in[0], d_in[1], d_in[2], d_in[4], d_in[5], d_in[6], d_in[7], d_out, (char*)d_ws);
}
```

```cpp
#include <hip/hip_runtime.h>
#include <hip/hip_bf16.h>
#include <stdint.h>
#include <type_traits>

typedef unsigned v4u __attribute__((ext_vector_type(4)));
typedef unsigned v2u __attribute__((ext_vector_type(2)));
typedef float f32x2 __attribute__((ext_vector_type(2)));
typedef float f32x4 __attribute__((ext_vector_type(4)));
typedef float f32x16 __attribute__((ext_vector_type(16)));
typedef short bf16x8 __attribute__((ext_vector_type(8)));
typedef short s16x4 __attribute__((ext_vector_type(4)));
typedef __bf16 bf16x2_t __attribute__((ext_vector_type(2)));
typedef unsigned long long u64;
#define GAS __attribute__((address_space(1)))
#define LAS __attribute__((address_space(3)))
#define DI __device__ __forceinline__
#define RLX_AGENT __ATOMIC_RELAXED, __HIP_MEMORY_SCOPE_AGENT

constexpr int SEQ = 2048, DM = 512, FF = 1792;
constexpr unsigned WS_EPOCH = 0;
constexpr unsigned WS_STATS = 32768;
constexpr unsigned WS_FLAG = 65536;
constexpr unsigned WS_KV = WS_FLAG + 5 * 1024 * 128;
constexpr int AFT = 145;
constexpr unsigned WS_AF = WS_KV + 16 * 64 * 4096;
constexpr unsigned WS_END = WS_AF + 64 * AFT * 1024;
constexpr unsigned WS_STAMP = WS_END;
enum { FL_KV = 0, FL_H0 = 1, FL_H1 = 2, FL_O0 = 3, FL_O1 = 4 };
constexpr int L_WA = 0;
constexpr int L_WG = 86016;
constexpr int VB = 1040;
constexpr int KVT = 2048 + 2 * VB;
constexpr int L_KV = 32768;
constexpr int RD = 8, NR = 12;
constexpr int PART = 16 * 66 * 16;
constexpr int L_CS = L_WG + 4 * PART;
constexpr int L_RS = L_CS + 3584;
constexpr int L_CNT = L_RS + 512;
constexpr int LDS_BYTES = L_CNT + 64;
constexpr int xbuf(int s) { return ((s + 1) % 3) * 16384; }
constexpr int qbuf(int s) { return 49152 + (s % 3) * 12288; }
constexpr int w1buf(int s) { return L_WG + (s % 3) * PART + 8192; }
static_assert(L_KV + 8 * KVT >= 65536 && xbuf(7) == 32768 && qbuf(2) + 12288 <= L_WG && L_KV + 12 * KVT <= L_WG && PART >= RD * 2048 && LDS_BYTES <= 163840, "lds map");
#define AWIN 6
constexpr int AW = AWIN;
constexpr unsigned SPIN_CAP = 1u << 23;

#define MFMA32(a, b, c) __builtin_amdgcn_mfma_f32_32x32x16_bf16(__builtin_bit_cast(bf16x8, a), __builtin_bit_cast(bf16x8, b), c, 0, 0, 0)
#define MFMA16(a, b, c) __builtin_amdgcn_mfma_f32_16x16x32_bf16(__builtin_bit_cast(bf16x8, a), __builtin_bit_cast(bf16x8, b), c, 0, 0, 0)
#define SBAR() __builtin_amdgcn_sched_barrier(0)

#define STAMP_AT(i) do { } while (0)

DI float asf(unsigned u) { return __builtin_bit_cast(float, u); }
DI unsigned asu(float f) { return __builtin_bit_cast(unsigned, f); }
DI unsigned pk2(float lo, float hi) { return __builtin_bit_cast(unsigned, __builtin_convertvector((f32x2){lo, hi}, bf16x2_t)); }
DI float bflo(unsigned u) { return __builtin_bit_cast(float, u << 16); }
DI float bfhi(unsigned u) { return __builtin_bit_cast(float, u & 0xffff0000u); }
DI v4u make_srd(const void* p, unsigned bytes) { const u64 a = (u64)p; return (v4u){(unsigned)a, (unsigned)(a >> 32) & 0xffffu, bytes, 0x00020000u}; }
DI void dma16(v4u srd, unsigned voff, unsigned soff, unsigned lds) {
  asm volatile("s_mov_b32 m0, %3\n\ts_nop 0\n\tbuffer_load_dwordx4 %0, %1, %2 offen lds" :: "v"(voff), "s"(srd), "s"(soff), "s"(lds) : "memory");
}
DI void dma16_sc1(v4u srd, unsigned voff, unsigned soff, unsigned lds) {
  asm volatile("s_mov_b32 m0, %3\n\ts_nop 0\n\tbuffer_load_dwordx4 %0, %1, %2 offen sc1 lds" :: "v"(voff), "s"(srd), "s"(soff), "s"(lds) : "memory");
}
DI void dma16_a(v4u srd, unsigned voff, unsigned soff, unsigned lds) { dma16(srd, voff, soff, lds); }
DI void dma16_b(v4u srd, unsigned voff1, unsigned soff) {
  asm volatile("buffer_load_dwordx4 %0, %1, %2 offen offset:1024 lds" :: "v"(voff1), "s"(srd), "s"(soff) : "memory");
}
DI v4u ld16_sc1_async(v4u srd, unsigned voff, unsigned soff) {
  v4u r; asm volatile("buffer_load_dwordx4 %0, %1, %2, %3 offen sc1" : "=v"(r) : "v"(voff), "s"(srd), "s"(soff) : "memory"); return r;
}
DI v4u ld16_async(v4u srd, unsigned voff, unsigned soff) {
  v4u r; asm volatile("buffer_load_dwordx4 %0, %1, %2, %3 offen" : "=v"(r) : "v"(voff), "s"(srd), "s"(soff) : "memory"); return r;
}
DI unsigned ld2_async(v4u srd, unsigned voff, unsigned soff) {
  unsigned r; asm volatile("buffer_load_ushort %0, %1, %2, %3 offen" : "=v"(r) : "v"(voff), "s"(srd), "s"(soff) : "memory"); return r;
}
DI void st16_sc1(v4u v, v4u srd, unsigned voff, unsigned soff) {
  asm volatile("buffer_store_dwordx4 %0, %1, %2, %3 offen sc1\n\ts_nop 1" :: "v"(v), "v"(voff), "s"(srd), "s"(soff) : "memory");
}
template <int N> DI void wait_vm1(v4u& a) { asm volatile("s_waitcnt vmcnt(%1)" : "+v"(a) : "i"(N) : "memory"); }
template <int N> DI void wait_vm2(v4u& a, v4u& b) { asm volatile("s_waitcnt vmcnt(%2)" : "+v"(a), "+v"(b) : "i"(N) : "memory"); }
template <int N> DI void wait_vm3(v4u& a, v4u& b, v4u& c) { asm volatile("s_waitcnt vmcnt(%3)" : "+v"(a), "+v"(b), "+v"(c) : "i"(N) : "memory"); }
DI void pin_s(u64& v) { asm volatile("" : "+s"(v)); }
template <class V> DI void pin(V& v) { asm volatile("" : "+v"(v)); }
template <int N> DI void wait_vm() { asm volatile("s_waitcnt vmcnt(%0)" :: "i"(N) : "memory"); }
DI void bar() { asm volatile("s_waitcnt lgkmcnt(0)\n\ts_barrier" ::: "memory"); }
DI v4u lds128(unsigned a) { return *(const LAS v4u*)(size_t)a; }
DI void sts128(unsigned a, v4u v) { *(LAS v4u*)(size_t)a = v; }
DI v2u ldtr(unsigned a) { return __builtin_bit_cast(v2u, __builtin_amdgcn_ds_read_tr16_b64_v4i16((LAS s16x4*)(size_t)a)); }
DI v4u ldtr2(unsigned a, int second) { const v2u lo = ldtr(a), hi = ldtr(a + second); return (v4u){lo.x, lo.y, hi.x, hi.y}; }
template <int CTRL> DI float dpp(float v) { return __builtin_bit_cast(float, __builtin_amdgcn_update_dpp(0, __builtin_bit_cast(int, v), CTRL, 0xf, 0xf, true)); }
DI float rdl(float v, int l) { return __builtin_bit_cast(float, __builtin_amdgcn_readlane(__builtin_bit_cast(int, v), l)); }
DI v4u pack8(const f32x16& a, int s) { return (v4u){pk2(a[8 * s], a[8 * s + 1]), pk2(a[8 * s + 2], a[8 * s + 3]), pk2(a[8 * s + 4], a[8 * s + 5]), pk2(a[8 * s + 6], a[8 * s + 7])}; }
DI void poll_flags(const GAS u64* p, u64 tag) {
#pragma unroll 1
  for (unsigned spin = 0; spin < SPIN_CAP; ++spin) {
    const u64 v = __hip_atomic_load(p, RLX_AGENT);
    if (__all(v == tag)) break;
    __builtin_amdgcn_s_sleep(1);
  }
  asm volatile("" ::: "memory");
}
constexpr u64 upper_mask(int reg) {
  u64 m = 0;
  for (int l = 0; l < 64; ++l) { const int row = (reg & 3) + 8 * (reg >> 2) + 4 * (l >> 5); if (row > (l & 31)) m |= 1ull << l; }
  return m;
}
template <int REG> DI bool upper() { return __builtin_amdgcn_inverse_ballot_w64(upper_mask(REG)); }
template <int I, int N, class Fn> DI void static_for(Fn&& f) { if constexpr (I < N) { f(std::integral_constant<int, I>{}); static_for<I + 1, N>(f); } }
DI float max3(float a, float b, float c) { return __builtin_fmaxf(__builtin_fmaxf(a, b), c); }

#define FLAHEAD 4
constexpr int FLA = FLAHEAD, H1_CK = 16 - AW, O0_CK = 28 - AW, O1_CK = 32 - AW, RES_LD = 12;
constexpr bool fl_load(int t) { return t == H1_CK - FLA || t == O0_CK - FLA || t == O1_CK - FLA; }
constexpr int g2_issued(int t) { return ((t >= 1 && t - 1 + NR + RD < 36) ? 2 : 0) + ((t + AW < 36) ? 2 : 0) + fl_load(t) + (t == RES_LD ? 2 : 0); }
constexpr int g2_range(int a, int b) { int n = 0; for (int t = a; t < b; ++t) n += g2_issued(t); return n; }
#define GELU_PK(P) (P == 1)
#define PRIO_G2 2
#define PRIO_ATT 3
#define PRIO_G1 2
#define ROT r
#define CODEWARM 32000
#define MIDWARM 1
#define KVS 1
constexpr int HLD = 9;
#define KVPUB 2
#define EXP2(v) __builtin_amdgcn_exp2f(v)
#define PIN(v) pin(v)
extern "C" __device__ float mul_unpacked(float, float) __asm("llvm.amdgcn.fmul.legacy");
#define MUL1(a, b) mul_unpacked(a, b)
#define FMA1(a, b, c) __builtin_amdgcn_div_fmasf(a, b, c, false)
DI v2u ld8_sc1_async(v4u srd, unsigned voff) {
  v2u r; asm volatile("buffer_load_dwordx2 %0, %1, %2, 0 offen sc1" : "=v"(r) : "v"(voff), "s"(srd) : "memory"); return r;
}
template <int N> DI void wait_vmf(v2u& a) { asm volatile("s_waitcnt vmcnt(%1)" : "+v"(a) : "i"(N) : "memory"); }
DI void arrive(unsigned cnt, int lane) {
  asm volatile("s_waitcnt lgkmcnt(0)" ::: "memory");
  if (lane == 0) __hip_atomic_fetch_add((LAS unsigned*)(size_t)cnt, 1u, __ATOMIC_RELAXED, __HIP_MEMORY_SCOPE_WORKGROUP);
}
DI void arrive_nowait(unsigned cnt) {
  asm volatile("s_mov_b64 exec, 1\n\tds_add_u32 %0, %1\n\ts_mov_b64 exec, -1" :: "v"(cnt), "v"(1u) : "memory");
}
DI unsigned peek(unsigned cnt) { return __hip_atomic_load((LAS unsigned*)(size_t)cnt, __ATOMIC_RELAXED, __HIP_MEMORY_SCOPE_WORKGROUP); }
DI void await(unsigned cnt, unsigned n) {
#pragma unroll 1
  for (unsigned spin = 0; spin < SPIN_CAP * 32; ++spin) {
    if (__builtin_amdgcn_readfirstlane(peek(cnt)) >= n) break;
    __builtin_amdgcn_s_sleep(1);
  }
  asm volatile("" ::: "memory");
}

__global__ __launch_bounds__(512, 2) void fused_block(const void* __restrict__ x, const void* __restrict__ wqkv, const void* __restrict__ wo,
                                                      const void* __restrict__ w1, const void* __restrict__ b1, const void* __restrict__ w2,
                                                      const void* __restrict__ b2, void* __restrict__ out, char* __restrict__ ws) {
  constexpr unsigned lds0 = 0;
  u64 pc;
  asm volatile("s_getpc_b64 %0" : "=s"(pc));
  const int wave = __builtin_amdgcn_readfirstlane(threadIdx.x >> 6);
  const int m = wave & 3;
  const bool isG = wave < 4;
  const int xcd = blockIdx.x & 7, idx = blockIdx.x >> 3;
  const int r = (xcd >> 2) * 8 + (idx >> 2), j = (xcd & 3) * 4 + (idx & 3);
  const int rt = (xcd >> 1) * 8 + (idx >> 2), ct = (xcd & 1) * 4 + (idx & 3);
  int lane = threadIdx.x & 63, r32, h, g, q4, p4;
  STAMP_AT(0);
  asm volatile("" :: "s"(x), "s"(wqkv), "s"(wo), "s"(w1), "s"(b1), "s"(w2), "s"(b2), "s"(out), "s"(ws));

  const v4u sx = make_srd(x, SEQ * DM * 2), sws = make_srd(ws, WS_END);
  const __amdgpu_buffer_rsrc_t rws = __builtin_amdgcn_make_buffer_rsrc(ws, 0, WS_END, 0x00020000);
  const unsigned one2 = 0x3f803f80u;
  const int T = 4 * r + m;
  const int rot = ROT;
  auto flag_off = [&](int kind, int tile, int jj) { return WS_FLAG + ((kind * 64 + tile) * 16 + jj) * 128; };
  auto flag = [&](int kind, int tile, int jj) { return (GAS u64*)(ws + flag_off(kind, tile, jj)); };
  u64 tag;
  unsigned taglo, tag32;
  auto get_tag = [&]() {
    const v2u tg = *(const LAS v2u*)(size_t)(lds0 + L_CNT + 32);
    taglo = __builtin_amdgcn_readfirstlane(tg[0]); tag32 = __builtin_amdgcn_readfirstlane(tg[1]);
    tag = ((u64)tag32 << 32) | taglo;
  };
  v4u xres, bres, xaug, st;
  float mean, var, rstd, sd;
  v4u sel;
  unsigned xo[4], swz;
  auto lane_setup = [&]() {
    lane = threadIdx.x & 63; PIN(lane);
    r32 = lane & 31; h = lane >> 5; g = lane >> 4; q4 = (lane >> 2) & 3; p4 = lane & 3;
    const unsigned selv = ((lane & 15) == (g & 1)) ? one2 : 0u;
    sel = (v4u){selv, selv, selv, selv};
#pragma unroll
    for (int p = 0; p < 4; ++p) xo[p] = (32 * m + r32) * 128 + (((2 * ((p + m) & 3) + h) ^ ((r32 >> 1) & 7)) * 16);
    swz = ((((lane >> 2) & 1) ^ (g & 1)) * 64) + p4 * 16;
  };

  auto stat_load = [&]() { st = ld16_sc1_async(sws, WS_STATS + (128 * r + 32 * m + r32) * 16, 0); };
  auto res_loads = [&]() {
    int t2 = threadIdx.x; PIN(t2);
    const int row = 64 * rt + (t2 >> 3), col = 64 * ct + (t2 & 7) * 8;
    xres = ld16_async(sx, row * 1024 + col * 2, 0);
    bres = ld16_async(make_srd(b2, DM * 2), col * 2, 0);
  };
  f32x4 lo, hi;
  auto res_sum = [&]() {
    PIN(xres); PIN(bres);
    lo = (f32x4){bflo(xres[0]) + bflo(bres[0]), bfhi(xres[0]) + bfhi(bres[0]), bflo(xres[1]) + bflo(bres[1]), bfhi(xres[1]) + bfhi(bres[1])};
    hi = (f32x4){bflo(xres[2]) + bflo(bres[2]), bfhi(xres[2]) + bfhi(bres[2]), bflo(xres[3]) + bflo(bres[3]), bfhi(xres[3]) + bfhi(bres[3])};
    PIN(lo); PIN(hi);
  };
  auto row_stats = [&]() {
    if (!__all(st[1] == tag32 && st[3] == tag32)) {
#pragma unroll 1
      for (unsigned spin = 0; spin < SPIN_CAP; ++spin) {
        st = ld16_sc1_async(sws, WS_STATS + (128 * r + 32 * m + r32) * 16, 0);
        wait_vm1<0>(st);
        if (__all(st[1] == tag32 && st[3] == tag32)) break;
      }
    }
    mean = asf(st[0]); var = asf(st[2]);
    PIN(mean); PIN(var);
  };
  auto make_xaug = [&]() {
    PIN(mean); PIN(var);
    rstd = __builtin_amdgcn_rsqf(var); sd = var * rstd;
    const unsigned uh = pk2(-mean, sd);
    const unsigned ul = pk2(-mean - bflo(uh), FMA1(bfhi(uh), -1.f, sd));
    xaug = (v4u){(uh & 0xffffu) | (uh << 16), (ul & 0xffffu) | (uh & 0xffff0000u), ul >> 16, 0u};
    if (h) xaug = (v4u){0u, 0u, 0u, 0u};
    PIN(xaug);
  };
  auto cswr = [&](int tile, f32x4 c) {
    if (lane < 16) { LAS float* p = (LAS float*)(size_t)(lds0 + L_CS + (tile * 32 + lane) * 16 + m * 4); p[0] = c[0]; p[64] = c[1]; }
  };
  auto csrd = [&](int tile) { return __builtin_bit_cast(f32x4, lds128(lds0 + L_CS + (tile * 32 + r32) * 16)); };
  auto waug = [&](f32x4 c4, unsigned b16) {
    const float c = ((c4[0] + c4[1]) + c4[2]) + c4[3];
    const unsigned uh = pk2(c, 0.f);
    const unsigned ul = pk2(c - bflo(uh), 0.f);
    return (v4u){uh | (ul << 16), uh | (b16 << 16), b16, 0u};
  };

  const v4u sw1 = make_srd(w1, DM * FF * 2);
  const int hcol0 = 448 * (j >> 2) + 128 * (j & 3);
  const bool heavy = (j & 3) != 3;
  unsigned bias0, bias1;
  auto bias_load = [&](int P) {
    bias0 = ld2_async(make_srd(b1, FF * 2), r32 * 2, (hcol0 + 64 * P) * 2);
    bias1 = ld2_async(make_srd(b1, FF * 2), r32 * 2, (hcol0 + 64 * P + 32) * 2);
  };
  auto w1dma = [&](int P, int c, unsigned dst, int half) {
    const unsigned vw1 = (lane >> 3) * (FF * 2) + swz, vw1b = vw1 + 8 * (FF * 2) - 1024;
    const unsigned so = (64 * ((c + rot) & 7) + 16 * m) * (FF * 2) + (hcol0 + 64 * P) * 2;
    if (half == 0) dma16_a(sw1, vw1, so, dst + m * 2048); else dma16_b(sw1, vw1b, so);
  };
  f32x16 hac[2];
  unsigned rk[4][2];
  auto hsetup = [&](int P) {
#pragma unroll
    for (int p = 0; p < 4; ++p)
#pragma unroll
      for (int c = 0; c < 2; ++c) {
        rk[p][c] = lds0 + (P ? L_WA : w1buf(0)) + ((p + m) & 3) * 2048 + h * 1024 + q4 * 128 + ((c ^ (q4 >> 1)) * 64) + (g & 1) * 32 + p4 * 8;
        PIN(rk[p][c]);
      }
  };
  auto hmm = [&](auto pc, const v4u (&xf)[32], auto&& hook) {
    constexpr int P = pc.value;
    constexpr int D = 4;
    const unsigned cnt = lds0 + L_CNT + 4;
    f32x4 cs[2] = {}, c0, c1;
    v4u wf[D][2], au0, au1;
    unsigned seen = 0;
    hac[0] = (f32x16){}; hac[1] = (f32x16){};
    auto rd = [&](int ks, int t) { wf[ks % D][t] = ldtr2(rk[ks & 3][t] + ((ks >> 2) ^ 4) * 8192, 512); };
#pragma unroll
    for (int ks = 0; ks < D; ++ks) { rd(ks, 0); rd(ks, 1); }
    SBAR();
    static_for<0, 32>([&](auto kc) {
      constexpr int ks = kc.value;
      hook(kc);
      if constexpr (ks == 12) arrive_nowait(cnt + 16);
      if constexpr (ks == 29) seen = peek(cnt);
#pragma unroll
      for (int t = 0; t < 2; ++t) {
        hac[t] = MFMA32(wf[ks % D][t], xf[ks ^ 16], hac[t]);
        if ((ks & 3) == 0 && ks < 28) { cs[t] = MFMA16(sel, wf[ks % D][t], cs[t]); PIN(cs[t]); }
        if (ks == 26) { cs[t] = MFMA16(sel, wf[28 % D][t], cs[t]); PIN(cs[t]); }
        SBAR();
        if constexpr (ks + D < 32) rd(ks + D, t);
        if constexpr (ks == 31) { if (t == 0) { au0 = waug(c0, bias0); PIN(au0); } else { au1 = waug(c1, bias1); PIN(au1); } }
        SBAR();
      }
      if constexpr (ks == 30) {
        PIN(hac[0]); PIN(hac[1]);
        if (__builtin_amdgcn_readfirstlane(seen) < 4) await(cnt, 4);
        asm volatile("" ::: "memory");
        if constexpr (ks == 30) { c0 = csrd(2 * P); c1 = csrd(2 * P + 1); }
      }
      if constexpr (ks == 27) { cswr(2 * P, cs[0]); cswr(2 * P + 1, cs[1]); arrive_nowait(cnt); }
      SBAR();
    });
    hac[0] = MFMA32(au0, xaug, hac[0]);
    hac[1] = MFMA32(au1, xaug, hac[1]);
    SBAR();
  };
  auto hgelu = [&](auto pc, auto&& mid) {
    constexpr int P = pc.value;
    constexpr bool PK = GELU_PK(P);
    const float C0 = 2.3022081986f, C1 = C0 * 0.044715f;
    const float ga = -C1 * rstd * rstd * rstd, gb = -C0 * rstd;
    auto mul = [&](f32x2 a, f32x2 b) { return PK ? a * b : (f32x2){MUL1(a[0], b[0]), MUL1(a[1], b[1])}; };
    auto fma = [&](f32x2 a, float b, float c) {
      return PK ? __builtin_elementwise_fma(a, (f32x2){b, b}, (f32x2){c, c}) : (f32x2){FMA1(a[0], b, c), FMA1(a[1], b, c)}; };
#pragma unroll
    for (int t = 0; t < 2; ++t) {
      f32x2 v[8], z[8];
#pragma unroll
      for (int e = 0; e < 8; ++e) { v[e] = (f32x2){hac[t][2 * e], hac[t][2 * e + 1]}; z[e] = mul(v[e], v[e]); }
      SBAR();
#pragma unroll
      for (int e = 0; e < 8; ++e) z[e] = fma(z[e], ga, gb);
      SBAR();
#pragma unroll
      for (int e = 0; e < 8; ++e) z[e] = mul(z[e], v[e]);
      SBAR();
#pragma unroll
      for (int e = 0; e < 8; ++e) z[e] = (f32x2){EXP2(z[e][0]), EXP2(z[e][1])};
      SBAR();
#pragma unroll
      for (int e = 0; e < 8; ++e) z[e] = fma(z[e], sd, sd);
      SBAR();
#pragma unroll
      for (int e = 0; e < 8; ++e) z[e] = (f32x2){__builtin_amdgcn_rcpf(z[e][0]), __builtin_amdgcn_rcpf(z[e][1])};
      SBAR();
#pragma unroll
      for (int e = 0; e < 8; ++e) z[e] = mul(z[e], v[e]);
      SBAR();
#pragma unroll
      for (int s = 0; s < 2; ++s) {
        const v4u f = {pk2(z[4 * s][0], z[4 * s][1]), pk2(z[4 * s + 1][0], z[4 * s + 1][1]), pk2(z[4 * s + 2][0], z[4 * s + 2][1]), pk2(z[4 * s + 3][0], z[4 * s + 3][1])};
        st16_sc1(f, sws, lane * 16, WS_AF + (T * AFT + (hcol0 >> 4) + 4 * P + 2 * t + s) * 1024);
      }
      if (t == 0) mid();
      SBAR();
    }
  };

  if (isG) {
    lane_setup();
    const v4u sw2 = make_srd(w2, FF * DM * 2), swo = make_srd(wo, DM * DM * 2);
    const unsigned vw2 = (lane >> 3) * 1024 + swz, vw2b = vw2 + 7 * 1024;
    const int ro3 = rt % 3;
    auto kmap = [&](int kk) {
      if (kk < 16) return 8 * (((kk >> 2) + rt) & 3) + (kk & 3);
      const int p = ((kk - 16) >> 2) + ro3;
      return 8 * (p >= 3 ? p - 3 : p) + 4 + (kk & 3);
    };
    auto omap = [&](int i) { return 2 * ((i + rt) & 3) + (i >> 2); };
    auto wdmah = [&](int kk, int half) {
      const unsigned so = (kk < 28 ? 448 * m + 16 * kmap(kk) : 128 * m + 16 * omap(kk - 28)) * 1024 + 128 * ct;
      if (half == 0) dma16_a(kk < 28 ? sw2 : swo, vw2, so, lds0 + L_WG + m * PART + (kk % RD) * 2048); else dma16_b(kk < 28 ? sw2 : swo, vw2b, so);
    };
    auto wdma = [&](int kk) { wdmah(kk, 0); wdmah(kk, 1); };
    {
      const v4u swq = make_srd(wqkv, DM * 3 * DM * 2);
      const unsigned vx0 = (lane >> 3) * 1024 + (((lane & 7) ^ g) * 16), vx1 = (7 + (lane >> 3)) * 1024 + (((lane & 7) ^ (4 + g)) * 16);
      const unsigned vwq = (lane >> 2) * (3 * DM * 2) + p4 * 16;
      auto piece = [&](int s, int i) {
        const int kc = (s + rot) & 7;
        if (i < 4) {
          const unsigned so = (128 * r + 32 * m + 16 * (i >> 1)) * 1024 + kc * 128;
          if (i & 1) dma16_b(sx, vx1, so); else dma16_a(sx, vx0, so, lds0 + xbuf(s) + m * 4096 + (i >> 1) * 2048);
        } else if (i < 7) dma16(swq, vwq, (64 * kc + 16 * m) * (3 * DM * 2) + (512 * (i - 4) + 32 * j) * 2, lds0 + qbuf(s) + ((i - 4) * 4 + m) * 1024);
        else w1dma(0, s, lds0 + w1buf(s), i - 7);
      };
#pragma unroll
      for (int i = 0; i < 18; ++i) { piece(i / 9, i % 9); if (i == 8) __builtin_amdgcn_s_barrier(); }
      __builtin_amdgcn_s_setprio(PRIO_G1);
      hsetup(0);
      f32x4 cs[2] = {};
      v4u xf[4], wf[4][2];
      hac[0] = (f32x16){}; hac[1] = (f32x16){};
      static_for<0, 8>([&](auto sc) {
        constexpr int s = sc.value;
        auto fill = [&](int i) {
          if constexpr (s < 6) piece(s + 2, i);
          if constexpr (s == 6) {
            if (i == 0) { stat_load(); bias_load(0); }
            else if (i < 5) { if (heavy) w1dma(1, (i - 1) >> 1, lds0 + L_WA + ((i - 1) >> 1) * 8192, (i - 1) & 1); }
            else wdmah((i - 5) >> 1, (i - 5) & 1);
          }
          if constexpr (s == 7) {
            if (heavy && i < 4) w1dma(1, 2 + (i >> 1), lds0 + L_WA + (2 + (i >> 1)) * 8192, i & 1);
            if (heavy && i >= 4) { w1dma(1, i, lds0 + L_WA + i * 8192, 0); w1dma(1, i, 0, 1); }
          }
          SBAR();
        };
        auto rd = [&](int p) {
          xf[p] = lds128(xbuf(s) + xo[p]);
          wf[p][0] = ldtr2(rk[p][0] + (s % 3) * PART, 512); wf[p][1] = ldtr2(rk[p][1] + (s % 3) * PART, 512);
          SBAR();
        };
        if constexpr (s < 7) wait_vm<9>(); else { if (heavy) wait_vm<11>(); else wait_vm<7>(); }
        bar();
        STAMP_AT(10 + s);
        rd(0);
        if constexpr (s < 7) fill(0);
        rd(1);
        if constexpr (s < 7) fill(1);
        if constexpr (s == 2) get_tag();
#pragma unroll
        for (int p = 0; p < 4; ++p) {
#pragma unroll
          for (int t = 0; t < 2; ++t) {
            hac[t] = MFMA32(wf[p][t], xf[p], hac[t]);
            if (p == 0) { cs[t] = MFMA16(sel, wf[p][t], cs[t]); PIN(cs[t]); }
            SBAR();
            if (p < 2 && t == 0) rd(p + 2);
            if (s == 7) fill(2 * p + t); else if (2 * p + t < 7) fill(2 + 2 * p + t);
          }
          if (s == 7 && p == 1) {
            cswr(0, cs[0]); cswr(1, cs[1]);
            bar();
            STAMP_AT(1);
          }
        }
        if constexpr (s == 7) {
          const f32x4 c0 = csrd(0), c1 = csrd(1);
          if (heavy) wait_vm<20>(); else wait_vm<4>();
          PIN(st); PIN(bias0); PIN(bias1);
          row_stats(); make_xaug();
          hac[0] = MFMA32(waug(c0, bias0), xaug, hac[0]);
          hac[1] = MFMA32(waug(c1, bias1), xaug, hac[1]);
          SBAR();
        }
      });
    }
    STAMP_AT(18);
    if (heavy) { wait_vm<0>(); arrive_nowait(lds0 + L_CNT + 16); }
    hgelu(std::integral_constant<int, 0>{}, [&]() {});
    __builtin_amdgcn_s_setprio(0);
    STAMP_AT(19);
    unsigned rb2[2];
#pragma unroll
    for (int c = 0; c < 2; ++c) rb2[c] = lds0 + L_WG + m * PART + h * 512 + q4 * 128 + ((c ^ (q4 >> 1)) * 64) + (g & 1) * 32 + p4 * 8;
    v4u wq[36][2];
    auto wrd = [&](int kk) {
#pragma unroll
      for (int c = 0; c < 2; ++c) wq[kk][c] = ldtr2(rb2[c] + (kk % RD) * 2048, 1024);
    };
    auto wgot = [&](int kk) { PIN(wq[kk][0]); PIN(wq[kk][1]); };
    const int ftile = 2 * rt + ((lane >> 2) & 1);
    const int T0 = 4 * r - 8 + 2 * m;
    const bool live = T0 >= 0;
    auto kv_fetch = [&]() {
      const unsigned src = WS_KV + (j * 64 + (live ? T0 : 0)) * 4096, vk = live ? lane * 16 : h * 512, vv = live ? lane * 16 : 0x40000000u;
#pragma unroll
      for (int i = 0; i < 8; ++i)
        dma16_sc1(sws, (i & 2) ? vv : vk, src + (live ? i : (i & 3)) * 1024, lds0 + L_KV + (2 * m + (i >> 2)) * KVT + (i & 3) * 1024 + ((i & 3) == 3 ? VB - 1024 : 0));
      const unsigned on = (lane < 2 || live) ? one2 : 0u;
      if (lane < 6) sts128(lds0 + L_KV + (lane < 2 ? 8 + m : 2 * m + ((lane - 2) >> 1)) * KVT + 2048 + (lane & 1) * VB + 1024, (v4u){on, on, on, on});
    };
    v4u af[36][2];
    v2u fl;
    const unsigned a0 = WS_AF + (2 * rt * AFT + 28 * m) * 1024, o0 = WS_AF + (2 * rt * AFT + 112 + 8 * m) * 1024;
    fl = ld8_sc1_async(sws, flag_off(FL_KV, live ? T0 + (lane & 1) : 0, j));
    for (int kk = 2; kk < 6; ++kk) wdma(kk);
    wait_vmf<8>(fl);
    if (lane == 0) __hip_atomic_store(flag(FL_H0, T, j), tag, RLX_AGENT);
    STAMP_AT(2);
    if (!__all(fl[0] == taglo && fl[1] == tag32)) poll_flags(flag(FL_KV, live ? T0 + (lane & 1) : 0, j), tag);
    if (heavy) await(lds0 + L_CNT + 20, 4);
    kv_fetch();
    wdma(6); wdma(7);
    static_for<0, NR + 1>([&](auto kc) {
      constexpr int k = kc.value;
      constexpr int refills = k >= 2 ? 2 * (k - 1) : 0;
      if constexpr (k < NR) {
        wait_vm<(k < 6 ? 2 * (5 - (k < 2 ? 1 : k)) + 13 : 2 * (7 - k)) + refills + (k > HLD ? 1 : 0)>();
        wrd(k);
      }
      if constexpr (k == KVS) { wait_vm<4 + refills>(); arrive_nowait(lds0 + L_CNT); }
      if constexpr (k == HLD) fl = ld8_sc1_async(sws, flag_off(FL_H0, ftile, 4 * m + p4));
      if constexpr (k >= 1) { wgot(k - 1); wdma(k - 1 + RD); }
    });
    STAMP_AT(3);
    wait_vmf<2 * (NR + 1 - HLD)>(fl);
    if (!__all(fl[0] == taglo && fl[1] == tag32)) poll_flags(flag(FL_H0, ftile, 4 * m + p4), tag);
    STAMP_AT(4);
#pragma unroll
    for (int kk = 0; kk < AW; ++kk) {
      af[kk][0] = ld16_sc1_async(sws, lane * 16, a0 + kmap(kk) * 1024);
      af[kk][1] = ld16_sc1_async(sws, lane * 16, a0 + (AFT + kmap(kk)) * 1024);
    }
    __builtin_amdgcn_s_setprio(PRIO_G2);
    f32x16 c2[2][2] = {};
    auto mm2 = [&](int kk, const v4u (&a)[2], auto&& fill) {
      SBAR();
#pragma unroll
      for (int i = 0; i < 4; ++i) { c2[i >> 1][i & 1] = MFMA32(wq[kk][i & 1], a[i >> 1], c2[i >> 1][i & 1]); SBAR(); fill(i); SBAR(); }
    };
    static_assert(FLA <= 4, "one flag register");
    static_for<0, 32>([&](auto kc) {
      constexpr int kk = kc.value;
      wait_vm2<(kk < AW) ? 2 * (AW - 1 - kk) + g2_range(0, kk) : g2_range(kk - AW + 1, kk)>(af[kk][0], af[kk][1]);
      if constexpr (kk == H1_CK || kk == O0_CK || kk == O1_CK) {
        constexpr int kind = kk == H1_CK ? FL_H1 : kk == O0_CK ? FL_O0 : FL_O1;
        if constexpr (kk == O1_CK) STAMP_AT(5);
        wait_vmf<g2_range(kk - FLA, kk) - 1>(fl);
        if (!__all(fl[0] == taglo && fl[1] == tag32)) poll_flags(flag(kind, ftile, 4 * m + p4), tag);
        if constexpr (kk == O1_CK) STAMP_AT(6);
      }
      if constexpr (fl_load(kk)) fl = ld8_sc1_async(sws, flag_off(kk + FLA == H1_CK ? FL_H1 : kk + FLA == O0_CK ? FL_O0 : FL_O1, ftile, 4 * m + p4));
      if constexpr (kk == RES_LD) res_loads();
      if constexpr (kk == 20) res_sum();
      constexpr bool dw = kk >= 1 && kk - 1 + NR + RD < 36;
      if constexpr (dw) wgot(kk - 1 + NR);
      mm2(kk, af[kk], [&](int i) {
        if constexpr (dw) { if (i < 2) wdmah(kk - 1 + NR + RD, i); }
        if constexpr (kk + NR < 28) { if (i == 1) wrd(kk + NR); }
        if constexpr (kk >= 24) { if (i == 0) wrd(kk + 4); }
        if constexpr (kk + AW < 36) {
          if (i >= 2) af[kk + AW][i - 2] = ld16_sc1_async(sws, lane * 16, (kk + AW < 28 ? a0 + kmap(kk + AW) * 1024 : o0 + omap(kk + AW - 28) * 1024) + AFT * (i - 2) * 1024);
        }
        if constexpr (kk == 31) PIN(c2[i >> 1][i & 1]);
      });
    });
    for (int kk = 32; kk < 36; ++kk) wgot(kk);
    unsigned pa = lds0 + L_WG + m * PART + lane * 16;
    PIN(pa);
    auto put = [&](int qn, int qd) {
      const f32x16& c = c2[qn >> 1][qn & 1];
      sts128(pa + (qn * 4 + qd) * 66 * 16, __builtin_bit_cast(v4u, (f32x4){c[4 * qd], c[4 * qd + 1], c[4 * qd + 2], c[4 * qd + 3]}));
    };
    static_for<0, 4>([&](auto qc) {
      constexpr int qn = qc.value;
      if constexpr ((qn & 1) == 0) { wait_vm2<0>(af[32][qn >> 1], af[33][qn >> 1]); PIN(af[34][qn >> 1]); PIN(af[35][qn >> 1]); }
      SBAR();
#pragma unroll
      for (int i = 0; i < 4; ++i) {
        c2[qn >> 1][qn & 1] = MFMA32(wq[32 + i][qn & 1], af[32 + i][qn >> 1], c2[qn >> 1][qn & 1]);
        SBAR();
        if (qn > 0 && i >= 2) { put(qn - 1, 2 * i - 4); put(qn - 1, 2 * i - 3); }
        SBAR();
      }
    });
#pragma unroll
    for (int qd = 0; qd < 4; ++qd) put(3, qd);
    STAMP_AT(7);
  } else {
    __builtin_amdgcn_s_setprio(1);
    lane_setup();
    v4u xr[2];
    xr[0] = ld16_async(sx, lane * 16, (128 * r + 8 * j + 2 * m) * 1024); xr[1] = ld16_async(sx, lane * 16, (128 * r + 8 * j + 2 * m + 1) * 1024);
    unsigned warm;
    {
      warm = ld2_async(make_srd((const void*)(pc & ~127ull), CODEWARM), lane < 2 ? lane * 128 : 0x40000000u, (idx * 4 + m) * 256);
    }
    __builtin_amdgcn_s_barrier();
    v2u ep = ld8_sc1_async(sws, m == 0 ? WS_EPOCH : 0x40000000u);
    float sval;
    const int wi = blockIdx.x * 4 + m;
    const u64 wbase = wi < 448 ? (u64)w1 + wi * 4096 : wi < 896 ? (u64)w2 + (wi - 448) * 4096 : (u64)wo + (wi - 896) * 4096;
    auto make_stats = [&]() {
      wait_vm2<1>(xr[0], xr[1]);
      PIN(warm);
      float piv[2], a[4];
#pragma unroll
      for (int u = 0; u < 2; ++u) {
        piv[u] = rdl(bflo(xr[u][0]), 0);
        float s1 = 0.f, s2 = 0.f;
#pragma unroll
        for (int i = 0; i < 8; ++i) { const float d = ((i & 1) ? bfhi(xr[u][i >> 1]) : bflo(xr[u][i >> 1])) - piv[u]; s1 += d; s2 = __builtin_fmaf(d, d, s2); }
        a[2 * u] = s1; a[2 * u + 1] = s2;
      }
#pragma unroll
      for (int i = 0; i < 4; ++i) a[i] += dpp<0xB1>(a[i]);
#pragma unroll
      for (int i = 0; i < 4; ++i) a[i] += dpp<0x4E>(a[i]);
      float t = p4 == 0 ? a[0] : p4 == 1 ? a[1] : p4 == 2 ? a[2] : a[3];
      t += dpp<0x114>(t); t += dpp<0x118>(t);
      const auto s16 = __builtin_amdgcn_permlane16_swap(asu(t), asu(t), false, false);
      t = asf(s16[0]) + asf(s16[1]);
      const auto s32 = __builtin_amdgcn_permlane32_swap(asu(t), asu(t), false, false);
      t = (asf(s32[0]) + asf(s32[1])) * (1.f / DM);
      const float e1 = dpp<0xA0>(t);
      sval = (p4 & 1) ? __builtin_fmaxf(__builtin_fmaf(-e1, e1, t), 0.f) + 1e-5f : t + (p4 ? piv[1] : piv[0]);
      PIN(sval);
    };
    const unsigned acnt = lds0 + L_CNT;
    const unsigned vpos = 2048 + (g & 1) * 512 + h * 256 + (lane & 15) * 16;
    v4u qf[2], am0, am1;
    u64 um[16];
    auto masks = [&]() {
      const int dq = r32 - 4 * h;
#pragma unroll
      for (int u = 0; u < 16; ++u) { um[u] = __builtin_amdgcn_ballot_w64((u & 3) + 8 * (u >> 2) > dq); pin_s(um[u]); }
#pragma unroll
      for (int d = 0; d < 4; ++d) {
        am0[d] = __builtin_amdgcn_inverse_ballot_w64(um[2 * d]) ? 0xffffffffu : __builtin_amdgcn_inverse_ballot_w64(um[2 * d + 1]) ? 0xffff0000u : 0u;
        am1[d] = __builtin_amdgcn_inverse_ballot_w64(um[8 + 2 * d]) ? 0xffffffffu : __builtin_amdgcn_inverse_ballot_w64(um[9 + 2 * d]) ? 0xffff0000u : 0u;
      }
      PIN(am0); PIN(am1);
    };
    {
      v4u xf[32];
      f32x16 acc[3] = {};
      f32x4 cs[3] = {};
      v4u wf[4][3];
      unsigned wb[4];
#pragma unroll
      for (int p = 0; p < 4; ++p) { wb[p] = qbuf(0) + ((p + m) & 3) * 1024 + (8 * h + q4) * 64 + (g & 1) * 32 + p4 * 8; PIN(wb[p]); }
      auto rd = [&](int ks, int t) {
        if (t == 0) xf[ks] = lds128(xbuf(ks >> 2) + xo[ks & 3]);
        wf[ks & 3][t] = ldtr2(wb[ks & 3] + ((ks >> 2) % 3) * 12288 + t * 4096, 256);
      };
      auto mm = [&](int ks, int t) {
        acc[t] = t == 2 ? MFMA32(xf[ks], wf[ks & 3][t], acc[t]) : MFMA32(wf[ks & 3][t], xf[ks], acc[t]);
        if ((ks & 3) == 0) { cs[t] = MFMA16(sel, wf[ks & 3][t], cs[t]); PIN(cs[t]); }
        SBAR();
      };
      make_stats();
      bar();
      STAMP_AT(10);
#pragma unroll
      for (int t = 0; t < 6; ++t) rd(t / 3, t % 3);
      SBAR();
      static_for<0, 8>([&](auto sc) {
        constexpr int s = sc.value;
#pragma unroll
        for (int p = 0; p < 2; ++p) {
#pragma unroll
          for (int t = 0; t < 3; ++t) { mm(4 * s + p, t); rd(4 * s + p + 2, t); SBAR(); }
          if (s == 7 && p == 0) { cswr(4, cs[0]); cswr(5, cs[1]); cswr(6, cs[2]); SBAR(); }
        }
        if constexpr (s == 0) {
          wait_vmf<0>(ep);
          if (m == 0 && lane < 10)
            *(LAS unsigned*)(size_t)(lds0 + L_CNT + lane * 4) = lane == 8 ? ep[0] * 0x9E3779B1u + 0x7F4A7C15u : lane == 9 ? ep[1] * 0x85EBCA77u + 0x165667B1u : 0u;
        }
        if constexpr (s == 1) {
          get_tag();
          if ((lane >> 2) == 3) {
            const v2u gr = {asu(sval), tag32};
            __builtin_amdgcn_raw_buffer_store_b64(gr, rws, WS_STATS + (128 * r + 8 * j + 2 * m) * 16 + p4 * 8, 0, 16);
          }
        }
        if constexpr (s == MIDWARM) {
          warm = ld2_async(make_srd((const void*)wbase, 4096), lane * 128, 0);
        }
        if constexpr (s == 5) stat_load();
        if constexpr (s == 6) {
          wait_vm1<0>(st);
          PIN(warm);
          row_stats();
        }
        mm(4 * s + 2, 0); mm(4 * s + 2, 1);
        bar();
        if constexpr (s < 7) {
          STAMP_AT(11 + s);
          mm(4 * s + 2, 2);
          rd(4 * s + 4, 0); rd(4 * s + 4, 1); rd(4 * s + 4, 2); SBAR();
#pragma unroll
          for (int t = 0; t < 3; ++t) { mm(4 * s + 3, t); rd(4 * s + 5, t); SBAR(); }
        }
      });
      STAMP_AT(1);
      __builtin_amdgcn_s_setprio(3);
      const f32x4 c5 = csrd(5), c6 = csrd(6), c4 = csrd(4);
      SBAR();
      mm(30, 2); mm(31, 1);
      make_xaug();
      SBAR();
      mm(31, 2); mm(31, 0);
      if (h == 0) *(LAS float*)(size_t)(lds0 + L_RS + (m * 32 + r32) * 4) = rstd;
      acc[1] = MFMA32(waug(c5, 0u), xaug, acc[1]);
      acc[2] = MFMA32(xaug, waug(c6, 0u), acc[2]);
      acc[0] = MFMA32(waug(c4, 0u), xaug, acc[0]);
      SBAR();
      asm volatile("" ::: "memory");
      f32x4 rs[4];
#pragma unroll
      for (int qd = 0; qd < 4; ++qd) rs[qd] = __builtin_bit_cast(f32x4, lds128(lds0 + L_RS + (m * 32 + 8 * qd + 4 * h) * 4));
      const unsigned kvo = WS_KV + (j * 64 + T) * 4096;
#pragma unroll
      for (int e = 0; e < 16; ++e) acc[1][e] = MUL1(acc[1][e], rstd);
#pragma unroll
      for (int e = 0; e < 2; ++e) {
        const v4u f = pack8(acc[1], e);
        st16_sc1(f, sws, lane * 16, kvo + e * 1024);
        sts128(lds0 + L_KV + (8 + m) * KVT + e * 1024 + lane * 16, f);
      }
#pragma unroll
      for (int qd = 0; qd < 4; ++qd)
#pragma unroll
        for (int e = 0; e < 4; ++e) acc[2][4 * qd + e] = MUL1(acc[2][4 * qd + e], rs[qd][e]);
#pragma unroll
      for (int s = 0; s < 2; ++s) {
        const v4u f = pack8(acc[2], s);
        st16_sc1(f, sws, vpos, kvo + s * 1024);
        sts128(lds0 + L_KV + (8 + m) * KVT + s * VB + vpos, f);
      }
      SBAR();
      const float qs = rstd * (0.25f * 1.4426950408889634f);
#pragma unroll
      for (int e = 0; e < 16; ++e) acc[0][e] = MUL1(acc[0][e], qs);
      qf[0] = pack8(acc[0], 0); qf[1] = pack8(acc[0], 1);
      PIN(qf[0]); PIN(qf[1]);
      arrive_nowait(acnt);
      STAMP_AT(2);
      if (heavy) {
        hsetup(1);
        await(acnt + 16, 4);
        __builtin_amdgcn_s_setprio(1);
        STAMP_AT(3);
        hmm(std::integral_constant<int, 1>{}, xf, [&](auto kc) {
          constexpr int ks = kc.value;
          if constexpr (ks == KVPUB) {
            wait_vm<0>();
            if (lane == 0) __hip_atomic_store(flag(FL_KV, T, j), tag, RLX_AGENT);
          }
          if constexpr (ks == KVPUB + 2) bias_load(1);
          if constexpr (ks == 31) { wait_vm<0>(); PIN(bias0); PIN(bias1); }
        });
        STAMP_AT(18);
        __builtin_amdgcn_s_setprio(3);
        hgelu(std::integral_constant<int, 1>{}, [&]() {});
        STAMP_AT(4);
      } else {
        wait_vm<0>();
        if (lane == 0) __hip_atomic_store(flag(FL_KV, T, j), tag, RLX_AGENT);
      }
    }
    masks();
    STAMP_AT(19);
    const unsigned kb = lds0 + L_KV + m * KVT + lane * 16;
    unsigned vb[2];
#pragma unroll
    for (int e = 0; e < 2; ++e) { vb[e] = lds0 + L_KV + m * KVT + 2048 + ((g & 1) ? 1024 : e * 512 + h * 256 + (lane & 15) * 16); PIN(vb[e]); }
    bool pend = true;
    if (__builtin_amdgcn_readfirstlane(peek(acnt)) < 8) {
      wait_vm<0>();
      if (lane == 0) __hip_atomic_store(flag(FL_H1, T, j), tag, RLX_AGENT);
      pend = false;
      await(acnt, 8);
    }
    asm volatile("" ::: "memory");
    __builtin_amdgcn_s_setprio(PRIO_ATT);
    STAMP_AT(5);
    const f32x16 zero = {};
    auto o_store = [&](f32x16& o, int e) {
      const float rl = __builtin_amdgcn_rcpf(o[8]);
#pragma unroll
      for (int u = 0; u < 8; ++u) o[u] = MUL1(o[u], rl);
      st16_sc1(pack8(o, 0), sws, lane * 16, WS_AF + (T * AFT + 112 + 2 * j + e) * 1024);
    };
    int redo = 0;
    {
      f32x16 o[2], sa, sb, su;
      v4u kn, ku, va, vc, vn0, vn1, vu0, vu1, p0, p1, pu;
      auto kaddr = [&](int n) { return kb + ((n & 7) + 1) * KVT + (n >> 3) * 1024; };
      auto vaddr = [&](int n) { return vb[n >> 3] + ((n & 7) + 1) * KVT; };
      kn = lds128(kaddr(0)); vn0 = lds128(vaddr(0)); vn1 = lds128(vaddr(0) + VB);
      sa = MFMA32(kn, qf[0], zero);
      kn = lds128(kaddr(1));
      SBAR();
      auto step = [&](auto nc, f32x16& sc, f32x16& sn) {
        constexpr int n = nc.value, e = n >> 3, i = n & 7;
        if constexpr (n < 15) sn = MFMA32(kn, qf[(n + 1) >> 3], zero);
        if constexpr (n < 14) kn = lds128(kaddr(n + 2));
        if constexpr (i == 5) ku = lds128(kb + e * 1024);
        v4u v0 = vn0, v1 = vn1;
        if constexpr (n < 15) { vn0 = lds128(vaddr(n + 1)); vn1 = lds128(vaddr(n + 1) + VB); }
        if constexpr (i == 6) { vu0 = lds128(vb[e]); vu1 = lds128(vb[e] + VB); }
        SBAR();
        if constexpr (i == 7) {
#pragma unroll
          for (int u = 0; u < 16; ++u) sc[u] = __builtin_amdgcn_inverse_ballot_w64(um[u]) ? su[u] : sc[u];
          PIN(sc); SBAR();
        }
#pragma unroll
        for (int u = 0; u < 6; ++u) sc[u] = EXP2(sc[u]);
        PIN(sc); SBAR();
        if constexpr (n == 8) { o[0] = MFMA32(vc, p1, o[0]); SBAR(); }
        else if constexpr (n > 0) { o[e] = MFMA32(vc, p1, o[e]); SBAR(); }
        sc[6] = EXP2(sc[6]); sc[7] = EXP2(sc[7]);
        p0 = pack8(sc, 0);
        sc[8] = EXP2(sc[8]); sc[9] = EXP2(sc[9]);
        if constexpr (i == 7) { va = p0 & am0; p0 &= ~am0; PIN(va); }
        PIN(sc); PIN(p0); SBAR();
        if constexpr (n == 8) { o[0] = MFMA32(vu1, pu, o[0]); SBAR(); }
        o[e] = MFMA32(v0, p0, i == 0 ? zero : o[e]);
        SBAR();
        if constexpr (i == 6) { su = MFMA32(ku, qf[e], zero); SBAR(); }
#pragma unroll
        for (int u = 10; u < 13; ++u) sc[u] = EXP2(sc[u]);
        if constexpr (i == 7) { PIN(sc); SBAR(); o[e] = MFMA32(vu0, va, o[e]); SBAR(); }
#pragma unroll
        for (int u = 13; u < 16; ++u) sc[u] = EXP2(sc[u]);
        p1 = pack8(sc, 1); vc = v1;
        if constexpr (i == 7) { pu = p1 & am1; p1 &= ~am1; PIN(pu); }
        PIN(p1); SBAR();
        if constexpr (n == 8) {
          if (__all(o[0][8] >= 0x1p-30f && o[0][8] <= 0x1p30f)) o_store(o[0], 0); else redo = 1;
          SBAR();
        }
        if constexpr (n == 10) pend = !redo;
        if constexpr (i == 3 || i == 5 || i == 7) {
          if (pend && (i == 7 || (__builtin_amdgcn_s_getreg(7 | (3 << 11)) & 15) == 0)) {
            wait_vm<0>();
            if (lane == 0) __hip_atomic_store(flag(FL_H1 + e, T, j), tag, RLX_AGENT);
            pend = false;
          }
        }
      };
      static_for<0, 8>([&](auto hc) {
        step(std::integral_constant<int, 2 * hc.value>{}, sa, sb);
        step(std::integral_constant<int, 2 * hc.value + 1>{}, sb, sa);
      });
      o[1] = MFMA32(vc, p1, o[1]);
      o[1] = MFMA32(vu1, pu, o[1]);
      if (__all(o[1][8] >= 0x1p-30f && o[1][8] <= 0x1p30f)) o_store(o[1], 1); else redo |= 2;
    }
    if (redo) {
#pragma unroll 1
      for (int e = 0; e < 2; ++e) {
        if (!((redo >> e) & 1)) continue;
        const v4u q = e ? qf[1] : qf[0];
        const unsigned vbe = e ? vb[1] : vb[0];
        auto merged = [&](const f32x16& c) {
          f32x16 sl = MFMA32(lds128(kb + 8 * KVT + e * 1024), q, c);
          const f32x16 su = MFMA32(lds128(kb + e * 1024), q, c);
          static_for<0, 16>([&](auto u) { sl[u.value] = upper<u.value>() ? su[u.value] : sl[u.value]; });
          return sl;
        };
        float mx = -INFINITY;
#pragma unroll 1
        for (int i = 0; i < 8; ++i) {
          const f32x16 sc = i ? MFMA32(lds128(kb + i * KVT + e * 1024), q, zero) : merged(zero);
#pragma unroll
          for (int u = 0; u < 16; u += 2) mx = max3(mx, sc[u], sc[u + 1]);
        }
        const auto sw = __builtin_amdgcn_permlane32_swap(asu(mx), asu(mx), false, false);
        mx = __builtin_fmaxf(asf(sw[0]), asf(sw[1]));
        f32x16 negm, o;
#pragma unroll
        for (int u = 0; u < 16; ++u) negm[u] = -mx;
        {
          f32x16 sc = merged(negm);
#pragma unroll
          for (int u = 0; u < 16; ++u) sc[u] = EXP2(sc[u]);
          const v4u p0 = pack8(sc, 0), p1 = pack8(sc, 1);
          o = MFMA32(lds128(vbe), p0 & am0, zero);
          o = MFMA32(lds128(vbe + VB), p1 & am1, o);
          o = MFMA32(lds128(vbe + 8 * KVT), p0 & ~am0, o);
          o = MFMA32(lds128(vbe + 8 * KVT + VB), p1 & ~am1, o);
        }
#pragma unroll 1
        for (int i = 1; i < 8; ++i) {
          f32x16 sc = MFMA32(lds128(kb + i * KVT + e * 1024), q, negm);
#pragma unroll
          for (int u = 0; u < 16; ++u) sc[u] = EXP2(sc[u]);
          o = MFMA32(lds128(vbe + i * KVT), pack8(sc, 0), o);
          o = MFMA32(lds128(vbe + i * KVT + VB), pack8(sc, 1), o);
        }
        o_store(o, e);
      }
      wait_vm<0>();
      if (lane == 0) __hip_atomic_store(flag(FL_O0, T, j), tag, RLX_AGENT);
    }
    STAMP_AT(6);
    wait_vm<0>();
    if (lane == 0) __hip_atomic_store(flag(FL_O1, T, j), tag, RLX_AGENT);
    STAMP_AT(7);
    res_loads();
    wait_vm<0>();
    res_sum();
    if (blockIdx.x == 0 && wave == 7) {
#pragma unroll 1
      for (int i = 0; i < 4; ++i) poll_flags(flag(FL_KV, 4 * (4 * i + g), lane & 15), tag);
      if (lane == 0) __hip_atomic_store((GAS u64*)(ws + WS_EPOCH), tag, RLX_AGENT);
    }
  }
  {
    int t2 = threadIdx.x; PIN(t2);
    const int row = t2 >> 3, ch = t2 & 7;
    unsigned a = lds0 + L_WG + (((row >> 5) * 8 + ch) * 66 + (row & 31)) * 16, oo = (64 * rt + row) * 1024 + (64 * ct + ch * 8) * 2;
    PIN(a); PIN(oo);
    bar();
    f32x4 pl[4], ph[4];
#pragma unroll
    for (int w = 0; w < 4; ++w) { pl[w] = __builtin_bit_cast(f32x4, lds128(a + w * PART)); ph[w] = __builtin_bit_cast(f32x4, lds128(a + w * PART + 512)); }
    SBAR();
#pragma unroll
    for (int w = 0; w < 4; ++w) { lo += pl[w]; hi += ph[w]; }
    const v4u o = {pk2(lo[0], lo[1]), pk2(lo[2], lo[3]), pk2(hi[0], hi[1]), pk2(hi[2], hi[3])};
    __builtin_amdgcn_raw_buffer_store_b128(o, __builtin_amdgcn_make_buffer_rsrc(out, 0, SEQ * DM * 2, 0x00020000), oo, 0, 16);
  }
  STAMP_AT(8);
}

extern "C" void kernel_launch(void* const* d_in, const int* in_sizes, int n_in, void* d_out, int out_size, void* d_ws, size_t ws_size,
                              hipStream_t stream) {
  (void)in_sizes; (void)n_in; (void)out_size; (void)ws_size;
  (void)hipFuncSetAttribute((const void*)fused_block, hipFuncAttributeMaxDynamicSharedMemorySize, LDS_BYTES);
  fused_block<<<256, 512, LDS_BYTES, stream>>>(d_in[0], d_in[1], d_in[2], d_in[4], d_in[5], d_in[6], d_in[7], d_out, (char*)d_ws);
}
```

```cpp
#include <hip/hip_runtime.h>
#include <hip/hip_bf16.h>
#include <stdint.h>
#include <type_traits>

typedef unsigned v4u __attribute__((ext_vector_type(4)));
typedef unsigned v2u __attribute__((ext_vector_type(2)));
typedef float f32x2 __attribute__((ext_vector_type(2)));
typedef float f32x4 __attribute__((ext_vector_type(4)));
typedef float f32x16 __attribute__((ext_vector_type(16)));
typedef short bf16x8 __attribute__((ext_vector_type(8)));
typedef short s16x4 __attribute__((ext_vector_type(4)));
typedef __bf16 bf16x2_t __attribute__((ext_vector_type(2)));
typedef unsigned long long u64;
#define GAS __attribute__((address_space(1)))
#define LAS __attribute__((address_space(3)))
#define DI __device__ __forceinline__
#define RLX_AGENT __ATOMIC_RELAXED, __HIP_MEMORY_SCOPE_AGENT

constexpr int SEQ = 2048, DM = 512, FF = 1792;
constexpr unsigned WS_EPOCH = 0;
constexpr unsigned WS_STATS = 32768;
constexpr unsigned WS_FLAG = 65536;
constexpr unsigned WS_KV = WS_FLAG + 5 * 1024 * 128;
constexpr int AFT = 145;
constexpr unsigned WS_AF = WS_KV + 16 * 64 * 4096;
constexpr unsigned WS_END = WS_AF + 64 * AFT * 1024;
enum { FL_KV = 0, FL_H0 = 1, FL_H1 = 2, FL_O0 = 3, FL_O1 = 4 };
constexpr int L_WA = 0;
constexpr int L_WG = 86016;
constexpr int VB = 1040;
constexpr int KVT = 2048 + 2 * VB;
constexpr int L_KV = 32768;
constexpr int RD = 8, NR = 12;
constexpr int PART = 16 * 66 * 16;
constexpr int L_CS = L_WG + 4 * PART;
constexpr int L_RS = L_CS + 3584;
constexpr int L_CNT = L_RS + 512;
constexpr int LDS_BYTES = L_CNT + 64;
constexpr int xbuf(int s) { return ((s + 1) % 3) * 16384; }
constexpr int qbuf(int s) { return 49152 + (s % 3) * 12288; }
constexpr int w1buf(int s) { return L_WG + (s % 3) * PART + 8192; }
static_assert(L_KV + 8 * KVT >= 65536 && xbuf(7) == 32768 && qbuf(2) + 12288 <= L_WG && L_KV + 12 * KVT <= L_WG && PART >= RD * 2048 && LDS_BYTES <= 163840, "lds map");
constexpr int AW = 6;
constexpr unsigned SPIN_CAP = 1u << 23;

#define MFMA32(a, b, c) __builtin_amdgcn_mfma_f32_32x32x16_bf16(__builtin_bit_cast(bf16x8, a), __builtin_bit_cast(bf16x8, b), c, 0, 0, 0)
#define MFMA16(a, b, c) __builtin_amdgcn_mfma_f32_16x16x32_bf16(__builtin_bit_cast(bf16x8, a), __builtin_bit_cast(bf16x8, b), c, 0, 0, 0)
#define SBAR() __builtin_amdgcn_sched_barrier(0)

DI float asf(unsigned u) { return __builtin_bit_cast(float, u); }
DI unsigned asu(float f) { return __builtin_bit_cast(unsigned, f); }
DI unsigned pk2(float lo, float hi) { return __builtin_bit_cast(unsigned, __builtin_convertvector((f32x2){lo, hi}, bf16x2_t)); }
DI float bflo(unsigned u) { return __builtin_bit_cast(float, u << 16); }
DI float bfhi(unsigned u) { return __builtin_bit_cast(float, u & 0xffff0000u); }
DI v4u make_srd(const void* p, unsigned bytes) { const u64 a = (u64)p; return (v4u){(unsigned)a, (unsigned)(a >> 32) & 0xffffu, bytes, 0x00020000u}; }
DI void dma16(v4u srd, unsigned voff, unsigned soff, unsigned lds) {
  asm volatile("s_mov_b32 m0, %3\n\ts_nop 0\n\tbuffer_load_dwordx4 %0, %1, %2 offen lds" :: "v"(voff), "s"(srd), "s"(soff), "s"(lds) : "memory");
}
DI void dma16_sc1(v4u srd, unsigned voff, unsigned soff, unsigned lds) {
  asm volatile("s_mov_b32 m0, %3\n\ts_nop 0\n\tbuffer_load_dwordx4 %0, %1, %2 offen sc1 lds" :: "v"(voff), "s"(srd), "s"(soff), "s"(lds) : "memory");
}
DI void dma16_a(v4u srd, unsigned voff, unsigned soff, unsigned lds) { dma16(srd, voff, soff, lds); }
DI void dma16_b(v4u srd, unsigned voff1, unsigned soff) {
  asm volatile("buffer_load_dwordx4 %0, %1, %2 offen offset:1024 lds" :: "v"(voff1), "s"(srd), "s"(soff) : "memory");
}
DI v4u ld16_sc1_async(v4u srd, unsigned voff, unsigned soff) {
  v4u r; asm volatile("buffer_load_dwordx4 %0, %1, %2, %3 offen sc1" : "=v"(r) : "v"(voff), "s"(srd), "s"(soff) : "memory"); return r;
}
DI v4u ld16_async(v4u srd, unsigned voff, unsigned soff) {
  v4u r; asm volatile("buffer_load_dwordx4 %0, %1, %2, %3 offen" : "=v"(r) : "v"(voff), "s"(srd), "s"(soff) : "memory"); return r;
}
DI unsigned ld2_async(v4u srd, unsigned voff, unsigned soff) {
  unsigned r; asm volatile("buffer_load_ushort %0, %1, %2, %3 offen" : "=v"(r) : "v"(voff), "s"(srd), "s"(soff) : "memory"); return r;
}
DI void st16_sc1(v4u v, v4u srd, unsigned voff, unsigned soff) {
  asm volatile("buffer_store_dwordx4 %0, %1, %2, %3 offen sc1\n\ts_nop 1" :: "v"(v), "v"(voff), "s"(srd), "s"(soff) : "memory");
}
template <int N> DI void wait_vm1(v4u& a) { asm volatile("s_waitcnt vmcnt(%1)" : "+v"(a) : "i"(N) : "memory"); }
template <int N> DI void wait_vm2(v4u& a, v4u& b) { asm volatile("s_waitcnt vmcnt(%2)" : "+v"(a), "+v"(b) : "i"(N) : "memory"); }
DI void pin_s(u64& v) { asm volatile("" : "+s"(v)); }
template <class V> DI void pin(V& v) { asm volatile("" : "+v"(v)); }
template <int N> DI void wait_vm() { asm volatile("s_waitcnt vmcnt(%0)" :: "i"(N) : "memory"); }
DI void bar() { asm volatile("s_waitcnt lgkmcnt(0)\n\ts_barrier" ::: "memory"); }
DI v4u lds128(unsigned a) { return *(const LAS v4u*)(size_t)a; }
DI void sts128(unsigned a, v4u v) { *(LAS v4u*)(size_t)a = v; }
DI v2u ldtr(unsigned a) { return __builtin_bit_cast(v2u, __builtin_amdgcn_ds_read_tr16_b64_v4i16((LAS s16x4*)(size_t)a)); }
DI v4u ldtr2(unsigned a, int second) { const v2u lo = ldtr(a), hi = ldtr(a + second); return (v4u){lo.x, lo.y, hi.x, hi.y}; }
template <int CTRL> DI float dpp(float v) { return __builtin_bit_cast(float, __builtin_amdgcn_update_dpp(0, __builtin_bit_cast(int, v), CTRL, 0xf, 0xf, true)); }
DI float rdl(float v, int l) { return __builtin_bit_cast(float, __builtin_amdgcn_readlane(__builtin_bit_cast(int, v), l)); }
DI v4u pack8(const f32x16& a, int s) { return (v4u){pk2(a[8 * s], a[8 * s + 1]), pk2(a[8 * s + 2], a[8 * s + 3]), pk2(a[8 * s + 4], a[8 * s + 5]), pk2(a[8 * s + 6], a[8 * s + 7])}; }
DI void poll_flags(const GAS u64* p, u64 tag) {
#pragma unroll 1
  for (unsigned spin = 0; spin < SPIN_CAP; ++spin) {
    const u64 v = __hip_atomic_load(p, RLX_AGENT);
    if (__all(v == tag)) break;
    __builtin_amdgcn_s_sleep(1);
  }
  asm volatile("" ::: "memory");
}
constexpr u64 upper_mask(int reg) {
  u64 m = 0;
  for (int l = 0; l < 64; ++l) { const int row = (reg & 3) + 8 * (reg >> 2) + 4 * (l >> 5); if (row > (l & 31)) m |= 1ull << l; }
  return m;
}
template <int REG> DI bool upper() { return __builtin_amdgcn_inverse_ballot_w64(upper_mask(REG)); }
template <int I, int N, class Fn> DI void static_for(Fn&& f) { if constexpr (I < N) { f(std::integral_constant<int, I>{}); static_for<I + 1, N>(f); } }
DI float max3(float a, float b, float c) { return __builtin_fmaxf(__builtin_fmaxf(a, b), c); }

constexpr int FLA = 4, H1_CK = 16 - AW, O0_CK = 28 - AW, O1_CK = 32 - AW, RES_LD = 12;
constexpr bool fl_load(int t) { return t == H1_CK - FLA || t == O0_CK - FLA || t == O1_CK - FLA; }
constexpr int g2_issued(int t) { return ((t >= 1 && t - 1 + NR + RD < 36) ? 2 : 0) + ((t + AW < 36) ? 2 : 0) + fl_load(t) + (t == RES_LD ? 2 : 0); }
constexpr int g2_range(int a, int b) { int n = 0; for (int t = a; t < b; ++t) n += g2_issued(t); return n; }
constexpr unsigned CODE_BYTES = 32000;
constexpr int KVPUB = 2, KVS = 1, HLD = 9;
#define EXP2(v) __builtin_amdgcn_exp2f(v)
#define PIN(v) pin(v)
extern "C" __device__ float mul_unpacked(float, float) __asm("llvm.amdgcn.fmul.legacy");
#define MUL1(a, b) mul_unpacked(a, b)
#define FMA1(a, b, c) __builtin_amdgcn_div_fmasf(a, b, c, false)
DI v2u ld8_sc1_async(v4u srd, unsigned voff) {
  v2u r; asm volatile("buffer_load_dwordx2 %0, %1, %2, 0 offen sc1" : "=v"(r) : "v"(voff), "s"(srd) : "memory"); return r;
}
template <int N> DI void wait_vmf(v2u& a) { asm volatile("s_waitcnt vmcnt(%1)" : "+v"(a) : "i"(N) : "memory"); }
DI void arrive_nowait(unsigned cnt) {
  asm volatile("s_mov_b64 exec, 1\n\tds_add_u32 %0, %1\n\ts_mov_b64 exec, -1" :: "v"(cnt), "v"(1u) : "memory");
}
DI unsigned peek(unsigned cnt) { return __hip_atomic_load((LAS unsigned*)(size_t)cnt, __ATOMIC_RELAXED, __HIP_MEMORY_SCOPE_WORKGROUP); }
DI void await(unsigned cnt, unsigned n) {
#pragma unroll 1
  for (unsigned spin = 0; spin < SPIN_CAP * 32; ++spin) {
    if (__builtin_amdgcn_readfirstlane(peek(cnt)) >= n) break;
    __builtin_amdgcn_s_sleep(1);
  }
  asm volatile("" ::: "memory");
}

__global__ __launch_bounds__(512, 2) void fused_block(const void* __restrict__ x, const void* __restrict__ wqkv, const void* __restrict__ wo,
                                                      const void* __restrict__ w1, const void* __restrict__ b1, const void* __restrict__ w2,
                                                      const void* __restrict__ b2, void* __restrict__ out, char* __restrict__ ws) {
  constexpr unsigned lds0 = 0;
  u64 pc;
  asm volatile("s_getpc_b64 %0" : "=s"(pc));
  const int wave = __builtin_amdgcn_readfirstlane(threadIdx.x >> 6);
  const int m = wave & 3;
  const bool isG = wave < 4;
  const int xcd = blockIdx.x & 7, idx = blockIdx.x >> 3;
  const int r = (xcd >> 2) * 8 + (idx >> 2), j = (xcd & 3) * 4 + (idx & 3);
  const int rt = (xcd >> 1) * 8 + (idx >> 2), ct = (xcd & 1) * 4 + (idx & 3);
  int lane = threadIdx.x & 63, r32, h, g, q4, p4;
  asm volatile("" :: "s"(x), "s"(wqkv), "s"(wo), "s"(w1), "s"(b1), "s"(w2), "s"(b2), "s"(out), "s"(ws));

  const v4u sx = make_srd(x, SEQ * DM * 2), sws = make_srd(ws, WS_END);
  const __amdgpu_buffer_rsrc_t rws = __builtin_amdgcn_make_buffer_rsrc(ws, 0, WS_END, 0x00020000);
  const unsigned one2 = 0x3f803f80u;
  const int T = 4 * r + m;
  auto flag_off = [&](int kind, int tile, int jj) { return WS_FLAG + ((kind * 64 + tile) * 16 + jj) * 128; };
  auto flag = [&](int kind, int tile, int jj) { return (GAS u64*)(ws + flag_off(kind, tile, jj)); };
  u64 tag;
  unsigned taglo, tag32;
  auto get_tag = [&]() {
    const v2u tg = *(const LAS v2u*)(size_t)(lds0 + L_CNT + 32);
    taglo = __builtin_amdgcn_readfirstlane(tg[0]); tag32 = __builtin_amdgcn_readfirstlane(tg[1]);
    tag = ((u64)tag32 << 32) | taglo;
  };
  v4u xres, bres, xaug, st;
  float mean, var, rstd, sd;
  v4u sel;
  unsigned xo[4], swz;
  auto lane_setup = [&]() {
    lane = threadIdx.x & 63; PIN(lane);
    r32 = lane & 31; h = lane >> 5; g = lane >> 4; q4 = (lane >> 2) & 3; p4 = lane & 3;
    const unsigned selv = ((lane & 15) == (g & 1)) ? one2 : 0u;
    sel = (v4u){selv, selv, selv, selv};
#pragma unroll
    for (int p = 0; p < 4; ++p) xo[p] = (32 * m + r32) * 128 + (((2 * ((p + m) & 3) + h) ^ ((r32 >> 1) & 7)) * 16);
    swz = ((((lane >> 2) & 1) ^ (g & 1)) * 64) + p4 * 16;
  };

  auto stat_load = [&]() { st = ld16_sc1_async(sws, WS_STATS + (128 * r + 32 * m + r32) * 16, 0); };
  auto res_loads = [&]() {
    int t2 = threadIdx.x; PIN(t2);
    const int row = 64 * rt + (t2 >> 3), col = 64 * ct + (t2 & 7) * 8;
    xres = ld16_async(sx, row * 1024 + col * 2, 0);
    bres = ld16_async(make_srd(b2, DM * 2), col * 2, 0);
  };
  f32x4 lo, hi;
  auto res_sum = [&]() {
    PIN(xres); PIN(bres);
    lo = (f32x4){bflo(xres[0]) + bflo(bres[0]), bfhi(xres[0]) + bfhi(bres[0]), bflo(xres[1]) + bflo(bres[1]), bfhi(xres[1]) + bfhi(bres[1])};
    hi = (f32x4){bflo(xres[2]) + bflo(bres[2]), bfhi(xres[2]) + bfhi(bres[2]), bflo(xres[3]) + bflo(bres[3]), bfhi(xres[3]) + bfhi(bres[3])};
    PIN(lo); PIN(hi);
  };
  auto row_stats = [&]() {
    if (!__all(st[1] == tag32 && st[3] == tag32)) {
#pragma unroll 1
      for (unsigned spin = 0; spin < SPIN_CAP; ++spin) {
        st = ld16_sc1_async(sws, WS_STATS + (128 * r + 32 * m + r32) * 16, 0);
        wait_vm1<0>(st);
        if (__all(st[1] == tag32 && st[3] == tag32)) break;
      }
    }
    mean = asf(st[0]); var = asf(st[2]);
    PIN(mean); PIN(var);
  };
  auto make_xaug = [&]() {
    PIN(mean); PIN(var);
    rstd = __builtin_amdgcn_rsqf(var); sd = var * rstd;
    const unsigned uh = pk2(-mean, sd);
    const unsigned ul = pk2(-mean - bflo(uh), FMA1(bfhi(uh), -1.f, sd));
    xaug = (v4u){(uh & 0xffffu) | (uh << 16), (ul & 0xffffu) | (uh & 0xffff0000u), ul >> 16, 0u};
    if (h) xaug = (v4u){0u, 0u, 0u, 0u};
    PIN(xaug);
  };
  auto cswr = [&](int tile, f32x4 c) {
    if (lane < 16) { LAS float* p = (LAS float*)(size_t)(lds0 + L_CS + (tile * 32 + lane) * 16 + m * 4); p[0] = c[0]; p[64] = c[1]; }
  };
  auto csrd = [&](int tile) { return __builtin_bit_cast(f32x4, lds128(lds0 + L_CS + (tile * 32 + r32) * 16)); };
  auto waug = [&](f32x4 c4, unsigned b16) {
    const float c = ((c4[0] + c4[1]) + c4[2]) + c4[3];
    const unsigned uh = pk2(c, 0.f);
    const unsigned ul = pk2(c - bflo(uh), 0.f);
    return (v4u){uh | (ul << 16), uh | (b16 << 16), b16, 0u};
  };

  const v4u sw1 = make_srd(w1, DM * FF * 2);
  const int hcol0 = 448 * (j >> 2) + 128 * (j & 3);
  const bool heavy = (j & 3) != 3;
  unsigned bias0, bias1;
  auto bias_load = [&](int P) {
    bias0 = ld2_async(make_srd(b1, FF * 2), r32 * 2, (hcol0 + 64 * P) * 2);
    bias1 = ld2_async(make_srd(b1, FF * 2), r32 * 2, (hcol0 + 64 * P + 32) * 2);
  };
  auto w1dma = [&](int P, int c, unsigned dst, int half) {
    const unsigned vw1 = (lane >> 3) * (FF * 2) + swz, vw1b = vw1 + 8 * (FF * 2) - 1024;
    const unsigned so = (64 * ((c + r) & 7) + 16 * m) * (FF * 2) + (hcol0 + 64 * P) * 2;
    if (half == 0) dma16_a(sw1, vw1, so, dst + m * 2048); else dma16_b(sw1, vw1b, so);
  };
  f32x16 hac[2];
  unsigned rk[4][2];
  auto hsetup = [&](int P) {
#pragma unroll
    for (int p = 0; p < 4; ++p)
#pragma unroll
      for (int c = 0; c < 2; ++c) {
        rk[p][c] = lds0 + (P ? L_WA : w1buf(0)) + ((p + m) & 3) * 2048 + h * 1024 + q4 * 128 + ((c ^ (q4 >> 1)) * 64) + (g & 1) * 32 + p4 * 8;
        PIN(rk[p][c]);
      }
  };
  auto hmm = [&](auto pc, const v4u (&xf)[32], auto&& hook) {
    constexpr int P = pc.value;
    constexpr int D = 4;
    const unsigned cnt = lds0 + L_CNT + 4;
    f32x4 cs[2] = {}, c0, c1;
    v4u wf[D][2], au0, au1;
    unsigned seen = 0;
    hac[0] = (f32x16){}; hac[1] = (f32x16){};
    auto rd = [&](int ks, int t) { wf[ks % D][t] = ldtr2(rk[ks & 3][t] + ((ks >> 2) ^ 4) * 8192, 512); };
#pragma unroll
    for (int ks = 0; ks < D; ++ks) { rd(ks, 0); rd(ks, 1); }
    SBAR();
    static_for<0, 32>([&](auto kc) {
      constexpr int ks = kc.value;
      hook(kc);
      if constexpr (ks == 12) arrive_nowait(cnt + 16);
      if constexpr (ks == 29) seen = peek(cnt);
#pragma unroll
      for (int t = 0; t < 2; ++t) {
        hac[t] = MFMA32(wf[ks % D][t], xf[ks ^ 16], hac[t]);
        if ((ks & 3) == 0 && ks < 28) { cs[t] = MFMA16(sel, wf[ks % D][t], cs[t]); PIN(cs[t]); }
        if (ks == 26) { cs[t] = MFMA16(sel, wf[28 % D][t], cs[t]); PIN(cs[t]); }
        SBAR();
        if constexpr (ks + D < 32) rd(ks + D, t);
        if constexpr (ks == 31) { if (t == 0) { au0 = waug(c0, bias0); PIN(au0); } else { au1 = waug(c1, bias1); PIN(au1); } }
        SBAR();
      }
      if constexpr (ks == 30) {
        PIN(hac[0]); PIN(hac[1]);
        if (__builtin_amdgcn_readfirstlane(seen) < 4) await(cnt, 4);
        asm volatile("" ::: "memory");
        if constexpr (ks == 30) { c0 = csrd(2 * P); c1 = csrd(2 * P + 1); }
      }
      if constexpr (ks == 27) { cswr(2 * P, cs[0]); cswr(2 * P + 1, cs[1]); arrive_nowait(cnt); }
      SBAR();
    });
    hac[0] = MFMA32(au0, xaug, hac[0]);
    hac[1] = MFMA32(au1, xaug, hac[1]);
    SBAR();
  };
  auto hgelu = [&](auto pc, auto&& mid) {
    constexpr int P = pc.value;
    constexpr bool PK = P == 1;
    const float C0 = 2.3022081986f, C1 = C0 * 0.044715f;
    const float ga = -C1 * rstd * rstd * rstd, gb = -C0 * rstd;
    auto mul = [&](f32x2 a, f32x2 b) { return PK ? a * b : (f32x2){MUL1(a[0], b[0]), MUL1(a[1], b[1])}; };
    auto fma = [&](f32x2 a, float b, float c) {
      return PK ? __builtin_elementwise_fma(a, (f32x2){b, b}, (f32x2){c, c}) : (f32x2){FMA1(a[0], b, c), FMA1(a[1], b, c)}; };
#pragma unroll
    for (int t = 0; t < 2; ++t) {
      f32x2 v[8], z[8];
#pragma unroll
      for (int e = 0; e < 8; ++e) { v[e] = (f32x2){hac[t][2 * e], hac[t][2 * e + 1]}; z[e] = mul(v[e], v[e]); }
      SBAR();
#pragma unroll
      for (int e = 0; e < 8; ++e) z[e] = fma(z[e], ga, gb);
      SBAR();
#pragma unroll
      for (int e = 0; e < 8; ++e) z[e] = mul(z[e], v[e]);
      SBAR();
#pragma unroll
      for (int e = 0; e < 8; ++e) z[e] = (f32x2){EXP2(z[e][0]), EXP2(z[e][1])};
      SBAR();
#pragma unroll
      for (int e = 0; e < 8; ++e) z[e] = fma(z[e], sd, sd);
      SBAR();
#pragma unroll
      for (int e = 0; e < 8; ++e) z[e] = (f32x2){__builtin_amdgcn_rcpf(z[e][0]), __builtin_amdgcn_rcpf(z[e][1])};
      SBAR();
#pragma unroll
      for (int e = 0; e < 8; ++e) z[e] = mul(z[e], v[e]);
      SBAR();
#pragma unroll
      for (int s = 0; s < 2; ++s) {
        const v4u f = {pk2(z[4 * s][0], z[4 * s][1]), pk2(z[4 * s + 1][0], z[4 * s + 1][1]), pk2(z[4 * s + 2][0], z[4 * s + 2][1]), pk2(z[4 * s + 3][0], z[4 * s + 3][1])};
        st16_sc1(f, sws, lane * 16, WS_AF + (T * AFT + (hcol0 >> 4) + 4 * P + 2 * t + s) * 1024);
      }
      if (t == 0) mid();
      SBAR();
    }
  };

  if (isG) {
    lane_setup();
    const v4u sw2 = make_srd(w2, FF * DM * 2), swo = make_srd(wo, DM * DM * 2);
    const unsigned vw2 = (lane >> 3) * 1024 + swz, vw2b = vw2 + 7 * 1024;
    const int ro3 = rt % 3;
    auto kmap = [&](int kk) {
      if (kk < 16) return 8 * (((kk >> 2) + rt) & 3) + (kk & 3);
      const int p = ((kk - 16) >> 2) + ro3;
      return 8 * (p >= 3 ? p - 3 : p) + 4 + (kk & 3);
    };
    auto omap = [&](int i) { return 2 * ((i + rt) & 3) + (i >> 2); };
    auto wdmah = [&](int kk, int half) {
      const unsigned so = (kk < 28 ? 448 * m + 16 * kmap(kk) : 128 * m + 16 * omap(kk - 28)) * 1024 + 128 * ct;
      if (half == 0) dma16_a(kk < 28 ? sw2 : swo, vw2, so, lds0 + L_WG + m * PART + (kk % RD) * 2048); else dma16_b(kk < 28 ? sw2 : swo, vw2b, so);
    };
    auto wdma = [&](int kk) { wdmah(kk, 0); wdmah(kk, 1); };
    {
      const v4u swq = make_srd(wqkv, DM * 3 * DM * 2);
      const unsigned vx0 = (lane >> 3) * 1024 + (((lane & 7) ^ g) * 16), vx1 = (7 + (lane >> 3)) * 1024 + (((lane & 7) ^ (4 + g)) * 16);
      const unsigned vwq = (lane >> 2) * (3 * DM * 2) + p4 * 16;
      auto piece = [&](int s, int i) {
        const int kc = (s + r) & 7;
        if (i < 4) {
          const unsigned so = (128 * r + 32 * m + 16 * (i >> 1)) * 1024 + kc * 128;
          if (i & 1) dma16_b(sx, vx1, so); else dma16_a(sx, vx0, so, lds0 + xbuf(s) + m * 4096 + (i >> 1) * 2048);
        } else if (i < 7) dma16(swq, vwq, (64 * kc + 16 * m) * (3 * DM * 2) + (512 * (i - 4) + 32 * j) * 2, lds0 + qbuf(s) + ((i - 4) * 4 + m) * 1024);
        else w1dma(0, s, lds0 + w1buf(s), i - 7);
      };
#pragma unroll
      for (int i = 0; i < 18; ++i) { piece(i / 9, i % 9); if (i == 8) __builtin_amdgcn_s_barrier(); }
      __builtin_amdgcn_s_setprio(2);
      hsetup(0);
      f32x4 cs[2] = {};
      v4u xf[4], wf[4][2];
      hac[0] = (f32x16){}; hac[1] = (f32x16){};
      static_for<0, 8>([&](auto sc) {
        constexpr int s = sc.value;
        auto fill = [&](int i) {
          if constexpr (s < 6) piece(s + 2, i);
          if constexpr (s == 6) {
            if (i == 0) { stat_load(); bias_load(0); }
            else if (i < 5) { if (heavy) w1dma(1, (i - 1) >> 1, lds0 + L_WA + ((i - 1) >> 1) * 8192, (i - 1) & 1); }
            else wdmah((i - 5) >> 1, (i - 5) & 1);
          }
          if constexpr (s == 7) {
            if (heavy && i < 4) w1dma(1, 2 + (i >> 1), lds0 + L_WA + (2 + (i >> 1)) * 8192, i & 1);
            if (heavy && i >= 4) { w1dma(1, i, lds0 + L_WA + i * 8192, 0); w1dma(1, i, 0, 1); }
          }
          SBAR();
        };
        auto rd = [&](int p) {
          xf[p] = lds128(xbuf(s) + xo[p]);
          wf[p][0] = ldtr2(rk[p][0] + (s % 3) * PART, 512); wf[p][1] = ldtr2(rk[p][1] + (s % 3) * PART, 512);
          SBAR();
        };
        if constexpr (s < 7) wait_vm<9>(); else { if (heavy) wait_vm<11>(); else wait_vm<7>(); }
        bar();
        rd(0);
        if constexpr (s < 7) fill(0);
        rd(1);
        if constexpr (s < 7) fill(1);
        if constexpr (s == 2) get_tag();
#pragma unroll
        for (int p = 0; p < 4; ++p) {
#pragma unroll
          for (int t = 0; t < 2; ++t) {
            hac[t] = MFMA32(wf[p][t], xf[p], hac[t]);
            if (p == 0) { cs[t] = MFMA16(sel, wf[p][t], cs[t]); PIN(cs[t]); }
            SBAR();
            if (p < 2 && t == 0) rd(p + 2);
            if (s == 7) fill(2 * p + t); else if (2 * p + t < 7) fill(2 + 2 * p + t);
          }
          if (s == 7 && p == 1) {
            cswr(0, cs[0]); cswr(1, cs[1]);
            bar();
          }
        }
        if constexpr (s == 7) {
          const f32x4 c0 = csrd(0), c1 = csrd(1);
          if (heavy) wait_vm<20>(); else wait_vm<4>();
          PIN(st); PIN(bias0); PIN(bias1);
          row_stats(); make_xaug();
          hac[0] = MFMA32(waug(c0, bias0), xaug, hac[0]);
          hac[1] = MFMA32(waug(c1, bias1), xaug, hac[1]);
          SBAR();
        }
      });
    }
    if (heavy) { wait_vm<0>(); arrive_nowait(lds0 + L_CNT + 16); }
    hgelu(std::integral_constant<int, 0>{}, [&]() {});
    __builtin_amdgcn_s_setprio(0);
    unsigned rb2[2];
#pragma unroll
    for (int c = 0; c < 2; ++c) rb2[c] = lds0 + L_WG + m * PART + h * 512 + q4 * 128 + ((c ^ (q4 >> 1)) * 64) + (g & 1) * 32 + p4 * 8;
    v4u wq[36][2];
    auto wrd = [&](int kk) {
#pragma unroll
      for (int c = 0; c < 2; ++c) wq[kk][c] = ldtr2(rb2[c] + (kk % RD) * 2048, 1024);
    };
    auto wgot = [&](int kk) { PIN(wq[kk][0]); PIN(wq[kk][1]); };
    const int ftile = 2 * rt + ((lane >> 2) & 1);
    const int T0 = 4 * r - 8 + 2 * m;
    const bool live = T0 >= 0;
    auto kv_fetch = [&]() {
      const unsigned src = WS_KV + (j * 64 + (live ? T0 : 0)) * 4096, vk = live ? lane * 16 : h * 512, vv = live ? lane * 16 : 0x40000000u;
#pragma unroll
      for (int i = 0; i < 8; ++i)
        dma16_sc1(sws, (i & 2) ? vv : vk, src + (live ? i : (i & 3)) * 1024, lds0 + L_KV + (2 * m + (i >> 2)) * KVT + (i & 3) * 1024 + ((i & 3) == 3 ? VB - 1024 : 0));
      const unsigned on = (lane < 2 || live) ? one2 : 0u;
      if (lane < 6) sts128(lds0 + L_KV + (lane < 2 ? 8 + m : 2 * m + ((lane - 2) >> 1)) * KVT + 2048 + (lane & 1) * VB + 1024, (v4u){on, on, on, on});
    };
    v4u af[36][2];
    v2u fl;
    const unsigned a0 = WS_AF + (2 * rt * AFT + 28 * m) * 1024, o0 = WS_AF + (2 * rt * AFT + 112 + 8 * m) * 1024;
    fl = ld8_sc1_async(sws, flag_off(FL_KV, live ? T0 + (lane & 1) : 0, j));
    for (int kk = 2; kk < 6; ++kk) wdma(kk);
    wait_vmf<8>(fl);
    if (lane == 0) __hip_atomic_store(flag(FL_H0, T, j), tag, RLX_AGENT);
    if (!__all(fl[0] == taglo && fl[1] == tag32)) poll_flags(flag(FL_KV, live ? T0 + (lane & 1) : 0, j), tag);
    if (heavy) await(lds0 + L_CNT + 20, 4);
    kv_fetch();
    wdma(6); wdma(7);
    static_for<0, NR + 1>([&](auto kc) {
      constexpr int k = kc.value;
      constexpr int refills = k >= 2 ? 2 * (k - 1) : 0;
      if constexpr (k < NR) {
        wait_vm<(k < 6 ? 2 * (5 - (k < 2 ? 1 : k)) + 13 : 2 * (7 - k)) + refills + (k > HLD ? 1 : 0)>();
        wrd(k);
      }
      if constexpr (k == KVS) { wait_vm<4 + refills>(); arrive_nowait(lds0 + L_CNT); }
      if constexpr (k == HLD) fl = ld8_sc1_async(sws, flag_off(FL_H0, ftile, 4 * m + p4));
      if constexpr (k >= 1) { wgot(k - 1); wdma(k - 1 + RD); }
    });
    wait_vmf<2 * (NR + 1 - HLD)>(fl);
    if (!__all(fl[0] == taglo && fl[1] == tag32)) poll_flags(flag(FL_H0, ftile, 4 * m + p4), tag);
#pragma unroll
    for (int kk = 0; kk < AW; ++kk) {
      af[kk][0] = ld16_sc1_async(sws, lane * 16, a0 + kmap(kk) * 1024);
      af[kk][1] = ld16_sc1_async(sws, lane * 16, a0 + (AFT + kmap(kk)) * 1024);
    }
    __builtin_amdgcn_s_setprio(2);
    f32x16 c2[2][2] = {};
    auto mm2 = [&](int kk, const v4u (&a)[2], auto&& fill) {
      SBAR();
#pragma unroll
      for (int i = 0; i < 4; ++i) { c2[i >> 1][i & 1] = MFMA32(wq[kk][i & 1], a[i >> 1], c2[i >> 1][i & 1]); SBAR(); fill(i); SBAR(); }
    };
    static_assert(FLA <= 4, "one flag register");
    static_for<0, 32>([&](auto kc) {
      constexpr int kk = kc.value;
      wait_vm2<(kk < AW) ? 2 * (AW - 1 - kk) + g2_range(0, kk) : g2_range(kk - AW + 1, kk)>(af[kk][0], af[kk][1]);
      if constexpr (kk == H1_CK || kk == O0_CK || kk == O1_CK) {
        constexpr int kind = kk == H1_CK ? FL_H1 : kk == O0_CK ? FL_O0 : FL_O1;
        wait_vmf<g2_range(kk - FLA, kk) - 1>(fl);
        if (!__all(fl[0] == taglo && fl[1] == tag32)) poll_flags(flag(kind, ftile, 4 * m + p4), tag);
      }
      if constexpr (fl_load(kk)) fl = ld8_sc1_async(sws, flag_off(kk + FLA == H1_CK ? FL_H1 : kk + FLA == O0_CK ? FL_O0 : FL_O1, ftile, 4 * m + p4));
      if constexpr (kk == RES_LD) res_loads();
      if constexpr (kk == 20) res_sum();
      constexpr bool dw = kk >= 1 && kk - 1 + NR + RD < 36;
      if constexpr (dw) wgot(kk - 1 + NR);
      mm2(kk, af[kk], [&](int i) {
        if constexpr (dw) { if (i < 2) wdmah(kk - 1 + NR + RD, i); }
        if constexpr (kk + NR < 28) { if (i == 1) wrd(kk + NR); }
        if constexpr (kk >= 24) { if (i == 0) wrd(kk + 4); }
        if constexpr (kk + AW < 36) {
          if (i >= 2) af[kk + AW][i - 2] = ld16_sc1_async(sws, lane * 16, (kk + AW < 28 ? a0 + kmap(kk + AW) * 1024 : o0 + omap(kk + AW - 28) * 1024) + AFT * (i - 2) * 1024);
        }
        if constexpr (kk == 31) PIN(c2[i >> 1][i & 1]);
      });
    });
    for (int kk = 32; kk < 36; ++kk) wgot(kk);
    unsigned pa = lds0 + L_WG + m * PART + lane * 16;
    PIN(pa);
    auto put = [&](int qn, int qd) {
      const f32x16& c = c2[qn >> 1][qn & 1];
      sts128(pa + (qn * 4 + qd) * 66 * 16, __builtin_bit_cast(v4u, (f32x4){c[4 * qd], c[4 * qd + 1], c[4 * qd + 2], c[4 * qd + 3]}));
    };
    static_for<0, 4>([&](auto qc) {
      constexpr int qn = qc.value;
      if constexpr ((qn & 1) == 0) { wait_vm2<0>(af[32][qn >> 1], af[33][qn >> 1]); PIN(af[34][qn >> 1]); PIN(af[35][qn >> 1]); }
      SBAR();
#pragma unroll
      for (int i = 0; i < 4; ++i) {
        c2[qn >> 1][qn & 1] = MFMA32(wq[32 + i][qn & 1], af[32 + i][qn >> 1], c2[qn >> 1][qn & 1]);
        SBAR();
        if (qn > 0 && i >= 2) { put(qn - 1, 2 * i - 4); put(qn - 1, 2 * i - 3); }
        SBAR();
      }
    });
#pragma unroll
    for (int qd = 0; qd < 4; ++qd) put(3, qd);
  } else {
    __builtin_amdgcn_s_setprio(1);
    lane_setup();
    v4u xr[2];
    xr[0] = ld16_async(sx, lane * 16, (128 * r + 8 * j + 2 * m) * 1024); xr[1] = ld16_async(sx, lane * 16, (128 * r + 8 * j + 2 * m + 1) * 1024);
    unsigned warm;
    {
      warm = ld2_async(make_srd((const void*)(pc & ~127ull), CODE_BYTES), lane < 2 ? lane * 128 : 0x40000000u, (idx * 4 + m) * 256);
    }
    __builtin_amdgcn_s_barrier();
    v2u ep = ld8_sc1_async(sws, m == 0 ? WS_EPOCH : 0x40000000u);
    float sval;
    const int wi = blockIdx.x * 4 + m;
    const u64 wbase = wi < 448 ? (u64)w1 + wi * 4096 : wi < 896 ? (u64)w2 + (wi - 448) * 4096 : (u64)wo + (wi - 896) * 4096;
    auto make_stats = [&]() {
      wait_vm2<1>(xr[0], xr[1]);
      PIN(warm);
      float piv[2], a[4];
#pragma unroll
      for (int u = 0; u < 2; ++u) {
        piv[u] = rdl(bflo(xr[u][0]), 0);
        float s1 = 0.f, s2 = 0.f;
#pragma unroll
        for (int i = 0; i < 8; ++i) { const float d = ((i & 1) ? bfhi(xr[u][i >> 1]) : bflo(xr[u][i >> 1])) - piv[u]; s1 += d; s2 = __builtin_fmaf(d, d, s2); }
        a[2 * u] = s1; a[2 * u + 1] = s2;
      }
#pragma unroll
      for (int i = 0; i < 4; ++i) a[i] += dpp<0xB1>(a[i]);
#pragma unroll
      for (int i = 0; i < 4; ++i) a[i] += dpp<0x4E>(a[i]);
      float t = p4 == 0 ? a[0] : p4 == 1 ? a[1] : p4 == 2 ? a[2] : a[3];
      t += dpp<0x114>(t); t += dpp<0x118>(t);
      const auto s16 = __builtin_amdgcn_permlane16_swap(asu(t), asu(t), false, false);
      t = asf(s16[0]) + asf(s16[1]);
      const auto s32 = __builtin_amdgcn_permlane32_swap(asu(t), asu(t), false, false);
      t = (asf(s32[0]) + asf(s32[1])) * (1.f / DM);
      const float e1 = dpp<0xA0>(t);
      sval = (p4 & 1) ? __builtin_fmaxf(__builtin_fmaf(-e1, e1, t), 0.f) + 1e-5f : t + (p4 ? piv[1] : piv[0]);
      PIN(sval);
    };
    const unsigned acnt = lds0 + L_CNT;
    const unsigned vpos = 2048 + (g & 1) * 512 + h * 256 + (lane & 15) * 16;
    v4u qf[2], am0, am1;
    u64 um[16];
    auto masks = [&]() {
      const int dq = r32 - 4 * h;
#pragma unroll
      for (int u = 0; u < 16; ++u) { um[u] = __builtin_amdgcn_ballot_w64((u & 3) + 8 * (u >> 2) > dq); pin_s(um[u]); }
#pragma unroll
      for (int d = 0; d < 4; ++d) {
        am0[d] = __builtin_amdgcn_inverse_ballot_w64(um[2 * d]) ? 0xffffffffu : __builtin_amdgcn_inverse_ballot_w64(um[2 * d + 1]) ? 0xffff0000u : 0u;
        am1[d] = __builtin_amdgcn_inverse_ballot_w64(um[8 + 2 * d]) ? 0xffffffffu : __builtin_amdgcn_inverse_ballot_w64(um[9 + 2 * d]) ? 0xffff0000u : 0u;
      }
      PIN(am0); PIN(am1);
    };
    {
      v4u xf[32];
      f32x16 acc[3] = {};
      f32x4 cs[3] = {};
      v4u wf[4][3];
      unsigned wb[4];
#pragma unroll
      for (int p = 0; p < 4; ++p) { wb[p] = qbuf(0) + ((p + m) & 3) * 1024 + (8 * h + q4) * 64 + (g & 1) * 32 + p4 * 8; PIN(wb[p]); }
      auto rd = [&](int ks, int t) {
        if (t == 0) xf[ks] = lds128(xbuf(ks >> 2) + xo[ks & 3]);
        wf[ks & 3][t] = ldtr2(wb[ks & 3] + ((ks >> 2) % 3) * 12288 + t * 4096, 256);
      };
      auto mm = [&](int ks, int t) {
        acc[t] = t == 2 ? MFMA32(xf[ks], wf[ks & 3][t], acc[t]) : MFMA32(wf[ks & 3][t], xf[ks], acc[t]);
        if ((ks & 3) == 0) { cs[t] = MFMA16(sel, wf[ks & 3][t], cs[t]); PIN(cs[t]); }
        SBAR();
      };
      make_stats();
      bar();
#pragma unroll
      for (int t = 0; t < 6; ++t) rd(t / 3, t % 3);
      SBAR();
      static_for<0, 8>([&](auto sc) {
        constexpr int s = sc.value;
#pragma unroll
        for (int p = 0; p < 2; ++p) {
#pragma unroll
          for (int t = 0; t < 3; ++t) { mm(4 * s + p, t); rd(4 * s + p + 2, t); SBAR(); }
          if (s == 7 && p == 0) { cswr(4, cs[0]); cswr(5, cs[1]); cswr(6, cs[2]); SBAR(); }
        }
        if constexpr (s == 0) {
          wait_vmf<0>(ep);
          if (m == 0 && lane < 10)
            *(LAS unsigned*)(size_t)(lds0 + L_CNT + lane * 4) = lane == 8 ? ep[0] * 0x9E3779B1u + 0x7F4A7C15u : lane == 9 ? ep[1] * 0x85EBCA77u + 0x165667B1u : 0u;
        }
        if constexpr (s == 1) {
          get_tag();
          if ((lane >> 2) == 3) {
            const v2u gr = {asu(sval), tag32};
            __builtin_amdgcn_raw_buffer_store_b64(gr, rws, WS_STATS + (128 * r + 8 * j + 2 * m) * 16 + p4 * 8, 0, 16);
          }
        }
        if constexpr (s == 1) {
          warm = ld2_async(make_srd((const void*)wbase, 4096), lane * 128, 0);
        }
        if constexpr (s == 5) stat_load();
        if constexpr (s == 6) {
          wait_vm1<0>(st);
          PIN(warm);
          row_stats();
        }
        mm(4 * s + 2, 0); mm(4 * s + 2, 1);
        bar();
        if constexpr (s < 7) {
          mm(4 * s + 2, 2);
          rd(4 * s + 4, 0); rd(4 * s + 4, 1); rd(4 * s + 4, 2); SBAR();
#pragma unroll
          for (int t = 0; t < 3; ++t) { mm(4 * s + 3, t); rd(4 * s + 5, t); SBAR(); }
        }
      });
      __builtin_amdgcn_s_setprio(3);
      const f32x4 c5 = csrd(5), c6 = csrd(6), c4 = csrd(4);
      SBAR();
      mm(30, 2); mm(31, 1);
      make_xaug();
      SBAR();
      mm(31, 2); mm(31, 0);
      if (h == 0) *(LAS float*)(size_t)(lds0 + L_RS + (m * 32 + r32) * 4) = rstd;
      acc[1] = MFMA32(waug(c5, 0u), xaug, acc[1]);
      acc[2] = MFMA32(xaug, waug(c6, 0u), acc[2]);
      acc[0] = MFMA32(waug(c4, 0u), xaug, acc[0]);
      SBAR();
      asm volatile("" ::: "memory");
      f32x4 rs[4];
#pragma unroll
      for (int qd = 0; qd < 4; ++qd) rs[qd] = __builtin_bit_cast(f32x4, lds128(lds0 + L_RS + (m * 32 + 8 * qd + 4 * h) * 4));
      const unsigned kvo = WS_KV + (j * 64 + T) * 4096;
#pragma unroll
      for (int e = 0; e < 16; ++e) acc[1][e] = MUL1(acc[1][e], rstd);
#pragma unroll
      for (int e = 0; e < 2; ++e) {
        const v4u f = pack8(acc[1], e);
        st16_sc1(f, sws, lane * 16, kvo + e * 1024);
        sts128(lds0 + L_KV + (8 + m) * KVT + e * 1024 + lane * 16, f);
      }
#pragma unroll
      for (int qd = 0; qd < 4; ++qd)
#pragma unroll
        for (int e = 0; e < 4; ++e) acc[2][4 * qd + e] = MUL1(acc[2][4 * qd + e], rs[qd][e]);
#pragma unroll
      for (int s = 0; s < 2; ++s) {
        const v4u f = pack8(acc[2], s);
        st16_sc1(f, sws, vpos, kvo + s * 1024);
        sts128(lds0 + L_KV + (8 + m) * KVT + s * VB + vpos, f);
      }
      SBAR();
      const float qs = rstd * (0.25f * 1.4426950408889634f);
#pragma unroll
      for (int e = 0; e < 16; ++e) acc[0][e] = MUL1(acc[0][e], qs);
      qf[0] = pack8(acc[0], 0); qf[1] = pack8(acc[0], 1);
      PIN(qf[0]); PIN(qf[1]);
      arrive_nowait(acnt);
      if (heavy) {
        hsetup(1);
        await(acnt + 16, 4);
        __builtin_amdgcn_s_setprio(1);
        hmm(std::integral_constant<int, 1>{}, xf, [&](auto kc) {
          constexpr int ks = kc.value;
          if constexpr (ks == KVPUB) {
            wait_vm<0>();
            if (lane == 0) __hip_atomic_store(flag(FL_KV, T, j), tag, RLX_AGENT);
          }
          if constexpr (ks == KVPUB + 2) bias_load(1);
          if constexpr (ks == 31) { wait_vm<0>(); PIN(bias0); PIN(bias1); }
        });
        __builtin_amdgcn_s_setprio(3);
        hgelu(std::integral_constant<int, 1>{}, [&]() {});
      } else {
        wait_vm<0>();
        if (lane == 0) __hip_atomic_store(flag(FL_KV, T, j), tag, RLX_AGENT);
      }
    }
    masks();
    const unsigned kb = lds0 + L_KV + m * KVT + lane * 16;
    unsigned vb[2];
#pragma unroll
    for (int e = 0; e < 2; ++e) { vb[e] = lds0 + L_KV + m * KVT + 2048 + ((g & 1) ? 1024 : e * 512 + h * 256 + (lane & 15) * 16); PIN(vb[e]); }
    bool pend = true;
    if (__builtin_amdgcn_readfirstlane(peek(acnt)) < 8) {
      wait_vm<0>();
      if (lane == 0) __hip_atomic_store(flag(FL_H1, T, j), tag, RLX_AGENT);
      pend = false;
      await(acnt, 8);
    }
    asm volatile("" ::: "memory");
    __builtin_amdgcn_s_setprio(3);
    const f32x16 zero = {};
    auto o_store = [&](f32x16& o, int e) {
      const float rl = __builtin_amdgcn_rcpf(o[8]);
#pragma unroll
      for (int u = 0; u < 8; ++u) o[u] = MUL1(o[u], rl);
      st16_sc1(pack8(o, 0), sws, lane * 16, WS_AF + (T * AFT + 112 + 2 * j + e) * 1024);
    };
    int redo = 0;
    {
      f32x16 o[2], sa, sb, su;
      v4u kn, ku, va, vc, vn0, vn1, vu0, vu1, p0, p1, pu;
      auto kaddr = [&](int n) { return kb + ((n & 7) + 1) * KVT + (n >> 3) * 1024; };
      auto vaddr = [&](int n) { return vb[n >> 3] + ((n & 7) + 1) * KVT; };
      kn = lds128(kaddr(0)); vn0 = lds128(vaddr(0)); vn1 = lds128(vaddr(0) + VB);
      sa = MFMA32(kn, qf[0], zero);
      kn = lds128(kaddr(1));
      SBAR();
      auto step = [&](auto nc, f32x16& sc, f32x16& sn) {
        constexpr int n = nc.value, e = n >> 3, i = n & 7;
        if constexpr (n < 15) sn = MFMA32(kn, qf[(n + 1) >> 3], zero);
        if constexpr (n < 14) kn = lds128(kaddr(n + 2));
        if constexpr (i == 5) ku = lds128(kb + e * 1024);
        v4u v0 = vn0, v1 = vn1;
        if constexpr (n < 15) { vn0 = lds128(vaddr(n + 1)); vn1 = lds128(vaddr(n + 1) + VB); }
        if constexpr (i == 6) { vu0 = lds128(vb[e]); vu1 = lds128(vb[e] + VB); }
        SBAR();
        if constexpr (i == 7) {
#pragma unroll
          for (int u = 0; u < 16; ++u) sc[u] = __builtin_amdgcn_inverse_ballot_w64(um[u]) ? su[u] : sc[u];
          PIN(sc); SBAR();
        }
#pragma unroll
        for (int u = 0; u < 6; ++u) sc[u] = EXP2(sc[u]);
        PIN(sc); SBAR();
        if constexpr (n == 8) { o[0] = MFMA32(vc, p1, o[0]); SBAR(); }
        else if constexpr (n > 0) { o[e] = MFMA32(vc, p1, o[e]); SBAR(); }
        sc[6] = EXP2(sc[6]); sc[7] = EXP2(sc[7]);
        p0 = pack8(sc, 0);
        sc[8] = EXP2(sc[8]); sc[9] = EXP2(sc[9]);
        if constexpr (i == 7) { va = p0 & am0; p0 &= ~am0; PIN(va); }
        PIN(sc); PIN(p0); SBAR();
        if constexpr (n == 8) { o[0] = MFMA32(vu1, pu, o[0]); SBAR(); }
        o[e] = MFMA32(v0, p0, i == 0 ? zero : o[e]);
        SBAR();
        if constexpr (i == 6) { su = MFMA32(ku, qf[e], zero); SBAR(); }
#pragma unroll
        for (int u = 10; u < 13; ++u) sc[u] = EXP2(sc[u]);
        if constexpr (i == 7) { PIN(sc); SBAR(); o[e] = MFMA32(vu0, va, o[e]); SBAR(); }
#pragma unroll
        for (int u = 13; u < 16; ++u) sc[u] = EXP2(sc[u]);
        p1 = pack8(sc, 1); vc = v1;
        if constexpr (i == 7) { pu = p1 & am1; p1 &= ~am1; PIN(pu); }
        PIN(p1); SBAR();
        if constexpr (n == 8) {
          if (__all(o[0][8] >= 0x1p-30f && o[0][8] <= 0x1p30f)) o_store(o[0], 0); else redo = 1;
          SBAR();
        }
        if constexpr (n == 10) pend = !redo;
        if constexpr (i == 3 || i == 5 || i == 7) {
          if (pend && (i == 7 || (__builtin_amdgcn_s_getreg(7 | (3 << 11)) & 15) == 0)) {
            wait_vm<0>();
            if (lane == 0) __hip_atomic_store(flag(FL_H1 + e, T, j), tag, RLX_AGENT);
            pend = false;
          }
        }
      };
      static_for<0, 8>([&](auto hc) {
        step(std::integral_constant<int, 2 * hc.value>{}, sa, sb);
        step(std::integral_constant<int, 2 * hc.value + 1>{}, sb, sa);
      });
      o[1] = MFMA32(vc, p1, o[1]);
      o[1] = MFMA32(vu1, pu, o[1]);
      if (__all(o[1][8] >= 0x1p-30f && o[1][8] <= 0x1p30f)) o_store(o[1], 1); else redo |= 2;
    }
    if (redo) {
#pragma unroll 1
      for (int e = 0; e < 2; ++e) {
        if (!((redo >> e) & 1)) continue;
        const v4u q = e ? qf[1] : qf[0];
        const unsigned vbe = e ? vb[1] : vb[0];
        auto merged = [&](const f32x16& c) {
          f32x16 sl = MFMA32(lds128(kb + 8 * KVT + e * 1024), q, c);
          const f32x16 su = MFMA32(lds128(kb + e * 1024), q, c);
          static_for<0, 16>([&](auto u) { sl[u.value] = upper<u.value>() ? su[u.value] : sl[u.value]; });
          return sl;
        };
        float mx = -INFINITY;
#pragma unroll 1
        for (int i = 0; i < 8; ++i) {
          const f32x16 sc = i ? MFMA32(lds128(kb + i * KVT + e * 1024), q, zero) : merged(zero);
#pragma unroll
          for (int u = 0; u < 16; u += 2) mx = max3(mx, sc[u], sc[u + 1]);
        }
        const auto sw = __builtin_amdgcn_permlane32_swap(asu(mx), asu(mx), false, false);
        mx = __builtin_fmaxf(asf(sw[0]), asf(sw[1]));
        f32x16 negm, o;
#pragma unroll
        for (int u = 0; u < 16; ++u) negm[u] = -mx;
        {
          f32x16 sc = merged(negm);
#pragma unroll
          for (int u = 0; u < 16; ++u) sc[u] = EXP2(sc[u]);
          const v4u p0 = pack8(sc, 0), p1 = pack8(sc, 1);
          o = MFMA32(lds128(vbe), p0 & am0, zero);
          o = MFMA32(lds128(vbe + VB), p1 & am1, o);
          o = MFMA32(lds128(vbe + 8 * KVT), p0 & ~am0, o);
          o = MFMA32(lds128(vbe + 8 * KVT + VB), p1 & ~am1, o);
        }
#pragma unroll 1
        for (int i = 1; i < 8; ++i) {
          f32x16 sc = MFMA32(lds128(kb + i * KVT + e * 1024), q, negm);
#pragma unroll
          for (int u = 0; u < 16; ++u) sc[u] = EXP2(sc[u]);
          o = MFMA32(lds128(vbe + i * KVT), pack8(sc, 0), o);
          o = MFMA32(lds128(vbe + i * KVT + VB), pack8(sc, 1), o);
        }
        o_store(o, e);
      }
      wait_vm<0>();
      if (lane == 0) __hip_atomic_store(flag(FL_O0, T, j), tag, RLX_AGENT);
    }
    wait_vm<0>();
    if (lane == 0) __hip_atomic_store(flag(FL_O1, T, j), tag, RLX_AGENT);
    res_loads();
    wait_vm<0>();
    res_sum();
    if (blockIdx.x == 24 && wave == 7) {
#pragma unroll 1
      for (int i = 0; i < 4; ++i) poll_flags(flag(FL_KV, 4 * (4 * i + g), lane & 15), tag);
      if (lane == 0) __hip_atomic_store((GAS u64*)(ws + WS_EPOCH), tag, RLX_AGENT);
    }
  }
  {
    int t2 = threadIdx.x; PIN(t2);
    const int row = t2 >> 3, ch = t2 & 7;
    unsigned a = lds0 + L_WG + (((row >> 5) * 8 + ch) * 66 + (row & 31)) * 16, oo = (64 * rt + row) * 1024 + (64 * ct + ch * 8) * 2;
    PIN(a); PIN(oo);
    bar();
    f32x4 pl[4], ph[4];
#pragma unroll
    for (int w = 0; w < 4; ++w) { pl[w] = __builtin_bit_cast(f32x4, lds128(a + w * PART)); ph[w] = __builtin_bit_cast(f32x4, lds128(a + w * PART + 512)); }
    SBAR();
#pragma unroll
    for (int w = 0; w < 4; ++w) { lo += pl[w]; hi += ph[w]; }
    const v4u o = {pk2(lo[0], lo[1]), pk2(lo[2], lo[3]), pk2(hi[0], hi[1]), pk2(hi[2], hi[3])};
    __builtin_amdgcn_raw_buffer_store_b128(o, __builtin_amdgcn_make_buffer_rsrc(out, 0, SEQ * DM * 2, 0x00020000), oo, 0, 16);
  }
}

extern "C" void kernel_launch(void* const* d_in, const int* in_sizes, int n_in, void* d_out, int out_size, void* d_ws, size_t ws_size,
                              hipStream_t stream) {
  (void)in_sizes; (void)n_in; (void)out_size; (void)ws_size;
  (void)hipFuncSetAttribute((const void*)fused_block, hipFuncAttributeMaxDynamicSharedMemorySize, LDS_BYTES);
  fused_block<<<256, 512, LDS_BYTES, stream>>>(d_in[0], d_in[1], d_in[2], d_in[4], d_in[5], d_in[6], d_in[7], d_out, (char*)d_ws);
}
```
